```python
import torch

def _softmax(x, axis=-1):
    return torch.softmax(x, dim=axis)
def _gelu(x):
    return torch.nn.functional.gelu(x, approximate="tanh")
def _silu(x):
    return torch.nn.functional.silu(x)
def _sigmoid(x):
    return torch.sigmoid(x)
def _layernorm(x, axis=-1, eps=1e-5):
    m = x.mean(dim=axis, keepdim=True)
    v = x.var(dim=axis, keepdim=True, unbiased=False)
    return (x - m) / torch.sqrt(v + eps)
def _scatter_add(tgt, idx, src, axis=0):
    return tgt.clone().index_add_(axis, idx, src)
def _segment_sum(src, seg, n):
    out = torch.zeros((n,) + src.shape[1:], dtype=src.dtype)
    return out.index_add_(0, seg, src)
def _pad(x, pw):
    flat = tuple(v for pair in reversed(pw) for v in pair)
    return torch.nn.functional.pad(x, flat)

kernel_name = 'interleave_layers__sliding_block__swiglu_ffn__prompt'

def setup_inputs():

    _w = (lambda a: (lambda t: t.to(torch.bfloat16) if t.is_floating_point() else t)(torch.as_tensor(a)))
    def _A_setup():
        B, S, D = 1, 2048, 512
        return {
            "x": _w((lambda *s: torch.randn(*s, dtype=torch.bfloat16) * 0.05)(B, S, D)),
            "wqkv": _w((lambda *s: torch.randn(*s, dtype=torch.bfloat16) * 0.05)(D, 3 * D)),
            "wo": _w((lambda *s: torch.randn(*s, dtype=torch.bfloat16) * 0.05)(D, D)),
        }
    _A_in = _A_setup()

    def _B_setup():
        B, S, D, F = 1, 2048, 512, 1792
        return {
            "x": _w((lambda *s: torch.randn(*s, dtype=torch.bfloat16) * 0.05)(B, S, D)),
            "w_gate": _w((lambda *s: torch.randn(*s, dtype=torch.bfloat16) * 0.05)(D, F)),
            "w_up": _w((lambda *s: torch.randn(*s, dtype=torch.bfloat16) * 0.05)(D, F)),
            "w_down": _w((lambda *s: torch.randn(*s, dtype=torch.bfloat16) * 0.05)(F, D)),
        }
    _B_in = _B_setup()

    return {
        "A_x": _A_in["x"],
        "A_wqkv": _A_in["wqkv"],
        "A_wo": _A_in["wo"],
        "B_x": _B_in["x"],
        "B_w_gate": _B_in["w_gate"],
        "B_w_up": _B_in["w_up"],
        "B_w_down": _B_in["w_down"],
    }

def reference(A_x, A_wqkv, A_wo, B_x, B_w_gate, B_w_up, B_w_down):
    def _A_ref(x, wqkv, wo):
        B, S, D = x.shape
        H = 32; Dh = D // H; W = 256
        qkv = (lambda x, *d: x.permute(*d))((x @ wqkv).reshape(B, S, 3, H, Dh), 2, 0, 3, 1, 4)
        q, k, v = qkv[0], qkv[1], qkv[2]
        scores = torch.einsum("bhqd,bhkd->bhqk", q, k) * (Dh ** -0.5)
        idx = torch.arange(S)
        mask = (idx[:, None] - idx[None, :] >= 0) & (idx[:, None] - idx[None, :] < W)
        scores = torch.where(mask[None, None], scores, -1e9)
        p = _softmax(scores, axis=-1)
        o = torch.einsum("bhqk,bhkd->bhqd", p, v)
        return (lambda x, *d: x.permute(*d))(o, 0, 2, 1, 3).reshape(B, S, D) @ wo

    def _B_ref(x, w_gate, w_up, w_down):
        g = _silu(x @ w_gate)
        u = x @ w_up
        return (g * u) @ w_down

    x = A_x
    x = x + _A_ref(_layernorm(x, axis=-1), A_wqkv, A_wo)
    x = x + _B_ref(_layernorm(x, axis=-1), B_w_gate, B_w_up, B_w_down)
    x = x + _A_ref(_layernorm(x, axis=-1), A_wqkv, A_wo)
    x = x + _B_ref(_layernorm(x, axis=-1), B_w_gate, B_w_up, B_w_down)
    return x
```

```cpp
#include <hip/hip_runtime.h>
#include <hip/hip_bf16.h>
#include <stdint.h>

typedef unsigned short u16;
__device__ __forceinline__ float bf2f(u16 b) { return __uint_as_float(((unsigned)b) << 16); }
__device__ __forceinline__ u16 f2bf(float f) { unsigned u = __float_as_uint(f); u += 0x7FFFu + ((u >> 16) & 1u); return (u16)(u >> 16); }

constexpr int S = 2048, D = 512, F = 1792, H = 32, DH = 16, W = 256;

__global__ void k_ln(const u16* x, u16* y) {
  int r = blockIdx.x, t = threadIdx.x;
  float v[8]; float s = 0;
  for (int i = 0; i < 8; ++i) { v[i] = bf2f(x[r * D + t * 8 + i]); s += v[i]; }
  for (int m = 32; m >= 1; m >>= 1) s += __shfl_xor(s, m);
  float mean = s / D; float q = 0;
  for (int i = 0; i < 8; ++i) { float d = v[i] - mean; q += d * d; }
  for (int m = 32; m >= 1; m >>= 1) q += __shfl_xor(q, m);
  float rstd = 1.0f / sqrtf(q / D + 1e-5f);
  for (int i = 0; i < 8; ++i) y[r * D + t * 8 + i] = f2bf((v[i] - mean) * rstd);
}
__global__ void k_gemm(const u16* A, const u16* B, const u16* res, u16* C, int M, int N, int K) {
  int n = blockIdx.x * 64 + threadIdx.x, m = blockIdx.y * 4 + threadIdx.y;
  float acc = 0;
  for (int k = 0; k < K; ++k) acc += bf2f(A[m * K + k]) * bf2f(B[k * N + n]);
  if (res) acc = bf2f(f2bf(acc)) + bf2f(res[m * N + n]);
  C[m * N + n] = f2bf(acc);
}
__global__ void k_swiglu(const u16* g, const u16* u, u16* h, int n) {
  int i = blockIdx.x * 256 + threadIdx.x;
  if (i < n) { float gg = bf2f(g[i]); float s = gg / (1.0f + __expf(-gg)); h[i] = f2bf(bf2f(f2bf(s)) * bf2f(u[i])); }
}
__global__ void k_attn(const u16* qkv, u16* o) {
  int i = blockIdx.x * 64 + threadIdx.x; int h = blockIdx.y;
  float q[DH]; for (int d = 0; d < DH; ++d) q[d] = bf2f(qkv[i * 3 * D + h * DH + d]);
  int j0 = i - (W - 1); if (j0 < 0) j0 = 0;
  float m = -1e30f;
  for (int j = j0; j <= i; ++j) { float s = 0; for (int d = 0; d < DH; ++d) s += q[d] * bf2f(qkv[j * 3 * D + D + h * DH + d]); s *= 0.25f; m = fmaxf(m, s); }
  float l = 0; float acc[DH]; for (int d = 0; d < DH; ++d) acc[d] = 0;
  for (int j = j0; j <= i; ++j) { float s = 0; for (int d = 0; d < DH; ++d) s += q[d] * bf2f(qkv[j * 3 * D + D + h * DH + d]); s *= 0.25f; float p = __expf(s - m); l += p;
    for (int d = 0; d < DH; ++d) acc[d] += p * bf2f(qkv[j * 3 * D + 2 * D + h * DH + d]); }
  for (int d = 0; d < DH; ++d) o[i * D + h * DH + d] = f2bf(acc[d] / l);
}

extern "C" void kernel_launch(void* const* d_in, const int* in_sizes, int n_in, void* d_out, int out_size, void* d_ws, size_t ws_size, hipStream_t stream) {
  const u16* x = (const u16*)d_in[0]; const u16* wqkv = (const u16*)d_in[1]; const u16* wo = (const u16*)d_in[2];
  const u16* wg = (const u16*)d_in[4]; const u16* wu = (const u16*)d_in[5]; const u16* wd = (const u16*)d_in[6];
  u16* ws = (u16*)d_ws;
  u16* xn = ws; u16* qkv = xn + S * D; u16* o = qkv + S * 3 * D; u16* x1 = o + S * D; u16* g = x1 + S * D; u16* u = g + S * F; u16* hh = u + S * F; u16* x2 = hh + S * F;
  const u16* cur = x;
  for (int l = 0; l < 2; ++l) {
    k_ln<<<S, 64, 0, stream>>>(cur, xn);
    k_gemm<<<dim3(3 * D / 64, S / 4), dim3(64, 4), 0, stream>>>(xn, wqkv, nullptr, qkv, S, 3 * D, D);
    k_attn<<<dim3(S / 64, H), 64, 0, stream>>>(qkv, o);
    k_gemm<<<dim3(D / 64, S / 4), dim3(64, 4), 0, stream>>>(o, wo, cur, x1, S, D, D);
    k_ln<<<S, 64, 0, stream>>>(x1, xn);
    k_gemm<<<dim3(F / 64, S / 4), dim3(64, 4), 0, stream>>>(xn, wg, nullptr, g, S, F, D);
    k_gemm<<<dim3(F / 64, S / 4), dim3(64, 4), 0, stream>>>(xn, wu, nullptr, u, S, F, D);
    k_swiglu<<<(S * F + 255) / 256, 256, 0, stream>>>(g, u, hh, S * F);
    u16* dst = (l == 1) ? (u16*)d_out : x2;
    k_gemm<<<dim3(D / 64, S / 4), dim3(64, 4), 0, stream>>>(hh, wd, x1, dst, S, D, F);
    cur = dst;
  }
}
```

```cpp
#include <hip/hip_runtime.h>
#include <stdint.h>

#define LAS __attribute__((address_space(3)))
#define GAS __attribute__((address_space(1)))
#define DI __device__ __forceinline__
#define RLX_AGENT __ATOMIC_RELAXED, __HIP_MEMORY_SCOPE_AGENT
typedef unsigned short u16;
typedef unsigned v4u __attribute__((ext_vector_type(4)));
typedef unsigned v2u __attribute__((ext_vector_type(2)));
typedef float f32x2 __attribute__((ext_vector_type(2)));
typedef float f32x4 __attribute__((ext_vector_type(4)));
typedef float f32x16 __attribute__((ext_vector_type(16)));
typedef __bf16 bf16x2 __attribute__((ext_vector_type(2)));
typedef __bf16 bf16x8 __attribute__((ext_vector_type(8)));
typedef GAS unsigned gu32;

namespace {

constexpr int D_ = 512, F_ = 1792;
constexpr unsigned MB2 = 2097152u;
constexpr unsigned FLAG_OFF = 0, FLAG_LAYER = 65536, FLAG_BYTES = 131072;
constexpr unsigned CS_OFF = FLAG_BYTES;
constexpr unsigned WQKV_OFF = CS_OFF + 32768;
constexpr unsigned WO_OFF = WQKV_OFF + 16 * 98304;
constexpr unsigned WGU_OFF = WO_OFF + 8 * 65536;
constexpr unsigned WDN_OFF = WGU_OFF + 16 * 229376;
constexpr unsigned X0_OFF = WDN_OFF + 8 * 229376;
constexpr unsigned LAY_OFF = X0_OFF + MB2;
constexpr unsigned L_K = 0, L_V = MB2, L_O = 2 * MB2, L_X1 = 3 * MB2, L_X2 = 4 * MB2, L_H = 5 * MB2;
constexpr unsigned LAY_BYTES = 5 * MB2 + 64 * 112 * 1024;
constexpr unsigned WS_BYTES = LAY_OFF + 2 * LAY_BYTES;
constexpr int FL_XN = 0, FL_O = 16, FL_X1 = 32, FL_H = 48, FL_KV = 176, FL_TMO = 511;
constexpr int SLAB = 16384;
constexpr int LDS_KL = 98304, LDS_VL = LDS_KL + 24576, LDS_QL = LDS_VL + 24576;
constexpr int LDS_RED = 131072;
constexpr int LDS_CS = 155648, LDS_ST = LDS_CS + 1280, LDS_ONES = LDS_ST + 1024;
constexpr int LDS_BYTES = 163840;
constexpr unsigned SPIN_CAP = 1u << 16;
constexpr int AUX_SC1 = 16;

DI f32x16 mfma(v4u a, v4u b, f32x16 c) {
  return __builtin_amdgcn_mfma_f32_32x32x16_bf16(__builtin_bit_cast(bf16x8, a), __builtin_bit_cast(bf16x8, b), c, 0, 0, 0);
}
DI unsigned pk2(float lo, float hi) {
  f32x2 p = {lo, hi};
  return __builtin_bit_cast(unsigned, __builtin_convertvector(p, bf16x2));
}
DI float bfr(float x) { return __uint_as_float(pk2(x, 0.f) << 16); }
DI float bflo(unsigned u) { return __uint_as_float(u << 16); }
DI float bfhi(unsigned u) { return __uint_as_float(u & 0xffff0000u); }
DI float dot2(unsigned a, unsigned b, float c) {
  return __builtin_amdgcn_fdot2_f32_bf16(__builtin_bit_cast(bf16x2, a), __builtin_bit_cast(bf16x2, b), c, false);
}
DI float xhalf_sum(float v) {
  auto r = __builtin_amdgcn_permlane32_swap(__float_as_uint(v), __float_as_uint(v), false, false);
  return __uint_as_float(r[0]) + __uint_as_float(r[1]);
}
DI float xhalf_max(float v) {
  auto r = __builtin_amdgcn_permlane32_swap(__float_as_uint(v), __float_as_uint(v), false, false);
  return fmaxf(__uint_as_float(r[0]), __uint_as_float(r[1]));
}
DI f32x16 zero16() {
  f32x16 z;
#pragma unroll
  for (int i = 0; i < 16; ++i) z[i] = 0.f;
  return z;
}
DI int vopq(int x) { asm volatile("" : "+v"(x)); return x; }
DI int sopq(int x) { asm volatile("" : "+s"(x)); return x; }
#define COORDS() \
  const int lane = vopq(lane0), lo = lane * 16, hh = lane >> 5; \
  const int w = sopq(w0), p = sopq(p0), c = sopq(c0), ct = c >> 1, rh = c & 1; \
  const unsigned lay = (unsigned)sopq((int)lay0), xin = (unsigned)sopq((int)xin0); \
  (void)lo; (void)hh; (void)ct; (void)rh; (void)lay; (void)xin; (void)p; (void)c; (void)w;
#define BAR() asm volatile("s_waitcnt lgkmcnt(0)\n\ts_barrier" ::: "memory")
#define DRAIN() asm volatile("s_waitcnt vmcnt(0)" ::: "memory")
template <int N> DI void wait_vm() { asm volatile("s_waitcnt vmcnt(%0)" ::"i"(N) : "memory"); }

typedef __amdgpu_buffer_rsrc_t rsrc_t;
DI v4u ld16(rsrc_t rs, unsigned voff, unsigned soff) { return __builtin_amdgcn_raw_buffer_load_b128(rs, voff, soff, AUX_SC1); }
DI void st16(rsrc_t rs, v4u v, unsigned voff, unsigned soff) { __builtin_amdgcn_raw_buffer_store_b128(v, rs, voff, soff, AUX_SC1); }
DI v4u lds16(LAS unsigned char* L, int off) { return *(LAS v4u*)(L + off); }
DI f32x4 lds4f(LAS unsigned char* L, int off) { return *(LAS f32x4*)(L + off); }

__global__ void __launch_bounds__(256) k_prep(const u16* __restrict__ x, const u16* __restrict__ wqkv, const u16* __restrict__ wo,
                                              const u16* __restrict__ wg, const u16* __restrict__ wu, const u16* __restrict__ wd,
                                              unsigned char* __restrict__ ws) {
  __shared__ float red[4][32];
  const int job = blockIdx.x, tid = threadIdx.x;
  if (job < 240) {
    const int gl = tid >> 3, hh = (tid >> 2) & 1, nc = tid & 3;
    const u16* src; int ld, col0, g = gl; unsigned dst; int csidx = -1; bool act = true;
    if (job < 48) {
      const int c = job / 3, tile = job % 3;
      src = wqkv; ld = 3 * D_; col0 = tile * D_ + 32 * c + 8 * nc;
      dst = WQKV_OFF + c * 98304 + (3 * g + tile) * 1024; csidx = c * 320 + tile * 32;
    } else if (job < 160) {
      const int jj = job - 48, c = jj / 7, j = jj % 7;
      src = (nc < 2) ? wg : wu; ld = F_; col0 = 112 * c + 16 * j + 8 * (nc & 1);
      dst = WGU_OFF + c * 229376 + (j < 3 ? (3 * g + j) : (96 + (j - 3) * 32 + g)) * 1024; csidx = c * 320 + 96 + 32 * j;
    } else if (job < 176) {
      const int ntg = job - 160, ct = ntg >> 1, nt = ntg & 1;
      src = wo; ld = D_; col0 = 32 * ntg + 8 * nc;
      const int kh = g >> 4, i = (g >> 2) & 3, kk = g & 3;
      dst = WO_OFF + ct * 65536 + (i * 16 + (kk * 2 + kh) * 2 + nt) * 1024;
    } else {
      const int jj = job - 176, ntg = jj >> 2, q = jj & 3, ct = ntg >> 1, nt = ntg & 1;
      act = gl < 28; g = 28 * q + (act ? gl : 0);
      src = wd; ld = D_; col0 = 32 * ntg + 8 * nc;
      const int cp = g / 7, j = g % 7, kh = cp >> 3, cc = (cp >> 2) & 1, kk = cp & 3, i = 2 * j + cc;
      dst = WDN_OFF + ct * 229376 + (i * 16 + (kk * 2 + kh) * 2 + nt) * 1024;
    }
    v4u in[8];
#pragma unroll
    for (int j = 0; j < 8; ++j) {
      const int k = 16 * g + 8 * (j >> 2) + 4 * hh + (j & 3);
      in[j] = *(const v4u*)(src + (size_t)k * ld + col0);
    }
    if (act) {
#pragma unroll
      for (int i = 0; i < 8; ++i) {
        v4u o;
#pragma unroll
        for (int m = 0; m < 4; ++m) o[m] = __builtin_amdgcn_perm(in[2 * m + 1][i >> 1], in[2 * m][i >> 1], (i & 1) ? 0x07060302u : 0x05040100u);
        *(v4u*)(ws + dst + (hh * 32 + 8 * nc + i) * 16) = o;
      }
    }
    if (csidx >= 0) {
      float s[8];
#pragma unroll
      for (int i = 0; i < 8; ++i) {
        float a = 0.f;
#pragma unroll
        for (int j = 0; j < 8; ++j) a += (i & 1) ? bfhi(in[j][i >> 1]) : bflo(in[j][i >> 1]);
        a += __shfl_xor(a, 4); a += __shfl_xor(a, 8); a += __shfl_xor(a, 16); a += __shfl_xor(a, 32);
        s[i] = a;
      }
      if ((tid & 63) < 4) {
#pragma unroll
        for (int i = 0; i < 8; ++i) red[tid >> 6][8 * nc + i] = s[i];
      }
      __syncthreads();
      if (tid < 32) ((float*)(ws + CS_OFF))[csidx + tid] = (red[0][tid] + red[1][tid]) + (red[2][tid] + red[3][tid]);
    }
  } else if (job < 304) {
    const int T = job - 240, t = tid & 31, gq = tid >> 5;
    const v4u* row = (const v4u*)(x + (size_t)(32 * T + t) * D_ + 64 * gq);
    v4u in[8];
#pragma unroll
    for (int i = 0; i < 8; ++i) in[i] = row[i];
#pragma unroll
    for (int u = 0; u < 4; ++u) {
      const v4u a = in[2 * u], b = in[2 * u + 1];
      unsigned char* d = ws + X0_OFF + ((T * 32 + 4 * gq + u) * 64 + t) * 16;
      *(v4u*)d = (v4u){a.x, a.y, b.x, b.y};
      *(v4u*)(d + 512) = (v4u){a.z, a.w, b.z, b.w};
    }
  } else {
#pragma unroll
    for (int i = 0; i < 4; ++i) *(unsigned*)(ws + FLAG_OFF + (tid * 4 + i) * 128) = 0u;
  }
}

DI void poll_ge(gu32* word, unsigned want, bool active, gu32* tmo) {
  for (unsigned spins = 0;;) {
    unsigned v = want;
    if (active) v = __hip_atomic_load(word, RLX_AGENT);
    if (__all(v >= want)) break;
    ++spins;
    if (spins > SPIN_CAP) { __hip_atomic_store(tmo, 1u, RLX_AGENT); break; }
    if ((spins & 255u) == 0u && __builtin_amdgcn_readfirstlane(__hip_atomic_load(tmo, RLX_AGENT))) break;
  }
  __builtin_amdgcn_fence(__ATOMIC_ACQUIRE, "wavefront");
}

template <int I> DI void dma1(rsrc_t rs, LAS unsigned char* dst, unsigned voff, unsigned soff) {
  __builtin_amdgcn_raw_ptr_buffer_load_lds(rs, (LAS void*)dst, 16, voff, soff, I * 1024, 0);
}
DI void dma_half(rsrc_t rs, LAS unsigned char* dst, unsigned voff, unsigned soff) {
  dma1<0>(rs, dst, voff, soff); dma1<1>(rs, dst, voff, soff); dma1<2>(rs, dst, voff, soff); dma1<3>(rs, dst, voff, soff);
  dma1<0>(rs, dst + 4096, voff, soff + 4096); dma1<1>(rs, dst + 4096, voff, soff + 4096);
  dma1<2>(rs, dst + 4096, voff, soff + 4096); dma1<3>(rs, dst + 4096, voff, soff + 4096);
}

struct Stats { float m, rden; };
DI Stats ln_stats(float s1, float s2) {
  s1 = xhalf_sum(s1); s2 = xhalf_sum(s2);
  const float mean = s1 * (1.0f / D_);
  const float var = fmaxf(s2 * (1.0f / D_) - mean * mean, 0.f);
  const float den = bfr(sqrtf(bfr(bfr(var) + 1e-5f)));
  Stats st; st.m = bfr(mean); st.rden = 1.0f / den;
  return st;
}

template <bool QKV, int SLOT0, int NEXT>
DI void gemm3(LAS unsigned char* L, int lo, const v4u (&xa)[32], f32x16& a0, f32x16& a1, f32x16& a2, v4u (&wf)[2][4], float& s1o, float& s2o) {
  float s1a = 0.f, s1b = 0.f, s2a = 0.f, s2b = 0.f;
  a0 = zero16(); a1 = zero16(); a2 = zero16();
  BAR();
#pragma unroll
  for (int u = 0; u < 4; ++u) wf[0][u] = lds16(L, lo + (SLOT0 & 7) * SLAB + u * 1024);
#pragma unroll
  for (int G = 0; G < 24; ++G) {
    if (G + 1 < 24) {
      if ((G + 1) % 4 == 0) BAR();
#pragma unroll
      for (int u = 0; u < 4; ++u) wf[(G + 1) & 1][u] = lds16(L, lo + ((SLOT0 + (G + 1) / 4) & 7) * SLAB + (((G + 1) % 4) * 4 + u) * 1024);
    } else if (NEXT >= 0) {
      BAR();
#pragma unroll
      for (int u = 0; u < 4; ++u) wf[0][u] = lds16(L, lo + (NEXT & 7) * SLAB + u * 1024);
    }
#pragma unroll
    for (int u = 0; u < 4; ++u) {
      const int f = 4 * G + u, ks = f / 3, tile = f % 3;
      if (tile == 0) {
        a0 = mfma(wf[G & 1][u], xa[ks], a0);
        s1a = dot2(xa[ks].x, 0x3f803f80u, s1a); s2a = dot2(xa[ks].x, xa[ks].x, s2a);
        s1b = dot2(xa[ks].y, 0x3f803f80u, s1b); s2b = dot2(xa[ks].y, xa[ks].y, s2b);
      } else if (tile == 1) {
        a1 = mfma(wf[G & 1][u], xa[ks], a1);
        s1a = dot2(xa[ks].z, 0x3f803f80u, s1a); s2a = dot2(xa[ks].z, xa[ks].z, s2a);
        s1b = dot2(xa[ks].w, 0x3f803f80u, s1b); s2b = dot2(xa[ks].w, xa[ks].w, s2b);
      } else {
        a2 = QKV ? mfma(xa[ks], wf[G & 1][u], a2) : mfma(wf[G & 1][u], xa[ks], a2);
      }
    }
  }
  s1o = s1a + s1b; s2o = s2a + s2b;
}

DI void ln_fold(LAS unsigned char* L, int csoff, int hh, const f32x16& a, const Stats& st, float (&y)[16]) {
#pragma unroll
  for (int q = 0; q < 4; ++q) {
    const f32x4 cs = lds4f(L, csoff + (8 * q + 4 * hh) * 4);
#pragma unroll
    for (int i = 0; i < 4; ++i) y[4 * q + i] = (a[4 * q + i] - st.m * cs[i]) * st.rden;
  }
}
DI v4u pack8(const float* y) { return (v4u){pk2(y[0], y[1]), pk2(y[2], y[3]), pk2(y[4], y[5]), pk2(y[6], y[7])}; }

DI void gu_epi(LAS unsigned char* L, rsrc_t rs, int j, int hh, unsigned lo, unsigned hbase, const f32x16& a, const Stats& st) {
  float y[16];
  ln_fold(L, LDS_CS + (96 + 32 * j) * 4, hh, a, st, y);
  float h[8];
#pragma unroll
  for (int i = 0; i < 8; ++i) {
    const float e = __builtin_amdgcn_exp2f(y[i] * -1.4426950408889634f);
    h[i] = y[i] * __builtin_amdgcn_rcpf(1.0f + e) * y[8 + i];
  }
  st16(rs, pack8(h), lo, hbase + j * 1024);
}

template <class EPI>
DI void chain32(LAS unsigned char* L, int lo, int sb, int nsb, bool confirm, const v4u (&xa)[32], f32x16& a, v4u (&wf)[2][4], EPI epi) {
  a = zero16();
  const int base = lo + sb;
#pragma unroll
  for (int G = 0; G < 8; ++G) {
    if (G + 1 < 8) {
      if (G + 1 == 4) { if (confirm) DRAIN(); BAR(); }
#pragma unroll
      for (int u = 0; u < 4; ++u) wf[(G + 1) & 1][u] = lds16(L, base + ((G + 1) * 4 + u) * 1024);
    } else if (nsb >= 0) {
      BAR();
#pragma unroll
      for (int u = 0; u < 4; ++u) wf[0][u] = lds16(L, lo + nsb + u * 1024);
    }
#pragma unroll
    for (int u = 0; u < 4; ++u) a = mfma(wf[G & 1][u], xa[4 * G + u], a);
    epi(G);
  }
}

template <int NS, int SLOT0, int RING, class AOFF>
DI void gemm64(LAS unsigned char* L, rsrc_t rs, int lo, int kh, AOFF aoff, f32x16& keep, f32x16& send) {
  keep = zero16(); send = zero16();
  v4u ha[RING];
  const int bk = lo + (kh * 2 + kh) * 1024, bs = lo + (kh * 2 + 1 - kh) * 1024;
  BAR();
#pragma unroll
  for (int s = 0; s < RING; ++s) ha[s] = ld16(rs, lo, aoff(s));
  v4u wk[2][4], wsn[2][4];
#pragma unroll
  for (int kk = 0; kk < 4; ++kk) {
    wk[0][kk] = lds16(L, bk + (SLOT0 & 7) * SLAB + kk * 4096);
    wsn[0][kk] = lds16(L, bs + (SLOT0 & 7) * SLAB + kk * 4096);
  }
#pragma unroll
  for (int i = 0; i < NS; ++i) {
    if (i + 1 < NS) {
      BAR();
#pragma unroll
      for (int kk = 0; kk < 4; ++kk) {
        wk[(i + 1) & 1][kk] = lds16(L, bk + ((SLOT0 + i + 1) & 7) * SLAB + kk * 4096);
        wsn[(i + 1) & 1][kk] = lds16(L, bs + ((SLOT0 + i + 1) & 7) * SLAB + kk * 4096);
      }
    }
#pragma unroll
    for (int kk = 0; kk < 4; ++kk) {
      const int s = 4 * i + kk;
      keep = mfma(wk[i & 1][kk], ha[s % RING], keep);
      send = mfma(wsn[i & 1][kk], ha[s % RING], send);
      if (s + RING < 4 * NS) ha[s % RING] = ld16(rs, lo, aoff(s + RING));
    }
  }
}
DI void finish64(LAS unsigned char* L, int lo, int tt, int kh, f32x16& keep, const f32x16& send, v4u r0, v4u r1, v4u& o0, v4u& o1) {
#pragma unroll
  for (int q = 0; q < 4; ++q)
    *(LAS f32x4*)(L + LDS_RED + (tt * 2 + 1 - kh) * 4096 + q * 1024 + lo) = (f32x4){send[4 * q], send[4 * q + 1], send[4 * q + 2], send[4 * q + 3]};
  BAR();
  float y[16];
#pragma unroll
  for (int q = 0; q < 4; ++q) {
    const f32x4 o = lds4f(L, LDS_RED + (tt * 2 + kh) * 4096 + q * 1024 + lo);
#pragma unroll
    for (int i = 0; i < 4; ++i) y[4 * q + i] = keep[4 * q + i] + o[i];
  }
#pragma unroll
  for (int m = 0; m < 4; ++m) {
    y[2 * m] += bflo(r0[m]); y[2 * m + 1] += bfhi(r0[m]);
    y[8 + 2 * m] += bflo(r1[m]); y[8 + 2 * m + 1] += bfhi(r1[m]);
  }
  o0 = pack8(y); o1 = pack8(y + 8);
}

__global__ void __launch_bounds__(512, 2) k_main(unsigned char* __restrict__ ws, u16* __restrict__ out) {
  extern __shared__ __attribute__((aligned(16))) unsigned char smem[];
  LAS unsigned char* L = (LAS unsigned char*)smem;
  const int tid = threadIdx.x, lane0 = tid & 63, w0 = __builtin_amdgcn_readfirstlane(tid >> 6);
  const int b = blockIdx.x, xcd = b & 7, idx = b >> 3;
  const int p0 = 4 * (xcd >> 1) + (idx >> 3), c0 = (idx & 7) + 8 * (xcd & 1);
  const rsrc_t rs = __builtin_amdgcn_make_buffer_rsrc((void*)ws, 0, (int)WS_BYTES, 0x00020000);
  const bool compute = w0 < 4;
  gu32* const tmo = (gu32*)(ws + FLAG_OFF) + FL_TMO * 32;

  auto issue = [&](int m) {
    const int c = sopq(c0), ct = c >> 1, lo = lane0 * 16;
    const unsigned lhalf = (unsigned)sopq(w0 & 1) * 8192u;
    unsigned src;
    if (m < 8) src = WQKV_OFF + c * 98304 + m * SLAB;
    else if (m < 12) src = WO_OFF + ct * 65536 + (m - 8) * SLAB;
    else if (m < 26) src = WGU_OFF + c * 229376 + (m - 12) * SLAB;
    else src = WDN_OFF + ct * 229376 + (m - 26) * SLAB;
    dma_half(rs, L + (m & 7) * SLAB + lhalf, lo, src + lhalf);
  };

  if (w0 == 5 || w0 == 6) {
    for (int m = 0; m < 6; ++m) issue(m);
  } else if (w0 == 7) {
    const int lane = lane0;
    const f32x4* cs = (const f32x4*)(ws + CS_OFF + c0 * 1280);
    *(LAS f32x4*)(L + LDS_CS + lane * 16) = cs[lane];
    if (lane < 16) *(LAS f32x4*)(L + LDS_CS + 1024 + lane * 16) = cs[64 + lane];
    if (lane == 0) *(LAS v4u*)(L + LDS_ONES) = (v4u){0x3f803f80u, 0x3f803f80u, 0x3f803f80u, 0x3f803f80u};
  }

  for (int layer = 0; layer < 2; ++layer) {
    const bool last = layer == 1;
    const unsigned lay0 = LAY_OFF + layer * LAY_BYTES;
    const unsigned xin0 = layer == 0 ? X0_OFF : LAY_OFF + L_X2;
    gu32* const fl = (gu32*)(ws + FLAG_OFF + layer * FLAG_LAYER);

    if (compute) {
      COORDS();
      const int T = 4 * p + w;
      if (layer > 0) BAR();
      v4u xa[32];
#pragma unroll
      for (int ks = 0; ks < 32; ++ks) xa[ks] = ld16(rs, lo + (ks & 3) * 1024, xin + T * 32768 + (ks >> 2) * 4096);
      f32x16 aq, ak, av; v4u wf[2][4]; float s1, s2;
      gemm3<true, 0, -1>(L, lo, xa, aq, ak, av, wf, s1, s2);
      const Stats st = ln_stats(s1, s2);
      if (hh == 0) *(LAS f32x2*)(L + LDS_ST + w * 256 + lane * 8) = (f32x2){st.rden, st.m * st.rden};
      float y[16];
      ln_fold(L, LDS_CS + 32 * 4, hh, ak, st, y);
      {
        const v4u k0 = pack8(y), k1 = pack8(y + 8);
        st16(rs, k0, lo, lay + L_K + (T * 32 + 2 * c) * 1024);
        st16(rs, k1, lo + 1024, lay + L_K + (T * 32 + 2 * c) * 1024);
        *(LAS v4u*)(L + LDS_KL + (8 + w) * 1024 + lo) = k0;
        *(LAS v4u*)(L + LDS_KL + (12 + 8 + w) * 1024 + lo) = k1;
      }
      {
        const float csn = *(LAS float*)(L + LDS_CS + (64 + (lane & 31)) * 4);
#pragma unroll
        for (int q = 0; q < 4; ++q) {
          const f32x4 t0 = lds4f(L, LDS_ST + w * 256 + (8 * q + 4 * hh) * 8), t1 = lds4f(L, LDS_ST + w * 256 + (8 * q + 4 * hh) * 8 + 16);
          y[4 * q + 0] = av[4 * q + 0] * t0[0] - t0[1] * csn;
          y[4 * q + 1] = av[4 * q + 1] * t0[2] - t0[3] * csn;
          y[4 * q + 2] = av[4 * q + 2] * t1[0] - t1[1] * csn;
          y[4 * q + 3] = av[4 * q + 3] * t1[2] - t1[3] * csn;
        }
        const v4u v0 = pack8(y), v1 = pack8(y + 8);
        const int e = (lane >> 4) & 1, vo = hh * 256 + (lane & 15) * 16;
        st16(rs, v0, e * 1024 + vo, lay + L_V + (T * 32 + 2 * c) * 1024);
        st16(rs, v1, e * 1024 + vo + 512, lay + L_V + (T * 32 + 2 * c) * 1024);
        *(LAS v4u*)(L + LDS_VL + (e * 12 + 8 + w) * 1024 + vo) = v0;
        *(LAS v4u*)(L + LDS_VL + (e * 12 + 8 + w) * 1024 + vo + 512) = v1;
      }
      ln_fold(L, LDS_CS, hh, aq, st, y);
      *(LAS v4u*)(L + LDS_QL + w * 1024 + lo) = pack8(y);
      *(LAS v4u*)(L + LDS_QL + (4 + w) * 1024 + lo) = pack8(y + 8);
      DRAIN();
      BAR();
    } else {
      COORDS();
      const bool poller = w == 4, loader = (w == 5) || (w == 6);
      if (layer > 0) {
        if (poller) poll_ge(fl + (FL_XN + p) * 32, 16u, lane == 0, tmo);
        BAR();
      }
      if (loader) wait_vm<40>();
      BAR();
      for (int n = 1; n <= 5; ++n) {
        if (loader) wait_vm<32>();
        BAR();
        if (loader) issue(n + 7);
      }
      BAR();
      if (loader) issue(13);
      if (poller) {
        if (lane == 0) __hip_atomic_store(fl + (FL_KV + p * 16 + c) * 32, 1u, RLX_AGENT);
        const int pp = p - 1 - (lane & 1);
        poll_ge(fl + (FL_KV + (pp < 0 ? 0 : pp) * 16 + c) * 32, 1u, lane < 2 && pp >= 0, tmo);
      }
    }

    BAR();
    {
      COORDS();
      const int Tg = 4 * p - 8 + w;
      const unsigned so = lay + ((Tg >= 0 ? Tg : 0) * 32 + 2 * c) * 1024;
      const unsigned vo = Tg >= 0 ? (unsigned)lo : 0x40000000u;
      const v4u k0 = ld16(rs, vo, so + L_K), k1 = ld16(rs, vo + 1024, so + L_K);
      const v4u v0 = ld16(rs, vo, so + L_V), v1 = ld16(rs, vo + 1024, so + L_V);
      *(LAS v4u*)(L + LDS_KL + w * 1024 + lo) = k0;
      *(LAS v4u*)(L + LDS_KL + (12 + w) * 1024 + lo) = k1;
      *(LAS v4u*)(L + LDS_VL + w * 1024 + lo) = v0;
      *(LAS v4u*)(L + LDS_VL + (12 + w) * 1024 + lo) = v1;
    }
    BAR();
    {
      COORDS();
      const int tt = w & 3, e = w >> 2, ql = lane & 31;
      const int t0 = 4 * p - 8 + tt;
      const v4u q = lds16(L, LDS_QL + (e * 4 + tt) * 1024 + lo);
      const int kb = LDS_KL + (e * 12 + tt) * 1024 + lo;
      f32x16 s[9];
#pragma unroll
      for (int i = 0; i < 9; ++i) s[i] = mfma(lds16(L, kb + i * 1024), q, zero16());
      constexpr float NEG = -3.0e38f;
      if (t0 < 0) {
#pragma unroll
        for (int r = 0; r < 16; ++r) s[0][r] = NEG;
      }
#pragma unroll
      for (int r = 0; r < 16; ++r) {
        const int kl = (r & 3) + 8 * (r >> 2) + 4 * hh;
        s[8][r] = (kl <= ql) ? s[8][r] : s[0][r];
      }
      float m = NEG;
#pragma unroll
      for (int i = 8; i >= 1; --i) {
        float tm = fmaxf(s[i][0], s[i][1]);
#pragma unroll
        for (int r = 2; r < 16; r += 2) tm = fmaxf(fmaxf(tm, s[i][r]), s[i][r + 1]);
        m = (t0 + i >= 0) ? fmaxf(m, tm) : m;
      }
      m = xhalf_max(m);
      constexpr float CEXP = 0.25f * 1.4426950408889634f;
      const float mc = m * CEXP;
      const bool vl = (lane & 16) == 0;
      const int vstep = vl ? 512 : 0;
      const int vb0 = vl ? LDS_VL + (e * 12 + tt) * 1024 + hh * 256 + (lane & 15) * 16 : LDS_ONES;
      int va = vb0 + 2 * vstep;
      f32x16 o = zero16();
#pragma unroll
      for (int i = 1; i <= 8; ++i) {
        const float mi = (t0 + i >= 0) ? mc : 3.0e38f;
        float pr[16];
#pragma unroll
        for (int r = 0; r < 16; ++r) pr[r] = __builtin_amdgcn_exp2f(s[i][r] * CEXP - mi);
        if (i < 8) {
          o = mfma(lds16(L, va), pack8(pr), o); va += vstep;
          o = mfma(lds16(L, va), pack8(pr + 8), o); va += vstep;
        } else {
          float pl[16], pf[16];
#pragma unroll
          for (int r = 0; r < 16; ++r) {
            const int kl = (r & 3) + 8 * (r >> 2) + 4 * hh;
            pl[r] = (kl <= ql) ? pr[r] : 0.f;
            pf[r] = (kl <= ql) ? 0.f : pr[r];
          }
          o = mfma(lds16(L, va), pack8(pl), o); va += vstep;
          o = mfma(lds16(L, va), pack8(pl + 8), o);
          o = mfma(lds16(L, vb0), pack8(pf), o);
          o = mfma(lds16(L, vb0 + vstep), pack8(pf + 8), o);
        }
      }
      const float rl = __builtin_amdgcn_rcpf(o[8]);
      float y[8];
#pragma unroll
      for (int r = 0; r < 8; ++r) y[r] = o[r] * rl;
      st16(rs, pack8(y), lo, lay + L_O + ((4 * p + tt) * 32 + 2 * c + e) * 1024);
      DRAIN();
    }
    BAR();

    if (compute) {
      {
        COORDS();
        const int tt = w & 1, kh = w >> 1;
        const int T2 = 4 * p + 2 * rh + tt, og = 2 * (2 * ct + kh);
        const v4u r0 = ld16(rs, lo, xin + (T2 * 32 + og) * 1024), r1 = ld16(rs, lo + 1024, xin + (T2 * 32 + og) * 1024);
        f32x16 keep, send;
        const unsigned ab = lay + L_O + (T2 * 32 + 16 * kh) * 1024;
        gemm64<4, 8, 16>(L, rs, lo, kh, [&](int s) { return ab + s * 1024; }, keep, send);
        v4u o0, o1;
        finish64(L, lo, tt, kh, keep, send, r0, r1, o0, o1);
        st16(rs, o0, lo, lay + L_X1 + (T2 * 32 + og) * 1024);
        st16(rs, o1, lo + 1024, lay + L_X1 + (T2 * 32 + og) * 1024);
        DRAIN();
        BAR();
      }
      {
        COORDS();
        const int T = 4 * p + w;
        BAR();
        v4u xa[32];
#pragma unroll
        for (int ks = 0; ks < 32; ++ks) xa[ks] = ld16(rs, lo + (ks & 3) * 1024, lay + L_X1 + T * 32768 + (ks >> 2) * 4096);
        f32x16 a0, a1, a2, a; v4u wf[2][4]; float s1, s2;
        gemm3<false, 12, 18>(L, lo, xa, a0, a1, a2, wf, s1, s2);
        const Stats st = ln_stats(s1, s2);
        const unsigned hb = lay + L_H + (T * 112 + 7 * c) * 1024;
        chain32(L, lo, 2 * SLAB, 4 * SLAB, false, xa, a, wf, [&](int G) {
          if (G == 1) gu_epi(L, rs, 0, hh, lo, hb, a0, st);
          if (G == 3) gu_epi(L, rs, 1, hh, lo, hb, a1, st);
          if (G == 5) gu_epi(L, rs, 2, hh, lo, hb, a2, st);
        });
        for (int jj = 1; jj <= 3; ++jj) {
          const f32x16 old = a;
          chain32(L, lo, ((2 + 2 * jj) & 7) * SLAB, jj < 3 ? ((4 + 2 * jj) & 7) * SLAB : -1, true, xa, a, wf, [&](int G) {
            if (G == 1) gu_epi(L, rs, 2 + jj, hh, lo, hb, old, st);
          });
        }
        gu_epi(L, rs, 6, hh, lo, hb, a, st);
        DRAIN();
        BAR();
      }
      {
        COORDS();
        const int tt = w & 1, kh = w >> 1;
        const int T2 = 4 * p + 2 * rh + tt, og = 2 * (2 * ct + kh);
        const v4u r0 = ld16(rs, lo, lay + L_X1 + (T2 * 32 + og) * 1024), r1 = ld16(rs, lo + 1024, lay + L_X1 + (T2 * 32 + og) * 1024);
        f32x16 keep, send;
        const unsigned ab = lay + L_H + (T2 * 112 + 56 * kh) * 1024;
        gemm64<14, 26, 24>(L, rs, lo, kh, [&](int s) { return ab + (7 * (4 * ((s >> 2) & 1) + (s & 3)) + (s >> 3)) * 1024; }, keep, send);
        v4u o0, o1;
        finish64(L, lo, tt, kh, keep, send, r0, r1, o0, o1);
        if (!last) {
          st16(rs, o0, lo, lay + L_X2 + (T2 * 32 + og) * 1024);
          st16(rs, o1, lo + 1024, lay + L_X2 + (T2 * 32 + og) * 1024);
          DRAIN();
          BAR();
        } else {
          u16* orow = out + (size_t)(32 * T2 + (lane & 31)) * D_ + 16 * og + 4 * hh;
          *(v2u*)(orow) = (v2u){o0.x, o0.y};
          *(v2u*)(orow + 8) = (v2u){o0.z, o0.w};
          *(v2u*)(orow + 16) = (v2u){o1.x, o1.y};
          *(v2u*)(orow + 24) = (v2u){o1.z, o1.w};
        }
      }
    } else {
      COORDS();
      const bool poller = w == 4, loader = (w == 5) || (w == 6);
      if (loader) { issue(14); issue(15); }
      if (poller) {
        if (lane == 0) __hip_atomic_fetch_add(fl + (FL_O + p) * 32, 1u, RLX_AGENT);
        poll_ge(fl + (FL_O + p) * 32, 16u, lane == 0, tmo);
      }
      if (loader) wait_vm<48>();
      BAR();
      for (int n = 9; n <= 11; ++n) {
        if (loader) wait_vm<48>();
        BAR();
        if (loader) issue(n + 7);
      }
      BAR();
      if (loader) issue(19);
      BAR();
      if (poller) {
        if (lane == 0) __hip_atomic_fetch_add(fl + (FL_X1 + p) * 32, 1u, RLX_AGENT);
        poll_ge(fl + (FL_X1 + p) * 32, 16u, lane == 0, tmo);
      }
      BAR();
      if (loader) wait_vm<48>();
      BAR();
      for (int n = 13; n <= 25; ++n) {
        if (loader) wait_vm<48>();
        BAR();
        if (loader) issue(n + 7);
        if (poller && n >= 21 && (n & 1) && lane == 0) __hip_atomic_fetch_add(fl + (FL_H + p * 8 + ((n - 21) >> 1)) * 32, 1u, RLX_AGENT);
      }
      BAR();
      if (loader) issue(33);
      if (poller) {
        if (lane == 0) __hip_atomic_fetch_add(fl + (FL_H + p * 8 + 3) * 32, 1u, RLX_AGENT);
        poll_ge(fl + (FL_H + p * 8 + (lane & 1)) * 32, 16u, lane < 2, tmo);
      }
      if (loader) wait_vm<48>();
      BAR();
      for (int n = 27; n <= 33; ++n) {
        if (poller && n == 28) poll_ge(fl + (FL_H + p * 8 + 2) * 32, 16u, lane == 0, tmo);
        if (poller && n == 30) poll_ge(fl + (FL_H + p * 8 + 3) * 32, 16u, lane == 0, tmo);
        if (loader) wait_vm<48>();
        BAR();
        if (loader) { if (n < 33) issue(n + 7); else if (!last) issue(0); }
      }
      if (!last) {
        for (int n = 34; n <= 39; ++n) {
          if (loader) wait_vm<48>();
          BAR();
          if (loader && n < 39) issue(n - 33);
        }
        BAR();
        BAR();
        if (poller && lane == 0) __hip_atomic_fetch_add(fl + FLAG_LAYER / 4 + (FL_XN + p) * 32, 1u, RLX_AGENT);
      } else {
        if (loader) wait_vm<40>();
        BAR();
        if (loader) wait_vm<32>();
        BAR();
        if (loader) wait_vm<24>();
        BAR();
        if (loader) wait_vm<16>();
        BAR();
        if (loader) wait_vm<8>();
        BAR();
        if (loader) wait_vm<0>();
        BAR();
        BAR();
      }
    }
  }
}

}

extern "C" void kernel_launch(void* const* d_in, const int* in_sizes, int n_in, void* d_out, int out_size, void* d_ws, size_t ws_size,
                              hipStream_t stream) {
  (void)in_sizes; (void)n_in; (void)out_size; (void)ws_size;
  (void)hipFuncSetAttribute((const void*)k_main, hipFuncAttributeMaxDynamicSharedMemorySize, LDS_BYTES);
  k_prep<<<305, 256, 0, stream>>>((const u16*)d_in[0], (const u16*)d_in[1], (const u16*)d_in[2], (const u16*)d_in[4], (const u16*)d_in[5],
                                  (const u16*)d_in[6], (unsigned char*)d_ws);
  k_main<<<256, 512, LDS_BYTES, stream>>>((unsigned char*)d_ws, (u16*)d_out);
}
```

```cpp
#include <hip/hip_runtime.h>
#include <stdint.h>
#define NO_TLB_WARM
#include <utility>

#define LAS __attribute__((address_space(3)))
#define GAS __attribute__((address_space(1)))
#define DI __device__ __forceinline__
#define RLX_AGENT __ATOMIC_RELAXED, __HIP_MEMORY_SCOPE_AGENT
typedef unsigned short u16;
typedef unsigned v4u __attribute__((ext_vector_type(4)));
typedef unsigned v2u __attribute__((ext_vector_type(2)));
typedef float f32x2 __attribute__((ext_vector_type(2)));
typedef float f32x4 __attribute__((ext_vector_type(4)));
typedef float f32x16 __attribute__((ext_vector_type(16)));
typedef __bf16 bf16x2 __attribute__((ext_vector_type(2)));
typedef __bf16 bf16x8 __attribute__((ext_vector_type(8)));
typedef GAS unsigned gu32;

namespace {

constexpr int D_ = 512, F_ = 1792;
constexpr unsigned MB2 = 2097152u, STB = 262144u;
constexpr unsigned FLAG_OFF = 0, FLAG_LAYER = 65536, FLAG_BYTES = 131072;
constexpr unsigned CS_OFF = FLAG_BYTES;
constexpr unsigned WQKV_OFF = CS_OFF + 16 * 320 * 64;
constexpr unsigned WO_OFF = WQKV_OFF + 16 * 98304;
constexpr unsigned WGU_OFF = WO_OFF + 8 * 65536;
constexpr unsigned WDN_OFF = WGU_OFF + 16 * 229376;
constexpr unsigned X0_OFF = WDN_OFF + 8 * 229376;
constexpr unsigned LAY_OFF = X0_OFF + MB2 + STB;
constexpr unsigned L_K = 0, L_V = MB2, L_O = 2 * MB2, L_X1 = 3 * MB2, L_X2 = 4 * MB2 + STB, L_H = 5 * MB2 + 2 * STB;
constexpr unsigned LAY_BYTES = L_H + 64 * 112 * 1024;
constexpr unsigned WS_BYTES = LAY_OFF + 2 * LAY_BYTES;
constexpr int FL_XN = 0, FL_O = 16, FL_X1 = 32, FL_H = 48, FL_KV = 176, FL_SC = 432, FL_SW = 448, FL_WR = 464, FL_EP = 510, FL_TMO = 511;
constexpr int SLAB = 16384;
constexpr int VBLK = 528;
constexpr int LDS_KL = 98304, LDS_VL = LDS_KL + 24576, LDS_QL = LDS_VL + 48 * VBLK;
constexpr int LDS_RED = 131072;
constexpr int LDS_CS = LDS_QL + 8192, LDS_ST = LDS_CS + 2560, LDS_MK = LDS_ST + 512;
constexpr int LDS_BYTES = 163840;
constexpr unsigned SPIN_CAP = 1u << 16;
constexpr int AUX_SC1 = 16;
constexpr float LOG2E = 1.4426950408889634f;

DI f32x16 mfma(v4u a, v4u b, f32x16 c) {
  return __builtin_amdgcn_mfma_f32_32x32x16_bf16(__builtin_bit_cast(bf16x8, a), __builtin_bit_cast(bf16x8, b), c, 0, 0, 0);
}
DI unsigned pk2(float lo, float hi) {
  f32x2 p = {lo, hi};
  return __builtin_bit_cast(unsigned, __builtin_convertvector(p, bf16x2));
}
DI float bfr(float x) { return __uint_as_float(pk2(x, 0.f) << 16); }
DI float bflo(unsigned u) { return __uint_as_float(u << 16); }
DI float bfhi(unsigned u) { return __uint_as_float(u & 0xffff0000u); }
DI float amul(float a, float b) { float d; asm("v_mul_f32 %0, %1, %2" : "=v"(d) : "v"(a), "v"(b)); return d; }
DI float afma(float a, float b, float c) { float d; asm("v_fma_f32 %0, %1, %2, %3" : "=v"(d) : "v"(a), "v"(b), "v"(c)); return d; }
DI float afms(float a, float b, float c) { float d; asm("v_fma_f32 %0, %1, %2, -%3" : "=v"(d) : "v"(a), "v"(b), "v"(c)); return d; }
DI float aadd(float a, float b) { float d; asm("v_add_f32 %0, %1, %2" : "=v"(d) : "v"(a), "v"(b)); return d; }
DI float xhalf_sum(float v) {
  auto r = __builtin_amdgcn_permlane32_swap(__float_as_uint(v), __float_as_uint(v), false, false);
  return __uint_as_float(r[0]) + __uint_as_float(r[1]);
}
DI float xhalf_max(float v) {
  auto r = __builtin_amdgcn_permlane32_swap(__float_as_uint(v), __float_as_uint(v), false, false);
  return fmaxf(__uint_as_float(r[0]), __uint_as_float(r[1]));
}
DI f32x16 splat16(float v) {
  f32x16 z;
#pragma unroll
  for (int i = 0; i < 16; ++i) z[i] = v;
  return z;
}
DI int vopq(int x) { asm volatile("" : "+v"(x)); return x; }
DI int lane_id() { int l; asm volatile("v_mbcnt_lo_u32_b32 %0, -1, 0\n\tv_mbcnt_hi_u32_b32 %0, -1, %0" : "=v"(l)); return l; }
DI int sopq(int x) { asm volatile("" : "+s"(x)); return x; }
#define COORDS() \
  const int lane = lane_id(), lo = lane * 16, hh = lane >> 5; \
  const int w = sopq(w0), p = sopq(p0), c = sopq(c0), ct = c >> 1, rh = c & 1; \
  const unsigned lay = (unsigned)sopq((int)lay0), xin = (unsigned)sopq((int)xin0); \
  (void)lo; (void)hh; (void)ct; (void)rh; (void)lay; (void)xin; (void)p; (void)c; (void)w;
#define BAR() asm volatile("s_waitcnt lgkmcnt(0)\n\ts_barrier" ::: "memory")
#define BARX() asm volatile("s_barrier" ::: "memory")
#define BARS(c) BARX()
#define DRAIN() asm volatile("s_waitcnt vmcnt(0)" ::: "memory")
#define PIN() __builtin_amdgcn_sched_barrier(0)
template <int N> DI void wait_vm() { asm volatile("s_waitcnt vmcnt(%0)" ::"i"(N) : "memory"); }

typedef __amdgpu_buffer_rsrc_t rsrc_t;
DI v4u ld16(rsrc_t rs, unsigned voff, unsigned soff) { return __builtin_amdgcn_raw_buffer_load_b128(rs, voff, soff, AUX_SC1); }
DI f32x2 ld8f(rsrc_t rs, unsigned voff, unsigned soff) { return __builtin_bit_cast(f32x2, __builtin_amdgcn_raw_buffer_load_b64(rs, voff, soff, AUX_SC1)); }
DI void st16(rsrc_t rs, v4u v, unsigned voff, unsigned soff) { __builtin_amdgcn_raw_buffer_store_b128(v, rs, voff, soff, AUX_SC1); }
DI void st8f(rsrc_t rs, f32x2 v, unsigned voff, unsigned soff) { __builtin_amdgcn_raw_buffer_store_b64(__builtin_bit_cast(v2u, v), rs, voff, soff, AUX_SC1); }
DI v4u lds16(LAS unsigned char* L, int off) { return *(LAS v4u*)(L + off); }
DI f32x4 lds4f(LAS unsigned char* L, int off) { return *(LAS f32x4*)(L + off); }

DI void poll(gu32* word, unsigned want, bool exact, bool active, gu32* tmo, unsigned tag) {
  for (unsigned spins = 0;;) {
    unsigned v = want;
    if (active) v = __hip_atomic_load(word, RLX_AGENT);
    if (__all(exact ? v == want : v >= want)) break;
    ++spins;
    if (spins > SPIN_CAP) { __hip_atomic_store(tmo, tag, RLX_AGENT); break; }
    if ((spins & 255u) == 0u && (unsigned)__builtin_amdgcn_readfirstlane(__hip_atomic_load(tmo, RLX_AGENT)) == tag) break;
  }
  __builtin_amdgcn_fence(__ATOMIC_ACQUIRE, "wavefront");
}
#define poll_ge(word, want, active, tmo) poll(word, want, false, active, tmo, tag)

template <int I> DI void dma1(rsrc_t rs, LAS unsigned char* dst, unsigned voff, unsigned soff) {
  __builtin_amdgcn_raw_ptr_buffer_load_lds(rs, (LAS void*)dst, 16, voff, soff, I * 1024, AUX_SC1);
}
DI void dma_half(rsrc_t rs, LAS unsigned char* dst, unsigned voff, unsigned soff) {
  dma1<0>(rs, dst, voff, soff); dma1<1>(rs, dst, voff, soff); dma1<2>(rs, dst, voff, soff); dma1<3>(rs, dst, voff, soff);
  dma1<0>(rs, dst + 4096, voff, soff + 4096); dma1<1>(rs, dst + 4096, voff, soff + 4096);
  dma1<2>(rs, dst + 4096, voff, soff + 4096); dma1<3>(rs, dst + 4096, voff, soff + 4096);
}

struct Stats {
  f32x2 sp[8];
  float mean, q, m, den, rden;
  v4u mf;
  DI void load(rsrc_t rs, int lane, unsigned base) {
#pragma unroll
    for (int i = 0; i < 8; ++i) sp[i] = ld8f(rs, (lane >> 5) * 2048 + (lane & 31) * 8 + i * 256, base);
  }
  DI void slice(int k, int hh) {
    if (k == 0) mean = aadd(aadd(aadd(sp[0].x, sp[1].x), aadd(sp[2].x, sp[3].x)), aadd(aadd(sp[4].x, sp[5].x), aadd(sp[6].x, sp[7].x)));
    if (k == 1) { mean = xhalf_sum(mean) * (1.0f / D_); q = aadd(aadd(sp[0].y, sp[1].y), aadd(sp[2].y, sp[3].y)); }
    if (k == 2) { q = aadd(q, aadd(aadd(sp[4].y, sp[5].y), aadd(sp[6].y, sp[7].y))) * 32.0f; m = mean * -32.0f; }
    if (k >= 3 && k < 7) {
      const float d0 = aadd(sp[2 * k - 6].x, m), d1 = aadd(sp[2 * k - 5].x, m);
      q = afma(d0, d0, q); q = afma(d1, d1, q);
    }
    if (k == 7) { q = xhalf_sum(q) * (1.0f / (32.0f * D_)); m = bfr(mean); }
    if (k == 8) den = bfr(__builtin_amdgcn_sqrtf(bfr(bfr(q) + 1e-5f)));
    if (k == 9) {
      rden = __builtin_amdgcn_rcpf(den);
      const unsigned nb = hh ? 0u : (__float_as_uint(m) ^ 0x80000000u) >> 16;
      mf = (v4u){nb * 0x10001u, nb, 0u, 0u};
    }
  }
};

template <int V> using IC = std::integral_constant<int, V>;
template <class FN, int... I> DI void static_for_(FN fn, std::integer_sequence<int, I...>) { (fn(IC<I>{}), ...); }
template <int N, class FN> DI void static_for(FN fn) { static_for_(fn, std::make_integer_sequence<int, N>{}); }
template <int NF, class RD, class STEP>
DI void pipe(RD rd, STEP step) {
  constexpr int LA = 6;
  v4u wf[8];
  static_for<LA>([&](auto i) { wf[i.value] = rd(i); });
  PIN();
  static_for<NF>([&](auto fc) {
    constexpr int f = fc.value;
    step(fc, wf[f & 7], IC<0>{});
    if constexpr (f + LA < NF) wf[(f + LA) & 7] = rd(IC<f + LA>{});
    step(fc, wf[f & 7], IC<1>{});
    PIN();
  });
}
DI v4u csfrag(LAS unsigned char* L, int lane, int tile) {
  const v2u t = *(LAS v2u*)(L + LDS_CS + tile * 256 + (lane & 31) * 8);
  return (v4u){t.x, t.y, 0u, 0u};
}
DI unsigned mulpk(float a, float s0, float b, float s1) {
  unsigned d; float t;
  asm("v_mul_f32 %0, %2, %3\n\tv_mul_f32 %1, %4, %5\n\tv_cvt_pk_bf16_f32 %0, %0, %1" : "=&v"(d), "=&v"(t) : "v"(a), "v"(s0), "v"(b), "v"(s1));
  return d;
}
DI v4u pack8(const float* y) { return (v4u){pk2(y[0], y[1]), pk2(y[2], y[3]), pk2(y[4], y[5]), pk2(y[6], y[7])}; }

struct Fin { v4u o0, o1; f32x2 st; };

constexpr unsigned long long low_mask(int r) {
  unsigned long long m = 0;
  for (int l = 0; l < 64; ++l)
    if ((r & 3) + 8 * (r >> 2) + 4 * (l >> 5) <= (l & 31)) m |= 1ull << l;
  return m;
}
template <int R> DI float sel_low(float lowv, float upv) {
  float d;
  asm("v_cndmask_b32_e64 %0, %1, %2, %3" : "=v"(d) : "v"(upv), "v"(lowv), "s"(low_mask(R)));
  return d;
}
DI float max16(float m, const f32x16& s) {
#pragma unroll
  for (int r = 0; r < 16; r += 2) m = __builtin_fmaxf(__builtin_fmaxf(m, s[r]), s[r + 1]);
  return m;
}
DI v4u exp_pack(const f32x16& s, int h) {
  float pr[8];
#pragma unroll
  for (int r = 0; r < 8; ++r) pr[r] = __builtin_amdgcn_exp2f(s[8 * h + r]);
  return pack8(pr);
}
template <bool EDGE>
DI void attn_unit(LAS unsigned char* L, rsrc_t rs, int lane, int tt, int e, int t0, unsigned obase) {
  const int lo = lane * 16;
  const int kb = LDS_KL + (e * 12 + tt) * 1024 + lo;
  const v4u q = lds16(L, LDS_QL + (e * 4 + tt) * 1024 + lo);
  v4u kf[9];
  kf[8] = lds16(L, kb + 8192); kf[0] = lds16(L, kb);
#pragma unroll
  for (int i = 7; i >= 1; --i) kf[i] = lds16(L, kb + i * 1024);
#define VALID(i) (!EDGE || t0 + (i) >= 0)
  f32x16 s[9];
  s[8] = mfma(kf[8], q, splat16(0.f));
  if (VALID(0)) s[0] = mfma(kf[0], q, splat16(0.f));
  else s[0] = splat16(__int_as_float(vopq(0xff61b1e6)));
  if (VALID(7)) s[7] = mfma(kf[7], q, splat16(0.f));
  if (VALID(6)) s[6] = mfma(kf[6], q, splat16(0.f));
  asm volatile("s_nop 7\n\ts_nop 7" : "+v"(s[8]), "+v"(s[0]));
  PIN();
  static_for<16>([&](auto r) { s[8][r.value] = sel_low<r.value>(s[8][r.value], s[0][r.value]); });
  float m = max16(s[8][0], s[8]);
  PIN();
  static_for<7>([&](auto ic) {
    constexpr int i = 7 - ic.value;
    if constexpr (i - 2 >= 1) { if (VALID(i - 2)) s[i - 2] = mfma(kf[i - 2], q, splat16(0.f)); }
    if (VALID(i)) m = max16(m, s[i]);
    PIN();
  });
  m = xhalf_max(m);
  const f32x16 cm = splat16(-m);
  const int vb = LDS_VL + (e * 24 + 2 * tt) * VBLK + ((lane & 16) ? 512 : (lane >> 5) * 256 + (lane & 15) * 16);
  s[8] = mfma(kf[8], q, cm);
  if (VALID(0)) s[0] = mfma(kf[0], q, cm);
  if (VALID(7)) s[7] = mfma(kf[7], q, cm);
  const v4u mk0 = lds16(L, LDS_MK + lo), mk1 = lds16(L, LDS_MK + 1024 + lo);
  v4u va = lds16(L, vb + 16 * VBLK), vc = lds16(L, vb + 17 * VBLK), v0a = lds16(L, vb), v0c = lds16(L, vb + VBLK);
  asm volatile("s_nop 7\n\ts_nop 7" : "+v"(s[8]), "+v"(s[0]));
  PIN();
  f32x16 o;
  {
    static_for<16>([&](auto r) { s[8][r.value] = sel_low<r.value>(s[8][r.value], s[0][r.value]); });
    const v4u p0 = exp_pack(s[8], 0), l0 = p0 & mk0;
    o = mfma(va, l0, splat16(0.f));
    if (VALID(0)) o = mfma(v0a, p0 ^ l0, o);
    const v4u p1 = exp_pack(s[8], 1), l1 = p1 & mk1;
    o = mfma(vc, l1, o);
    if (VALID(0)) o = mfma(v0c, p1 ^ l1, o);
    va = lds16(L, vb + 14 * VBLK); vc = lds16(L, vb + 15 * VBLK);
    PIN();
  }
  static_for<7>([&](auto ic) {
    constexpr int i = 7 - ic.value;
    if constexpr (i > 1) { if (VALID(i - 1)) s[i - 1] = mfma(kf[i - 1], q, cm); }
    PIN();
    if (VALID(i)) {
      o = mfma(va, exp_pack(s[i], 0), o);
      if constexpr (i > 1) va = lds16(L, vb + (2 * i - 2) * VBLK);
      o = mfma(vc, exp_pack(s[i], 1), o);
      if constexpr (i > 1) vc = lds16(L, vb + (2 * i - 1) * VBLK);
    }
    PIN();
  });
#undef VALID
  const float rl = __builtin_amdgcn_rcpf(o[8]);
  float y[8];
#pragma unroll
  for (int r = 0; r < 8; ++r) y[r] = o[r] * rl;
  st16(rs, pack8(y), lo, obase);
}

struct Retile {
  v4u in[8];
  DI void load(const u16* src, int ld) {
#pragma unroll
    for (int j = 0; j < 8; ++j) in[j] = *(const v4u*)(src + (size_t)(8 * (j >> 2) + (j & 3)) * ld);
  }
  template <int NCS> DI void store(LAS unsigned char* t, rsrc_t rs, int lane, unsigned dst, unsigned cst) {
#pragma unroll
    for (int i = 0; i < 8; ++i) {
      v4u o;
#pragma unroll
      for (int m = 0; m < 4; ++m) o[m] = __builtin_amdgcn_perm(in[2 * m + 1][i >> 1], in[2 * m][i >> 1], (i & 1) ? 0x07060302u : 0x05040100u);
      *(LAS v4u*)(t + (lane >> 3) * 1024 + ((8 * (lane & 7) + i) ^ (lane & 7)) * 16) = o;
    }
    asm volatile("s_waitcnt lgkmcnt(0)" ::: "memory");
    float cs = 0.f;
#pragma unroll
    for (int i = 0; i < 8; ++i) {
      const v4u o = *(LAS v4u*)(t + i * 1024 + (lane ^ (lane >> 3)) * 16);
      st16(rs, o, lane * 16 + (unsigned)__builtin_amdgcn_readlane((int)dst, 8 * i), 0);
      if (i < 2 * NCS) {
#pragma unroll
        for (int m = 0; m < 4; ++m) cs += bflo(o[m]) + bfhi(o[m]);
        if (i & 1) {
          cs = xhalf_sum(cs);
          if (lane < 32) __builtin_amdgcn_raw_buffer_store_b32(__float_as_uint(cs), rs, lane * 4, cst + (i >> 1) * 128, AUX_SC1);
          cs = 0.f;
        }
      }
    }
  }
};
DI void cs_gather(LAS unsigned char* L, rsrc_t rs, int c, int v) {
  float a[16];
#pragma unroll
  for (int q = 0; q < 16; ++q) a[q] = __uint_as_float(__builtin_amdgcn_raw_buffer_load_b32(rs, v * 4, CS_OFF + c * 20480 + q * 1280, AUX_SC1));
  float cs = 0.f;
#pragma unroll
  for (int q = 0; q < 16; ++q) cs += a[q];
  const float hi = bfr(cs), mid = bfr(cs - hi), lw = (cs - hi) - mid;
  *(LAS v2u*)(L + LDS_CS + v * 8) = (v2u){pk2(hi, mid), pk2(lw, 0.f)};
}

__global__ void __launch_bounds__(512, 2) k_main(const u16* __restrict__ x, const u16* __restrict__ wqkv, const u16* __restrict__ wo, const u16* __restrict__ wg,
                                                 const u16* __restrict__ wu, const u16* __restrict__ wd, unsigned char* __restrict__ ws,
                                                 u16* __restrict__ out) {
  extern __shared__ __attribute__((aligned(16))) unsigned char smem[];
  LAS unsigned char* L = (LAS unsigned char*)smem;
  const int tid = threadIdx.x, lane0 = tid & 63, w0 = __builtin_amdgcn_readfirstlane(tid >> 6);
  const int b = blockIdx.x, xcd = b & 7, idx = b >> 3;
  const int p0 = 4 * (xcd >> 1) + (idx >> 3), c0 = (idx & 7) + 8 * (xcd & 1);
  const rsrc_t rs = __builtin_amdgcn_make_buffer_rsrc((void*)ws, 0, (int)WS_BYTES, 0x00020000);
  const bool compute = w0 < 4;
  gu32* const tmo = (gu32*)(ws + FLAG_OFF) + FL_TMO * 32;
  auto issue = [&](int m) {
    const int c = sopq(c0), ct = c >> 1, lo = lane_id() * 16;
    const unsigned lhalf = (unsigned)sopq(w0 & 1) * 8192u;
    unsigned src;
    if (m < 8) src = WQKV_OFF + c * 98304 + m * SLAB;
    else if (m < 12) src = WO_OFF + ct * 65536 + (m - 8) * SLAB;
    else if (m < 26) src = WGU_OFF + c * 229376 + (m - 12) * SLAB;
    else src = WDN_OFF + ct * 229376 + (m - 26) * SLAB;
    dma_half(rs, L + (m & 7) * SLAB + lhalf, lo, src + lhalf);
  };
#define GEMM64_PRE(AOFF) \
    _Pragma("unroll") for (int s = 0; s < 8; ++s) ha[s] = ld16(rs, lo, AOFF);
#define GEMM64(NS, SLOT0, RING, FIRSTBAR, AOFF, FIN) \
  { \
    f32x16 keep, send; \
    const int bk = lo + kh * 3072, bs = lo + kh * 1024 + 1024; \
    pipe<8 * NS>( \
        [&](auto fc) { \
          constexpr int f = fc.value; \
          if (f % 8 == 0 && (f > 0 || FIRSTBAR)) BARX(); \
          if (f == 0 && FIRSTBAR) { GEMM64_PRE(AOFF) } \
          return lds16(L, ((f & 1) ? bs : bk) + ((SLOT0 + f / 8) & 7) * SLAB + ((f >> 1) & 3) * 4096); \
        }, \
        [&](auto fc, const v4u& wfr, auto pc) { \
          constexpr int f = fc.value, part = pc.value, s0 = f >> 1; \
          if (part == 0) { \
            if (f & 1) send = mfma(wfr, ha[s0 % RING], f == 1 ? splat16(0.f) : send); \
            else keep = mfma(wfr, ha[s0 % RING], f == 0 ? splat16(0.f) : keep); \
          } else { \
            constexpr int s = f < 32 ? f + 8 : (f >= 35 && (f & 1)) ? (f - 3) / 2 + 24 : 1000; \
            if constexpr (s < 4 * NS) ha[s % RING] = ld16(rs, lo, AOFF); \
          } \
        }); \
    asm volatile("" : "+v"(keep), "+v"(send)); \
    _Pragma("unroll") for (int q = 0; q < 4; ++q) \
      *(LAS f32x4*)(L + LDS_RED + (tt * 2 + 1 - kh) * 4096 + q * 1024 + lo) = (f32x4){send[4 * q], send[4 * q + 1], send[4 * q + 2], send[4 * q + 3]}; \
    float y[16]; \
    _Pragma("unroll") for (int m = 0; m < 4; ++m) { \
      y[2 * m] = keep[2 * m] + bflo(r0[m]); y[2 * m + 1] = keep[2 * m + 1] + bfhi(r0[m]); \
      y[8 + 2 * m] = keep[8 + 2 * m] + bflo(r1[m]); y[8 + 2 * m + 1] = keep[8 + 2 * m + 1] + bfhi(r1[m]); \
    } \
    BAR(); \
    _Pragma("unroll") for (int q = 0; q < 4; ++q) { \
      const f32x4 o = lds4f(L, LDS_RED + (tt * 2 + kh) * 4096 + q * 1024 + lo); \
      _Pragma("unroll") for (int i = 0; i < 4; ++i) y[4 * q + i] += o[i]; \
    } \
    FIN.o0 = pack8(y); FIN.o1 = pack8(y + 8); \
  }
#define ROWSTAT(FIN) \
  { \
    float z[16]; \
    _Pragma("unroll") for (int m = 0; m < 4; ++m) { \
      z[2 * m] = bflo(FIN.o0[m]); z[2 * m + 1] = bfhi(FIN.o0[m]); z[8 + 2 * m] = bflo(FIN.o1[m]); z[8 + 2 * m + 1] = bfhi(FIN.o1[m]); \
    } \
    float s = 0.f; \
    _Pragma("unroll") for (int i = 0; i < 16; ++i) s += z[i]; \
    s = xhalf_sum(s); \
    const float mu = s * (1.0f / 32.0f); \
    float q = 0.f; \
    _Pragma("unroll") for (int i = 0; i < 16; ++i) q += (z[i] - mu) * (z[i] - mu); \
    FIN.st = (f32x2){s, xhalf_sum(q)}; \
  }

  unsigned tag = 0;
  Retile rt, rt2;
  constexpr unsigned NONE = 0x40000000u;
  {
    const int lane = lane0, p = p0, c = c0;
    const int fl = lane >> 3, hh = (lane >> 2) & 1, nc = lane & 3;
    gu32* const f0 = (gu32*)(ws + FLAG_OFF);
    if (w0 < 4) {
      const int T = 4 * p + w0, t = lane & 31, gh = lane >> 5;
      const v4u* row = (const v4u*)(x + (size_t)(32 * T + t) * D_ + 32 * c + 16 * gh);
      const v4u a = row[0], bq = row[1];
      const unsigned xo = X0_OFF + (T * 32 + 2 * c) * 1024;
      st16(rs, (v4u){a.x, a.y, bq.x, bq.y}, gh * 1024 + t * 16, xo);
      st16(rs, (v4u){a.z, a.w, bq.z, bq.w}, gh * 1024 + 512 + t * 16, xo);
      float s = 0.f;
#pragma unroll
      for (int m = 0; m < 4; ++m) s += (bflo(a[m]) + bfhi(a[m])) + (bflo(bq[m]) + bfhi(bq[m]));
      s = xhalf_sum(s);
      const float mu = s * (1.0f / 32.0f);
      float q = 0.f;
#pragma unroll
      for (int m = 0; m < 4; ++m) {
        const float e0 = bflo(a[m]) - mu, e1 = bfhi(a[m]) - mu, e2 = bflo(bq[m]) - mu, e3 = bfhi(bq[m]) - mu;
        q += (e0 * e0 + e1 * e1) + (e2 * e2 + e3 * e3);
      }
      q = xhalf_sum(q);
      if (lane < 32) st8f(rs, (f32x2){s, q}, lane * 8, X0_OFF + MB2 + T * 4096 + c * 256);
      DRAIN();
    } else if (w0 == 5) {
      const int tile = fl >> 1, g = 2 * p + (fl & 1), ld = tile < 3 ? 3 * D_ : D_;
      rt.load((tile < 3 ? wqkv + tile * D_ : wo) + (size_t)(16 * g + 4 * hh) * ld + 32 * c + 8 * nc, ld);
      rt.store<3>(L + LDS_KL, rs, lane,
                  tile < 3 ? WQKV_OFF + c * 98304 + (tile == 0 ? 64 + g : 2 * g + tile - 1) * 1024
                           : WO_OFF + (c >> 1) * 65536 + (((g >> 2) & 3) * 16 + ((g & 3) * 2 + (g >> 4)) * 2 + (c & 1)) * 1024,
                  CS_OFF + c * 20480 + p * 1280);
      DRAIN();
    } else if (w0 == 4) {
      tag = __builtin_amdgcn_readfirstlane(__hip_atomic_load(f0 + FL_EP * 32, RLX_AGENT)) + 1u;
      if (c == 0 && lane < 14) {
        const int l = lane / 7, i = lane % 7;
        __hip_atomic_store(f0 + l * (FLAG_LAYER / 4) + (i < 3 ? 16 * i + p : FL_H + p * 8 + i - 3) * 32, 0u, RLX_AGENT);
      }
      v4u mk[2];
#pragma unroll
      for (int k = 0; k < 8; ++k) {
        const int kl = ((2 * k) & 3) + 8 * ((2 * k) >> 2) + 4 * (lane >> 5), ql = lane & 31;
        mk[k >> 2][k & 3] = (kl <= ql ? 0xffffu : 0u) | (kl + 1 <= ql ? 0xffff0000u : 0u);
      }
      *(LAS v4u*)(L + LDS_MK + lane * 16) = mk[0];
      *(LAS v4u*)(L + LDS_MK + 1024 + lane * 16) = mk[1];
      DRAIN();
    } else {
      const int j = 4 * (w0 - 6) + (fl < 6 || w0 == 6 ? fl >> 1 : 0), g = 2 * p + (fl & 1);
      rt.load(((nc < 2) ? wg : wu) + 112 * c + 16 * j + 8 * (nc & 1) + (size_t)(16 * g + 4 * hh) * F_, F_);
      if (w0 == 7) rt2.load(wd + (size_t)(16 * (7 * p + (fl < 7 ? fl : 0)) + 4 * hh) * D_ + 32 * c + 8 * nc, D_);
    }
    BAR();
    if (w0 == 4 && lane < 2) __hip_atomic_store(f0 + (lane ? (FL_SW + c) * 32 + p : (FL_SC + p) * 32 + c), tag, RLX_AGENT);
  }

#define LAYER_VARS() \
  const bool last = layer == 1; \
  const unsigned lay0 = LAY_OFF + layer * LAY_BYTES; \
  const unsigned xin0 = layer == 0 ? X0_OFF : LAY_OFF + L_X2; \
  gu32* const fl = (gu32*)(ws + FLAG_OFF + layer * FLAG_LAYER); \
  (void)last; (void)fl; (void)xin0;
  auto attention = [&](int layer) {
    LAYER_VARS();
    BAR();
    {
      COORDS();
      const int Tg = 4 * p - 8 + w;
      if (Tg >= 0) {
        const unsigned so = lay + (Tg * 32 + 2 * c) * 1024;
        const v4u k0 = ld16(rs, lo, so + L_K), k1 = ld16(rs, lo + 1024, so + L_K);
        const v4u v0 = ld16(rs, lo, so + L_V), v1 = ld16(rs, lo + 1024, so + L_V);
        *(LAS v4u*)(L + LDS_KL + w * 1024 + lo) = k0;
        *(LAS v4u*)(L + LDS_KL + (12 + w) * 1024 + lo) = k1;
        *(LAS v4u*)(L + LDS_VL + (2 * w + hh) * VBLK + (lane & 31) * 16) = v0;
        *(LAS v4u*)(L + LDS_VL + (24 + 2 * w + hh) * VBLK + (lane & 31) * 16) = v1;
      }
    }
    BAR();
    {
      COORDS();
      const int tt = w & 3, e = w >> 2;
      const unsigned ob = lay + L_O + ((4 * p + tt) * 32 + 2 * c + e) * 1024;
      if (p >= 2) attn_unit<false>(L, rs, lane, tt, e, 0, ob);
      else attn_unit<true>(L, rs, lane, tt, e, 4 * p - 8 + tt, ob);
      DRAIN();
    }
    BAR();

  };

  if (compute) {
    for (int layer = 0; layer < 2; ++layer) {
    LAYER_VARS();
    {
      COORDS();
      const int T = 4 * p + w;
      BAR();
      Stats st;
      st.load(rs, lane, xin + MB2 + T * 4096);
      v4u xa[32];
      const unsigned xb = xin + T * 32768;
#pragma unroll
      for (int ks = 0; ks < 8; ++ks) xa[ks] = ld16(rs, lo + (ks & 3) * 1024, xb + (ks >> 2) * 4096);
      f32x16 aq, ak, av;
      v4u o0, o1; f32x4 rt[4];
      const unsigned kvb = lay + (T * 32 + 2 * c) * 1024;
      const int ve = (lane >> 4) & 1, vo = hh * 256 + (lane & 15) * 16;
      pipe<99>(
          [&](auto fc) {
            constexpr int t = fc.value, f = t < 64 ? t : t - 2;
            if constexpr (t == 64 || t == 65) return csfrag(L, lane, t - 63);
            else if constexpr (t == 98) return csfrag(L, lane, 0);
            else {
              if (f % 16 == 0) BARX();
              return lds16(L, lo + f * 1024);
            }
          },
          [&](auto fc, const v4u& wfr, auto pc) {
            constexpr int t = fc.value, part = pc.value;
            if constexpr (part == 0) {
              if constexpr (t == 98) aq = mfma(wfr, st.mf, aq);
              else if constexpr (t >= 66) aq = mfma(wfr, xa[t - 66], t == 66 ? splat16(0.f) : aq);
              else if constexpr (t == 65) av = mfma(st.mf, wfr, av);
              else if constexpr (t == 64) ak = mfma(wfr, st.mf, ak);
              else if constexpr (t & 1) av = mfma(xa[t >> 1], wfr, t == 1 ? splat16(0.f) : av);
              else ak = mfma(wfr, xa[t >> 1], t == 0 ? splat16(0.f) : ak);
              return;
            }
            if constexpr (t < 24) xa[t + 8] = ld16(rs, lo + (t & 3) * 1024, xb + ((t + 8) >> 2) * 4096);
            if (t >= 28 && t < 38) st.slice(t - 28, hh);
            if (t == 38) { if (hh == 0) *(LAS float*)(L + LDS_ST + w * 128 + lane * 4) = st.rden; }
            if constexpr (t >= 67 && t < 75) {
              constexpr int r = t - 67;
              const unsigned v = mulpk(ak[2 * r], st.rden, ak[2 * r + 1], st.rden);
              if constexpr (r < 4) o0[r] = v; else o1[r - 4] = v;
              if constexpr (r == 3) { st16(rs, o0, lo, kvb + L_K); *(LAS v4u*)(L + LDS_KL + (8 + w) * 1024 + lo) = o0; }
              if constexpr (r == 7) { st16(rs, o1, lo + 1024, kvb + L_K); *(LAS v4u*)(L + LDS_KL + (12 + 8 + w) * 1024 + lo) = o1; }
            }
            if constexpr (t >= 69 && t < 77 && (t & 1)) rt[(t - 69) >> 1] = lds4f(L, LDS_ST + w * 128 + ((t - 69) >> 1) * 32 + hh * 16);
            if constexpr (t >= 75 && t < 83) {
              constexpr int r = t - 75;
              const unsigned v = mulpk(av[2 * r], rt[r >> 1][2 * (r & 1)], av[2 * r + 1], rt[r >> 1][2 * (r & 1) + 1]);
              if constexpr (r < 4) o0[r] = v; else o1[r - 4] = v;
              if constexpr (r == 3) { st16(rs, o0, ve * 1024 + vo, kvb + L_V); *(LAS v4u*)(L + LDS_VL + (ve * 24 + 16 + 2 * w) * VBLK + vo) = o0; }
              if constexpr (r == 7) { st16(rs, o1, ve * 1024 + vo + 512, kvb + L_V); *(LAS v4u*)(L + LDS_VL + (ve * 24 + 17 + 2 * w) * VBLK + vo) = o1; }
            }
          });
      asm volatile("" : "+v"(aq));
      DRAIN();
      BAR();
      {
        const float qs = st.rden * (0.25f * LOG2E);
        float y[16];
#pragma unroll
        for (int r = 0; r < 16; ++r) y[r] = aq[r] * qs;
        *(LAS v4u*)(L + LDS_QL + w * 1024 + lo) = pack8(y);
        *(LAS v4u*)(L + LDS_QL + (4 + w) * 1024 + lo) = pack8(y + 8);
      }
    }
    attention(layer);
    {
      {
        COORDS();
        const int tt = w & 1, kh = w >> 1;
        const int T2 = 4 * p + 2 * rh + tt, og = 2 * (2 * ct + kh);
        const v4u r0 = ld16(rs, lo, xin + (T2 * 32 + og) * 1024), r1 = ld16(rs, lo + 1024, xin + (T2 * 32 + og) * 1024);
        const unsigned ab = lay + L_O + (T2 * 32 + 16 * kh) * 1024;
        Fin fin;
        v4u ha[16];
        GEMM64(4, 8, 16, true, ab + s * 1024, fin);
        st16(rs, fin.o0, lo, lay + L_X1 + (T2 * 32 + og) * 1024);
        st16(rs, fin.o1, lo + 1024, lay + L_X1 + (T2 * 32 + og) * 1024);
        ROWSTAT(fin);
        if (hh == 0) st8f(rs, fin.st, lane * 8, lay + L_X1 + MB2 + T2 * 4096 + og * 128);
        DRAIN();
        BAR();
      }
      {
        COORDS();
        const int T = 4 * p + w;
        BAR();
        Stats st;
        st.load(rs, lane, lay + L_X1 + MB2 + T * 4096);
        v4u xa[32];
        const unsigned xb = lay + L_X1 + T * 32768;
#pragma unroll
        for (int ks = 0; ks < 8; ++ks) xa[ks] = ld16(rs, lo + (ks & 3) * 1024, xb + (ks >> 2) * 4096);
        f32x16 acc[3];
        v4u hv; float k1, den2, t0, t1, p0_, p1_;
        const unsigned hb = lay + L_H + (T * 112 + 7 * c) * 1024;
        auto epi = [&](auto jc, auto kc) {
          constexpr int j = jc.value, k = kc.value, i = k / 3;
          const f32x16& a = acc[j % 3];
          if constexpr (k % 3 == 0)
            asm("v_mul_f32 %0, %2, %4\n\tv_mul_f32 %1, %3, %4\n\tv_exp_f32 %0, %0\n\tv_exp_f32 %1, %1"
                : "=&v"(t0), "=&v"(t1) : "v"(a[2 * i]), "v"(a[2 * i + 1]), "v"(k1));
          else if constexpr (k % 3 == 1)
            asm("v_mul_f32 %2, %4, %6\n\tv_fma_f32 %0, %0, %8, %8\n\tv_mul_f32 %3, %5, %7\n\tv_fma_f32 %1, %1, %8, %8"
                : "+v"(t0), "+v"(t1), "=&v"(p0_), "=&v"(p1_) : "v"(a[2 * i]), "v"(a[2 * i + 1]), "v"(a[8 + 2 * i]), "v"(a[8 + 2 * i + 1]), "v"(den2));
          else {
            unsigned d;
            asm("v_rcp_f32 %1, %1\n\tv_rcp_f32 %2, %2\n\tv_mul_f32 %1, %1, %3\n\tv_mul_f32 %2, %2, %4\n\tv_cvt_pk_bf16_f32 %0, %1, %2"
                : "=v"(d), "+v"(t0), "+v"(t1) : "v"(p0_), "v"(p1_));
            hv[i] = d;
            if constexpr (k == 11) st16(rs, hv, lo, hb + j * 1024);
          }
        };
        pipe<231>(
            [&](auto fc) {
              constexpr int t = fc.value, j = t < 66 ? (t & 1) : 2 + (t - 66) / 33, ks = t < 66 ? t >> 1 : (t - 66) % 33;
              if constexpr (ks == 32) return csfrag(L, lane, 3 + j);
              else {
                constexpr int f = t < 64 ? t : 32 * j + ks;
                if (f % 16 == 0) {
                  if (f == 144 || f == 176 || f == 208) DRAIN();
                  BARX();
                }
                return lds16(L, lo + ((12 + f / 16) & 7) * SLAB + (f % 16) * 1024);
              }
            },
            [&](auto fc, const v4u& wfr, auto pc) {
              constexpr int t = fc.value, part = pc.value, j = t < 66 ? (t & 1) : 2 + (t - 66) / 33, ks = t < 66 ? t >> 1 : (t - 66) % 33;
              if constexpr (part == 0) {
                if constexpr (ks == 32) acc[j % 3] = mfma(wfr, st.mf, acc[j % 3]);
                else acc[j % 3] = mfma(wfr, xa[ks], ks == 0 ? splat16(0.f) : acc[j % 3]);
                return;
              }
              if constexpr (t < 24) xa[t + 8] = ld16(rs, lo + (t & 3) * 1024, xb + ((t + 8) >> 2) * 4096);
              if (t >= 28 && t < 38) st.slice(t - 28, hh);
              if (t == 38) { k1 = st.rden * -LOG2E; den2 = st.den * st.den; }
              if constexpr (t >= 66) {
                if constexpr (j == 2) {
                  if constexpr (ks >= 1 && ks < 13) epi(IC<0>{}, IC<ks - 1>{});
                  if constexpr (ks >= 13 && ks < 25) epi(IC<1>{}, IC<ks - 13>{});
                } else if constexpr (ks >= 12 && ks < 24) epi(IC<j - 1>{}, IC<ks - 12>{});
              }
            });
        asm volatile("s_nop 7\n\ts_nop 7" : "+v"(acc[0]));
        static_for<12>([&](auto kc) { epi(IC<6>{}, kc); });
      }
      {
        COORDS();
        const int tt = w & 1, kh = w >> 1;
        const int T2 = 4 * p + 2 * rh + tt, og = 2 * (2 * ct + kh);
        const unsigned ab = lay + L_H + (T2 * 112 + 56 * kh) * 1024;
#define DN_AOFF ab + (7 * (4 * ((s >> 2) & 1) + (s & 3)) + (s >> 3)) * 1024
        v4u ha[24];
        GEMM64_PRE(DN_AOFF)
        const v4u r0 = ld16(rs, lo, lay + L_X1 + (T2 * 32 + og) * 1024), r1 = ld16(rs, lo + 1024, lay + L_X1 + (T2 * 32 + og) * 1024);
        wait_vm<10>();
        BAR();
        Fin fin;
        GEMM64(14, 26, 24, false, DN_AOFF, fin);
        if (!last) {
          st16(rs, fin.o0, lo, lay + L_X2 + (T2 * 32 + og) * 1024);
          st16(rs, fin.o1, lo + 1024, lay + L_X2 + (T2 * 32 + og) * 1024);
          ROWSTAT(fin);
          if (hh == 0) st8f(rs, fin.st, lane * 8, lay + L_X2 + MB2 + T2 * 4096 + og * 128);
          DRAIN();
          BAR();
        } else {
          u16* orow = out + (size_t)(32 * T2 + (lane & 31)) * D_ + 16 * og + 4 * hh;
          *(v2u*)(orow) = (v2u){fin.o0.x, fin.o0.y};
          *(v2u*)(orow + 8) = (v2u){fin.o0.z, fin.o0.w};
          *(v2u*)(orow + 16) = (v2u){fin.o1.x, fin.o1.y};
          *(v2u*)(orow + 24) = (v2u){fin.o1.z, fin.o1.w};
        }
      }
    }
    }
  } else {
    auto qkv_helper = [&](auto lc) {
      constexpr int layer = lc.value;
      LAYER_VARS();
      COORDS();
      const bool poller = w == 4, loader = (w == 5) || (w == 6);
      if (poller) {
        if (layer == 0) poll(fl + (lane < 16 ? (FL_SC + p) * 32 + lane : (FL_SW + 2 * ct + (lane >> 5)) * 32 + (lane & 15)), tag, true, lane < 48, tmo, tag);
        else poll_ge(fl + (FL_XN + p) * 32, 16u, lane == 0, tmo);
      }
      BAR();
      if (layer == 0) {
        if (loader) { for (int m = 0; m < 6; ++m) issue(m); }
        if (w == 7) { cs_gather(L, rs, c, lane); if (lane < 32) cs_gather(L, rs, c, 64 + lane); }
      }
      if (loader) wait_vm<40>();
      BAR();
      if (loader) wait_vm<32>();
      BARX();
      for (int n = 2; n <= 5; ++n) {
        if (loader) wait_vm<24>();
        BARS(n == 4);
        if (loader) issue(n + 6);
      }
      BAR();
      if (layer == 0) {
        const int fr = lane >> 3;
        if (w >= 6) {
          const int j = 4 * (w - 6) + (fr >> 1), g = 2 * p + (fr & 1);
          const unsigned dst = j < 7 ? WGU_OFF + c * 229376 + (j < 2 ? 2 * g + j : 32 * j + g) * 1024 : NONE;
          const unsigned cst = CS_OFF + c * 20480 + p * 1280 + (3 + 4 * (w - 6)) * 128;
          if (w == 6) rt.store<4>(L + 4 * SLAB, rs, lane, dst, cst); else rt.store<3>(L + 5 * SLAB, rs, lane, dst, cst);
        }
        if (w == 7)
          rt2.store<0>(L + 5 * SLAB + 8192, rs, lane,
                       fr < 7 ? WDN_OFF + ct * 229376 + ((2 * fr + ((p >> 2) & 1)) * 16 + ((p & 3) * 2 + (p >> 3)) * 2 + rh) * 1024 : NONE, 0);
      } else if (loader) { issue(12); issue(13); }
      if (w == 7 && lane < 48) { const unsigned one = (unsigned)vopq(0x3f803f80); *(LAS v4u*)(L + LDS_VL + lane * VBLK + 512) = (v4u){one, one, one, one}; }
      if (poller) {
        if (lane == 0) __hip_atomic_store(fl + (FL_KV + p * 16 + c) * 32, tag, RLX_AGENT);
        const int pp = p - 1 - (lane & 1);
        poll(fl + (FL_KV + (pp < 0 ? 0 : pp) * 16 + c) * 32, tag, true, lane < 2 && pp >= 0, tmo, tag);
      }
    };
    qkv_helper(IC<0>{});
    for (int layer = 0;;) {
      attention(layer);
      {
      LAYER_VARS();
      COORDS();
      const bool poller = w == 4, loader = (w == 5) || (w == 6);
      if (loader && layer > 0) { issue(14); issue(15); }
      if (poller) {
        if (lane == 0) __hip_atomic_fetch_add(fl + (FL_O + p) * 32, 1u, RLX_AGENT);
        if (lane == 1 && layer == 0) __hip_atomic_store(fl + (FL_WR + ct) * 32 + p * 2 + rh, tag, RLX_AGENT);
        poll(fl + (lane < 32 ? (FL_O + p) * 32 : (FL_WR + ct) * 32 + (lane & 31)), lane < 32 ? 16u : tag, true, lane < 32 ? lane == 0 : layer == 0, tmo, tag);
      }
      for (int n = 8; n <= 11; ++n) {
        if (loader) wait_vm<40>();
        BARS(n == 8);
        if (loader && n == 8 && layer == 0) { issue(12); issue(13); issue(14); issue(15); }
        if (loader && n >= 10) issue(n + 6);
      }
      BAR();
      if (loader) { issue(18); issue(19); }
      BAR();
      if (w == 7 && layer == 0) {
        cs_gather(L, rs, c, 96 + lane); cs_gather(L, rs, c, 160 + lane); cs_gather(L, rs, c, 224 + lane);
        if (lane < 32) cs_gather(L, rs, c, 288 + lane);
      }
      if (poller) {
        if (lane == 0) __hip_atomic_fetch_add(fl + (FL_X1 + p) * 32, 1u, RLX_AGENT);
        poll_ge(fl + (FL_X1 + p) * 32, 16u, lane == 0, tmo);
      }
      BAR();
      gu32* const hw = fl + (FL_H + p * 8 + (lane & 3)) * 32;
      unsigned hv = 0;
#define GATE(s_) do { if (!(__builtin_amdgcn_readlane(hv, s_) >= 16u)) poll_ge(hw, 16u, lane == s_, tmo); } while (0)
      for (int n = 12; n <= 25; ++n) {
        if (poller) {
          if (n == 25) GATE(0);
          if (n == 24) hv = __hip_atomic_load(hw, RLX_AGENT);
        }
        if (loader) wait_vm<40>();
        BARS(n == 12 || n == 16 || n == 20);
        if (loader && n >= 14) issue(n + 6);
        if (poller && n >= 21 && (n & 1) && lane == 0) __hip_atomic_fetch_add(fl + (FL_H + p * 8 + ((n - 21) >> 1)) * 32, 1u, RLX_AGENT);
      }
      if (loader) wait_vm<40>();
      BAR();
      if (loader) { issue(32); issue(33); }
      if (poller && lane == 0) __hip_atomic_fetch_add(fl + (FL_H + p * 8 + 3) * 32, 1u, RLX_AGENT);
      for (int n = 27; n <= 34; ++n) {
        if (poller) {
          if (n == 28) GATE(1);
          if (n == 29) GATE(2);
          if (n == 31) GATE(3);
          if (n == 27 || n == 30) hv = __hip_atomic_load(hw, RLX_AGENT);
        }
        if (loader) wait_vm<40>();
        BARS(n == 30);
        if (loader && n >= 28) { if (n < 34) issue(n + 6); else if (!last) issue(0); }
      }
      if (!last) {
        for (int n = 35; n <= 39; ++n) {
          if (loader) wait_vm<40>();
          BARX();
          if (loader) issue(n - 34);
        }
        BAR();
        BAR();
        if (poller && lane == 0) __hip_atomic_fetch_add(fl + FLAG_LAYER / 4 + (FL_XN + p) * 32, 1u, RLX_AGENT);
      } else {
        if (loader) wait_vm<32>();
        BARX();
        if (loader) wait_vm<24>();
        BARX();
        if (loader) wait_vm<16>();
        BARX();
        if (loader) wait_vm<8>();
        BARX();
        if (loader) wait_vm<0>();
        BARX();
        BAR();
        if (poller && blockIdx.x == 0) {
          gu32* const f0 = (gu32*)(ws + FLAG_OFF);
          for (int q = 0; q < 4; ++q) poll(f0 + (FL_SC + 4 * q + (lane >> 4)) * 32 + (lane & 15), tag, true, true, tmo, tag);
          if (lane == 0) __hip_atomic_store(f0 + FL_EP * 32, tag, RLX_AGENT);
        }
      }
          }
      if (layer == 1) break;
      layer = 1;
      qkv_helper(IC<1>{});
    }
  }
}

}

extern "C" void kernel_launch(void* const* d_in, const int* in_sizes, int n_in, void* d_out, int out_size, void* d_ws, size_t ws_size,
                              hipStream_t stream) {
  (void)in_sizes; (void)n_in; (void)out_size; (void)ws_size;
  (void)hipFuncSetAttribute((const void*)k_main, hipFuncAttributeMaxDynamicSharedMemorySize, LDS_BYTES);
  k_main<<<256, 512, LDS_BYTES, stream>>>((const u16*)d_in[0], (const u16*)d_in[1], (const u16*)d_in[2], (const u16*)d_in[4], (const u16*)d_in[5],
                                          (const u16*)d_in[6], (unsigned char*)d_ws, (u16*)d_out);
}
```

```cpp
#include <hip/hip_runtime.h>
#include <stdint.h>
#include <utility>

#define LAS __attribute__((address_space(3)))
#define GAS __attribute__((address_space(1)))
#define DI __device__ __forceinline__
#define RLX_AGENT __ATOMIC_RELAXED, __HIP_MEMORY_SCOPE_AGENT
typedef unsigned short u16;
typedef unsigned v4u __attribute__((ext_vector_type(4)));
typedef unsigned v2u __attribute__((ext_vector_type(2)));
typedef float f32x2 __attribute__((ext_vector_type(2)));
typedef float f32x4 __attribute__((ext_vector_type(4)));
typedef float f32x16 __attribute__((ext_vector_type(16)));
typedef __bf16 bf16x2 __attribute__((ext_vector_type(2)));
typedef __bf16 bf16x8 __attribute__((ext_vector_type(8)));
typedef GAS unsigned gu32;
typedef GAS unsigned long long gu64;

namespace {

constexpr int D_ = 512, F_ = 1792;
constexpr unsigned MB2 = 2097152u, STB = 262144u;
constexpr unsigned FLAG_LAYER = 1048576u, FLAG_G = 2 * FLAG_LAYER, FLAG_BYTES = FLAG_G + 65536;
constexpr unsigned WQKV_OFF = FLAG_BYTES;
constexpr unsigned WO_OFF = WQKV_OFF + 32 * 49152;
constexpr unsigned WGU_OFF = WO_OFF + 16 * 32768;
constexpr unsigned WDN_OFF = WGU_OFF + 32 * 114688;
constexpr unsigned LAY_OFF = WDN_OFF + 16 * 114688;
constexpr unsigned L_K = 0, L_V = MB2, L_O = 2 * MB2, L_X1 = 3 * MB2, L_X = 4 * MB2 + STB, L_H = 5 * MB2 + 2 * STB;
constexpr unsigned LAY_BYTES = L_H + 128 * 57344;
constexpr unsigned WS_BYTES = LAY_OFF + 2 * LAY_BYTES;
constexpr unsigned NONE = 0x40000000u;
constexpr int FL_KV = 0, FL_O = 2048, FL_X1B = 4096, FL_X1A = 6144, FL_HB = 6400, FL_HA = 6656, FL_XB = 6912, FL_XA = 7168, FL_HA2 = 7424;
constexpr int FL_WR = 0, FL_EP = 256, FL_TMO = 257;
constexpr int VBLK = 528;
constexpr int LDS_KL = 114688, LDS_VL = LDS_KL + 16384, LDS_QL = LDS_VL + 32 * VBLK, LDS_CS = LDS_QL + 8192, LDS_ST = LDS_CS + 1536;
constexpr int LDS_MK = LDS_ST + 1536, LDS_TAG = LDS_MK + 2048;
constexpr int LDS_WO = 81920, LDS_TB = 49152;
constexpr int LDS_BYTES = 163840;
#define TSTR 1048576u
constexpr unsigned SPIN_CAP = 1u << 16;
constexpr int AUX_SC1 = 16;
constexpr float LOG2E = 1.4426950408889634f;

DI f32x16 mfma(v4u a, v4u b, f32x16 c) {
  return __builtin_amdgcn_mfma_f32_32x32x16_bf16(__builtin_bit_cast(bf16x8, a), __builtin_bit_cast(bf16x8, b), c, 0, 0, 0);
}
DI f32x4 mfma16(v4u a, v4u b, f32x4 c) {
  return __builtin_amdgcn_mfma_f32_16x16x32_bf16(__builtin_bit_cast(bf16x8, a), __builtin_bit_cast(bf16x8, b), c, 0, 0, 0);
}
DI unsigned pk2(float lo, float hi) {
  f32x2 p = {lo, hi};
  return __builtin_bit_cast(unsigned, __builtin_convertvector(p, bf16x2));
}
DI float bfr(float x) { return __uint_as_float(pk2(x, 0.f) << 16); }
DI float bflo(unsigned u) { return __uint_as_float(u << 16); }
DI float bfhi(unsigned u) { return __uint_as_float(u & 0xffff0000u); }
DI float amul(float a, float b) { float d; asm("v_mul_f32 %0, %1, %2" : "=v"(d) : "v"(a), "v"(b)); return d; }
DI float afma(float a, float b, float c) { float d; asm("v_fma_f32 %0, %1, %2, %3" : "=v"(d) : "v"(a), "v"(b), "v"(c)); return d; }
DI float aadd(float a, float b) { float d; asm("v_add_f32 %0, %1, %2" : "=v"(d) : "v"(a), "v"(b)); return d; }
DI float asub(float a, float b) { float d; asm("v_sub_f32 %0, %1, %2" : "=v"(d) : "v"(a), "v"(b)); return d; }
DI float xhalf_sum(float v) {
  float t;
  asm("v_mov_b32 %1, %0\n\ts_nop 1\n\tv_permlane32_swap_b32 %0, %1\n\tv_add_f32 %0, %0, %1" : "+v"(v), "=&v"(t));
  return v;
}
DI float xrow_sum(float v) {
  float t;
  asm("v_mov_b32 %1, %0\n\ts_nop 1\n\tv_permlane16_swap_b32 %0, %1\n\tv_add_f32 %0, %0, %1" : "+v"(v), "=&v"(t));
  return v;
}
DI float xhalf_max(float v) {
  auto r = __builtin_amdgcn_permlane32_swap(__float_as_uint(v), __float_as_uint(v), false, false);
  return fmaxf(__uint_as_float(r[0]), __uint_as_float(r[1]));
}
DI void swap16(unsigned& a, unsigned& b) { asm("s_nop 1\n\tv_permlane16_swap_b32 %0, %1" : "+v"(a), "+v"(b)); }
DI void swap32(unsigned& a, unsigned& b) { asm("s_nop 1\n\tv_permlane32_swap_b32 %0, %1" : "+v"(a), "+v"(b)); }
DI f32x16 splat16(float v) {
  f32x16 z;
#pragma unroll
  for (int i = 0; i < 16; ++i) z[i] = v;
  return z;
}
DI f32x4 zero4() { return (f32x4){0.f, 0.f, 0.f, 0.f}; }
DI int vopq(int x) { asm volatile("" : "+v"(x)); return x; }
DI int lane_id() { int l; asm volatile("v_mbcnt_lo_u32_b32 %0, -1, 0\n\tv_mbcnt_hi_u32_b32 %0, -1, %0" : "=v"(l)); return l; }
template <int LN> DI unsigned wrl(unsigned v, unsigned s) { asm volatile("v_writelane_b32 %0, %1, %2" : "+v"(v) : "s"(s), "i"(LN)); return v; }
template <class T> DI void undef(T& x) { asm volatile("" : "=v"(x)); }
DI int sopq(int x) { asm volatile("" : "+s"(x)); return x; }
#define SWZ(l) ((l) ^ (((l) >> 3) & 7))
#define COORDS() \
  const int lane = lane_id(), lo = lane * 16, hh = lane >> 5, t16 = lane & 15, g4 = lane >> 4, lsw = SWZ(lane) * 16; \
  const int w = sopq(w0) & 3, sp = sopq(sp0), c = sopq(c0), n32 = c >> 1, rh = c & 1; \
  const unsigned lay = (unsigned)sopq((int)lay0); \
  const unsigned flo = sopq(layer) * FLAG_LAYER; \
  gu32* const fl = (gu32*)(ws + flo); \
  (void)lo; (void)hh; (void)t16; (void)g4; (void)lsw; (void)n32; (void)rh; (void)lay; (void)sp; (void)c; (void)w; (void)fl;
#define HBARS(c) asm volatile("s_waitcnt lgkmcnt(0)\n\ts_barrier" ::: "memory")
#define CST() do {} while (0)
#define HBAR() asm volatile("s_waitcnt lgkmcnt(0)\n\ts_barrier" ::: "memory")
#define BAR() asm volatile("s_waitcnt lgkmcnt(0)\n\ts_barrier" ::: "memory")
#define BARX() asm volatile("s_barrier" ::: "memory")
#define DRAIN() asm volatile("s_waitcnt vmcnt(0)" ::: "memory")
#define PIN() __builtin_amdgcn_sched_barrier(0)
#define SETPRIO(n) __builtin_amdgcn_s_setprio(n)
template <int N> DI void wait_vm() { static_assert(N >= 0 && N < 64, ""); asm volatile("s_waitcnt vmcnt(%0)" ::"i"(N) : "memory"); }

typedef __amdgpu_buffer_rsrc_t rsrc_t;
DI v4u ld16(rsrc_t rs, unsigned voff, unsigned soff) { return __builtin_amdgcn_raw_buffer_load_b128(rs, voff, soff, AUX_SC1); }
DI f32x2 ld8f(rsrc_t rs, unsigned voff, unsigned soff) { return __builtin_bit_cast(f32x2, __builtin_amdgcn_raw_buffer_load_b64(rs, voff, soff, AUX_SC1)); }
DI void st16(rsrc_t rs, v4u v, unsigned voff, unsigned soff) { __builtin_amdgcn_raw_buffer_store_b128(v, rs, voff, soff, AUX_SC1); }
DI void st8(rsrc_t rs, v2u v, unsigned voff, unsigned soff) { __builtin_amdgcn_raw_buffer_store_b64(v, rs, voff, soff, AUX_SC1); }
DI void st4(rsrc_t rs, unsigned v, unsigned voff, unsigned soff) { __builtin_amdgcn_raw_buffer_store_b32(v, rs, voff, soff, AUX_SC1); }
DI v4u lds16(LAS unsigned char* L, int off) { return *(LAS v4u*)(L + off); }
DI f32x4 lds4f(LAS unsigned char* L, int off) { return *(LAS f32x4*)(L + off); }

DI void poll(gu32* word, bool active, gu32* tmo, unsigned tag) {
  for (unsigned spins = 0;;) {
    unsigned v = tag;
    if (active) v = __hip_atomic_load(word, RLX_AGENT);
    if (__all(v == tag)) break;
    ++spins;
    if (spins > SPIN_CAP) { __hip_atomic_store(tmo, tag, RLX_AGENT); break; }
    if ((spins & 255u) == 0u && (unsigned)__builtin_amdgcn_readfirstlane(__hip_atomic_load(tmo, RLX_AGENT)) == tag) break;
  }
  __builtin_amdgcn_fence(__ATOMIC_ACQUIRE, "wavefront");
}

template <int I> DI void dma1(rsrc_t rs, LAS unsigned char* dst, unsigned voff, unsigned soff) {
  __builtin_amdgcn_raw_ptr_buffer_load_lds(rs, (LAS void*)dst, 16, voff, soff, I * 1024, AUX_SC1);
}
template <int V> using IC = std::integral_constant<int, V>;
template <class FN, int... I> DI void static_for_(FN fn, std::integer_sequence<int, I...>) { (fn(IC<I>{}), ...); }
template <int N, class FN> DI void static_for(FN fn) { static_for_(fn, std::make_integer_sequence<int, N>{}); }
template <int N> DI void dma(rsrc_t rs, LAS unsigned char* dst, unsigned lo, unsigned src) {
  static_for<N>([&](auto i) { dma1<i.value & 3>(rs, dst + (i.value >> 2) * 4096, lo, src + (i.value >> 2) * 4096); });
}

struct Stats {
  f32x4 p[16];
  float m0, m1, q0, q1;
  DI void load(rsrc_t rs, int lane, unsigned base, int i0 = 0, int i1 = 16) {
#pragma unroll
    for (int i = i0; i < i1; ++i) p[i] = __builtin_bit_cast(f32x4, ld16(rs, (lane >> 4) * 4096 + (lane & 15) * 16 + i * 256, base));
    PIN();
  }
  DI void part1() {
    m0 = m1 = q0 = q1 = 0.f;
#pragma unroll
    for (int i = 0; i < 16; ++i) { m0 += p[i][0]; q0 += p[i][1]; m1 += p[i][2]; q1 += p[i][3]; }
    asm volatile("" : "+v"(m0), "+v"(m1), "+v"(q0), "+v"(q1));
  }
  DI void part2() {
    const float a0 = m0 * (1.0f / 16.0f), a1 = m1 * (1.0f / 16.0f);
    q0 *= 32.0f; q1 *= 32.0f;
#pragma unroll
    for (int i = 0; i < 16; ++i) { const float d0 = p[i][0] - a0, d1 = p[i][2] - a1; q0 += d0 * d0; q1 += d1 * d1; }
    asm volatile("" : "+v"(q0), "+v"(q1));
  }
  DI void all(LAS unsigned char* L, int lane) { part1(); part2(); part3(L, lane); }
  DI void part3(LAS unsigned char* L, int lane) {
    const float den0 = bfr(__builtin_amdgcn_sqrtf(bfr(bfr(q0 * (1.0f / (32.0f * D_))) + 1e-5f)));
    const float den1 = bfr(__builtin_amdgcn_sqrtf(bfr(bfr(q1 * (1.0f / (32.0f * D_))) + 1e-5f)));
    *(LAS f32x2*)(L + LDS_ST + lane * 8) = (f32x2){__builtin_amdgcn_rcpf(den0), __builtin_amdgcn_rcpf(den1)};
    *(LAS f32x2*)(L + LDS_ST + 512 + lane * 8) = (f32x2){den0 * den0, den1 * den1};
    *(LAS v2u*)(L + LDS_ST + 1024 + lane * 8) = (v2u){pk2(m0 * (-1.0f / D_), m0 * (-1.0f / D_)), pk2(m1 * (-1.0f / D_), m1 * (-1.0f / D_))};
  }
};
DI float lds1f(LAS unsigned char* L, int off) { return *(LAS float*)(L + off); }
DI v4u mfrag(LAS unsigned char* L, int off) { const unsigned n = *(LAS unsigned*)(L + off); return (v4u){n, n, 0u, 0u}; }

constexpr int LA = 6;
template <int NF, class RD, class STEP>
DI void pipe(RD rd, STEP step) {
  v4u wf[8];
  static_for<LA>([&](auto i) { wf[i.value] = rd(i); });
  step(IC<-1>{}, wf[0], IC<1>{});
  PIN();
  static_for<NF>([&](auto fc) {
    constexpr int f = fc.value;
    if constexpr ((f & 1) == 0 && f + LA <= NF) __builtin_amdgcn_s_waitcnt(0xc07f | ((LA - 2) << 8));
    step(fc, wf[f & 7], IC<0>{});
    if constexpr (f + LA < NF) wf[(f + LA) & 7] = rd(IC<f + LA>{});
    step(fc, wf[f & 7], IC<1>{});
    PIN();
  });
}
DI v4u csfrag(LAS unsigned char* L, int entry) {
  const v2u t = *(LAS v2u*)(L + LDS_CS + entry * 8);
  return (v4u){t.x, t.y, 0u, 0u};
}
DI void cs_put(LAS unsigned char* L, int entry, float cs) {
  const float hi = bfr(cs), mid = bfr(cs - hi), lw = (cs - hi) - mid;
  *(LAS v2u*)(L + LDS_CS + entry * 8) = (v2u){pk2(hi, mid), pk2(lw, 0.f)};
}
DI unsigned mulpk(float a, float s0, float b, float s1) {
  unsigned d; float t;
  asm("v_mul_f32 %0, %2, %3\n\tv_mul_f32 %1, %4, %5\n\tv_cvt_pk_bf16_f32 %0, %0, %1" : "=&v"(d), "=&v"(t) : "v"(a), "v"(s0), "v"(b), "v"(s1));
  return d;
}
DI v4u pack8(const float* y) { return (v4u){pk2(y[0], y[1]), pk2(y[2], y[3]), pk2(y[4], y[5]), pk2(y[6], y[7])}; }

struct Fin {
  float y[8], s;
  v4u o;
  template <int K> DI void chunk(const f32x4& a0, const f32x4& a1, const v4u& r, rsrc_t ro, unsigned vo, unsigned so, rsrc_t rs, unsigned vs, unsigned ss) {
    if constexpr (K == 0) { y[0] = aadd(a0[0], bflo(r[0])); y[1] = aadd(a0[1], bfhi(r[0])); y[2] = aadd(a0[2], bflo(r[1])); y[3] = aadd(a0[3], bfhi(r[1])); }
    if constexpr (K == 1) { y[4] = aadd(a1[0], bflo(r[2])); y[5] = aadd(a1[1], bfhi(r[2])); y[6] = aadd(a1[2], bflo(r[3])); y[7] = aadd(a1[3], bfhi(r[3])); }
    if constexpr (K == 2) { o = pack8(y); st16(ro, o, vo, so); }
    if constexpr (K == 3) {
#pragma unroll
      for (int m = 0; m < 4; ++m) { y[2 * m] = bflo(o[m]); y[2 * m + 1] = bfhi(o[m]); }
    }
    if constexpr (K == 4) s = xrow_sum(aadd(aadd(aadd(y[0], y[1]), aadd(y[2], y[3])), aadd(aadd(y[4], y[5]), aadd(y[6], y[7]))));
    if constexpr (K == 5) {
      s = xhalf_sum(s);
      const float mu = s * (1.0f / 32.0f);
#pragma unroll
      for (int i = 0; i < 8; ++i) y[i] = asub(y[i], mu);
    }
    if constexpr (K == 6) {
      y[0] = amul(y[0], y[0]); y[1] = amul(y[1], y[1]);
#pragma unroll
      for (int i = 2; i < 8; ++i) y[i & 1] = afma(y[i], y[i], y[i & 1]);
    }
    if constexpr (K == 7) st8(rs, (v2u){__float_as_uint(s), __float_as_uint(xhalf_sum(xrow_sum(aadd(y[0], y[1]))))}, vs, ss);
  }
};

constexpr unsigned long long low_mask(int r) {
  unsigned long long m = 0;
  for (int l = 0; l < 64; ++l)
    if ((r & 3) + 8 * (r >> 2) + 4 * (l >> 5) <= (l & 31)) m |= 1ull << l;
  return m;
}
DI float sel(unsigned long long mask, float set, float unset) {
  float d;
  asm("v_cndmask_b32_e64 %0, %1, %2, %3" : "=v"(d) : "v"(unset), "v"(set), "s"(mask));
  return d;
}
DI float max16(float m, const f32x16& s) {
#pragma unroll
  for (int r = 0; r < 16; r += 2) m = __builtin_fmaxf(__builtin_fmaxf(m, s[r]), s[r + 1]);
  return m;
}
DI v4u exp_pack(const f32x16& s, int h) {
  float pr[8];
#pragma unroll
  for (int r = 0; r < 8; ++r) pr[r] = __builtin_amdgcn_exp2f(s[8 * h + r]);
  return pack8(pr);
}
template <class MID>
DI v4u attn_unit(LAS unsigned char* L, int lane, int tl, int nv, MID mid) {
  const int lo = lane * 16;
  const int kb = LDS_KL + tl * 1024 + lo, kf0 = LDS_KL + (tl + 8) * 1024 + (lane >> 5) * 512;
  const v4u q = lds16(L, LDS_QL + tl * 1024 + lo);
  v4u kf[9];
  kf[8] = lds16(L, kb + 8192); kf[0] = lds16(L, nv <= 0 ? kb : kf0);
#pragma unroll
  for (int i = 7; i >= 1; --i) kf[i] = lds16(L, (nv <= i ? kb : kf0 - i * 1024) + i * 1024);
  f32x16 s[9];
  s[8] = mfma(kf[8], q, splat16(0.f));
  s[0] = mfma(kf[0], q, splat16(0.f));
  s[7] = mfma(kf[7], q, splat16(0.f));
  s[6] = mfma(kf[6], q, splat16(0.f));
  asm volatile("s_nop 7\n\ts_nop 7" : "+v"(s[8]), "+v"(s[0]));
  PIN();
  static_for<16>([&](auto r) { s[8][r.value] = sel(low_mask(r.value), s[8][r.value], s[0][r.value]); });
  float m = max16(s[8][0], s[8]);
  mid();
  PIN();
  static_for<7>([&](auto ic) {
    constexpr int i = 7 - ic.value;
    if constexpr (i - 2 >= 1) s[i - 2] = mfma(kf[i - 2], q, splat16(0.f));
    m = max16(m, s[i]);
    PIN();
  });
  m = xhalf_max(m);
  const f32x16 cm = splat16(-m);
  const int vb = LDS_VL + 2 * tl * VBLK + ((lane & 16) ? 512 : (lane >> 5) * 256 + (lane & 15) * 16);
  s[8] = mfma(kf[8], q, cm);
  s[0] = mfma(kf[0], q, cm);
  s[7] = mfma(kf[7], q, cm);
  const v4u mk0 = lds16(L, LDS_MK + lo), mk1 = lds16(L, LDS_MK + 1024 + lo);
  v4u va = lds16(L, vb + 16 * VBLK), vc = lds16(L, vb + 17 * VBLK), v0a = lds16(L, vb), v0c = lds16(L, vb + VBLK);
  asm volatile("s_nop 7\n\ts_nop 7" : "+v"(s[8]), "+v"(s[0]));
  PIN();
  f32x16 o;
  {
    static_for<16>([&](auto r) { s[8][r.value] = sel(low_mask(r.value), s[8][r.value], s[0][r.value]); });
    const v4u p0 = exp_pack(s[8], 0), l0 = p0 & mk0;
    o = mfma(va, l0, splat16(0.f));
    o = mfma(v0a, p0 ^ l0, o);
    const v4u p1 = exp_pack(s[8], 1), l1 = p1 & mk1;
    o = mfma(vc, l1, o);
    o = mfma(v0c, p1 ^ l1, o);
    va = lds16(L, vb + 14 * VBLK); vc = lds16(L, vb + 15 * VBLK);
    PIN();
  }
  static_for<7>([&](auto ic) {
    constexpr int i = 7 - ic.value;
    if constexpr (i > 1) s[i - 1] = mfma(kf[i - 1], q, cm);
    PIN();
    o = mfma(va, exp_pack(s[i], 0), o);
    if constexpr (i > 1) va = lds16(L, vb + (2 * i - 2) * VBLK);
    o = mfma(vc, exp_pack(s[i], 1), o);
    if constexpr (i > 1) vc = lds16(L, vb + (2 * i - 1) * VBLK);
    PIN();
  });
  const float rl = __builtin_amdgcn_rcpf(o[8]);
  float y[8];
#pragma unroll
  for (int r = 0; r < 8; ++r) y[r] = o[r] * rl;
  return pack8(y);
}

struct Retile {
  v4u in[8];
  DI void load(const u16* src, int ld, int rs4) {
#pragma unroll
    for (int j = 0; j < 8; ++j) in[j] = *(const v4u*)(src + (size_t)(((j & 3) + rs4 * (j >> 2)) * ld));
    PIN();
  }
  unsigned d[8];
  DI void out(rsrc_t rs) {
#pragma unroll
    for (int i = 0; i < 8; ++i) st16(rs, in[i], d[i], 0);
  }
  DI void lines(LAS unsigned char* t, int lane, int sw, unsigned dst) {
#pragma unroll
    for (int e = 0; e < 8; ++e) {
      v4u o;
#pragma unroll
      for (int m = 0; m < 4; ++m) o[m] = __builtin_amdgcn_perm(in[2 * m + 1][e >> 1], in[2 * m][e >> 1], (e & 1) ? 0x07060302u : 0x05040100u);
      *(LAS v4u*)(t + lane * 128 + (e ^ sw ^ (lane & 7)) * 16) = o;
    }
    asm volatile("s_waitcnt lgkmcnt(0)" ::: "memory");
#pragma unroll
    for (int i = 0; i < 8; ++i) {
      in[i] = *(LAS v4u*)(t + i * 1024 + (lane ^ (lane >> 3)) * 16);
      d[i] = (unsigned)__builtin_amdgcn_ds_bpermute((8 * i + (lane >> 3)) * 4, (int)dst) + (lane & 7) * 16;
    }
    PIN();
  }
  DI void put(LAS unsigned char* L, int pb, int dd, int lp, int dl) {
#pragma unroll
    for (int e = 0; e < 8; ++e) {
      v4u o;
#pragma unroll
      for (int m = 0; m < 4; ++m) o[m] = __builtin_amdgcn_perm(in[2 * m + 1][e >> 1], in[2 * m][e >> 1], (e & 1) ? 0x07060302u : 0x05040100u);
      const int s = lp + (e & 3) + (e >> 2) * dl;
      *(LAS v4u*)(L + pb + (e >> 2) * dd + (s ^ ((s >> 3) & 7)) * 16) = o;
    }
  }
};

struct GS { int cs, j, kap, off; };
constexpr GS gu_step(int u) {
  if (u < 64) { const int k = u >> 2, j = u & 3; return GS{0, j, k, j < 3 ? (7 * (k >> 1) + 3 * (k & 1) + j) * 1024 : (7 * (k >> 1) + 6) * 1024 + (k & 1) * 512}; }
  const int v = u - 64, j = v / 17, i = v % 17;
  return GS{i == 16, j, 16 + i, 57344 + j * 16384 + i * (j < 3 ? 1024 : 512)};
}
constexpr int GA = 132, DB = 264, DA = 376, NST = 488;
constexpr int NPG = 16, NPQ = 12, LDN = 12;
constexpr int ld_step(int id) {
  if (id < 32) return id < NPG ? -1 : 2 * (id - NPG);
  if (id < 64) { const int n = id - 32; return n < 12 ? 103 + (5 * n) / 2 : GA + 10 + 2 * (n - 12); }
  if (id < 176) {
    const int n = id - 64;
    return n < 12 ? GA + 103 + n : n < 26 ? DB - 15 + ((n - 12) * 7) / 4 : n < 56 ? DB + 27 + ((n - 26) * 5) / 3 : n < 84 ? DB + 79 + ((n - 56) * 4) / 3 : DA + 31 + 2 * (n - 84);
  }
  if (id < 208) { const int n = id - 176; return n < NPQ ? DA + 87 + 2 * n : -1; }
  if (id < 224) return -1;
  return id == 224 ? DB + 80 : DA + 72;
}
constexpr int NEP = 56;
constexpr int ep_step(int k) { return 83 + 17 * (k / 16) + k % 16; }
struct Tab { short ld[NST][2]; short ep[NST]; short nvm[NST]; };
constexpr int FINB = DA + 3;
constexpr Tab make_tab() {
  Tab t{};
  for (int s = 0; s < NST; ++s) { t.ld[s][0] = t.ld[s][1] = t.ep[s] = -1; t.nvm[s] = 0; }
  for (int id = 0; id < 226; ++id) {
    const int s = ld_step(id);
    if (s < 0) continue;
    t.ld[s][t.ld[s][0] >= 0] = (short)id; ++t.nvm[s];
  }
  for (int job = 0; job < 2; ++job)
    for (int k = 0; k < NEP; ++k) {
      const int s = job * GA + ep_step(k);
      t.ep[s] = (short)(job * 64 + k);
      if (k == NEP - 1) t.nvm[s] += 2;
      if (k == (job ? 31 : NEP - 1)) t.nvm[s] += 2;
    }
  ++t.nvm[FINB + 2]; ++t.nvm[FINB + 7];
  return t;
}
constexpr Tab TAB = make_tab();
constexpr int vm_between(int a, int b) {
  int n = 0;
  for (int s = a + 1; s <= b; ++s) n += TAB.nvm[s];
  return n;
}

__global__ void __launch_bounds__(512, 2) k_main(const u16* __restrict__ x, const u16* __restrict__ wqkv, const u16* __restrict__ wo, const u16* __restrict__ wg,
                                                 const u16* __restrict__ wu, const u16* __restrict__ wd, unsigned char* __restrict__ ws,
                                                 u16* __restrict__ out) {
  extern __shared__ __attribute__((aligned(16))) unsigned char smem[];
  LAS unsigned char* L = (LAS unsigned char*)smem;
  const int tid = threadIdx.x, w0 = __builtin_amdgcn_readfirstlane(tid >> 6);
  const int b = blockIdx.x;
  const int sp0 = (b & 6) + ((b >> 3) & 1), c0 = (b >> 4) + 16 * (b & 1);
  const rsrc_t rs = __builtin_amdgcn_make_buffer_rsrc((void*)ws, 0, (int)WS_BYTES, 0x00020000);
  const bool compute = w0 < 4;
  gu32* const fg = (gu32*)(ws + FLAG_G);
  gu32* const tmo = fg + FL_TMO * 32;
  unsigned tag = 0;
#define FLAG(blk, line) st4(rs, tag, lane ? NONE : 0u, (blk) + (line) * 128)
#define POLL(blk, line, n) poll((gu32*)(ws + (blk)) + ((line) + lane) * 32, lane < (n), tmo, tag)
#define GETTAG() tag = (unsigned)__builtin_amdgcn_readfirstlane(*(LAS unsigned*)(L + LDS_TAG))
  unsigned touch0 = 0;
  v4u xq[32];

  if (compute) {
    const int layer = 0; const unsigned lay0 = LAY_OFF;
    COORDS();
    SETPRIO(2);
    const int r = lane >> 3, j = lane & 7;
    const u16* src = x + (size_t)(256 * sp + 8 * c + r) * D_ + 128 * w + 8 * j;
    const v4u a[2] = {*(const v4u*)src, *(const v4u*)(src + 64)};
#pragma unroll
    for (int i = 0; i < 2; ++i) {
      st16(rs, a[i], (j >> 2) * 1024 + ((j & 3) * 16 + r) * 16, lay + L_X + (16 * sp + n32) * 16384 + (2 * w + i) * 2048 + rh * 128);
      auto quad = [](float v) {
        v += __int_as_float(__builtin_amdgcn_update_dpp(0, __float_as_int(v), 0xb1, 0xf, 0xf, true));
        return v + __int_as_float(__builtin_amdgcn_update_dpp(0, __float_as_int(v), 0x4e, 0xf, 0xf, true));
      };
      float sm = 0.f, q = 0.f;
#pragma unroll
      for (int m = 0; m < 4; ++m) sm += bflo(a[i][m]) + bfhi(a[i][m]);
      sm = quad(sm);
      const float mu = sm * (1.0f / 32.0f);
#pragma unroll
      for (int m = 0; m < 4; ++m) { const float e0 = bflo(a[i][m]) - mu, e1 = bfhi(a[i][m]) - mu; q += e0 * e0 + e1 * e1; }
      q = quad(q);
      st8(rs, (v2u){__float_as_uint(sm), __float_as_uint(q)}, (j & 3) ? NONE : (j >> 2) * 256 + r * 8,
          lay + L_X + MB2 + (8 * sp + (c >> 2)) * 4096 + (4 * w + 2 * i) * 256 + (c & 3) * 64);
    }
    DRAIN();
    BAR();
    BAR();
    GETTAG();
    const int TB = 8 * sp + 4 + w;
    static_for<NPQ>([&](auto nc) { constexpr int n = nc.value; xq[n] = ld16(rs, lo + ((n >> 1) & 3) * 1024, lay + L_X + TB * 32768 + (n & 1) * 16384 + (n >> 3) * 4096); });
    BARX();
  } else {
    const int layer = 0; const unsigned lay0 = LAY_OFF;
    COORDS();
    SETPRIO(3);
    if (w == 3) {
      unsigned long long pc;
      asm volatile("s_getpc_b64 %0" : "=s"(pc));
      const unsigned* cp = (const unsigned*)((pc & ~127ull) + ((b >> 3) & 3) * 12288 + lane * 128);
      touch0 = *cp + (lane < 32 ? cp[2048] : 0u);
    }
    Retile rt;
    LAS unsigned char* tb = L + LDS_TB + w * 8192;
    const int id = 2 * sp + rh + 16 * (w - 1);
    if (w == 0 || id < 28) {
      const u16* src; int ld, sw; unsigned dst;
      if (w == 0) {
        const int fi = lane >> 3, i8 = lane & 7, F = 6 * sp + (fi < 6 ? fi : 0);
        src = wqkv + (size_t)(32 * (F < 32 ? F >> 1 : F - 32) + 8 * (i8 >> 1)) * (3 * D_) + (F < 32 ? 1 + (F & 1) : 0) * D_ + 16 * c + 8 * (i8 & 1);
        ld = 3 * D_; sw = i8; dst = fi < 6 ? WQKV_OFF + c * 49152 + F * 1024 + i8 * 128 : NONE;
      } else {
        const int t = 64 * id + lane, mo = t / 14, ch = t - 14 * mo, mat = mo >> 6, o = mo & 63, c7 = ch >= 7 ? ch - 7 : ch, j = c7 >> 1, kap = o >> 1;
        src = (mat ? wu : wg) + (size_t)(8 * o) * F_ + 112 * n32 + 8 * ch;
        ld = F_;
        const int cell = kap < 16 ? 7 * (kap >> 1) + (j < 3 ? 3 * (kap & 1) + j : 6) : 56 + 16 * j + (j < 3 ? kap - 16 : (kap - 16) >> 1);
        const int rho = j < 3 ? 2 * mat + (c7 & 1) + 4 * (o & 1) : 4 * (kap & 1) + 2 * (o & 1) + mat;
        sw = j < 3 ? rho : 0; dst = WGU_OFF + (2 * n32 + (ch >= 7)) * 114688 + cell * 1024 + rho * 128;
      }
      unsigned ep = 0;
      if (w == 0) ep = __hip_atomic_load(fg + FL_EP * 32, RLX_AGENT);
      rt.load(src, ld, 4);
      if (w == 0) {
        tag = __builtin_amdgcn_readfirstlane(ep) + 1u;
        if (lane == 0) *(LAS v4u*)(L + LDS_TAG) = (v4u){tag, 0u, 0u, 0u};
        v4u mk[2];
#pragma unroll
        for (int k = 0; k < 8; ++k) {
          const int kq = ((2 * k) & 3) + 8 * ((2 * k) >> 2) + 4 * hh, ql = lane & 31;
          mk[k >> 2][k & 3] = (kq <= ql ? 0xffffu : 0u) | (kq + 1 <= ql ? 0xffff0000u : 0u);
        }
        *(LAS v4u*)(L + LDS_MK + lo) = mk[0];
        *(LAS v4u*)(L + LDS_MK + 1024 + lo) = mk[1];
        if (sp == 0) {
#pragma unroll
          for (int i = 0; i < 9; ++i) *(LAS v4u*)(L + LDS_VL + (i < 8 ? i * 1024 + lo : 8192 + t16 * 16)) = (v4u){0u, 0u, 0u, 0u};
        }
        const unsigned one = (sp == 0 && lane < 16) ? 0u : 0x3f803f80u;
        if (lane < 32) *(LAS v4u*)(L + LDS_VL + lane * VBLK + 512) = (v4u){one, one, one, one};
      }
      rt.lines(tb, lane, sw, dst);
      if (w == 0) rt.out(rs);
    } else if (id < 46) {
      const int slot = (lane >> 2) & 3, m4 = lane & 3, q = 16 * (id < 42 ? id - 28 : id - 42) + 4 * g4 + slot;
      const int cq = q < 128 ? q >> 2 : q < 192 ? (q - 128) >> 1 : q - 192, r = q < 128 ? q & 3 : q < 192 ? 4 + (q & 1) : 6;
      rt.load((id < 42 ? wd + (size_t)(56 * cq + (r < 6 ? 16 * (r >> 1) + 4 * (r & 1) : 48)) * D_ : wo + (size_t)(16 * (q >> 1) + 4 * (q & 1)) * D_) + 32 * n32 + 8 * m4,
              D_, (id < 42 && r == 6) ? 4 : 8);
      rt.put(tb, g4 * 2048, 1024, 4 * m4 + 16 * slot, 0);
      asm volatile("s_waitcnt lgkmcnt(0)" ::: "memory");
#pragma unroll
      for (int i = 0; i < 8; ++i) {
        rt.in[i] = lds16(tb, i * 1024 + lo);
        rt.d[i] = (id < 42 ? WDN_OFF + n32 * 114688 + (id - 28) * 8192 : WO_OFF + n32 * 32768 + (id - 42) * 8192) + i * 1024 + lo;
      }
    } else {
#pragma unroll
      for (int i = 0; i < 8; ++i) { undef(rt.in[i]); rt.d[i] = NONE; }
    }
    DRAIN();
    asm volatile("" ::"v"(touch0));
    HBAR();
    if (w == 0) {
      FLAG(0, FL_XB + sp * 32 + c);
      poll((gu32*)ws + (FL_XB + (lane < 32 ? sp * 32 + lane : (lane & 7) * 32 + c)) * 32, lane < 40, tmo, tag);
    }
    Stats st;
    if (w == 0) {
      st.load(rs, lane, lay + L_X + MB2 + (8 * sp + 4) * 4096);
    }
    HBAR();
    GETTAG();
    if (w) {
      const int l = w - 1;
      dma<4>(rs, L + l * 4096, lo, WQKV_OFF + c * 49152 + l * 4096);
      dma<7>(rs, L + (12 + 7 * l - (l >> 1)) * 1024, lo, WQKV_OFF + c * 49152 + (12 + 7 * l - (l >> 1)) * 1024);
      wait_vm<7>();
    }
    HBAR();
    if (w) { dma<6>(rs, L + (27 + 5 * w) * 1024, lo, WQKV_OFF + c * 49152 + (27 + 5 * w) * 1024);
      rt.out(rs); wait_vm<14>();
    }
    else {
      st.all(L, lane);
      PIN();
      const int i = lane & 31;
      const int line = i < 8 ? FL_KV + (8 * sp + i) * 32 + c : i < 16 ? FL_O + (8 * sp + i - 8) * 32 + c
                     : i < 20 ? FL_X1B + (8 * sp + 4 + 2 * rh + ((i >> 1) & 1)) * 32 + 2 * n32 + (i & 1) : FL_X1A + (i - 20 < 6 ? i - 20 : 5) * 256 + sp * 32 + c;
      touch0 = __builtin_amdgcn_raw_buffer_load_b32(rs, hh * FLAG_LAYER + line * 128, 0, AUX_SC1);
    }
  }

  for (int layer = 0; layer < 2; ++layer) {
    const bool last = layer == 1;
    const unsigned lay0 = LAY_OFF + layer * LAY_BYTES;
    auto ldw = [&](auto nc, unsigned src, int cell) {
      constexpr int N = nc.value, K = (N + 2) / 3;
      const int l = sopq(w0) - 5, c1 = cell + (l * K + K > N ? N - K : l * K);
      dma<K>(rs, L + c1 * 1024, lane_id() * 16, src + c1 * 1024);
    };
    auto ld_gu = [&](int s) { ldw(IC<14>{}, WGU_OFF + sopq(c0) * 114688, 14 * s); };
    auto ld_dn = [&](int s) { ldw(IC<14>{}, WDN_OFF + (sopq(c0) >> 1) * 114688, 14 * s); };
    if (compute) {
      COORDS();
      const int TB = 8 * sp + 4 + w, TA = TB - 4;
      v4u xa[32];
      auto ldx = [&](auto nc, int T) {
        constexpr int n = nc.value;
        return ld16(rs, lo + ((n >> 1) & 3) * 1024, lay + L_X + (2 * T + (n & 1)) * 16384 + (n >> 3) * 4096);
      };
      f32x4 aq[2], ak[2], av[2], r0, r1;
      v4u mf0, mf1; float rd0, rd1;
      unsigned o0, o1, o2, o3;
      const int kpos = (t16 + 16 * hh + 32 * (g4 & 1)) * 16;
      auto epi_kv = [&](int k, int T, int tl) {
        const unsigned kvb = lay + (T * 32 + c) * 1024;
        if (k == 0) { r0 = lds4f(L, LDS_ST + w * 128 + g4 * 16); r1 = lds4f(L, LDS_ST + w * 128 + 64 + g4 * 16); }
        if (k == 2) { o0 = mulpk(ak[0][0], rd0, ak[0][1], rd0); o1 = mulpk(ak[0][2], rd0, ak[0][3], rd0); }
        if (k == 3) { o2 = mulpk(ak[1][0], rd1, ak[1][1], rd1); o3 = mulpk(ak[1][2], rd1, ak[1][3], rd1); }
        if (k == 4) {
          swap32(o0, o2); swap32(o1, o3);
          st16(rs, (v4u){o0, o1, o2, o3}, kpos, kvb + L_K);
          *(LAS v4u*)(L + LDS_KL + (8 + tl) * 1024 + kpos) = (v4u){o0, o1, o2, o3};
        }
        if (k == 5) { o0 = mulpk(av[0][0], r0[0], av[0][1], r0[1]); o1 = mulpk(av[0][2], r0[2], av[0][3], r0[3]); }
        if (k == 6) { o2 = mulpk(av[1][0], r1[0], av[1][1], r1[1]); o3 = mulpk(av[1][2], r1[2], av[1][3], r1[3]); }
        if (k == 7) {
          swap32(o0, o2); swap32(o1, o3);
          st16(rs, (v4u){o0, o1, o2, o3}, lo, kvb + L_V);
          *(LAS v4u*)(L + LDS_VL + (16 + 2 * tl + hh) * VBLK + (lane & 31) * 16) = (v4u){o0, o1, o2, o3};
        }
      };
      auto epi_q = [&](int tl) {
        asm volatile("s_nop 7" : "+v"(aq[0]), "+v"(aq[1]));
        const float s0 = rd0 * (0.25f * LOG2E), s1 = rd1 * (0.25f * LOG2E);
        o0 = mulpk(aq[0][0], s0, aq[0][1], s0); o1 = mulpk(aq[0][2], s0, aq[0][3], s0);
        o2 = mulpk(aq[1][0], s1, aq[1][1], s1); o3 = mulpk(aq[1][2], s1, aq[1][3], s1);
        swap32(o0, o2); swap32(o1, o3);
        *(LAS v4u*)(L + LDS_QL + tl * 1024 + kpos) = (v4u){o0, o1, o2, o3};
      };
      pipe<102>(
          [&](auto fc) {
            constexpr int t = fc.value, u = t % 51;
            if constexpr (t == 28) { wait_vm<32 - NPQ>(); BARX(); CST(); }
            if constexpr (t == 12 || t == 46 || t == 79) BARX();
            if constexpr (u == 32 || u == 33 || u == 50) return csfrag(L, (u == 50 ? 0 : u - 31) * 16 + t16);
            else return lds16(L, (u < 32 ? u : u - 2) * 1024 + lsw);
          },
          [&](auto fc, const v4u& wfr, auto pc) {
            constexpr int t = fc.value, part = pc.value, u = t % 51, ks = (u < 32 ? u >> 1 : u - 34) & 15, m = u < 34 ? 1 + (u & 1) : 0;
            constexpr bool pa = t >= 51, cs = u == 32 || u == 33 || u == 50;
            if constexpr (t < 0) return;
            else if constexpr (part == 0) {
              const v4u& x0 = cs ? mf0 : pa ? xa[2 * ks] : xq[2 * ks];
              const v4u& x1 = cs ? mf1 : pa ? xa[2 * ks + 1] : xq[2 * ks + 1];
              if constexpr (m == 0) { aq[0] = mfma16(wfr, x0, u == 34 ? zero4() : aq[0]); aq[1] = mfma16(wfr, x1, u == 34 ? zero4() : aq[1]); }
              if constexpr (m == 1) { ak[0] = mfma16(wfr, x0, u == 0 ? zero4() : ak[0]); ak[1] = mfma16(wfr, x1, u == 0 ? zero4() : ak[1]); }
              if constexpr (m == 2) { av[0] = mfma16(x0, wfr, u == 1 ? zero4() : av[0]); av[1] = mfma16(x1, wfr, u == 1 ? zero4() : av[1]); }
            } else {
              if constexpr (t < 32 - NPQ) xq[NPQ + t] = ldx(IC<NPQ + t>{}, TB);
              if constexpr (u == 24) {
                rd0 = lds1f(L, LDS_ST + w * 128 + t16 * 4); rd1 = lds1f(L, LDS_ST + w * 128 + 64 + t16 * 4);
                const int a = g4 ? LDS_TAG + 8 : LDS_ST + 1024 + w * 128 + t16 * 4;
                mf0 = mfrag(L, a); mf1 = mfrag(L, g4 ? a : a + 64);
              }
              if constexpr (u >= 33 && u < 41) epi_kv(u - 33, pa ? TA : TB, pa ? w : 4 + w);
              if constexpr (t == 50) {
                epi_q(4 + w);
                CST();
                if (last) { DRAIN(); FLAG(flo, FL_KV + TB * 32 + c); }
                BARX();
                static_for<32>([&](auto n) { xa[n.value] = ldx(n, TA); });
              }
              if constexpr (t == 52) { if (!last) { wait_vm<32>(); FLAG(flo, FL_KV + TB * 32 + c); } }
            }
          });
      epi_q(w);
      BAR();
      CST();
    } else {
      COORDS();
      const bool loader = w != 0;
      auto cs_qkv = [&]() {
        const unsigned one = (unsigned)vopq(0x3f803f80);
        const v4u ones = (v4u){one, one, one, one};
        f32x4 a = zero4();
        v4u f[16];
        const int fa = (w < 3 ? w - 1 : 32) * 1024 + lsw, fs = w < 3 ? 2048 : 1024;
#pragma unroll
        for (int ks = 0; ks < 16; ++ks) asm volatile("ds_read_b128 %0, %1" : "=v"(f[ks]) : "v"(fa + ks * fs) : "memory");
        asm volatile("s_waitcnt lgkmcnt(0)" : "+v"(f[0]), "+v"(f[1]), "+v"(f[2]), "+v"(f[3]), "+v"(f[4]), "+v"(f[5]), "+v"(f[6]), "+v"(f[7]));
        asm volatile("" : "+v"(f[8]), "+v"(f[9]), "+v"(f[10]), "+v"(f[11]), "+v"(f[12]), "+v"(f[13]), "+v"(f[14]), "+v"(f[15]));
#pragma unroll
        for (int ks = 0; ks < 16; ++ks) a = mfma16(f[ks], ones, a);
        if (t16 == 0) {
#pragma unroll
          for (int r = 0; r < 4; ++r) cs_put(L, (w % 3) * 16 + 4 * g4 + r, a[r]);
        }
      };
      const unsigned wob = WO_OFF + n32 * 32768 - LDS_WO;
      HBARS(false);
      if (layer == 0) {
        if (w == 1 || w == 2) cs_qkv();
        if (loader) wait_vm<8>();
        HBAR();
        if (w == 3) cs_qkv();
        HBARS(false);
        HBAR();
      } else {
        if (loader) { ldw(IC<10>{}, wob, 80); wait_vm<13>(); }
        HBAR();
        if (loader) ldw(IC<10>{}, wob, 90);
        else FLAG(flo, FL_XA + sp * 32 + c);
        HBARS(false);
        if (loader) ldw(IC<12>{}, wob, 100);
        else POLL(flo, FL_XA + sp * 32, 32);
        HBAR();
      }
      for (int ph = 0; ph < 2; ++ph) {
        if (!loader && ph == 0) {
          Stats st;
          st.load(rs, lane, lay + L_X + MB2 + 8 * sp * 4096);
          st.all(L, lane);
        }
        if (loader && sp > 0 && ph != layer) {
          poll(fl + (FL_KV + (8 * sp - 5 + w + 3 * lane) * 32 + c) * 32, lane < (w == 1 ? 2 : 1), tmo, tag);
          const unsigned so = lay + ((8 * sp - 5 + w) * 32 + c) * 1024, s2 = w == 1 ? so + 3 * 32768 : NONE;
          const v4u k = ld16(rs, lo, so + L_K), v = ld16(rs, lo, so + L_V), k2 = ld16(rs, lo, s2 + L_K), v2 = ld16(rs, lo, s2 + L_V);
          PIN();
          *(LAS v4u*)(L + LDS_KL + (3 + w) * 1024 + lo) = k;
          *(LAS v4u*)(L + LDS_VL + (6 + 2 * w + hh) * VBLK + (lane & 31) * 16) = v;
          if (w == 1) {
            *(LAS v4u*)(L + LDS_KL + 7 * 1024 + lo) = k2;
            *(LAS v4u*)(L + LDS_VL + (14 + hh) * VBLK + (lane & 31) * 16) = v2;
          }
        }
        if (ph == 0) { DRAIN(); HBARS(false); }
      }
      if (layer == 0 && !loader) { asm volatile("" ::"v"(touch0)); FLAG(FLAG_G, FL_WR + n32 * 16 + 2 * sp + rh); }
      DRAIN();
      HBAR();
      CST(); CST();
      if (loader) {
        if (layer == 0) {
          POLL(FLAG_G, FL_WR + n32 * 16, 16);
          ldw(IC<10>{}, wob, 80); ldw(IC<10>{}, wob, 90); ldw(IC<12>{}, wob, 100);
          ld_gu(0);
          wait_vm<5>();
          if (lane == 0) asm volatile("ds_add_u32 %0, %1" ::"v"(LDS_TAG + 12), "v"(1) : "memory");
        } else ld_gu(0);
        ld_gu(1); ld_gu(2); ldw(IC<6>{}, WGU_OFF + c * 114688, 42);
      }
      if (sp > 0) {
        LAS unsigned* hw = (LAS unsigned*)(L + LDS_TAG + 4);
        if (!loader) {
          poll(fl + (FL_KV + (8 * sp - 8 + (lane & 3)) * 32 + c) * 32, lane < 4, tmo, tag);
          CST();
          v4u kk[4], vv[4];
#pragma unroll
          for (int t = 0; t < 4; ++t) {
            const unsigned so = lay + ((8 * sp - 8 + t) * 32 + c) * 1024;
            kk[t] = ld16(rs, lo, so + L_K); vv[t] = ld16(rs, lo, so + L_V);
          }
          PIN();
#pragma unroll
          for (int t = 0; t < 4; ++t) {
            *(LAS v4u*)(L + LDS_KL + t * 1024 + lo) = kk[t];
            *(LAS v4u*)(L + LDS_VL + (2 * t + hh) * VBLK + (lane & 31) * 16) = vv[t];
          }
          asm volatile("s_waitcnt lgkmcnt(0)" ::: "memory");
          __hip_atomic_store(hw, (unsigned)layer + 1u, __ATOMIC_RELAXED, __HIP_MEMORY_SCOPE_WORKGROUP);
        } else {
          CST();
          unsigned v;
          do {
            __builtin_amdgcn_s_sleep(1);
            asm volatile("ds_read_b32 %0, %1\n\ts_waitcnt lgkmcnt(0)" : "=v"(v) : "v"(LDS_TAG + 4) : "memory");
          } while ((unsigned)__builtin_amdgcn_readfirstlane(v) != (unsigned)layer + 1u);
        }
      } else CST();
      CST();
      if (layer == 0 && !loader) touch0 = __builtin_amdgcn_raw_buffer_load_b32(rs, LAY_OFF + L_O + lane * TSTR + c * 4096 + sp * 128, 0, AUX_SC1);
      SETPRIO(1);
    }
    {
      COORDS();
      const int tl = compute ? 4 + w : w, T = 8 * sp + tl;
      const v4u o = attn_unit(L, lane, tl, sp == 0 ? 8 - tl : 0, [&]() {
        if (compute) { DRAIN(); FLAG(flo, FL_KV + (T - 4) * 32 + c); }
      });
      st16(rs, o, (g4 & 1) * 16384 + hh * 256 + t16 * 16, lay + L_O + T * 32768 + c * 512);
      DRAIN();
      asm volatile("" ::"v"(touch0));
      FLAG(flo, FL_O + T * 32 + c);
      CST();
    }
    if (compute) {
      SETPRIO(0);
      v4u fr[176];
      {
        COORDS();
        const int TB = 8 * sp + 4 + 2 * rh + (w >> 1), SB = 2 * TB + (w & 1);
        const v4u rB = ld16(rs, lo, lay + L_X + (SB * 16 + n32) * 1024), rA = ld16(rs, lo, lay + L_X + (SB * 16 + n32) * 1024 - 8 * 16384);
        const unsigned xv = (g4 >> 1) * 1024 + (g4 & 1) * 512 + t16 * 16, sv = g4 ? NONE : t16 * 8;
        const unsigned xo = lay + L_X1 + (TB * 32 + 2 * n32) * 1024 + (w & 1) * 256, so = lay + L_X1 + MB2 + TB * 4096 + n32 * 256 + (w & 1) * 128;
        const int lswo = vopq(lsw + LDS_WO);
        while ((unsigned)__builtin_amdgcn_readfirstlane(__hip_atomic_load((LAS unsigned*)(L + LDS_TAG + 12), __ATOMIC_RELAXED, __HIP_MEMORY_SCOPE_WORKGROUP)) != 3u)
          __builtin_amdgcn_s_sleep(1);
        asm volatile("" ::: "memory");
        v4u of[16];
        unsigned pk;
        f32x4 a0, a1;
        Fin fin;
        pipe<64>(
            [&](auto fc) { return lds16(L, (fc.value & 31) * 1024 + lswo); },
            [&](auto fc, const v4u& wfr, auto pc) {
              constexpr int t = fc.value, part = pc.value, u = t & 31;
              if constexpr (part == 0) {
                if constexpr (u & 1) a1 = mfma16(wfr, of[u >> 1], u == 1 ? zero4() : a1);
                else a0 = mfma16(wfr, of[u >> 1], u == 0 ? zero4() : a0);
              } else {
                if constexpr (t == 31 || t == 63) {
                  asm volatile("s_nop 7" : "+v"(a0), "+v"(a1));
                  static_for<8>([&](auto k) { fin.template chunk<k.value>(a0, a1, t == 31 ? rB : rA, rs, xv, xo - (t == 31 ? 0 : 4 * 32768), rs, sv, so - (t == 31 ? 0 : 4 * 4096)); });
                  CST();
                }
                if constexpr (t == -1 || t == 31) {
                  if constexpr (t == 31) {
                    pk = __hip_atomic_load(fl + (FL_O + (TB - 4) * 32 + (lane & 31)) * 32, RLX_AGENT);
                    DRAIN();
                    FLAG(flo, FL_X1B + TB * 32 + 2 * n32 + (w & 1));
                  }
                  if (t == -1 || !__all(pk == tag)) POLL(flo, FL_O + (TB - (t == 31 ? 4 : 0)) * 32, 32);
                  CST();
                  static_for<16>([&](auto i) { of[i.value] = ld16(rs, lo + (i.value & 3) * 1024, lay + L_O + (SB - (t == 31 ? 8 : 0)) * 16384 + (i.value >> 2) * 4096); });
                }
                if constexpr (t == 55) pk = __hip_atomic_load(fl + (FL_X1B + (8 * sp + 4 + w) * 32 + (lane & 31)) * 32, RLX_AGENT);
                if constexpr (t == 63) {
                  const int T = 8 * sp + 4 + w;
                  if (!__all(pk == tag)) POLL(flo, FL_X1B + T * 32, 32);
                  CST();
                  static_for<NPG>([&](auto i) { fr[i.value] = ld16(rs, lo + (i.value & 3) * 1024, lay + L_X1 + T * 32768 + (i.value >> 2) * 4096); });
                }
              }
            });
      }
      SETPRIO(2);
      BAR();
      {
        COORDS();
        const int TB = 8 * sp + 4 + w;
        const int TD = 8 * sp + 4 + 2 * rh + (w >> 1), SD = 2 * TD + (w & 1);
        const unsigned x1b = lay + L_X1 + TB * 32768, hb = lay + L_H + SD * 57344, xn = last ? NONE : lay + LAY_BYTES + L_X;
        const unsigned hsv = (g4 & 1) * 57344 + hh * 256 + t16 * 16, hso = lay + L_H + TB * 114688;
        const rsrc_t ro = last ? __builtin_amdgcn_make_buffer_rsrc((void*)out, 0, (int)MB2, 0x00020000) : rs;
        const unsigned ob = last ? SD * 16384 + n32 * 64 : xn + (SD * 16 + n32) * 1024;
        const unsigned sb = xn + MB2 + TD * 4096 + n32 * 256 + (w & 1) * 128;
        unsigned ov, sv;
        const int lsw2 = vopq(lsw + 65536), lh = (hh * 16 + t16) * 16, lh2 = vopq(lh + 65536);
        f32x16 acc[2][4];
        f32x4 d0[2], d1[2];
        v4u mf, hv[4], res[2];
        float k1[2], den2[2], t0, t1, p0_, p1_;
        Fin fin;
        auto epi = [&](auto ec) {
          constexpr int job = ec.value >> 6, k = ec.value & 63, j = k >> 4, i = (k >> 2) & 3, uo = j < 3 ? 8 : 4;
          const f32x16& a = acc[job][j];
          if constexpr ((k & 3) == 0)
            asm volatile("v_mul_f32 %0, %2, %4\n\tv_mul_f32 %1, %3, %4\n\tv_exp_f32 %0, %0" : "=&v"(t0), "=&v"(t1) : "v"(a[2 * i]), "v"(a[2 * i + 1]), "v"(k1[job]));
          else if constexpr ((k & 3) == 1)
            asm volatile("v_exp_f32 %0, %0\n\tv_mul_f32 %1, %3, %5\n\tv_mul_f32 %2, %4, %6"
                : "+v"(t1), "=&v"(p0_), "=&v"(p1_) : "v"(a[2 * i]), "v"(a[2 * i + 1]), "v"(a[uo + 2 * i]), "v"(a[uo + 2 * i + 1]));
          else if constexpr ((k & 3) == 2)
            asm volatile("v_fma_f32 %0, %0, %2, %2\n\tv_fma_f32 %1, %1, %2, %2\n\tv_rcp_f32 %0, %0" : "+v"(t0), "+v"(t1) : "v"(den2[job]));
          else {
            unsigned d;
            asm volatile("v_rcp_f32 %2, %2\n\tv_mul_f32 %1, %1, %3\n\tv_mul_f32 %2, %2, %4\n\tv_cvt_pk_bf16_f32 %0, %1, %2" : "=v"(d), "+v"(t0), "+v"(t1) : "v"(p0_), "v"(p1_));
            hv[j][i] = d;
            if constexpr (k == (job ? 31 : NEP - 1)) { st16(rs, hv[0], hsv, hso - job * 4 * 114688 + c * 1024); st16(rs, hv[1], hsv + 512, hso - job * 4 * 114688 + c * 1024); }
            if constexpr (k == NEP - 1) {
              st16(rs, hv[2], hsv, hso - job * 4 * 114688 + 32768 + c * 512);
              st8(rs, (v2u){hv[3][0], hv[3][1]}, hsv - hh * 248, hso - job * 4 * 114688 + 49152 + c * 256);
            }
          }
        };
        auto vload = [&](auto ic) {
          constexpr int id = ic.value;
          if constexpr (id < 64) fr[id] = ld16(rs, lo + (id & 3) * 1024, x1b - (id >> 5) * 4 * 32768 + ((id & 31) >> 2) * 4096);
          else if constexpr (id < 176) {
            constexpr int n = (id - 64) % 56;
            fr[id] = ld16(rs, lo + (n & 3) * 1024, hb - (id >= 120 ? 8 * 57344 : 0) + (n >> 2) * 4096);
          } else if constexpr (id < 208) {
            constexpr int n = id - 176;
            xq[n] = ld16(rs, lo + ((n >> 1) & 3) * 1024, xn + TB * 32768 + (n & 1) * 16384 + (n >> 3) * 4096);
          } else res[id - 224] = ld16(rs, vopq(lo & 0x2f0) + ((lo & 256) << 1) + (lo & 512), lay + L_X1 + ((TD - 4 * (id - 224)) * 32 + 2 * n32) * 1024 + (w & 1) * 256);
        };
        pipe<NST>(
            [&](auto fc) {
              constexpr int t = fc.value;
              if constexpr (t == 32) wait_vm<vm_between(-1, t - LA - 1) + NPG>();
              if constexpr (t == GA + 48) wait_vm<vm_between(ep_step(NEP - 1), t - LA - 1)>();
              if constexpr (t == DB + 14) wait_vm<vm_between(GA + ep_step(31), t - LA - 1)>();
              if constexpr (t == DB + 56) wait_vm<vm_between(GA + ep_step(NEP - 1), t - LA - 1)>();
              if constexpr (t == DA + 42) wait_vm<vm_between(FINB + 7, t - LA - 1)>();
              if constexpr (t < DB) {
                constexpr GS s = gu_step(t % GA);
                if constexpr (s.cs) return csfrag(L, 48 + 32 * s.j + (s.j < 3 ? lane & 31 : t16));
                else {
                  if (s.off % 14336 == 0 && s.off >= (t < GA ? 14336 : 28672)) BARX();
                  return lds16(L, s.off < 65536 ? s.off + (s.j < 3 ? lsw : lh) : s.off - 65536 + (s.j < 3 ? lsw2 : lh2));
                }
              } else {
                constexpr int v = (t - DB) % 112;
                if (v % 14 == 0 && v >= (t < DA ? 0 : 28)) BARX();
                return lds16(L, v < 64 ? v * 1024 + lsw : (v - 64) * 1024 + lsw2);
              }
            },
            [&](auto fc, const v4u& wfr, auto pc) {
              constexpr int t = fc.value, part = pc.value;
              if constexpr (t < 0) return;
              else if constexpr (part == 0) {
                if constexpr (t < DB) {
                  constexpr int job = t / GA, u = t % GA;
                  constexpr GS s = gu_step(u);
                  acc[job][s.j] = mfma(wfr, s.cs ? mf : fr[32 * job + (s.kap & 31)], u < 4 ? splat16(0.f) : acc[job][s.j]);
                } else {
                  constexpr int job = t >= DA, v = (t - DB) % 112;
                  if constexpr (v & 1) d1[job] = mfma16(wfr, fr[64 + 56 * job + (v >> 1)], v == 1 ? zero4() : d1[job]);
                  else d0[job] = mfma16(wfr, fr[64 + 56 * job + (v >> 1)], v == 0 ? zero4() : d0[job]);
                }
              } else {
                if constexpr (t < DB) {
                  constexpr int job = t / GA, u = t % GA;
                  if constexpr (u == 74) {
                    const int a = LDS_ST + w * 128 + (lane & 31) * 4;
                    k1[job] = lds1f(L, a); den2[job] = lds1f(L, a + 512); mf = mfrag(L, hh ? LDS_TAG + 8 : a + 1024);
                  }
                  if constexpr (u == 79) k1[job] *= -LOG2E;
                }
                if constexpr (TAB.ld[t][0] >= 0) vload(IC<TAB.ld[t][0]>{});
                if constexpr (TAB.ld[t][1] >= 0) vload(IC<TAB.ld[t][1]>{});
                if constexpr (TAB.ep[t] >= 0) epi(IC<TAB.ep[t]>{});
                if constexpr (t == FINB - 1) { const int l = lane_id(); ov = l * 16 + ((((l & 15) * 1008) - (l >> 4) * 240) & sopq(-layer)); sv = l >= 16 ? NONE : l * 8; }
                if constexpr (t >= FINB && t < FINB + 8) fin.template chunk<t - FINB>(d0[0], d1[0], res[0], ro, ov, ob, rs, sv, sb);
              }
            });
        if (!last) BARX();
        CST();
        asm volatile("s_nop 7" : "+v"(d0[1]), "+v"(d1[1]));
        static_for<8>([&](auto k) { fin.template chunk<k.value>(d0[1], d1[1], res[1], ro, ov, ob - 8 * 16384, rs, sv, sb - 4 * 4096); });
      }
    } else {
      COORDS();
      const bool poller = w == 0, loader = w != 0;
      unsigned pv = 0;
#define PEEK(blk, line) pv = __hip_atomic_load((gu32*)(ws + (blk)) + ((line) + (lane & 31)) * 32, RLX_AGENT)
#define GATE(blk, line) do { if (!__all(pv == tag)) POLL(blk, line, 32); } while (0)
      if (layer == 0) {
        if (loader) { ldw(IC<8>{}, WGU_OFF + c * 114688, 48); ld_gu(4); ldw(IC<10>{}, WGU_OFF + c * 114688, 70); }
        f32x4 ca = zero4();
        const unsigned one = (unsigned)vopq((w == 3 || t16 == (g4 & 1)) ? 0x3f803f80 : 0), wb = WGU_OFF + c * 114688;
        for (int q = 0; q < (w < 3 ? 2 : 1); ++q) {
          v4u f[16];
          static_for<16>([&](auto ic) {
            constexpr int i = ic.value;
            f[i] = ld16(rs, w < 3 ? lsw : lo, wb + (w < 3 ? (q ? 56 + 16 * w + i : 7 * (i >> 1) + 3 * (i & 1) + w) : i < 8 ? 7 * i + 6 : 96 + i) * 1024);
          });
          PIN();
          static_for<16>([&](auto i) { ca = mfma16(f[i.value], (v4u){one, one, one, one}, ca); });
        }
        if (t16 < (w < 3 ? 2 : 1)) {
#pragma unroll
          for (int r = 0; r < 4; ++r) cs_put(L, 48 + 32 * w + 16 * t16 + 4 * g4 + r, ca[r]);
        }
      }
      SETPRIO(3);
      if (poller) {
        Stats st;
        SETPRIO(1);
        HBAR();
        static_for<7>([&](auto nc) {
          constexpr int n = nc.value + 1;
          if (n <= 2) st.load(rs, lane, lay + L_X1 + MB2 + (8 * sp + 4) * 4096, 8 * n - 8, 8 * n);
          if (n == 3) { FLAG(flo, FL_X1A + sp * 32 + c); st.part1(); }
          if (n == 4) st.part2();
          if (n == 5) st.part3(L, lane);
          if (n == 6) PEEK(flo, FL_X1A + sp * 32);
          if (n == 7) GATE(flo, FL_X1A + sp * 32);
          HBARS(n == 3 || n == 7);
        });
        st.load(rs, lane, lay + L_X1 + MB2 + 8 * sp * 4096);
        st.all(L, lane);
        static_for<6>([&](auto nc) {
          constexpr int n = nc.value + 2;
          if (n == 4) FLAG(flo, FL_HB + sp * 32 + c);
          if (n == 6) PEEK(flo, FL_HB + sp * 32);
          if (n == 7) GATE(flo, FL_HB + sp * 32);
          HBARS(n == 2 || n == 7);
        });
        static_for<8>([&](auto nc) {
          constexpr int n = nc.value;
          if (n == 2) FLAG(flo, FL_HA + sp * 32 + c);
          if (n == 5) { FLAG(flo, FL_HA2 + sp * 32 + c); PEEK(flo, FL_HA + sp * 32); }
          if (n == 6) GATE(flo, FL_HA + sp * 32);
          if (n == 7) PEEK(flo, FL_HA2 + sp * 32);
          HBARS(n == 0 || n == 3 || n == 6);
        });
        static_for<6>([&](auto nc) {
          constexpr int n = nc.value + 2;
          if (n == 2) GATE(flo, FL_HA2 + sp * 32);
          if (n == 4 && !last) FLAG(FLAG_LAYER, FL_XB + sp * 32 + c);
          if (n == 5) PEEK(FLAG_LAYER, FL_XB + sp * 32);
          if (n == 6) GATE(FLAG_LAYER, FL_XB + sp * 32);
          if (n == 7) st.load(rs, lane, last ? NONE : LAY_OFF + LAY_BYTES + L_X + MB2 + (8 * sp + 4) * 4096);
          HBARS(n == 2 || n == 6 || n == 7);
        });
        if (!last) { st.all(L, lane); }
        if (last && blockIdx.x == 0) {
          for (int q = 0; q < 4; ++q) POLL(flo, FL_HB + 64 * q, 64);
          if (lane == 0) __hip_atomic_store(fg + FL_EP * 32, tag, RLX_AGENT);
        }
      } else {
        const unsigned wq = WQKV_OFF + c * 49152, wb = WGU_OFF + c * 114688;
        DRAIN();
        HBAR();
        static_for<7>([&](auto nc) {
          constexpr int n = nc.value + 1;
          if (n >= 2 && n <= 4) ldw(IC<9>{}, wb, 62 + 9 * n);
          if (n == 5) { ldw(IC<5>{}, wb, 107); wait_vm<8>(); }
          if (n == 6) wait_vm<5>();
          if (n == 7) wait_vm<0>();
          HBARS(n == 3 || n == 7);
        });
        static_for<6>([&](auto nc) {
          constexpr int n = nc.value + 2;
          if (n == 5 || n == 6) { ld_dn(2 * n - 10); ld_dn(2 * n - 9); } else if (n > 6) ld_dn(n - 3);
          HBARS(n == 2 || n == 7);
        });
        static_for<8>([&](auto nc) {
          constexpr int n = nc.value;
          if (n <= 2) ld_dn(n + 5);
          wait_vm<(n <= 2 ? 25 : 35 - 5 * n)>();
          HBARS(n == 0 || n == 3 || n == 6);
        });
        static_for<6>([&](auto nc) {
          constexpr int n = nc.value + 2;
          if (!last) {
            if (n >= 3 && n <= 5) ldw(IC<14>{}, wq, 14 * (n - 3));
            if (n == 6) { ldw(IC<6>{}, wq, 42); ldw(IC<8>{}, wb, 48); }
            if (n == 7) ldw(IC<14>{}, wb, 56);
          }
          HBARS(n == 2 || n == 6 || n == 7);
        });
        if (!last) { ldw(IC<10>{}, wb, 70); wait_vm<14>(); }
      }
      SETPRIO(3);
      if (!last) HBAR();
      static_for<NPQ>([&](auto i) { undef(xq[i.value]); });
    }
  }
}

}

extern "C" void kernel_launch(void* const* d_in, const int* in_sizes, int n_in, void* d_out, int out_size, void* d_ws, size_t ws_size,
                              hipStream_t stream) {
  (void)in_sizes; (void)n_in; (void)out_size; (void)ws_size;
  (void)hipFuncSetAttribute((const void*)k_main, hipFuncAttributeMaxDynamicSharedMemorySize, LDS_BYTES);
  k_main<<<256, 512, LDS_BYTES, stream>>>((const u16*)d_in[0], (const u16*)d_in[1], (const u16*)d_in[2], (const u16*)d_in[4], (const u16*)d_in[5],
                                          (const u16*)d_in[6], (unsigned char*)d_ws, (u16*)d_out);
}
```

```cpp
#include <hip/hip_runtime.h>
#include <stdint.h>
#include <utility>

#define LAS __attribute__((address_space(3)))
#define GAS __attribute__((address_space(1)))
#define DI __device__ __forceinline__
#define RLX_AGENT __ATOMIC_RELAXED, __HIP_MEMORY_SCOPE_AGENT
typedef unsigned short u16;
typedef unsigned v4u __attribute__((ext_vector_type(4)));
typedef unsigned v2u __attribute__((ext_vector_type(2)));
typedef float f32x2 __attribute__((ext_vector_type(2)));
typedef float f32x4 __attribute__((ext_vector_type(4)));
typedef float f32x16 __attribute__((ext_vector_type(16)));
typedef __bf16 bf16x2 __attribute__((ext_vector_type(2)));
typedef __bf16 bf16x8 __attribute__((ext_vector_type(8)));
typedef GAS unsigned gu32;
typedef GAS unsigned long long gu64;

namespace {

constexpr int D_ = 512, F_ = 1792;
constexpr unsigned MB2 = 2097152u, STB = 262144u;
constexpr unsigned FLAG_LAYER = 1048576u, FLAG_G = 2 * FLAG_LAYER, FLAG_BYTES = FLAG_G + 65536;
constexpr unsigned WQKV_OFF = FLAG_BYTES;
constexpr unsigned WO_OFF = WQKV_OFF + 32 * 49152;
constexpr unsigned WGU_OFF = WO_OFF + 16 * 32768;
constexpr unsigned WDN_OFF = WGU_OFF + 32 * 114688;
constexpr unsigned LAY_OFF = WDN_OFF + 16 * 114688;
constexpr unsigned L_K = 0, L_V = MB2, L_O = 2 * MB2, L_X1 = 3 * MB2, L_X = 4 * MB2 + STB, L_H = 5 * MB2 + 2 * STB;
constexpr unsigned LAY_BYTES = L_H + 128 * 57344;
constexpr unsigned WS_BYTES = LAY_OFF + 2 * LAY_BYTES;
constexpr unsigned NONE = 0x40000000u;
constexpr int FL_KV = 0, FL_O = 2048, FL_X1B = 4096, FL_X1A = 6144, FL_HB = 6400, FL_HA = 6656, FL_XB = 6912, FL_XA = 7168, FL_HA2 = 7424;
constexpr int FL_WR = 0, FL_EP = 256, FL_TMO = 257;
constexpr int VBLK = 528;
constexpr int LDS_KL = 114688, LDS_VL = LDS_KL + 16384, LDS_QL = LDS_VL + 32 * VBLK, LDS_CS = LDS_QL + 8192, LDS_ST = LDS_CS + 1536;
constexpr int LDS_MK = LDS_ST + 1536, LDS_TAG = LDS_MK + 2048;
constexpr int LDS_WO = 81920, LDS_TB = 49152;
constexpr int LDS_BYTES = 163840;
#define TSTR 1048576u
constexpr unsigned SPIN_CAP = 1u << 16;
constexpr int AUX_SC1 = 16;
constexpr float LOG2E = 1.4426950408889634f;

DI f32x16 mfma(v4u a, v4u b, f32x16 c) {
  return __builtin_amdgcn_mfma_f32_32x32x16_bf16(__builtin_bit_cast(bf16x8, a), __builtin_bit_cast(bf16x8, b), c, 0, 0, 0);
}
DI f32x4 mfma16(v4u a, v4u b, f32x4 c) {
  return __builtin_amdgcn_mfma_f32_16x16x32_bf16(__builtin_bit_cast(bf16x8, a), __builtin_bit_cast(bf16x8, b), c, 0, 0, 0);
}
DI unsigned pk2(float lo, float hi) {
  f32x2 p = {lo, hi};
  return __builtin_bit_cast(unsigned, __builtin_convertvector(p, bf16x2));
}
DI float bfr(float x) { return __uint_as_float(pk2(x, 0.f) << 16); }
DI float bflo(unsigned u) { return __uint_as_float(u << 16); }
DI float bfhi(unsigned u) { return __uint_as_float(u & 0xffff0000u); }
DI float amul(float a, float b) { float d; asm("v_mul_f32 %0, %1, %2" : "=v"(d) : "v"(a), "v"(b)); return d; }
DI float afma(float a, float b, float c) { float d; asm("v_fma_f32 %0, %1, %2, %3" : "=v"(d) : "v"(a), "v"(b), "v"(c)); return d; }
DI float aadd(float a, float b) { float d; asm("v_add_f32 %0, %1, %2" : "=v"(d) : "v"(a), "v"(b)); return d; }
DI float asub(float a, float b) { float d; asm("v_sub_f32 %0, %1, %2" : "=v"(d) : "v"(a), "v"(b)); return d; }
DI float xhalf_sum(float v) {
  float t;
  asm("v_mov_b32 %1, %0\n\ts_nop 1\n\tv_permlane32_swap_b32 %0, %1\n\tv_add_f32 %0, %0, %1" : "+v"(v), "=&v"(t));
  return v;
}
DI float xrow_sum(float v) {
  float t;
  asm("v_mov_b32 %1, %0\n\ts_nop 1\n\tv_permlane16_swap_b32 %0, %1\n\tv_add_f32 %0, %0, %1" : "+v"(v), "=&v"(t));
  return v;
}
DI float xhalf_max(float v) {
  auto r = __builtin_amdgcn_permlane32_swap(__float_as_uint(v), __float_as_uint(v), false, false);
  return fmaxf(__uint_as_float(r[0]), __uint_as_float(r[1]));
}
DI void swap16(unsigned& a, unsigned& b) { asm("s_nop 1\n\tv_permlane16_swap_b32 %0, %1" : "+v"(a), "+v"(b)); }
DI void swap32(unsigned& a, unsigned& b) { asm("s_nop 1\n\tv_permlane32_swap_b32 %0, %1" : "+v"(a), "+v"(b)); }
DI f32x16 splat16(float v) {
  f32x16 z;
#pragma unroll
  for (int i = 0; i < 16; ++i) z[i] = v;
  return z;
}
DI f32x4 zero4() { return (f32x4){0.f, 0.f, 0.f, 0.f}; }
DI int vopq(int x) { asm volatile("" : "+v"(x)); return x; }
DI int lane_id() { int l; asm volatile("v_mbcnt_lo_u32_b32 %0, -1, 0\n\tv_mbcnt_hi_u32_b32 %0, -1, %0" : "=v"(l)); return l; }
template <int LN> DI unsigned wrl(unsigned v, unsigned s) { asm volatile("v_writelane_b32 %0, %1, %2" : "+v"(v) : "s"(s), "i"(LN)); return v; }
template <class T> DI void undef(T& x) { asm volatile("" : "=v"(x)); }
DI int sopq(int x) { asm volatile("" : "+s"(x)); return x; }
#define SWZ(l) ((l) ^ (((l) >> 3) & 7))
#define COORDS() \
  const int lane = lane_id(), lo = lane * 16, hh = lane >> 5, t16 = lane & 15, g4 = lane >> 4, lsw = SWZ(lane) * 16; \
  const int w = sopq(w0) & 3, sp = sopq(sp0), c = sopq(c0), n32 = c >> 1, rh = c & 1; \
  const unsigned lay = (unsigned)sopq((int)lay0); \
  const unsigned flo = sopq(layer) * FLAG_LAYER; \
  gu32* const fl = (gu32*)(ws + flo); \
  (void)lo; (void)hh; (void)t16; (void)g4; (void)lsw; (void)n32; (void)rh; (void)lay; (void)sp; (void)c; (void)w; (void)fl;
#define HBARS(c) asm volatile("s_waitcnt lgkmcnt(0)\n\ts_barrier" ::: "memory")
#define CST() do {} while (0)
#define DBGC(k) asm volatile("" ::: "memory")
#define HBAR() asm volatile("s_waitcnt lgkmcnt(0)\n\ts_barrier" ::: "memory")
#define BAR() asm volatile("s_waitcnt lgkmcnt(0)\n\ts_barrier" ::: "memory")
#define BARX() asm volatile("s_barrier" ::: "memory")
#define DRAIN() asm volatile("s_waitcnt vmcnt(0)" ::: "memory")
#define PIN() __builtin_amdgcn_sched_barrier(0)
#define SETPRIO(n) __builtin_amdgcn_s_setprio(n)
template <int N> DI void wait_vm() { static_assert(N >= 0 && N < 64, ""); asm volatile("s_waitcnt vmcnt(%0)" ::"i"(N) : "memory"); }

typedef __amdgpu_buffer_rsrc_t rsrc_t;
DI v4u ld16(rsrc_t rs, unsigned voff, unsigned soff) { return __builtin_amdgcn_raw_buffer_load_b128(rs, voff, soff, AUX_SC1); }
DI f32x2 ld8f(rsrc_t rs, unsigned voff, unsigned soff) { return __builtin_bit_cast(f32x2, __builtin_amdgcn_raw_buffer_load_b64(rs, voff, soff, AUX_SC1)); }
DI void st16(rsrc_t rs, v4u v, unsigned voff, unsigned soff) {
  __builtin_amdgcn_raw_buffer_store_b128(v, rs, voff, soff, AUX_SC1);
  asm volatile("s_nop 1" ::"v"(v));
}
DI void st8(rsrc_t rs, v2u v, unsigned voff, unsigned soff) { __builtin_amdgcn_raw_buffer_store_b64(v, rs, voff, soff, AUX_SC1); }
DI void st4(rsrc_t rs, unsigned v, unsigned voff, unsigned soff) { __builtin_amdgcn_raw_buffer_store_b32(v, rs, voff, soff, AUX_SC1); }
DI v4u lds16(LAS unsigned char* L, int off) { return *(LAS v4u*)(L + off); }
DI f32x4 lds4f(LAS unsigned char* L, int off) { return *(LAS f32x4*)(L + off); }

DI void poll(gu32* word, bool active, gu32* tmo, unsigned tag) {
  for (unsigned spins = 0;;) {
    unsigned v = tag;
    if (active) v = __hip_atomic_load(word, RLX_AGENT);
    if (__all(v == tag)) break;
    ++spins;
    if (spins > SPIN_CAP) { __hip_atomic_store(tmo, tag, RLX_AGENT); break; }
    if ((spins & 255u) == 0u && (unsigned)__builtin_amdgcn_readfirstlane(__hip_atomic_load(tmo, RLX_AGENT)) == tag) break;
  }
  __builtin_amdgcn_fence(__ATOMIC_ACQUIRE, "wavefront");
}

template <int I> DI void dma1(rsrc_t rs, LAS unsigned char* dst, unsigned voff, unsigned soff) {
  __builtin_amdgcn_raw_ptr_buffer_load_lds(rs, (LAS void*)dst, 16, voff, soff, I * 1024, AUX_SC1);
}
template <int V> using IC = std::integral_constant<int, V>;
template <class FN, int... I> DI void static_for_(FN fn, std::integer_sequence<int, I...>) { (fn(IC<I>{}), ...); }
template <int N, class FN> DI void static_for(FN fn) { static_for_(fn, std::make_integer_sequence<int, N>{}); }
template <int N> DI void dma(rsrc_t rs, LAS unsigned char* dst, unsigned lo, unsigned src) {
  static_for<N>([&](auto i) { dma1<i.value & 3>(rs, dst + (i.value >> 2) * 4096, lo, src + (i.value >> 2) * 4096); });
}

struct Stats {
  f32x4 p[16];
  float m0, m1, q0, q1;
  DI void load(rsrc_t rs, int lane, unsigned base, int i0 = 0, int i1 = 16) {
#pragma unroll
    for (int i = i0; i < i1; ++i) p[i] = __builtin_bit_cast(f32x4, ld16(rs, (lane >> 4) * 4096 + (lane & 15) * 16 + i * 256, base));
    PIN();
  }
  DI void part1() {
    m0 = m1 = q0 = q1 = 0.f;
#pragma unroll
    for (int i = 0; i < 16; ++i) { m0 += p[i][0]; q0 += p[i][1]; m1 += p[i][2]; q1 += p[i][3]; }
    asm volatile("" : "+v"(m0), "+v"(m1), "+v"(q0), "+v"(q1));
  }
  DI void part2() {
    const float a0 = m0 * (1.0f / 16.0f), a1 = m1 * (1.0f / 16.0f);
    q0 *= 32.0f; q1 *= 32.0f;
#pragma unroll
    for (int i = 0; i < 16; ++i) { const float d0 = p[i][0] - a0, d1 = p[i][2] - a1; q0 += d0 * d0; q1 += d1 * d1; }
    asm volatile("" : "+v"(q0), "+v"(q1));
  }
  DI void all(LAS unsigned char* L, int lane, int dst = LDS_ST) { part1(); part2(); part3(L, lane, dst); }
  DI void part3(LAS unsigned char* L, int lane, int dst = LDS_ST) {
    const float den0 = bfr(__builtin_amdgcn_sqrtf(bfr(bfr(q0 * (1.0f / (32.0f * D_))) + 1e-5f)));
    const float den1 = bfr(__builtin_amdgcn_sqrtf(bfr(bfr(q1 * (1.0f / (32.0f * D_))) + 1e-5f)));
    *(LAS f32x2*)(L + dst + lane * 8) = (f32x2){__builtin_amdgcn_rcpf(den0), __builtin_amdgcn_rcpf(den1)};
    *(LAS f32x2*)(L + dst + 512 + lane * 8) = (f32x2){den0 * den0, den1 * den1};
    *(LAS v2u*)(L + dst + 1024 + lane * 8) = (v2u){pk2(m0 * (-1.0f / D_), m0 * (-1.0f / D_)), pk2(m1 * (-1.0f / D_), m1 * (-1.0f / D_))};
  }
};
DI float lds1f(LAS unsigned char* L, int off) { return *(LAS float*)(L + off); }
DI v4u mfrag(LAS unsigned char* L, int off) { const unsigned n = *(LAS unsigned*)(L + off); return (v4u){n, n, 0u, 0u}; }

constexpr int LA = 6;
template <int NF, class RD, class STEP>
DI void pipe(RD rd, STEP step) {
  v4u wf[8];
  static_for<LA>([&](auto i) { wf[i.value] = rd(i); });
  step(IC<-1>{}, wf[0], IC<1>{});
  PIN();
  static_for<NF>([&](auto fc) {
    constexpr int f = fc.value;
    if constexpr ((f & 1) == 0 && f + LA <= NF) __builtin_amdgcn_s_waitcnt(0xc07f | ((LA - 2) << 8));
    step(fc, wf[f & 7], IC<0>{});
    if constexpr (f + LA < NF) wf[(f + LA) & 7] = rd(IC<f + LA>{});
    step(fc, wf[f & 7], IC<1>{});
    PIN();
  });
}
DI v4u csfrag(LAS unsigned char* L, int entry) {
  const v2u t = *(LAS v2u*)(L + LDS_CS + entry * 8);
  return (v4u){t.x, t.y, 0u, 0u};
}
DI void cs_put(LAS unsigned char* L, int entry, float cs) {
  const float hi = bfr(cs), mid = bfr(cs - hi), lw = (cs - hi) - mid;
  *(LAS v2u*)(L + LDS_CS + entry * 8) = (v2u){pk2(hi, mid), pk2(lw, 0.f)};
}
DI unsigned mulpk(float a, float s0, float b, float s1) {
  unsigned d; float t;
  asm("v_mul_f32 %0, %2, %3\n\tv_mul_f32 %1, %4, %5\n\tv_cvt_pk_bf16_f32 %0, %0, %1" : "=&v"(d), "=&v"(t) : "v"(a), "v"(s0), "v"(b), "v"(s1));
  return d;
}
DI unsigned nos(unsigned v) { return v < 0xfffffffeu ? v : 0xfffffffeu; }
DI v4u nos4(v4u v) { return (v4u){nos(v[0]), nos(v[1]), nos(v[2]), nos(v[3])}; }
DI unsigned umax3(unsigned a, unsigned b, unsigned c) { const unsigned t = a > b ? a : b; return t > c ? t : c; }
template <int N> DI bool missing(const v4u* f, int step = 1) {
  unsigned m0 = 0, m1 = 0;
#pragma unroll
  for (int i = 0; i < N; ++i) { m0 = umax3(m0, f[i * step][0], f[i * step][1]); m1 = umax3(m1, f[i * step][2], f[i * step][3]); }
  return __builtin_amdgcn_ballot_w64((m0 > m1 ? m0 : m1) == 0xffffffffu) != 0;
}
constexpr unsigned DATA_CAP = 1u << 14;
DI v4u pack8(const float* y) { return (v4u){pk2(y[0], y[1]), pk2(y[2], y[3]), pk2(y[4], y[5]), pk2(y[6], y[7])}; }

struct Fin {
  float y[8], s;
  v4u o;
  v2u sq;
  template <int K, bool ST = true> DI void chunk(const f32x4& a0, const f32x4& a1, const v4u& r, rsrc_t ro, unsigned vo, unsigned so, rsrc_t rs, unsigned vs, unsigned ss) {
    if constexpr (K == 0) { y[0] = aadd(a0[0], bflo(r[0])); y[1] = aadd(a0[1], bfhi(r[0])); y[2] = aadd(a0[2], bflo(r[1])); y[3] = aadd(a0[3], bfhi(r[1])); }
    if constexpr (K == 1) { y[4] = aadd(a1[0], bflo(r[2])); y[5] = aadd(a1[1], bfhi(r[2])); y[6] = aadd(a1[2], bflo(r[3])); y[7] = aadd(a1[3], bfhi(r[3])); }
    if constexpr (K == 2) { o = nos4(pack8(y)); if constexpr (ST) st16(ro, o, vo, so); }
    if constexpr (K == 3) {
#pragma unroll
      for (int m = 0; m < 4; ++m) { y[2 * m] = bflo(o[m]); y[2 * m + 1] = bfhi(o[m]); }
    }
    if constexpr (K == 4) s = xrow_sum(aadd(aadd(aadd(y[0], y[1]), aadd(y[2], y[3])), aadd(aadd(y[4], y[5]), aadd(y[6], y[7]))));
    if constexpr (K == 5) {
      s = xhalf_sum(s);
      const float mu = s * (1.0f / 32.0f);
#pragma unroll
      for (int i = 0; i < 8; ++i) y[i] = asub(y[i], mu);
    }
    if constexpr (K == 6) {
      y[0] = amul(y[0], y[0]); y[1] = amul(y[1], y[1]);
#pragma unroll
      for (int i = 2; i < 8; ++i) y[i & 1] = afma(y[i], y[i], y[i & 1]);
    }
    if constexpr (K == 7) {
      sq = (v2u){nos(__float_as_uint(s)), nos(__float_as_uint(xhalf_sum(xrow_sum(aadd(y[0], y[1])))))};
      if constexpr (ST) st8(rs, sq, vs, ss);
    }
  }
};

constexpr unsigned long long low_mask(int r) {
  unsigned long long m = 0;
  for (int l = 0; l < 64; ++l)
    if ((r & 3) + 8 * (r >> 2) + 4 * (l >> 5) <= (l & 31)) m |= 1ull << l;
  return m;
}
DI float sel(unsigned long long mask, float set, float unset) {
  float d;
  asm("v_cndmask_b32_e64 %0, %1, %2, %3" : "=v"(d) : "v"(unset), "v"(set), "s"(mask));
  return d;
}
DI float max16(float m, const f32x16& s) {
#pragma unroll
  for (int r = 0; r < 16; r += 2) m = __builtin_fmaxf(__builtin_fmaxf(m, s[r]), s[r + 1]);
  return m;
}
DI v4u exp_pack(const f32x16& s, int h) {
  float pr[8];
#pragma unroll
  for (int r = 0; r < 8; ++r) pr[r] = __builtin_amdgcn_exp2f(s[8 * h + r]);
  return pack8(pr);
}
template <bool EXACT>
DI f32x16 attn_pass(LAS unsigned char* L, int lane, int tl, int nv) {
  const int lo = lane * 16;
  const int kb = LDS_KL + tl * 1024 + lo, kf0 = LDS_KL + (tl + 8) * 1024 + (lane >> 5) * 512;
  const v4u q = lds16(L, LDS_QL + tl * 1024 + lo);
  v4u kf[9];
  kf[8] = lds16(L, kb + 8192); kf[0] = lds16(L, nv <= 0 ? kb : kf0);
#pragma unroll
  for (int i = 7; i >= 1; --i) kf[i] = lds16(L, (nv <= i ? kb : kf0 - i * 1024) + i * 1024);
  f32x16 s[9], cm = splat16(0.f);
  if constexpr (EXACT) {
    s[8] = mfma(kf[8], q, splat16(0.f));
    s[0] = mfma(kf[0], q, splat16(0.f));
    s[7] = mfma(kf[7], q, splat16(0.f));
    s[6] = mfma(kf[6], q, splat16(0.f));
    asm volatile("s_nop 7\n\ts_nop 7" : "+v"(s[8]), "+v"(s[0]));
    PIN();
    static_for<16>([&](auto r) { s[8][r.value] = sel(low_mask(r.value), s[8][r.value], s[0][r.value]); });
    float m = max16(s[8][0], s[8]);
    PIN();
    static_for<7>([&](auto ic) {
      constexpr int i = 7 - ic.value;
      if constexpr (i - 2 >= 1) s[i - 2] = mfma(kf[i - 2], q, splat16(0.f));
      m = max16(m, s[i]);
      PIN();
    });
    cm = splat16(-xhalf_max(m));
  }
  const int vb = LDS_VL + 2 * tl * VBLK + ((lane & 16) ? 512 : (lane >> 5) * 256 + (lane & 15) * 16);
  s[8] = mfma(kf[8], q, cm);
  s[0] = mfma(kf[0], q, cm);
  s[7] = mfma(kf[7], q, cm);
  const v4u mk0 = lds16(L, LDS_MK + lo), mk1 = lds16(L, LDS_MK + 1024 + lo);
  v4u va = lds16(L, vb + 16 * VBLK), vc = lds16(L, vb + 17 * VBLK), v0a = lds16(L, vb), v0c = lds16(L, vb + VBLK);
  asm volatile("s_nop 7\n\ts_nop 7" : "+v"(s[8]), "+v"(s[0]));
  PIN();
  f32x16 o;
  {
    static_for<16>([&](auto r) { s[8][r.value] = sel(low_mask(r.value), s[8][r.value], s[0][r.value]); });
    const v4u p0 = exp_pack(s[8], 0), l0 = p0 & mk0;
    o = mfma(va, l0, splat16(0.f));
    o = mfma(v0a, p0 ^ l0, o);
    const v4u p1 = exp_pack(s[8], 1), l1 = p1 & mk1;
    o = mfma(vc, l1, o);
    o = mfma(v0c, p1 ^ l1, o);
    va = lds16(L, vb + 14 * VBLK); vc = lds16(L, vb + 15 * VBLK);
    PIN();
  }
  static_for<7>([&](auto ic) {
    constexpr int i = 7 - ic.value;
    if constexpr (i > 1) s[i - 1] = mfma(kf[i - 1], q, cm);
    PIN();
    o = mfma(va, exp_pack(s[i], 0), o);
    if constexpr (i > 1) va = lds16(L, vb + (2 * i - 2) * VBLK);
    o = mfma(vc, exp_pack(s[i], 1), o);
    if constexpr (i > 1) vc = lds16(L, vb + (2 * i - 1) * VBLK);
    PIN();
  });
  return o;
}
DI v4u attn_unit(LAS unsigned char* L, int lane, int tl, int nv) {
  f32x16 o = attn_pass<false>(L, lane, tl, nv);
  float big = __builtin_fabsf(o[0]);
#pragma unroll
  for (int r = 1; r < 8; ++r) big += __builtin_fabsf(o[r]);
  if (__builtin_expect(!__all(o[8] > 0x1p-30f && o[8] < 0x1p60f && big < 0x1p120f), 0)) {
    asm volatile("" ::: "memory");
    o = attn_pass<true>(L, lane, tl, nv);
  }
  const float rl = __builtin_amdgcn_rcpf(o[8]);
  float y[8];
#pragma unroll
  for (int r = 0; r < 8; ++r) y[r] = o[r] * rl;
  return pack8(y);
}

struct Retile {
  v4u in[8];
  DI void load(const u16* src, int ld, int rs4) {
#pragma unroll
    for (int j = 0; j < 8; ++j) in[j] = *(const v4u*)(src + (size_t)(((j & 3) + rs4 * (j >> 2)) * ld));
    PIN();
  }
  unsigned d[8];
  DI void out(rsrc_t rs) {
#pragma unroll
    for (int i = 0; i < 8; ++i) st16(rs, in[i], d[i], 0);
  }
  DI void lines(LAS unsigned char* t, int lane, int sw, unsigned dst) {
#pragma unroll
    for (int e = 0; e < 8; ++e) {
      v4u o;
#pragma unroll
      for (int m = 0; m < 4; ++m) o[m] = __builtin_amdgcn_perm(in[2 * m + 1][e >> 1], in[2 * m][e >> 1], (e & 1) ? 0x07060302u : 0x05040100u);
      *(LAS v4u*)(t + lane * 128 + (e ^ sw ^ (lane & 7)) * 16) = o;
    }
    asm volatile("s_waitcnt lgkmcnt(0)" ::: "memory");
#pragma unroll
    for (int i = 0; i < 8; ++i) {
      in[i] = *(LAS v4u*)(t + i * 1024 + (lane ^ (lane >> 3)) * 16);
      d[i] = (unsigned)__builtin_amdgcn_ds_bpermute((8 * i + (lane >> 3)) * 4, (int)dst) + (lane & 7) * 16;
    }
    PIN();
  }
  DI void put(LAS unsigned char* L, int pb, int dd, int lp, int dl) {
#pragma unroll
    for (int e = 0; e < 8; ++e) {
      v4u o;
#pragma unroll
      for (int m = 0; m < 4; ++m) o[m] = __builtin_amdgcn_perm(in[2 * m + 1][e >> 1], in[2 * m][e >> 1], (e & 1) ? 0x07060302u : 0x05040100u);
      const int s = lp + (e & 3) + (e >> 2) * dl;
      *(LAS v4u*)(L + pb + (e >> 2) * dd + (s ^ ((s >> 3) & 7)) * 16) = o;
    }
  }
};

struct GS { int cs, j, kap, off; };
constexpr GS gu_step(int u) {
  if (u < 64) { const int k = u >> 2, j = u & 3; return GS{0, j, k, j < 3 ? (7 * (k >> 1) + 3 * (k & 1) + j) * 1024 : (7 * (k >> 1) + 6) * 1024 + (k & 1) * 512}; }
  const int v = u - 64, j = v / 17, i = v % 17;
  return GS{i == 16, j, 16 + i, 57344 + j * 16384 + i * (j < 3 ? 1024 : 512)};
}
constexpr int GA = 132, DB = 264, DA = 376, NST = 488;
constexpr int NPG = 16, NPQ = 12, LDN = 12;
constexpr int ld_step(int id) {
  if (id < 32) return id < NPG ? -1 : 2 * (id - NPG);
  if (id < 64) { const int n = id - 32; return n < 12 ? 103 + (5 * n) / 2 : GA + 10 + 2 * (n - 12); }
  if (id < 176) {
    const int n = id - 64;
    return n < 12 ? GA + 103 + n : n < 26 ? DB - 15 + ((n - 12) * 7) / 4 : n < 56 ? DB + 27 + ((n - 26) * 5) / 3 : n < 84 ? DB + 79 + ((n - 56) * 4) / 3 : DA + 31 + 2 * (n - 84);
  }
  if (id < 208) { const int n = id - 176; return n < NPQ ? DA + 87 + 2 * n : -1; }
  if (id < 224) return -1;
  return id == 224 ? DB + 80 : DA + 72;
}
constexpr int NEP = 56;
constexpr int ep_step(int k) { return 83 + 17 * (k / 16) + k % 16; }
struct Tab { short ld[NST][2]; short ep[NST]; short nvm[NST]; };
constexpr int FINB = DA + 3;
#define Q3T 75
#define XA0 18
constexpr int xa_step(int j) { const int a = XA0 + j, b = j < 12 ? 3 * j + 2 : 34 + j; return a > b ? a : b; }
constexpr Tab make_tab() {
  Tab t{};
  for (int s = 0; s < NST; ++s) { t.ld[s][0] = t.ld[s][1] = t.ep[s] = -1; t.nvm[s] = 0; }
  for (int id = 0; id < 226; ++id) {
    const int s = ld_step(id);
    if (s < 0) continue;
    t.ld[s][t.ld[s][0] >= 0] = (short)id; ++t.nvm[s];
  }
  for (int job = 0; job < 2; ++job)
    for (int k = 0; k < NEP; ++k) {
      const int s = job * GA + ep_step(k);
      t.ep[s] = (short)(job * 64 + k);
      if (k == NEP - 1) t.nvm[s] += 2;
      if (k == (job ? 31 : NEP - 1)) t.nvm[s] += 2;
    }
  ++t.nvm[FINB + 2]; ++t.nvm[FINB + 7];
  return t;
}
constexpr Tab TAB = make_tab();
constexpr int vm_between(int a, int b) {
  int n = 0;
  for (int s = a + 1; s <= b; ++s) n += TAB.nvm[s];
  return n;
}

__global__ void __launch_bounds__(512, 2) k_main(const u16* __restrict__ x, const u16* __restrict__ wqkv, const u16* __restrict__ wo, const u16* __restrict__ wg,
                                                 const u16* __restrict__ wu, const u16* __restrict__ wd, unsigned char* __restrict__ ws,
                                                 u16* __restrict__ out) {
  extern __shared__ __attribute__((aligned(16))) unsigned char smem[];
  LAS unsigned char* L = (LAS unsigned char*)smem;
  const int tid = threadIdx.x, w0 = __builtin_amdgcn_readfirstlane(tid >> 6);
  const int b = blockIdx.x;
  const int sp0 = (b & 6) + ((b >> 3) & 1), c0 = (b >> 4) + 16 * (b & 1);
  const rsrc_t rs = __builtin_amdgcn_make_buffer_rsrc((void*)ws, 0, (int)WS_BYTES, 0x00020000);
  const bool compute = w0 < 4;
  gu32* const fg = (gu32*)(ws + FLAG_G);
  gu32* const tmo = fg + FL_TMO * 32;
  unsigned tag = 0;
#define FLAG(blk, line) st4(rs, tag, lane ? NONE : 0u, (blk) + (line) * 128)
#define POLL(blk, line, n) poll((gu32*)(ws + (blk)) + ((line) + lane) * 32, lane < (n), tmo, tag)
#define GETTAG() tag = (unsigned)__builtin_amdgcn_readfirstlane(*(LAS unsigned*)(L + LDS_TAG))
  auto blank = [&](int it, unsigned ly) {
    const int lane = lane_id(), sp = sopq(sp0), c = sopq(c0);
    const unsigned o = (unsigned)vopq(-1);
    if (it < 20) st16(rs, (v4u){o, o, o, o}, lane * 16, ly + (it < 16 ? (it & 8 ? L_V : L_K) + ((8 * sp + (it & 7)) * 32 + c) * 1024 : L_X + ((16 * sp + 4 * (c & 1) + (it & 3)) * 16 + (c >> 1)) * 1024));
    else st8(rs, (v2u){o, o}, (lane >> 5) * 4096 + (lane & 31) * 8, ly + L_X + MB2 + (8 * sp + 2 * (c & 1)) * 4096 + (c >> 1) * 256);
  };
  unsigned touch0 = 0;
  v4u xq[32];

  if (compute) {
    const int layer = 0; const unsigned lay0 = LAY_OFF;
    COORDS();
    SETPRIO(2);
    const int r = lane >> 3, j = lane & 7;
    const u16* src = x + (size_t)(256 * sp + 8 * c + r) * D_ + 128 * w + 8 * j;
    v4u a[2] = {*(const v4u*)src, *(const v4u*)(src + 64)};
#pragma unroll
    for (int i = 0; i < 2; ++i) {
      a[i] = nos4(a[i]);
      st16(rs, a[i], (j >> 2) * 1024 + ((j & 3) * 16 + r) * 16, lay + L_X + (16 * sp + n32) * 16384 + (2 * w + i) * 2048 + rh * 128);
      auto quad = [](float v) {
        v += __int_as_float(__builtin_amdgcn_update_dpp(0, __float_as_int(v), 0xb1, 0xf, 0xf, true));
        return v + __int_as_float(__builtin_amdgcn_update_dpp(0, __float_as_int(v), 0x4e, 0xf, 0xf, true));
      };
      float sm = 0.f, q = 0.f;
#pragma unroll
      for (int m = 0; m < 4; ++m) sm += bflo(a[i][m]) + bfhi(a[i][m]);
      sm = quad(sm);
      const float mu = sm * (1.0f / 32.0f);
#pragma unroll
      for (int m = 0; m < 4; ++m) { const float e0 = bflo(a[i][m]) - mu, e1 = bfhi(a[i][m]) - mu; q += e0 * e0 + e1 * e1; }
      q = quad(q);
      st8(rs, (v2u){nos(__float_as_uint(sm)), nos(__float_as_uint(q))}, (j & 3) ? NONE : (j >> 2) * 256 + r * 8,
          lay + L_X + MB2 + (8 * sp + (c >> 2)) * 4096 + (4 * w + 2 * i) * 256 + (c & 3) * 64);
    }
    DRAIN();
    BAR();
    BAR();
    GETTAG();
    const int TB = 8 * sp + 4 + w;
    static_for<NPQ>([&](auto nc) { constexpr int n = nc.value; xq[n] = ld16(rs, lo + ((n >> 1) & 3) * 1024, lay + L_X + TB * 32768 + (n & 1) * 16384 + (n >> 3) * 4096); });
    BARX();
  } else {
    const int layer = 0; const unsigned lay0 = LAY_OFF;
    COORDS();
    SETPRIO(3);
    Retile rt;
    LAS unsigned char* tb = L + LDS_TB + w * 8192;
    const int id = 2 * sp + rh + 16 * (w - 1);
    auto fill0 = [&]() {
#pragma unroll 1
      for (int it = w - 1; it < 16; it += 3) blank(it, lay);
      PIN();
    };
    if (w == 0 || id < 28) {
      const u16* src; int ld, sw; unsigned dst;
      if (w == 0) {
        const int fi = lane >> 3, i8 = lane & 7, F = 6 * sp + (fi < 6 ? fi : 0);
        const int ks = F < 36 ? F / 3 : F < 44 ? (F - 12) >> 1 : F - 32, m = F < 36 ? (F - 3 * ks + 1) % 3 : F < 44 ? 1 + (F & 1) : 0;
        src = wqkv + (size_t)(32 * ks + 8 * (i8 >> 1)) * (3 * D_) + m * D_ + 16 * c + 8 * (i8 & 1);
        ld = 3 * D_; sw = i8; dst = fi < 6 ? WQKV_OFF + c * 49152 + F * 1024 + i8 * 128 : NONE;
      } else {
        const int t = 64 * id + lane, mo = t / 14, ch = t - 14 * mo, mat = mo >> 6, o = mo & 63, c7 = ch >= 7 ? ch - 7 : ch, j = c7 >> 1, kap = o >> 1;
        src = (mat ? wu : wg) + (size_t)(8 * o) * F_ + 112 * n32 + 8 * ch;
        ld = F_;
        const int cell = kap < 16 ? 7 * (kap >> 1) + (j < 3 ? 3 * (kap & 1) + j : 6) : 56 + 16 * j + (j < 3 ? kap - 16 : (kap - 16) >> 1);
        const int rho = j < 3 ? 2 * mat + (c7 & 1) + 4 * (o & 1) : 4 * (kap & 1) + 2 * (o & 1) + mat;
        sw = j < 3 ? rho : 0; dst = WGU_OFF + (2 * n32 + (ch >= 7)) * 114688 + cell * 1024 + rho * 128;
      }
      unsigned ep = 0;
      if (w == 0) ep = __hip_atomic_load(fg + FL_EP * 32, RLX_AGENT);
      rt.load(src, ld, 4);
      if (w) fill0();
      if (w == 0) {
        tag = __builtin_amdgcn_readfirstlane(ep) + 1u;
        if (lane == 0) *(LAS v4u*)(L + LDS_TAG) = (v4u){tag, 0u, 0u, 0u};
        v4u mk[2];
#pragma unroll
        for (int k = 0; k < 8; ++k) {
          const int kq = ((2 * k) & 3) + 8 * ((2 * k) >> 2) + 4 * hh, ql = lane & 31;
          mk[k >> 2][k & 3] = (kq <= ql ? 0xffffu : 0u) | (kq + 1 <= ql ? 0xffff0000u : 0u);
        }
        *(LAS v4u*)(L + LDS_MK + lo) = mk[0];
        *(LAS v4u*)(L + LDS_MK + 1024 + lo) = mk[1];
        if (sp == 0) {
#pragma unroll
          for (int i = 0; i < 9; ++i) *(LAS v4u*)(L + LDS_VL + (i < 8 ? i * 1024 + lo : 8192 + t16 * 16)) = (v4u){0u, 0u, 0u, 0u};
        }
        const unsigned one = (sp == 0 && lane < 16) ? 0u : 0x3f803f80u;
        if (lane < 32) *(LAS v4u*)(L + LDS_VL + lane * VBLK + 512) = (v4u){one, one, one, one};
      }
      rt.lines(tb, lane, sw, dst);
      if (w == 0) rt.out(rs);
    } else if (id < 46) {
      const int slot = (lane >> 2) & 3, m4 = lane & 3, q = 16 * (id < 42 ? id - 28 : id - 42) + 4 * g4 + slot;
      const int cq = q < 128 ? q >> 2 : q < 192 ? (q - 128) >> 1 : q - 192, r = q < 128 ? q & 3 : q < 192 ? 4 + (q & 1) : 6;
      rt.load((id < 42 ? wd + (size_t)(56 * cq + (r < 6 ? 16 * (r >> 1) + 4 * (r & 1) : 48)) * D_ : wo + (size_t)(16 * (q >> 1) + 4 * (q & 1)) * D_) + 32 * n32 + 8 * m4,
              D_, (id < 42 && r == 6) ? 4 : 8);
      fill0();
      rt.put(tb, g4 * 2048, 1024, 4 * m4 + 16 * slot, 0);
      asm volatile("s_waitcnt lgkmcnt(0)" ::: "memory");
#pragma unroll
      for (int i = 0; i < 8; ++i) {
        rt.in[i] = lds16(tb, i * 1024 + lo);
        rt.d[i] = (id < 42 ? WDN_OFF + n32 * 114688 + (id - 28) * 8192 : WO_OFF + n32 * 32768 + (id - 42) * 8192) + i * 1024 + lo;
      }
    } else {
#pragma unroll
      for (int i = 0; i < 8; ++i) { undef(rt.in[i]); rt.d[i] = NONE; }
      fill0();
    }
    if (w == 0) DRAIN();
    HBAR();
    if (w == 0) {
      FLAG(0, FL_XB + sp * 32 + c);
      poll((gu32*)ws + (FL_XB + (lane < 32 ? sp * 32 + lane : (lane & 7) * 32 + c)) * 32, lane < 40, tmo, tag);
    }
    Stats st;
    if (w == 0) {
      st.load(rs, lane, lay + L_X + MB2 + (8 * sp + 4) * 4096);
    }
    HBAR();
    GETTAG();
    if (w) {
      const int l = w - 1;
      dma<4>(rs, L + l * 4096, lo, WQKV_OFF + c * 49152 + l * 4096);
      dma<7>(rs, L + (12 + 7 * l - (l >> 1)) * 1024, lo, WQKV_OFF + c * 49152 + (12 + 7 * l - (l >> 1)) * 1024);
      wait_vm<7>();
    }
    HBAR();
    if (w) { dma<6>(rs, L + (27 + 5 * w) * 1024, lo, WQKV_OFF + c * 49152 + (27 + 5 * w) * 1024);
      rt.out(rs); wait_vm<14>();
    }
    else {
      st.all(L, lane);
      PIN();
      FLAG(0, FL_XA + sp * 32 + c);
      const int i = lane & 31;
      const int line = i < 8 ? FL_KV + (8 * sp + i) * 32 + c : i < 16 ? FL_O + (8 * sp + i - 8) * 32 + c
                     : i < 20 ? FL_X1B + (8 * sp + 4 + 2 * rh + ((i >> 1) & 1)) * 32 + 2 * n32 + (i & 1) : FL_X1A + (i - 20 < 6 ? i - 20 : 5) * 256 + sp * 32 + c;
      touch0 = __builtin_amdgcn_raw_buffer_load_b32(rs, hh * FLAG_LAYER + line * 128, 0, AUX_SC1);
    }
  }

  for (int layer = 0; layer < 2; ++layer) {
    const bool last = layer == 1;
    const unsigned lay0 = LAY_OFF + layer * LAY_BYTES;
    auto ldw = [&](auto nc, unsigned src, int cell) {
      constexpr int N = nc.value, K = (N + 2) / 3;
      const int l = sopq(w0) - 5, c1 = cell + (l * K + K > N ? N - K : l * K);
      dma<K>(rs, L + c1 * 1024, lane_id() * 16, src + c1 * 1024);
    };
    auto ld_gu = [&](int s) { ldw(IC<14>{}, WGU_OFF + sopq(c0) * 114688, 14 * s); };
    auto ld_dn = [&](int s) { ldw(IC<14>{}, WDN_OFF + (sopq(c0) >> 1) * 114688, 14 * s); };
    if (compute) {
      COORDS();
      const int TB = 8 * sp + 4 + w, TA = TB - 4;
      v4u xa[32];
      auto ldx = [&](auto nc, int T) {
        constexpr int n = nc.value;
        return ld16(rs, lo + ((n >> 1) & 3) * 1024, lay + L_X + (2 * T + (n & 1)) * 16384 + (n >> 3) * 4096);
      };
      f32x4 aq[2][2], ak[2][2], av[2][2], r0, r1;
      v4u mf0, mf1; float rd0, rd1;
      unsigned o0, o1, o2, o3;
      const int kpos = (t16 + 16 * hh + 32 * (g4 & 1)) * 16;
      auto epi = [&](int k, int pa) {
        const int tl = pa ? w : 4 + w, st = (pa ? LDS_KL : LDS_ST) + w * 128;
        const unsigned kvb = lay + ((8 * sp + tl) * 32 + c) * 1024;
        if (k == 0) { r0 = lds4f(L, st + g4 * 16); r1 = lds4f(L, st + 64 + g4 * 16); }
        if (k == 2) { o0 = mulpk(ak[pa][0][0], rd0, ak[pa][0][1], rd0); o1 = mulpk(ak[pa][0][2], rd0, ak[pa][0][3], rd0); }
        if (k == 3) { o2 = mulpk(ak[pa][1][0], rd1, ak[pa][1][1], rd1); o3 = mulpk(ak[pa][1][2], rd1, ak[pa][1][3], rd1); }
        if (k == 4) {
          swap32(o0, o2); swap32(o1, o3);
          st16(rs, nos4((v4u){o0, o1, o2, o3}), kpos, kvb + L_K);
          *(LAS v4u*)(L + LDS_KL + (8 + tl) * 1024 + kpos) = (v4u){o0, o1, o2, o3};
        }
        if (k == 5) { o0 = mulpk(av[pa][0][0], r0[0], av[pa][0][1], r0[1]); o1 = mulpk(av[pa][0][2], r0[2], av[pa][0][3], r0[3]); }
        if (k == 6) { o2 = mulpk(av[pa][1][0], r1[0], av[pa][1][1], r1[1]); o3 = mulpk(av[pa][1][2], r1[2], av[pa][1][3], r1[3]); }
        if (k == 7) {
          swap32(o0, o2); swap32(o1, o3);
          st16(rs, nos4((v4u){o0, o1, o2, o3}), lo, kvb + L_V);
          *(LAS v4u*)(L + LDS_VL + (16 + 2 * tl + hh) * VBLK + (lane & 31) * 16) = (v4u){o0, o1, o2, o3};
        }
        if (k == 8) {
          asm volatile("s_nop 7" : "+v"(aq[pa][0]), "+v"(aq[pa][1]));
          const float s0 = rd0 * (0.25f * LOG2E), s1 = rd1 * (0.25f * LOG2E);
          o0 = mulpk(aq[pa][0][0], s0, aq[pa][0][1], s0); o1 = mulpk(aq[pa][0][2], s0, aq[pa][0][3], s0);
          o2 = mulpk(aq[pa][1][0], s1, aq[pa][1][1], s1); o3 = mulpk(aq[pa][1][2], s1, aq[pa][1][3], s1);
          swap32(o0, o2); swap32(o1, o3);
          *(LAS v4u*)(L + LDS_QL + tl * 1024 + kpos) = (v4u){o0, o1, o2, o3};
        }
      };
      pipe<102>(
          [&](auto fc) {
            constexpr int t = fc.value, u = t % 51;
            if constexpr (t == 28) { BARX(); CST(); }
            if constexpr (t == 12 || t == 44 || t == Q3T) BARX();
            if constexpr (u == 44 || u == 45 || u == 50) return csfrag(L, (u == 50 ? 0 : u - 43) * 16 + t16);
            else return lds16(L, (u < 44 ? u : u - 2) * 1024 + lsw);
          },
          [&](auto fc, const v4u& wfr, auto pc) {
            constexpr int t = fc.value, part = pc.value, u = t % 51;
            constexpr int ks = (u < 36 ? u / 3 : u < 44 ? (u - 12) >> 1 : u - 34) & 15, m = u < 36 ? (u % 3 + 1) % 3 : u < 46 ? 1 + (u & 1) : 0;
            constexpr bool pa = t >= 51, cs = u == 44 || u == 45 || u == 50;
            if constexpr (t < 0) return;
            else if constexpr (part == 0) {
              if constexpr (pa && u < 44 && u % 12 == 0) {
                constexpr int n = u / 12 * 8;
                if (__builtin_expect(missing<8>(xa + n), 0)) {
                  unsigned spins = 0;
                  do { static_for<8>([&](auto i) { xa[n + i.value] = ldx(IC<n + i.value>{}, TA); }); DBGC(0); } while (missing<8>(xa + n) && ++spins < DATA_CAP);
                }
                PIN();
              }
              const v4u& x0 = cs ? mf0 : pa ? xa[2 * ks] : xq[2 * ks];
              const v4u& x1 = cs ? mf1 : pa ? xa[2 * ks + 1] : xq[2 * ks + 1];
              if constexpr (m == 0) { aq[pa][0] = mfma16(wfr, x0, u < 3 ? zero4() : aq[pa][0]); aq[pa][1] = mfma16(wfr, x1, u < 3 ? zero4() : aq[pa][1]); }
              if constexpr (m == 1) { ak[pa][0] = mfma16(wfr, x0, u < 3 ? zero4() : ak[pa][0]); ak[pa][1] = mfma16(wfr, x1, u < 3 ? zero4() : ak[pa][1]); }
              if constexpr (m == 2) { av[pa][0] = mfma16(x0, wfr, u < 3 ? zero4() : av[pa][0]); av[pa][1] = mfma16(x1, wfr, u < 3 ? zero4() : av[pa][1]); }
            } else {
              if constexpr (2 * t < 32 - NPQ) { xq[NPQ + 2 * t] = ldx(IC<NPQ + 2 * t>{}, TB); xq[NPQ + 2 * t + 1] = ldx(IC<NPQ + 2 * t + 1>{}, TB); }
              if constexpr (u == 40) {
                const int st = (pa ? LDS_KL : LDS_ST) + w * 128;
                rd0 = lds1f(L, st + t16 * 4); rd1 = lds1f(L, st + 64 + t16 * 4);
                const int a = g4 ? LDS_TAG + 8 : st + 1024 + t16 * 4;
                mf0 = mfrag(L, a); mf1 = mfrag(L, g4 ? a : a + 64);
              }
              if constexpr (t >= 45 && t < 54) epi(t - 45, 0);
              if constexpr (t >= 96) epi(t - 96, 1);
              static_for<16>([&](auto j) { if constexpr (xa_step(j.value) == t) { xa[2 * j.value] = ldx(IC<2 * j.value>{}, TA); xa[2 * j.value + 1] = ldx(IC<2 * j.value + 1>{}, TA); } });
              if constexpr (t == 50) CST();
            }
          });
      epi(6, 1); epi(7, 1); epi(8, 1);
      BAR();
      CST();
    } else {
      COORDS();
      const bool loader = w != 0;
      auto cs_qkv = [&]() {
        const unsigned one = (unsigned)vopq(0x3f803f80);
        const v4u ones = (v4u){one, one, one, one};
        f32x4 a = zero4();
        v4u f[16];
        const int fa = (w - 1) * 1024 + lsw, fb = (w < 3 ? 35 + w : 44) * 1024 + lsw, fs = w < 3 ? 2048 : 1024;
#pragma unroll
        for (int ks = 0; ks < 16; ++ks) asm volatile("ds_read_b128 %0, %1" : "=v"(f[ks]) : "v"(ks < 12 ? fa + ks * 3072 : fb + (ks - 12) * fs) : "memory");
        asm volatile("s_waitcnt lgkmcnt(0)" : "+v"(f[0]), "+v"(f[1]), "+v"(f[2]), "+v"(f[3]), "+v"(f[4]), "+v"(f[5]), "+v"(f[6]), "+v"(f[7]));
        asm volatile("" : "+v"(f[8]), "+v"(f[9]), "+v"(f[10]), "+v"(f[11]), "+v"(f[12]), "+v"(f[13]), "+v"(f[14]), "+v"(f[15]));
#pragma unroll
        for (int ks = 0; ks < 16; ++ks) a = mfma16(f[ks], ones, a);
        if (t16 == 0) {
#pragma unroll
          for (int r = 0; r < 4; ++r) cs_put(L, (w % 3) * 16 + 4 * g4 + r, a[r]);
        }
      };
      const unsigned wob = WO_OFF + n32 * 32768 - LDS_WO;
      if (last && !loader) {
        v4u o[4];
#pragma unroll
        for (int i = 0; i < 4; ++i) o[i] = lds16(L, LDS_QL + i * 1024 + lo);
        const v2u sq = *(LAS v2u*)(L + LDS_QL + 4096 + lane * 8);
#pragma unroll
        for (int i = 0; i < 4; ++i) st16(rs, o[i], lo, lay + L_X + ((16 * sp + 4 * rh + i) * 16 + n32) * 1024);
        st8(rs, sq, hh * 4096 + (g4 & 1) * 128 + t16 * 8, lay + L_X + MB2 + (8 * sp + 2 * rh) * 4096 + n32 * 256);
        PIN();
      }
      HBARS(false);
      if (layer == 0) {
        if (loader) wait_vm<8>();
        HBAR();
        if (loader) cs_qkv();
        HBAR();
      } else {
        if (loader) { ldw(IC<10>{}, wob, 80); wait_vm<13>(); }
        HBAR();
        if (loader) ldw(IC<10>{}, wob, 90);
        HBAR();
        if (loader) ldw(IC<12>{}, wob, 100);
      }
      {
        v4u kv[4];
        auto ldkv = [&]() {
          const unsigned so = lay + ((8 * sp - 5 + w) * 32 + c) * 1024, s2 = w == 1 ? so + 3 * 32768 : NONE;
          kv[0] = ld16(rs, lo, so + L_K); kv[1] = ld16(rs, lo, so + L_V); kv[2] = ld16(rs, lo, s2 + L_K); kv[3] = ld16(rs, lo, s2 + L_V);
          PIN();
          DBGC(0);
        };
        if (!loader) {
          Stats st;
          gu32* const bl = (gu32*)ws + (FL_XA + (sp ? sp - 1 : 0) * 32 + c) * 32;
          unsigned spins = 0, pv = tag;
          if (!last) pv = __hip_atomic_load(bl, RLX_AGENT);
          do { st.load(rs, lane, lay + L_X + MB2 + 8 * sp * 4096); DBGC(0); } while (missing<16>((const v4u*)st.p) && ++spins < DATA_CAP);
          st.all(L, lane, LDS_KL);
          if (!__all(pv == tag)) poll(bl, lane == 0, tmo, tag);
        }
        DRAIN();
        HBARS(false);
        if (loader && sp > 0) {
          unsigned spins = 0;
          do ldkv(); while (missing<4>(kv) && ++spins < DATA_CAP);
          *(LAS v4u*)(L + LDS_KL + (3 + w) * 1024 + lo) = kv[0];
          *(LAS v4u*)(L + LDS_VL + (6 + 2 * w + hh) * VBLK + (lane & 31) * 16) = kv[1];
          if (w == 1) {
            *(LAS v4u*)(L + LDS_KL + 7 * 1024 + lo) = kv[2];
            *(LAS v4u*)(L + LDS_VL + (14 + hh) * VBLK + (lane & 31) * 16) = kv[3];
          }
        }
      }
      if (layer == 0 && !loader) { asm volatile("" ::"v"(touch0)); FLAG(FLAG_G, FL_WR + n32 * 16 + 2 * sp + rh); }
      DRAIN();
      HBAR();
      CST(); CST();
      if (loader) {
        if (layer == 0) {
          POLL(FLAG_G, FL_WR + n32 * 16, 16);
          ldw(IC<10>{}, wob, 80); ldw(IC<10>{}, wob, 90); ldw(IC<12>{}, wob, 100);
          ld_gu(0);
          wait_vm<5>();
          if (lane == 0) asm volatile("ds_add_u32 %0, %1" ::"v"(LDS_TAG + 12), "v"(1) : "memory");
        } else ld_gu(0);
        ld_gu(1); ld_gu(2); ldw(IC<6>{}, WGU_OFF + c * 114688, 42);
      }
      if (sp > 0) {
        LAS unsigned* hw = (LAS unsigned*)(L + LDS_TAG + 4);
        CST();
        if (!loader) {
          v4u kv[8];
          unsigned spins = 0;
          do {
#pragma unroll
            for (int t = 0; t < 4; ++t) {
              const unsigned so = lay + ((8 * sp - 8 + t) * 32 + c) * 1024;
              kv[t] = ld16(rs, lo, so + L_K); kv[4 + t] = ld16(rs, lo, so + L_V);
            }
            PIN();
            DBGC(1);
          } while (missing<8>(kv) && ++spins < DATA_CAP);
#pragma unroll
          for (int t = 0; t < 4; ++t) {
            *(LAS v4u*)(L + LDS_KL + t * 1024 + lo) = kv[t];
            *(LAS v4u*)(L + LDS_VL + (2 * t + hh) * VBLK + (lane & 31) * 16) = kv[4 + t];
          }
          asm volatile("s_waitcnt lgkmcnt(0)" ::: "memory");
          __hip_atomic_store(hw, (unsigned)layer + 1u, __ATOMIC_RELAXED, __HIP_MEMORY_SCOPE_WORKGROUP);
        } else {
          unsigned v;
          do {
            __builtin_amdgcn_s_sleep(1);
            asm volatile("ds_read_b32 %0, %1\n\ts_waitcnt lgkmcnt(0)" : "=v"(v) : "v"(LDS_TAG + 4) : "memory");
          } while ((unsigned)__builtin_amdgcn_readfirstlane(v) != (unsigned)layer + 1u);
        }
      } else CST();
      CST();
      if (layer == 0 && !loader) touch0 = __builtin_amdgcn_raw_buffer_load_b32(rs, LAY_OFF + L_O + lane * TSTR + c * 4096 + sp * 128, 0, AUX_SC1);
      SETPRIO(1);
    }
    {
      COORDS();
      const int tl = compute ? 4 + w : w, T = 8 * sp + tl;
      const v4u o = attn_unit(L, lane, tl, sp == 0 ? 8 - tl : 0);
      st16(rs, o, (g4 & 1) * 16384 + hh * 256 + t16 * 16, lay + L_O + T * 32768 + c * 512);
      DRAIN();
      FLAG(flo, FL_O + T * 32 + c);
      CST();
    }
    if (compute) {
      SETPRIO(0);
      v4u fr[176];
      {
        COORDS();
        const int TB = 8 * sp + 4 + 2 * rh + (w >> 1), SB = 2 * TB + (w & 1);
        const v4u rB = ld16(rs, lo, lay + L_X + (SB * 16 + n32) * 1024), rA = ld16(rs, lo, lay + L_X + (SB * 16 + n32) * 1024 - 8 * 16384);
        const unsigned xv = (g4 >> 1) * 1024 + (g4 & 1) * 512 + t16 * 16, sv = g4 ? NONE : t16 * 8;
        const unsigned xo = lay + L_X1 + (TB * 32 + 2 * n32) * 1024 + (w & 1) * 256, so = lay + L_X1 + MB2 + TB * 4096 + n32 * 256 + (w & 1) * 128;
        const int lswo = vopq(lsw + LDS_WO);
        while ((unsigned)__builtin_amdgcn_readfirstlane(__hip_atomic_load((LAS unsigned*)(L + LDS_TAG + 12), __ATOMIC_RELAXED, __HIP_MEMORY_SCOPE_WORKGROUP)) != 3u)
          __builtin_amdgcn_s_sleep(1);
        asm volatile("" ::: "memory");
        v4u of[16];
        unsigned pk;
        f32x4 a0, a1;
        Fin fin;
        pipe<64>(
            [&](auto fc) { return lds16(L, (fc.value & 31) * 1024 + lswo); },
            [&](auto fc, const v4u& wfr, auto pc) {
              constexpr int t = fc.value, part = pc.value, u = t & 31;
              if constexpr (part == 0) {
                if constexpr (u & 1) a1 = mfma16(wfr, of[u >> 1], u == 1 ? zero4() : a1);
                else a0 = mfma16(wfr, of[u >> 1], u == 0 ? zero4() : a0);
              } else {
                if constexpr (t == 31 || t == 63) {
                  asm volatile("s_nop 7" : "+v"(a0), "+v"(a1));
                  static_for<8>([&](auto k) { fin.template chunk<k.value>(a0, a1, t == 31 ? rB : rA, rs, xv, xo - (t == 31 ? 0 : 4 * 32768), rs, sv, so - (t == 31 ? 0 : 4 * 4096)); });
                  CST();
                }
                if constexpr (t == -1 || t == 31) {
                  if constexpr (t == 31) {
                    pk = __hip_atomic_load(fl + (FL_O + (TB - 4) * 32 + (lane & 31)) * 32, RLX_AGENT);
                    DRAIN();
                    FLAG(flo, FL_X1B + TB * 32 + 2 * n32 + (w & 1));
                  }
                  if (t == -1 || !__all(pk == tag)) POLL(flo, FL_O + (TB - (t == 31 ? 4 : 0)) * 32, 32);
                  CST();
                  static_for<16>([&](auto i) { of[i.value] = ld16(rs, lo + (i.value & 3) * 1024, lay + L_O + (SB - (t == 31 ? 8 : 0)) * 16384 + (i.value >> 2) * 4096); });
                }
                if constexpr (t == 55) pk = __hip_atomic_load(fl + (FL_X1B + (8 * sp + 4 + w) * 32 + (lane & 31)) * 32, RLX_AGENT);
                if constexpr (t == 63) {
                  const int T = 8 * sp + 4 + w;
                  if (!__all(pk == tag)) POLL(flo, FL_X1B + T * 32, 32);
                  CST();
                  static_for<NPG>([&](auto i) { fr[i.value] = ld16(rs, lo + (i.value & 3) * 1024, lay + L_X1 + T * 32768 + (i.value >> 2) * 4096); });
                }
              }
            });
      }
      SETPRIO(2);
      BAR();
      {
        COORDS();
        const int TB = 8 * sp + 4 + w;
        const int TD = 8 * sp + 4 + 2 * rh + (w >> 1), SD = 2 * TD + (w & 1);
        const unsigned x1b = lay + L_X1 + TB * 32768, hb = lay + L_H + SD * 57344, xn = last ? NONE : lay + LAY_BYTES + L_X;
        const unsigned hsv = (g4 & 1) * 57344 + hh * 256 + t16 * 16, hso = lay + L_H + TB * 114688;
        const rsrc_t ro = last ? __builtin_amdgcn_make_buffer_rsrc((void*)out, 0, (int)MB2, 0x00020000) : rs;
        const unsigned ob = last ? SD * 16384 + n32 * 64 : xn + (SD * 16 + n32) * 1024;
        const unsigned sb = xn + MB2 + TD * 4096 + n32 * 256 + (w & 1) * 128;
        unsigned ov, sv;
        const int lsw2 = vopq(lsw + 65536), lh = (hh * 16 + t16) * 16, lh2 = vopq(lh + 65536);
        f32x16 acc[2][4];
        f32x4 d0[2], d1[2];
        v4u mf, hv[4], res[2];
        float k1[2], den2[2], t0, t1, p0_, p1_;
        Fin fin;
        auto epi = [&](auto ec) {
          constexpr int job = ec.value >> 6, k = ec.value & 63, j = k >> 4, i = (k >> 2) & 3, uo = j < 3 ? 8 : 4;
          const f32x16& a = acc[job][j];
          if constexpr ((k & 3) == 0)
            asm volatile("v_mul_f32 %0, %2, %4\n\tv_mul_f32 %1, %3, %4\n\tv_exp_f32 %0, %0" : "=&v"(t0), "=&v"(t1) : "v"(a[2 * i]), "v"(a[2 * i + 1]), "v"(k1[job]));
          else if constexpr ((k & 3) == 1)
            asm volatile("v_exp_f32 %0, %0\n\tv_mul_f32 %1, %3, %5\n\tv_mul_f32 %2, %4, %6"
                : "+v"(t1), "=&v"(p0_), "=&v"(p1_) : "v"(a[2 * i]), "v"(a[2 * i + 1]), "v"(a[uo + 2 * i]), "v"(a[uo + 2 * i + 1]));
          else if constexpr ((k & 3) == 2)
            asm volatile("v_fma_f32 %0, %0, %2, %2\n\tv_fma_f32 %1, %1, %2, %2\n\tv_rcp_f32 %0, %0" : "+v"(t0), "+v"(t1) : "v"(den2[job]));
          else {
            unsigned d;
            asm volatile("v_rcp_f32 %2, %2\n\tv_mul_f32 %1, %1, %3\n\tv_mul_f32 %2, %2, %4\n\tv_cvt_pk_bf16_f32 %0, %1, %2" : "=v"(d), "+v"(t0), "+v"(t1) : "v"(p0_), "v"(p1_));
            hv[j][i] = d;
            if constexpr (k == (job ? 31 : NEP - 1)) { st16(rs, hv[0], hsv, hso - job * 4 * 114688 + c * 1024); st16(rs, hv[1], hsv + 512, hso - job * 4 * 114688 + c * 1024); }
            if constexpr (k == NEP - 1) {
              st16(rs, hv[2], hsv, hso - job * 4 * 114688 + 32768 + c * 512);
              st8(rs, (v2u){hv[3][0], hv[3][1]}, hsv - hh * 248, hso - job * 4 * 114688 + 49152 + c * 256);
            }
          }
        };
        auto vload = [&](auto ic) {
          constexpr int id = ic.value;
          if constexpr (id < 64) fr[id] = ld16(rs, lo + (id & 3) * 1024, x1b - (id >> 5) * 4 * 32768 + ((id & 31) >> 2) * 4096);
          else if constexpr (id < 176) {
            constexpr int n = (id - 64) % 56;
            fr[id] = ld16(rs, lo + (n & 3) * 1024, hb - (id >= 120 ? 8 * 57344 : 0) + (n >> 2) * 4096);
          } else if constexpr (id < 208) {
            constexpr int n = id - 176;
            xq[n] = ld16(rs, lo + ((n >> 1) & 3) * 1024, xn + TB * 32768 + (n & 1) * 16384 + (n >> 3) * 4096);
          } else res[id - 224] = ld16(rs, vopq(lo & 0x2f0) + ((lo & 256) << 1) + (lo & 512), lay + L_X1 + ((TD - 4 * (id - 224)) * 32 + 2 * n32) * 1024 + (w & 1) * 256);
        };
        pipe<NST>(
            [&](auto fc) {
              constexpr int t = fc.value;
              if constexpr (t == 32) wait_vm<vm_between(-1, t - LA - 1) + NPG>();
              if constexpr (t == GA + 48) wait_vm<vm_between(ep_step(NEP - 1), t - LA - 1)>();
              if constexpr (t == DB + 14) wait_vm<vm_between(GA + ep_step(31), t - LA - 1)>();
              if constexpr (t == DB + 56) wait_vm<vm_between(GA + ep_step(NEP - 1), t - LA - 1)>();
              if constexpr (t == DA + 42) wait_vm<vm_between(FINB + 7, t - LA - 1)>();
              if constexpr (t < DB) {
                constexpr GS s = gu_step(t % GA);
                if constexpr (s.cs) return csfrag(L, 48 + 32 * s.j + (s.j < 3 ? lane & 31 : t16));
                else {
                  if (s.off % 14336 == 0 && s.off >= (t < GA ? 14336 : 28672)) BARX();
                  return lds16(L, s.off < 65536 ? s.off + (s.j < 3 ? lsw : lh) : s.off - 65536 + (s.j < 3 ? lsw2 : lh2));
                }
              } else {
                constexpr int v = (t - DB) % 112;
                if (v % 14 == 0 && v >= (t < DA ? 0 : 28)) BARX();
                return lds16(L, v < 64 ? v * 1024 + lsw : (v - 64) * 1024 + lsw2);
              }
            },
            [&](auto fc, const v4u& wfr, auto pc) {
              constexpr int t = fc.value, part = pc.value;
              if constexpr (t < 0) return;
              else if constexpr (part == 0) {
                if constexpr (t < DB) {
                  constexpr int job = t / GA, u = t % GA;
                  constexpr GS s = gu_step(u);
                  acc[job][s.j] = mfma(wfr, s.cs ? mf : fr[32 * job + (s.kap & 31)], u < 4 ? splat16(0.f) : acc[job][s.j]);
                } else {
                  constexpr int job = t >= DA, v = (t - DB) % 112;
                  if constexpr (v & 1) d1[job] = mfma16(wfr, fr[64 + 56 * job + (v >> 1)], v == 1 ? zero4() : d1[job]);
                  else d0[job] = mfma16(wfr, fr[64 + 56 * job + (v >> 1)], v == 0 ? zero4() : d0[job]);
                }
              } else {
                if constexpr (t < DB) {
                  constexpr int job = t / GA, u = t % GA;
                  if constexpr (u == 74) {
                    const int a = LDS_ST + w * 128 + (lane & 31) * 4;
                    k1[job] = lds1f(L, a); den2[job] = lds1f(L, a + 512); mf = mfrag(L, hh ? LDS_TAG + 8 : a + 1024);
                  }
                  if constexpr (u == 79) k1[job] *= -LOG2E;
                }
                if constexpr (TAB.ld[t][0] >= 0) vload(IC<TAB.ld[t][0]>{});
                if constexpr (TAB.ld[t][1] >= 0) vload(IC<TAB.ld[t][1]>{});
                if constexpr (TAB.ep[t] >= 0) epi(IC<TAB.ep[t]>{});
                if constexpr (t == FINB - 1) { const int l = lane_id(); ov = l * 16 + ((((l & 15) * 1008) - (l >> 4) * 240) & sopq(-layer)); sv = l >= 16 ? NONE : l * 8; }
                if constexpr (t >= FINB && t < FINB + 8) fin.template chunk<t - FINB>(d0[0], d1[0], res[0], ro, ov, ob, rs, sv, sb);
              }
            });
        CST();
        asm volatile("s_nop 7" : "+v"(d0[1]), "+v"(d1[1]));
        static_for<8>([&](auto k) { fin.template chunk<k.value, false>(d0[1], d1[1], res[1], ro, ov, ob - 8 * 16384, rs, sv, sb - 4 * 4096); });
        if (last) st16(ro, fin.o, ov, ob - 8 * 16384);
        else {
          *(LAS v4u*)(L + LDS_QL + w * 1024 + lo) = fin.o;
          if (lane < 16) *(LAS v2u*)(L + LDS_QL + 4096 + w * 128 + lane * 8) = fin.sq;
          BAR();
        }
      }
    } else {
      COORDS();
      const bool poller = w == 0, loader = w != 0;
      unsigned pv = 0;
#define PEEK(blk, line) pv = __hip_atomic_load((gu32*)(ws + (blk)) + ((line) + (lane & 31)) * 32, RLX_AGENT)
#define GATE(blk, line) do { if (!__all(pv == tag)) POLL(blk, line, 32); } while (0)
      if (layer == 0) {
        if (loader) { ldw(IC<8>{}, WGU_OFF + c * 114688, 48); ld_gu(4); ldw(IC<10>{}, WGU_OFF + c * 114688, 70); }
        f32x4 ca = zero4();
        const unsigned one = (unsigned)vopq((w == 3 || t16 == (g4 & 1)) ? 0x3f803f80 : 0), wb = WGU_OFF + c * 114688;
        for (int q = 0; q < (w < 3 ? 2 : 1); ++q) {
          v4u f[16];
          static_for<16>([&](auto ic) {
            constexpr int i = ic.value;
            f[i] = ld16(rs, w < 3 ? lsw : lo, wb + (w < 3 ? (q ? 56 + 16 * w + i : 7 * (i >> 1) + 3 * (i & 1) + w) : i < 8 ? 7 * i + 6 : 96 + i) * 1024);
          });
          PIN();
          static_for<16>([&](auto i) { ca = mfma16(f[i.value], (v4u){one, one, one, one}, ca); });
        }
        if (t16 < (w < 3 ? 2 : 1)) {
#pragma unroll
          for (int r = 0; r < 4; ++r) cs_put(L, 48 + 32 * w + 16 * t16 + 4 * g4 + r, ca[r]);
        }
      }
      SETPRIO(3);
      if (poller) {
        asm volatile("" ::"v"(touch0));
        Stats st;
        unsigned ev[4];
        const int h2l = lane < 32 ? FL_HA2 + sp * 32 + lane : FL_HB + (sp ? sp - 1 : 0) * 32 + c;
        SETPRIO(1);
        HBAR();
        static_for<7>([&](auto nc) {
          constexpr int n = nc.value + 1;
          if (n <= 2) st.load(rs, lane, lay + L_X1 + MB2 + (8 * sp + 4) * 4096, 8 * n - 8, 8 * n);
          if (n == 3) { FLAG(flo, FL_X1A + sp * 32 + c); st.part1(); }
          if (n == 4) st.part2();
          if (n == 5) st.part3(L, lane);
          if (n == 6) PEEK(flo, FL_X1A + sp * 32);
          if (n == 7) GATE(flo, FL_X1A + sp * 32);
          HBARS(n == 3 || n == 7);
        });
        st.load(rs, lane, lay + L_X1 + MB2 + 8 * sp * 4096);
        st.all(L, lane);
        static_for<6>([&](auto nc) {
          constexpr int n = nc.value + 2;
          if (n == 4) FLAG(flo, FL_HB + sp * 32 + c);
          if (n == 6) PEEK(flo, FL_HB + sp * 32);
          if (n == 7) GATE(flo, FL_HB + sp * 32);
          HBARS(n == 2 || n == 7);
        });
        static_for<8>([&](auto nc) {
          constexpr int n = nc.value;
          if (n == 2) FLAG(flo, FL_HA + sp * 32 + c);
          if (n == 5) { FLAG(flo, FL_HA2 + sp * 32 + c); PEEK(flo, FL_HA + sp * 32); }
          if (n == 6) GATE(flo, FL_HA + sp * 32);
          if (n == 7) pv = __hip_atomic_load(fl + h2l * 32, RLX_AGENT);
          HBARS(n == 0 || n == 3 || n == 6);
        });
        static_for<6>([&](auto nc) {
          constexpr int n = nc.value + 2;
          if (n == 2) { if (!__all(pv == tag)) poll(fl + h2l * 32, true, tmo, tag); }
          if (n == 3) {
#pragma unroll
            for (int q = 0; q < 4; ++q) ev[q] = __builtin_amdgcn_raw_buffer_load_b32(rs, lane * 128, last && blockIdx.x == 0 ? flo + (FL_HB + 64 * q) * 128 : NONE, AUX_SC1);
          }
          if (n == 4 && !last) FLAG(FLAG_LAYER, FL_XB + sp * 32 + c);
          if (n == 5) PEEK(FLAG_LAYER, FL_XB + sp * 32);
          if (n == 6) GATE(FLAG_LAYER, FL_XB + sp * 32);
          if (n == 7) st.load(rs, lane, last ? NONE : LAY_OFF + LAY_BYTES + L_X + MB2 + (8 * sp + 4) * 4096);
          HBARS(n == 2 || n == 6 || n == 7);
        });
        if (!last) { st.all(L, lane); }
        if (last && blockIdx.x == 0) {
          if (!__all(ev[0] == tag && ev[1] == tag && ev[2] == tag && ev[3] == tag))
            for (int q = 0; q < 4; ++q) POLL(flo, FL_HB + 64 * q, 64);
          if (lane == 0) __hip_atomic_store(fg + FL_EP * 32, tag, RLX_AGENT);
        }
      } else {
        const unsigned wq = WQKV_OFF + c * 49152, wb = WGU_OFF + c * 114688;
        DRAIN();
        HBAR();
        static_for<7>([&](auto nc) {
          constexpr int n = nc.value + 1;
          if (n == 7) wait_vm<1>();
          blank(3 * n - 4 + w, last ? NONE : LAY_OFF + LAY_BYTES);
          if (n >= 2 && n <= 4) ldw(IC<9>{}, wb, 62 + 9 * n);
          if (n == 5) { ldw(IC<5>{}, wb, 107); wait_vm<11>(); }
          if (n == 6) wait_vm<8>();
          HBARS(n == 3 || n == 7);
        });
        static_for<6>([&](auto nc) {
          constexpr int n = nc.value + 2;
          if (n == 3) wait_vm<0>();
          if (n == 5 || n == 6) { ld_dn(2 * n - 10); ld_dn(2 * n - 9); } else if (n > 6) ld_dn(n - 3);
          HBARS(n == 2 || n == 7);
        });
        static_for<8>([&](auto nc) {
          constexpr int n = nc.value;
          if (n <= 2) ld_dn(n + 5);
          wait_vm<(n <= 2 ? 25 : 35 - 5 * n)>();
          HBARS(n == 0 || n == 3 || n == 6);
        });
        static_for<6>([&](auto nc) {
          constexpr int n = nc.value + 2;
          if (!last) {
            if (n >= 3 && n <= 5) ldw(IC<14>{}, wq, 14 * (n - 3));
            if (n == 6) { ldw(IC<6>{}, wq, 42); ldw(IC<8>{}, wb, 48); }
            if (n == 7) ldw(IC<14>{}, wb, 56);
          }
          HBARS(n == 2 || n == 6 || n == 7);
        });
        if (!last) { ldw(IC<10>{}, wb, 70); wait_vm<14>(); }
      }
      SETPRIO(3);
      if (!last) {
        HBAR();
      }
      static_for<NPQ>([&](auto i) { undef(xq[i.value]); });
    }
  }
}

}

extern "C" void kernel_launch(void* const* d_in, const int* in_sizes, int n_in, void* d_out, int out_size, void* d_ws, size_t ws_size,
                              hipStream_t stream) {
  (void)in_sizes; (void)n_in; (void)out_size; (void)ws_size;
  (void)hipFuncSetAttribute((const void*)k_main, hipFuncAttributeMaxDynamicSharedMemorySize, LDS_BYTES);
  k_main<<<256, 512, LDS_BYTES, stream>>>((const u16*)d_in[0], (const u16*)d_in[1], (const u16*)d_in[2], (const u16*)d_in[4], (const u16*)d_in[5],
                                          (const u16*)d_in[6], (unsigned char*)d_ws, (u16*)d_out);
}
```

```cpp
#define X_AT 0
#define X_HOLD 600
#include <hip/hip_runtime.h>
#include <stdint.h>
#include <utility>

#define LAS __attribute__((address_space(3)))
#define GAS __attribute__((address_space(1)))
#define DI __device__ __forceinline__
#define RLX_AGENT __ATOMIC_RELAXED, __HIP_MEMORY_SCOPE_AGENT
typedef unsigned short u16;
typedef unsigned v4u __attribute__((ext_vector_type(4)));
typedef unsigned v2u __attribute__((ext_vector_type(2)));
typedef float f32x2 __attribute__((ext_vector_type(2)));
typedef float f32x4 __attribute__((ext_vector_type(4)));
typedef float f32x16 __attribute__((ext_vector_type(16)));
typedef __bf16 bf16x2 __attribute__((ext_vector_type(2)));
typedef __bf16 bf16x8 __attribute__((ext_vector_type(8)));
typedef GAS unsigned gu32;
typedef GAS unsigned long long gu64;

namespace {

constexpr int D_ = 512, F_ = 1792;
constexpr unsigned MB2 = 2097152u, STB = 262144u;
constexpr unsigned FLAG_LAYER = 1048576u, FLAG_G = 2 * FLAG_LAYER, FLAG_BYTES = FLAG_G + 65536;
constexpr unsigned WQKV_OFF = FLAG_BYTES;
constexpr unsigned WO_OFF = WQKV_OFF + 32 * 49152;
constexpr unsigned WGU_OFF = WO_OFF + 16 * 32768;
constexpr unsigned WDN_OFF = WGU_OFF + 32 * 114688;
constexpr unsigned LAY_OFF = WDN_OFF + 16 * 114688;
constexpr unsigned L_K = 0, L_V = MB2, L_O = 2 * MB2, L_X1 = 3 * MB2, L_X = 4 * MB2 + STB, L_H = 5 * MB2 + 2 * STB;
constexpr unsigned LAY_BYTES = L_H + 128 * 57344;
constexpr unsigned WS_BYTES = LAY_OFF + 2 * LAY_BYTES;
constexpr unsigned NONE = 0x40000000u;
constexpr int FL_KV = 0, FL_O = 2048, FL_X1B = 4096, FL_X1A = 6144, FL_HB = 6400, FL_HA = 6656, FL_XB = 6912, FL_XA = 7168, FL_HA2 = 7424;
constexpr int FL_WR = 0, FL_EP = 256, FL_TMO = 257;
constexpr int VBLK = 528;
constexpr int LDS_KL = 114688, LDS_VL = LDS_KL + 16384, LDS_QL = LDS_VL + 32 * VBLK, LDS_CS = LDS_QL + 8192, LDS_ST = LDS_CS + 1536;
constexpr int LDS_MK = LDS_ST + 1536, LDS_TAG = LDS_MK + 2048;
constexpr int LDS_WO = 81920, LDS_TB = 49152;
constexpr int LDS_BYTES = 163840;
#define TSTR 1048576u
constexpr unsigned SPIN_CAP = 1u << 16;
constexpr int AUX_SC1 = 16;
constexpr float LOG2E = 1.4426950408889634f;

DI f32x16 mfma(v4u a, v4u b, f32x16 c) {
  return __builtin_amdgcn_mfma_f32_32x32x16_bf16(__builtin_bit_cast(bf16x8, a), __builtin_bit_cast(bf16x8, b), c, 0, 0, 0);
}
DI f32x4 mfma16(v4u a, v4u b, f32x4 c) {
  return __builtin_amdgcn_mfma_f32_16x16x32_bf16(__builtin_bit_cast(bf16x8, a), __builtin_bit_cast(bf16x8, b), c, 0, 0, 0);
}
DI unsigned pk2(float lo, float hi) {
  f32x2 p = {lo, hi};
  return __builtin_bit_cast(unsigned, __builtin_convertvector(p, bf16x2));
}
DI float bfr(float x) { return __uint_as_float(pk2(x, 0.f) << 16); }
DI float bflo(unsigned u) { return __uint_as_float(u << 16); }
DI float bfhi(unsigned u) { return __uint_as_float(u & 0xffff0000u); }
DI float amul(float a, float b) { float d; asm("v_mul_f32 %0, %1, %2" : "=v"(d) : "v"(a), "v"(b)); return d; }
DI float afma(float a, float b, float c) { float d; asm("v_fma_f32 %0, %1, %2, %3" : "=v"(d) : "v"(a), "v"(b), "v"(c)); return d; }
DI float aadd(float a, float b) { float d; asm("v_add_f32 %0, %1, %2" : "=v"(d) : "v"(a), "v"(b)); return d; }
DI float asub(float a, float b) { float d; asm("v_sub_f32 %0, %1, %2" : "=v"(d) : "v"(a), "v"(b)); return d; }
DI float xhalf_sum(float v) {
  float t;
  asm("v_mov_b32 %1, %0\n\ts_nop 1\n\tv_permlane32_swap_b32 %0, %1\n\tv_add_f32 %0, %0, %1" : "+v"(v), "=&v"(t));
  return v;
}
DI float xrow_sum(float v) {
  float t;
  asm("v_mov_b32 %1, %0\n\ts_nop 1\n\tv_permlane16_swap_b32 %0, %1\n\tv_add_f32 %0, %0, %1" : "+v"(v), "=&v"(t));
  return v;
}
DI float xhalf_max(float v) {
  auto r = __builtin_amdgcn_permlane32_swap(__float_as_uint(v), __float_as_uint(v), false, false);
  return fmaxf(__uint_as_float(r[0]), __uint_as_float(r[1]));
}
DI void swap16(unsigned& a, unsigned& b) { asm("s_nop 1\n\tv_permlane16_swap_b32 %0, %1" : "+v"(a), "+v"(b)); }
DI void swap32(unsigned& a, unsigned& b) { asm("s_nop 1\n\tv_permlane32_swap_b32 %0, %1" : "+v"(a), "+v"(b)); }
DI f32x16 splat16(float v) {
  f32x16 z;
#pragma unroll
  for (int i = 0; i < 16; ++i) z[i] = v;
  return z;
}
DI f32x4 zero4() { return (f32x4){0.f, 0.f, 0.f, 0.f}; }
DI int vopq(int x) { asm volatile("" : "+v"(x)); return x; }
DI int lane_id() { int l; asm volatile("v_mbcnt_lo_u32_b32 %0, -1, 0\n\tv_mbcnt_hi_u32_b32 %0, -1, %0" : "=v"(l)); return l; }
template <int LN> DI unsigned wrl(unsigned v, unsigned s) { asm volatile("v_writelane_b32 %0, %1, %2" : "+v"(v) : "s"(s), "i"(LN)); return v; }
template <class T> DI void undef(T& x) { asm volatile("" : "=v"(x)); }
DI int sopq(int x) { asm volatile("" : "+s"(x)); return x; }
#define SWZ(l) ((l) ^ (((l) >> 3) & 7))
#define COORDS() \
  const int lane = lane_id(), lo = lane * 16, hh = lane >> 5, t16 = lane & 15, g4 = lane >> 4, lsw = SWZ(lane) * 16; \
  const int w = sopq(w0) & 3, sp = sopq(sp0), c = sopq(c0), n32 = c >> 1, rh = c & 1; \
  const unsigned lay = (unsigned)sopq((int)lay0); \
  const unsigned flo = sopq(layer) * FLAG_LAYER; \
  gu32* const fl = (gu32*)(ws + flo); \
  (void)lo; (void)hh; (void)t16; (void)g4; (void)lsw; (void)n32; (void)rh; (void)lay; (void)sp; (void)c; (void)w; (void)fl;
#define HBARS(c) asm volatile("s_waitcnt lgkmcnt(0)\n\ts_barrier" ::: "memory")
#define CST() do {} while (0)
#define DBGC(k) asm volatile("" ::: "memory")
#define HBAR() asm volatile("s_waitcnt lgkmcnt(0)\n\ts_barrier" ::: "memory")
#define BAR() asm volatile("s_waitcnt lgkmcnt(0)\n\ts_barrier" ::: "memory")
#define BARX() asm volatile("s_barrier" ::: "memory")
#define DRAIN() asm volatile("s_waitcnt vmcnt(0)" ::: "memory")
#define PIN() __builtin_amdgcn_sched_barrier(0)
#define SETPRIO(n) __builtin_amdgcn_s_setprio(n)
template <int N> DI void wait_vm() { static_assert(N >= 0 && N < 64, ""); asm volatile("s_waitcnt vmcnt(%0)" ::"i"(N) : "memory"); }

typedef __amdgpu_buffer_rsrc_t rsrc_t;
DI v4u ld16(rsrc_t rs, unsigned voff, unsigned soff) { return __builtin_amdgcn_raw_buffer_load_b128(rs, voff, soff, AUX_SC1); }
DI f32x2 ld8f(rsrc_t rs, unsigned voff, unsigned soff) { return __builtin_bit_cast(f32x2, __builtin_amdgcn_raw_buffer_load_b64(rs, voff, soff, AUX_SC1)); }
DI void st16(rsrc_t rs, v4u v, unsigned voff, unsigned soff) {
  __builtin_amdgcn_raw_buffer_store_b128(v, rs, voff, soff, AUX_SC1);
  asm volatile("s_nop 1" ::"v"(v));
}
DI void st8(rsrc_t rs, v2u v, unsigned voff, unsigned soff) { __builtin_amdgcn_raw_buffer_store_b64(v, rs, voff, soff, AUX_SC1); }
DI void st4(rsrc_t rs, unsigned v, unsigned voff, unsigned soff) { __builtin_amdgcn_raw_buffer_store_b32(v, rs, voff, soff, AUX_SC1); }
DI v4u lds16(LAS unsigned char* L, int off) { return *(LAS v4u*)(L + off); }
DI f32x4 lds4f(LAS unsigned char* L, int off) { return *(LAS f32x4*)(L + off); }

DI void poll(gu32* word, bool active, gu32* tmo, unsigned tag) {
  for (unsigned spins = 0;;) {
    unsigned v = tag;
    if (active) v = __hip_atomic_load(word, RLX_AGENT);
    if (__all(v == tag)) break;
    ++spins;
    if (spins > SPIN_CAP) { __hip_atomic_store(tmo, tag, RLX_AGENT); break; }
    if ((spins & 255u) == 0u && (unsigned)__builtin_amdgcn_readfirstlane(__hip_atomic_load(tmo, RLX_AGENT)) == tag) break;
  }
  __builtin_amdgcn_fence(__ATOMIC_ACQUIRE, "wavefront");
}

template <int I> DI void dma1(rsrc_t rs, LAS unsigned char* dst, unsigned voff, unsigned soff) {
  __builtin_amdgcn_raw_ptr_buffer_load_lds(rs, (LAS void*)dst, 16, voff, soff, I * 1024, AUX_SC1);
}
template <int V> using IC = std::integral_constant<int, V>;
template <class FN, int... I> DI void static_for_(FN fn, std::integer_sequence<int, I...>) { (fn(IC<I>{}), ...); }
template <int N, class FN> DI void static_for(FN fn) { static_for_(fn, std::make_integer_sequence<int, N>{}); }
template <int N> DI void dma(rsrc_t rs, LAS unsigned char* dst, unsigned lo, unsigned src) {
  static_for<N>([&](auto i) { dma1<i.value & 3>(rs, dst + (i.value >> 2) * 4096, lo, src + (i.value >> 2) * 4096); });
}

struct Stats {
  f32x4 p[16];
  float m0, m1, q0, q1;
  DI void load(rsrc_t rs, int lane, unsigned base, int i0 = 0, int i1 = 16) {
#pragma unroll
    for (int i = i0; i < i1; ++i) p[i] = __builtin_bit_cast(f32x4, ld16(rs, (lane >> 4) * 4096 + (lane & 15) * 16 + i * 256, base));
    PIN();
  }
  DI void part1() {
    m0 = m1 = q0 = q1 = 0.f;
#pragma unroll
    for (int i = 0; i < 16; ++i) { m0 += p[i][0]; q0 += p[i][1]; m1 += p[i][2]; q1 += p[i][3]; }
    asm volatile("" : "+v"(m0), "+v"(m1), "+v"(q0), "+v"(q1));
  }
  DI void part2() {
    const float a0 = m0 * (1.0f / 16.0f), a1 = m1 * (1.0f / 16.0f);
    q0 *= 32.0f; q1 *= 32.0f;
#pragma unroll
    for (int i = 0; i < 16; ++i) { const float d0 = p[i][0] - a0, d1 = p[i][2] - a1; q0 += d0 * d0; q1 += d1 * d1; }
    asm volatile("" : "+v"(q0), "+v"(q1));
  }
  DI void all(LAS unsigned char* L, int lane, int dst = LDS_ST) { part1(); part2(); part3(L, lane, dst); }
  DI void part3(LAS unsigned char* L, int lane, int dst = LDS_ST) {
    const float den0 = bfr(__builtin_amdgcn_sqrtf(bfr(bfr(q0 * (1.0f / (32.0f * D_))) + 1e-5f)));
    const float den1 = bfr(__builtin_amdgcn_sqrtf(bfr(bfr(q1 * (1.0f / (32.0f * D_))) + 1e-5f)));
    *(LAS f32x2*)(L + dst + lane * 8) = (f32x2){__builtin_amdgcn_rcpf(den0), __builtin_amdgcn_rcpf(den1)};
    *(LAS f32x2*)(L + dst + 512 + lane * 8) = (f32x2){den0 * den0, den1 * den1};
    *(LAS v2u*)(L + dst + 1024 + lane * 8) = (v2u){pk2(m0 * (-1.0f / D_), m0 * (-1.0f / D_)), pk2(m1 * (-1.0f / D_), m1 * (-1.0f / D_))};
  }
};
DI float lds1f(LAS unsigned char* L, int off) { return *(LAS float*)(L + off); }
DI v4u mfrag(LAS unsigned char* L, int off) { const unsigned n = *(LAS unsigned*)(L + off); return (v4u){n, n, 0u, 0u}; }

constexpr int LA = 6;
template <int NF, class RD, class STEP>
DI void pipe(RD rd, STEP step) {
  v4u wf[8];
  static_for<LA>([&](auto i) { wf[i.value] = rd(i); });
  step(IC<-1>{}, wf[0], IC<1>{});
  PIN();
  static_for<NF>([&](auto fc) {
    constexpr int f = fc.value;
    if constexpr ((f & 1) == 0 && f + LA <= NF) __builtin_amdgcn_s_waitcnt(0xc07f | ((LA - 2) << 8));
    step(fc, wf[f & 7], IC<0>{});
    if constexpr (f + LA < NF) wf[(f + LA) & 7] = rd(IC<f + LA>{});
    step(fc, wf[f & 7], IC<1>{});
    PIN();
  });
}
DI v4u csfrag(LAS unsigned char* L, int entry) {
  const v2u t = *(LAS v2u*)(L + LDS_CS + entry * 8);
  return (v4u){t.x, t.y, 0u, 0u};
}
DI void cs_put(LAS unsigned char* L, int entry, float cs) {
  const float hi = bfr(cs), mid = bfr(cs - hi), lw = (cs - hi) - mid;
  *(LAS v2u*)(L + LDS_CS + entry * 8) = (v2u){pk2(hi, mid), pk2(lw, 0.f)};
}
DI unsigned mulpk(float a, float s0, float b, float s1) {
  unsigned d; float t;
  asm("v_mul_f32 %0, %2, %3\n\tv_mul_f32 %1, %4, %5\n\tv_cvt_pk_bf16_f32 %0, %0, %1" : "=&v"(d), "=&v"(t) : "v"(a), "v"(s0), "v"(b), "v"(s1));
  return d;
}
DI unsigned nos(unsigned v) { return v < 0xfffffffeu ? v : 0xfffffffeu; }
DI v4u nos4(v4u v) { return (v4u){nos(v[0]), nos(v[1]), nos(v[2]), nos(v[3])}; }
DI unsigned umax3(unsigned a, unsigned b, unsigned c) { const unsigned t = a > b ? a : b; return t > c ? t : c; }
template <int N> DI bool missing(const v4u* f, int step = 1) {
  unsigned m0 = 0, m1 = 0;
#pragma unroll
  for (int i = 0; i < N; ++i) { m0 = umax3(m0, f[i * step][0], f[i * step][1]); m1 = umax3(m1, f[i * step][2], f[i * step][3]); }
  return __builtin_amdgcn_ballot_w64((m0 > m1 ? m0 : m1) == 0xffffffffu) != 0;
}
constexpr unsigned DATA_CAP = 1u << 14;
DI v4u pack8(const float* y) { return (v4u){pk2(y[0], y[1]), pk2(y[2], y[3]), pk2(y[4], y[5]), pk2(y[6], y[7])}; }

struct Fin {
  float y[8], s;
  v4u o;
  v2u sq;
  template <int K, bool ST = true> DI void chunk(const f32x4& a0, const f32x4& a1, const v4u& r, rsrc_t ro, unsigned vo, unsigned so, rsrc_t rs, unsigned vs, unsigned ss) {
    if constexpr (K == 0) { y[0] = aadd(a0[0], bflo(r[0])); y[1] = aadd(a0[1], bfhi(r[0])); y[2] = aadd(a0[2], bflo(r[1])); y[3] = aadd(a0[3], bfhi(r[1])); }
    if constexpr (K == 1) { y[4] = aadd(a1[0], bflo(r[2])); y[5] = aadd(a1[1], bfhi(r[2])); y[6] = aadd(a1[2], bflo(r[3])); y[7] = aadd(a1[3], bfhi(r[3])); }
    if constexpr (K == 2) { o = nos4(pack8(y)); if constexpr (ST) st16(ro, o, vo, so); }
    if constexpr (K == 3) {
#pragma unroll
      for (int m = 0; m < 4; ++m) { y[2 * m] = bflo(o[m]); y[2 * m + 1] = bfhi(o[m]); }
    }
    if constexpr (K == 4) s = xrow_sum(aadd(aadd(aadd(y[0], y[1]), aadd(y[2], y[3])), aadd(aadd(y[4], y[5]), aadd(y[6], y[7]))));
    if constexpr (K == 5) {
      s = xhalf_sum(s);
      const float mu = s * (1.0f / 32.0f);
#pragma unroll
      for (int i = 0; i < 8; ++i) y[i] = asub(y[i], mu);
    }
    if constexpr (K == 6) {
      y[0] = amul(y[0], y[0]); y[1] = amul(y[1], y[1]);
#pragma unroll
      for (int i = 2; i < 8; ++i) y[i & 1] = afma(y[i], y[i], y[i & 1]);
    }
    if constexpr (K == 7) {
      sq = (v2u){nos(__float_as_uint(s)), nos(__float_as_uint(xhalf_sum(xrow_sum(aadd(y[0], y[1])))))};
      if constexpr (ST) st8(rs, sq, vs, ss);
    }
  }
};

constexpr unsigned long long low_mask(int r) {
  unsigned long long m = 0;
  for (int l = 0; l < 64; ++l)
    if ((r & 3) + 8 * (r >> 2) + 4 * (l >> 5) <= (l & 31)) m |= 1ull << l;
  return m;
}
DI float sel(unsigned long long mask, float set, float unset) {
  float d;
  asm("v_cndmask_b32_e64 %0, %1, %2, %3" : "=v"(d) : "v"(unset), "v"(set), "s"(mask));
  return d;
}
DI float max16(float m, const f32x16& s) {
#pragma unroll
  for (int r = 0; r < 16; r += 2) m = __builtin_fmaxf(__builtin_fmaxf(m, s[r]), s[r + 1]);
  return m;
}
DI v4u exp_pack(const f32x16& s, int h) {
  float pr[8];
#pragma unroll
  for (int r = 0; r < 8; ++r) pr[r] = __builtin_amdgcn_exp2f(s[8 * h + r]);
  return pack8(pr);
}
template <bool EXACT>
DI f32x16 attn_pass(LAS unsigned char* L, int lane, int tl, int nv) {
  const int lo = lane * 16;
  const int kb = LDS_KL + tl * 1024 + lo, kf0 = LDS_KL + (tl + 8) * 1024 + (lane >> 5) * 512;
  const v4u q = lds16(L, LDS_QL + tl * 1024 + lo);
  v4u kf[9];
  kf[8] = lds16(L, kb + 8192); kf[0] = lds16(L, nv <= 0 ? kb : kf0);
#pragma unroll
  for (int i = 7; i >= 1; --i) kf[i] = lds16(L, (nv <= i ? kb : kf0 - i * 1024) + i * 1024);
  f32x16 s[9], cm = splat16(0.f);
  if constexpr (EXACT) {
    s[8] = mfma(kf[8], q, splat16(0.f));
    s[0] = mfma(kf[0], q, splat16(0.f));
    s[7] = mfma(kf[7], q, splat16(0.f));
    s[6] = mfma(kf[6], q, splat16(0.f));
    asm volatile("s_nop 7\n\ts_nop 7" : "+v"(s[8]), "+v"(s[0]));
    PIN();
    static_for<16>([&](auto r) { s[8][r.value] = sel(low_mask(r.value), s[8][r.value], s[0][r.value]); });
    float m = max16(s[8][0], s[8]);
    PIN();
    static_for<7>([&](auto ic) {
      constexpr int i = 7 - ic.value;
      if constexpr (i - 2 >= 1) s[i - 2] = mfma(kf[i - 2], q, splat16(0.f));
      m = max16(m, s[i]);
      PIN();
    });
    cm = splat16(-xhalf_max(m));
  }
  const int vb = LDS_VL + 2 * tl * VBLK + ((lane & 16) ? 512 : (lane >> 5) * 256 + (lane & 15) * 16);
  s[8] = mfma(kf[8], q, cm);
  s[0] = mfma(kf[0], q, cm);
  s[7] = mfma(kf[7], q, cm);
  const v4u mk0 = lds16(L, LDS_MK + lo), mk1 = lds16(L, LDS_MK + 1024 + lo);
  v4u va = lds16(L, vb + 16 * VBLK), vc = lds16(L, vb + 17 * VBLK), v0a = lds16(L, vb), v0c = lds16(L, vb + VBLK);
  asm volatile("s_nop 7\n\ts_nop 7" : "+v"(s[8]), "+v"(s[0]));
  PIN();
  f32x16 o;
  {
    static_for<16>([&](auto r) { s[8][r.value] = sel(low_mask(r.value), s[8][r.value], s[0][r.value]); });
    const v4u p0 = exp_pack(s[8], 0), l0 = p0 & mk0;
    o = mfma(va, l0, splat16(0.f));
    o = mfma(v0a, p0 ^ l0, o);
    const v4u p1 = exp_pack(s[8], 1), l1 = p1 & mk1;
    o = mfma(vc, l1, o);
    o = mfma(v0c, p1 ^ l1, o);
    va = lds16(L, vb + 14 * VBLK); vc = lds16(L, vb + 15 * VBLK);
    PIN();
  }
  static_for<7>([&](auto ic) {
    constexpr int i = 7 - ic.value;
    if constexpr (i > 1) s[i - 1] = mfma(kf[i - 1], q, cm);
    PIN();
    o = mfma(va, exp_pack(s[i], 0), o);
    if constexpr (i > 1) va = lds16(L, vb + (2 * i - 2) * VBLK);
    o = mfma(vc, exp_pack(s[i], 1), o);
    if constexpr (i > 1) vc = lds16(L, vb + (2 * i - 1) * VBLK);
    PIN();
  });
  return o;
}
DI v4u attn_unit(LAS unsigned char* L, int lane, int tl, int nv) {
  f32x16 o = attn_pass<false>(L, lane, tl, nv);
  float big = __builtin_fabsf(o[0]);
#pragma unroll
  for (int r = 1; r < 8; ++r) big += __builtin_fabsf(o[r]);
  if (__builtin_expect(!__all(o[8] > 0x1p-30f && o[8] < 0x1p60f && big < 0x1p120f), 0)) {
    asm volatile("" ::: "memory");
    o = attn_pass<true>(L, lane, tl, nv);
  }
  const float rl = __builtin_amdgcn_rcpf(o[8]);
  float y[8];
#pragma unroll
  for (int r = 0; r < 8; ++r) y[r] = o[r] * rl;
  return pack8(y);
}

struct Retile {
  v4u in[8];
  DI void load(const u16* src, int ld, int rs4) {
#pragma unroll
    for (int j = 0; j < 8; ++j) in[j] = *(const v4u*)(src + (size_t)(((j & 3) + rs4 * (j >> 2)) * ld));
    PIN();
  }
  unsigned d[8];
  DI void out(rsrc_t rs) {
#pragma unroll
    for (int i = 0; i < 8; ++i) st16(rs, in[i], d[i], 0);
  }
  DI void lines(LAS unsigned char* t, int lane, int sw, unsigned dst) {
#pragma unroll
    for (int e = 0; e < 8; ++e) {
      v4u o;
#pragma unroll
      for (int m = 0; m < 4; ++m) o[m] = __builtin_amdgcn_perm(in[2 * m + 1][e >> 1], in[2 * m][e >> 1], (e & 1) ? 0x07060302u : 0x05040100u);
      *(LAS v4u*)(t + lane * 128 + (e ^ sw ^ (lane & 7)) * 16) = o;
    }
    asm volatile("s_waitcnt lgkmcnt(0)" ::: "memory");
#pragma unroll
    for (int i = 0; i < 8; ++i) {
      in[i] = *(LAS v4u*)(t + i * 1024 + (lane ^ (lane >> 3)) * 16);
      d[i] = (unsigned)__builtin_amdgcn_ds_bpermute((8 * i + (lane >> 3)) * 4, (int)dst) + (lane & 7) * 16;
    }
    PIN();
  }
  DI void put(LAS unsigned char* L, int pb, int dd, int lp, int dl) {
#pragma unroll
    for (int e = 0; e < 8; ++e) {
      v4u o;
#pragma unroll
      for (int m = 0; m < 4; ++m) o[m] = __builtin_amdgcn_perm(in[2 * m + 1][e >> 1], in[2 * m][e >> 1], (e & 1) ? 0x07060302u : 0x05040100u);
      const int s = lp + (e & 3) + (e >> 2) * dl;
      *(LAS v4u*)(L + pb + (e >> 2) * dd + (s ^ ((s >> 3) & 7)) * 16) = o;
    }
  }
};

struct GS { int cs, j, kap, off; };
constexpr GS gu_step(int u) {
  if (u < 64) { const int k = u >> 2, j = u & 3; return GS{0, j, k, j < 3 ? (7 * (k >> 1) + 3 * (k & 1) + j) * 1024 : (7 * (k >> 1) + 6) * 1024 + (k & 1) * 512}; }
  const int v = u - 64, j = v / 17, i = v % 17;
  return GS{i == 16, j, 16 + i, 57344 + j * 16384 + i * (j < 3 ? 1024 : 512)};
}
constexpr int GA = 132, DB = 264, DA = 376, NST = 488;
constexpr int NPG = 16, NPQ = 12, LDN = 12;
constexpr int ld_step(int id) {
  if (id < 32) return id < NPG ? -1 : 2 * (id - NPG);
  if (id < 64) { const int n = id - 32; return n < 12 ? 103 + (5 * n) / 2 : GA + 10 + 2 * (n - 12); }
  if (id < 176) {
    const int n = id - 64;
    return n < 12 ? GA + 103 + n : n < 26 ? DB - 15 + ((n - 12) * 7) / 4 : n < 56 ? DB + 27 + ((n - 26) * 5) / 3 : n < 84 ? DB + 79 + ((n - 56) * 4) / 3 : DA + 31 + 2 * (n - 84);
  }
  if (id < 208) { const int n = id - 176; return n < NPQ ? DA + 87 + 2 * n : -1; }
  if (id < 224) return -1;
  return id == 224 ? DB + 80 : DA + 72;
}
constexpr int NEP = 56;
constexpr int ep_step(int k) { return 83 + 17 * (k / 16) + k % 16; }
struct Tab { short ld[NST][2]; short ep[NST]; short nvm[NST]; };
constexpr int FINB = DA + 3;
#define Q3T 75
#define XA0 18
constexpr int xa_step(int j) { const int a = XA0 + j, b = j < 12 ? 3 * j + 2 : 34 + j; return a > b ? a : b; }
constexpr Tab make_tab() {
  Tab t{};
  for (int s = 0; s < NST; ++s) { t.ld[s][0] = t.ld[s][1] = t.ep[s] = -1; t.nvm[s] = 0; }
  for (int id = 0; id < 226; ++id) {
    const int s = ld_step(id);
    if (s < 0) continue;
    t.ld[s][t.ld[s][0] >= 0] = (short)id; ++t.nvm[s];
  }
  for (int job = 0; job < 2; ++job)
    for (int k = 0; k < NEP; ++k) {
      const int s = job * GA + ep_step(k);
      t.ep[s] = (short)(job * 64 + k);
      if (k == NEP - 1) t.nvm[s] += 2;
      if (k == (job ? 31 : NEP - 1)) t.nvm[s] += 2;
    }
  ++t.nvm[FINB + 2]; ++t.nvm[FINB + 7];
  return t;
}
constexpr Tab TAB = make_tab();
constexpr int vm_between(int a, int b) {
  int n = 0;
  for (int s = a + 1; s <= b; ++s) n += TAB.nvm[s];
  return n;
}

__global__ void __launch_bounds__(512, 2) k_main(const u16* __restrict__ x, const u16* __restrict__ wqkv, const u16* __restrict__ wo, const u16* __restrict__ wg,
                                                 const u16* __restrict__ wu, const u16* __restrict__ wd, unsigned char* __restrict__ ws,
                                                 u16* __restrict__ out) {
  extern __shared__ __attribute__((aligned(16))) unsigned char smem[];
  LAS unsigned char* L = (LAS unsigned char*)smem;
  const int tid = threadIdx.x, w0 = __builtin_amdgcn_readfirstlane(tid >> 6);
  const int b = blockIdx.x;
  const int sp0 = (b & 6) + ((b >> 3) & 1), c0 = (b >> 4) + 16 * (b & 1);
  const rsrc_t rs = __builtin_amdgcn_make_buffer_rsrc((void*)ws, 0, (int)WS_BYTES, 0x00020000);
  const bool compute = w0 < 4;
  gu32* const fg = (gu32*)(ws + FLAG_G);
  gu32* const tmo = fg + FL_TMO * 32;
  unsigned tag = 0;
  const unsigned long long t_hold = __builtin_amdgcn_s_memrealtime();
#define HOLD(k) do { if (X_AT == (k)) while (__builtin_amdgcn_s_memrealtime() - t_hold < X_HOLD) __builtin_amdgcn_s_sleep(1); } while (0)
#define FLAG(blk, line) st4(rs, tag, lane ? NONE : 0u, (blk) + (line) * 128)
#define POLL(blk, line, n) poll((gu32*)(ws + (blk)) + ((line) + lane) * 32, lane < (n), tmo, tag)
#define GETTAG() tag = (unsigned)__builtin_amdgcn_readfirstlane(*(LAS unsigned*)(L + LDS_TAG))
  auto blank = [&](int it, unsigned ly) {
    const int lane = lane_id(), sp = sopq(sp0), c = sopq(c0);
    const unsigned o = (unsigned)vopq(-1);
    if (it < 20) st16(rs, (v4u){o, o, o, o}, lane * 16, ly + (it < 16 ? (it & 8 ? L_V : L_K) + ((8 * sp + (it & 7)) * 32 + c) * 1024 : L_X + ((16 * sp + 4 * (c & 1) + (it & 3)) * 16 + (c >> 1)) * 1024));
    else st8(rs, (v2u){o, o}, (lane >> 5) * 4096 + (lane & 31) * 8, ly + L_X + MB2 + (8 * sp + 2 * (c & 1)) * 4096 + (c >> 1) * 256);
  };
  unsigned touch0 = 0;
  v4u xq[32];

  if (compute) {
    const int layer = 0; const unsigned lay0 = LAY_OFF;
    COORDS();
    SETPRIO(2);
    const int r = lane >> 3, j = lane & 7;
    const u16* src = x + (size_t)(256 * sp + 8 * c + r) * D_ + 128 * w + 8 * j;
    v4u a[2] = {*(const v4u*)src, *(const v4u*)(src + 64)};
#pragma unroll
    for (int i = 0; i < 2; ++i) {
      a[i] = nos4(a[i]);
      st16(rs, a[i], (j >> 2) * 1024 + ((j & 3) * 16 + r) * 16, lay + L_X + (16 * sp + n32) * 16384 + (2 * w + i) * 2048 + rh * 128);
      auto quad = [](float v) {
        v += __int_as_float(__builtin_amdgcn_update_dpp(0, __float_as_int(v), 0xb1, 0xf, 0xf, true));
        return v + __int_as_float(__builtin_amdgcn_update_dpp(0, __float_as_int(v), 0x4e, 0xf, 0xf, true));
      };
      float sm = 0.f, q = 0.f;
#pragma unroll
      for (int m = 0; m < 4; ++m) sm += bflo(a[i][m]) + bfhi(a[i][m]);
      sm = quad(sm);
      const float mu = sm * (1.0f / 32.0f);
#pragma unroll
      for (int m = 0; m < 4; ++m) { const float e0 = bflo(a[i][m]) - mu, e1 = bfhi(a[i][m]) - mu; q += e0 * e0 + e1 * e1; }
      q = quad(q);
      st8(rs, (v2u){nos(__float_as_uint(sm)), nos(__float_as_uint(q))}, (j & 3) ? NONE : (j >> 2) * 256 + r * 8,
          lay + L_X + MB2 + (8 * sp + (c >> 2)) * 4096 + (4 * w + 2 * i) * 256 + (c & 3) * 64);
    }
    DRAIN();
    BAR();
    BAR();
    GETTAG();
    const int TB = 8 * sp + 4 + w;
    static_for<NPQ>([&](auto nc) { constexpr int n = nc.value; xq[n] = ld16(rs, lo + ((n >> 1) & 3) * 1024, lay + L_X + TB * 32768 + (n & 1) * 16384 + (n >> 3) * 4096); });
    BARX();
  } else {
    const int layer = 0; const unsigned lay0 = LAY_OFF;
    COORDS();
    SETPRIO(3);
    Retile rt;
    LAS unsigned char* tb = L + LDS_TB + w * 8192;
    const int id = 2 * sp + rh + 16 * (w - 1);
    auto fill0 = [&]() {
#pragma unroll 1
      for (int it = w - 1; it < 16; it += 3) blank(it, lay);
      PIN();
    };
    if (w == 0 || id < 28) {
      const u16* src; int ld, sw; unsigned dst;
      if (w == 0) {
        const int fi = lane >> 3, i8 = lane & 7, F = 6 * sp + (fi < 6 ? fi : 0);
        const int ks = F < 36 ? F / 3 : F < 44 ? (F - 12) >> 1 : F - 32, m = F < 36 ? (F - 3 * ks + 1) % 3 : F < 44 ? 1 + (F & 1) : 0;
        src = wqkv + (size_t)(32 * ks + 8 * (i8 >> 1)) * (3 * D_) + m * D_ + 16 * c + 8 * (i8 & 1);
        ld = 3 * D_; sw = i8; dst = fi < 6 ? WQKV_OFF + c * 49152 + F * 1024 + i8 * 128 : NONE;
      } else {
        const int t = 64 * id + lane, mo = t / 14, ch = t - 14 * mo, mat = mo >> 6, o = mo & 63, c7 = ch >= 7 ? ch - 7 : ch, j = c7 >> 1, kap = o >> 1;
        src = (mat ? wu : wg) + (size_t)(8 * o) * F_ + 112 * n32 + 8 * ch;
        ld = F_;
        const int cell = kap < 16 ? 7 * (kap >> 1) + (j < 3 ? 3 * (kap & 1) + j : 6) : 56 + 16 * j + (j < 3 ? kap - 16 : (kap - 16) >> 1);
        const int rho = j < 3 ? 2 * mat + (c7 & 1) + 4 * (o & 1) : 4 * (kap & 1) + 2 * (o & 1) + mat;
        sw = j < 3 ? rho : 0; dst = WGU_OFF + (2 * n32 + (ch >= 7)) * 114688 + cell * 1024 + rho * 128;
      }
      unsigned ep = 0;
      if (w == 0) ep = __hip_atomic_load(fg + FL_EP * 32, RLX_AGENT);
      rt.load(src, ld, 4);
      if (w) fill0();
      if (w == 0) {
        tag = __builtin_amdgcn_readfirstlane(ep) + 1u;
        if (lane == 0) *(LAS v4u*)(L + LDS_TAG) = (v4u){tag, 0u, 0u, 0u};
        v4u mk[2];
#pragma unroll
        for (int k = 0; k < 8; ++k) {
          const int kq = ((2 * k) & 3) + 8 * ((2 * k) >> 2) + 4 * hh, ql = lane & 31;
          mk[k >> 2][k & 3] = (kq <= ql ? 0xffffu : 0u) | (kq + 1 <= ql ? 0xffff0000u : 0u);
        }
        *(LAS v4u*)(L + LDS_MK + lo) = mk[0];
        *(LAS v4u*)(L + LDS_MK + 1024 + lo) = mk[1];
        if (sp == 0) {
#pragma unroll
          for (int i = 0; i < 9; ++i) *(LAS v4u*)(L + LDS_VL + (i < 8 ? i * 1024 + lo : 8192 + t16 * 16)) = (v4u){0u, 0u, 0u, 0u};
        }
        const unsigned one = (sp == 0 && lane < 16) ? 0u : 0x3f803f80u;
        if (lane < 32) *(LAS v4u*)(L + LDS_VL + lane * VBLK + 512) = (v4u){one, one, one, one};
      }
      rt.lines(tb, lane, sw, dst);
      if (w == 0) rt.out(rs);
    } else if (id < 46) {
      const int slot = (lane >> 2) & 3, m4 = lane & 3, q = 16 * (id < 42 ? id - 28 : id - 42) + 4 * g4 + slot;
      const int cq = q < 128 ? q >> 2 : q < 192 ? (q - 128) >> 1 : q - 192, r = q < 128 ? q & 3 : q < 192 ? 4 + (q & 1) : 6;
      rt.load((id < 42 ? wd + (size_t)(56 * cq + (r < 6 ? 16 * (r >> 1) + 4 * (r & 1) : 48)) * D_ : wo + (size_t)(16 * (q >> 1) + 4 * (q & 1)) * D_) + 32 * n32 + 8 * m4,
              D_, (id < 42 && r == 6) ? 4 : 8);
      fill0();
      rt.put(tb, g4 * 2048, 1024, 4 * m4 + 16 * slot, 0);
      asm volatile("s_waitcnt lgkmcnt(0)" ::: "memory");
#pragma unroll
      for (int i = 0; i < 8; ++i) {
        rt.in[i] = lds16(tb, i * 1024 + lo);
        rt.d[i] = (id < 42 ? WDN_OFF + n32 * 114688 + (id - 28) * 8192 : WO_OFF + n32 * 32768 + (id - 42) * 8192) + i * 1024 + lo;
      }
    } else {
#pragma unroll
      for (int i = 0; i < 8; ++i) { undef(rt.in[i]); rt.d[i] = NONE; }
      fill0();
    }
    if (w == 0) DRAIN();
    HBAR();
    if (w == 0) {
      FLAG(0, FL_XB + sp * 32 + c);
      poll((gu32*)ws + (FL_XB + (lane < 32 ? sp * 32 + lane : (lane & 7) * 32 + c)) * 32, lane < 40, tmo, tag);
    }
    Stats st;
    if (w == 0) {
      st.load(rs, lane, lay + L_X + MB2 + (8 * sp + 4) * 4096);
    }
    HBAR();
    GETTAG();
    if (w) {
      const int l = w - 1;
      dma<4>(rs, L + l * 4096, lo, WQKV_OFF + c * 49152 + l * 4096);
      dma<7>(rs, L + (12 + 7 * l - (l >> 1)) * 1024, lo, WQKV_OFF + c * 49152 + (12 + 7 * l - (l >> 1)) * 1024);
      wait_vm<7>();
    }
    HOLD(0);
    HBAR();
    if (w) { dma<6>(rs, L + (27 + 5 * w) * 1024, lo, WQKV_OFF + c * 49152 + (27 + 5 * w) * 1024);
      rt.out(rs); wait_vm<14>();
    }
    else {
      st.all(L, lane);
      PIN();
      FLAG(0, FL_XA + sp * 32 + c);
      const int i = lane & 31;
      const int line = i < 8 ? FL_KV + (8 * sp + i) * 32 + c : i < 16 ? FL_O + (8 * sp + i - 8) * 32 + c
                     : i < 20 ? FL_X1B + (8 * sp + 4 + 2 * rh + ((i >> 1) & 1)) * 32 + 2 * n32 + (i & 1) : FL_X1A + (i - 20 < 6 ? i - 20 : 5) * 256 + sp * 32 + c;
      touch0 = __builtin_amdgcn_raw_buffer_load_b32(rs, hh * FLAG_LAYER + line * 128, 0, AUX_SC1);
    }
  }

  for (int layer = 0; layer < 2; ++layer) {
    const bool last = layer == 1;
    const unsigned lay0 = LAY_OFF + layer * LAY_BYTES;
    auto ldw = [&](auto nc, unsigned src, int cell) {
      constexpr int N = nc.value, K = (N + 2) / 3;
      const int l = sopq(w0) - 5, c1 = cell + (l * K + K > N ? N - K : l * K);
      dma<K>(rs, L + c1 * 1024, lane_id() * 16, src + c1 * 1024);
    };
    auto ld_gu = [&](int s) { ldw(IC<14>{}, WGU_OFF + sopq(c0) * 114688, 14 * s); };
    auto ld_dn = [&](int s) { ldw(IC<14>{}, WDN_OFF + (sopq(c0) >> 1) * 114688, 14 * s); };
    if (compute) {
      COORDS();
      const int TB = 8 * sp + 4 + w, TA = TB - 4;
      v4u xa[32];
      auto ldx = [&](auto nc, int T) {
        constexpr int n = nc.value;
        return ld16(rs, lo + ((n >> 1) & 3) * 1024, lay + L_X + (2 * T + (n & 1)) * 16384 + (n >> 3) * 4096);
      };
      f32x4 aq[2][2], ak[2][2], av[2][2], r0, r1;
      v4u mf0, mf1; float rd0, rd1;
      unsigned o0, o1, o2, o3;
      const int kpos = (t16 + 16 * hh + 32 * (g4 & 1)) * 16;
      auto epi = [&](int k, int pa) {
        const int tl = pa ? w : 4 + w, st = (pa ? LDS_KL : LDS_ST) + w * 128;
        const unsigned kvb = lay + ((8 * sp + tl) * 32 + c) * 1024;
        if (k == 0) { r0 = lds4f(L, st + g4 * 16); r1 = lds4f(L, st + 64 + g4 * 16); }
        if (k == 2) { o0 = mulpk(ak[pa][0][0], rd0, ak[pa][0][1], rd0); o1 = mulpk(ak[pa][0][2], rd0, ak[pa][0][3], rd0); }
        if (k == 3) { o2 = mulpk(ak[pa][1][0], rd1, ak[pa][1][1], rd1); o3 = mulpk(ak[pa][1][2], rd1, ak[pa][1][3], rd1); }
        if (k == 4) {
          swap32(o0, o2); swap32(o1, o3);
          st16(rs, nos4((v4u){o0, o1, o2, o3}), kpos, kvb + L_K);
          *(LAS v4u*)(L + LDS_KL + (8 + tl) * 1024 + kpos) = (v4u){o0, o1, o2, o3};
        }
        if (k == 5) { o0 = mulpk(av[pa][0][0], r0[0], av[pa][0][1], r0[1]); o1 = mulpk(av[pa][0][2], r0[2], av[pa][0][3], r0[3]); }
        if (k == 6) { o2 = mulpk(av[pa][1][0], r1[0], av[pa][1][1], r1[1]); o3 = mulpk(av[pa][1][2], r1[2], av[pa][1][3], r1[3]); }
        if (k == 7) {
          swap32(o0, o2); swap32(o1, o3);
          st16(rs, nos4((v4u){o0, o1, o2, o3}), lo, kvb + L_V);
          *(LAS v4u*)(L + LDS_VL + (16 + 2 * tl + hh) * VBLK + (lane & 31) * 16) = (v4u){o0, o1, o2, o3};
        }
        if (k == 8) {
          asm volatile("s_nop 7" : "+v"(aq[pa][0]), "+v"(aq[pa][1]));
          const float s0 = rd0 * (0.25f * LOG2E), s1 = rd1 * (0.25f * LOG2E);
          o0 = mulpk(aq[pa][0][0], s0, aq[pa][0][1], s0); o1 = mulpk(aq[pa][0][2], s0, aq[pa][0][3], s0);
          o2 = mulpk(aq[pa][1][0], s1, aq[pa][1][1], s1); o3 = mulpk(aq[pa][1][2], s1, aq[pa][1][3], s1);
          swap32(o0, o2); swap32(o1, o3);
          *(LAS v4u*)(L + LDS_QL + tl * 1024 + kpos) = (v4u){o0, o1, o2, o3};
        }
      };
      pipe<102>(
          [&](auto fc) {
            constexpr int t = fc.value, u = t % 51;
            if constexpr (t == 28) { BARX(); CST(); }
            if constexpr (t == 12 || t == 44 || t == Q3T) BARX();
            if constexpr (u == 44 || u == 45 || u == 50) return csfrag(L, (u == 50 ? 0 : u - 43) * 16 + t16);
            else return lds16(L, (u < 44 ? u : u - 2) * 1024 + lsw);
          },
          [&](auto fc, const v4u& wfr, auto pc) {
            constexpr int t = fc.value, part = pc.value, u = t % 51;
            constexpr int ks = (u < 36 ? u / 3 : u < 44 ? (u - 12) >> 1 : u - 34) & 15, m = u < 36 ? (u % 3 + 1) % 3 : u < 46 ? 1 + (u & 1) : 0;
            constexpr bool pa = t >= 51, cs = u == 44 || u == 45 || u == 50;
            if constexpr (t < 0) return;
            else if constexpr (part == 0) {
              if constexpr (pa && u < 44 && u % 12 == 0) {
                constexpr int n = u / 12 * 8;
                if (__builtin_expect(missing<8>(xa + n), 0)) {
                  unsigned spins = 0;
                  do { static_for<8>([&](auto i) { xa[n + i.value] = ldx(IC<n + i.value>{}, TA); }); DBGC(0); } while (missing<8>(xa + n) && ++spins < DATA_CAP);
                }
                PIN();
              }
              const v4u& x0 = cs ? mf0 : pa ? xa[2 * ks] : xq[2 * ks];
              const v4u& x1 = cs ? mf1 : pa ? xa[2 * ks + 1] : xq[2 * ks + 1];
              if constexpr (m == 0) { aq[pa][0] = mfma16(wfr, x0, u < 3 ? zero4() : aq[pa][0]); aq[pa][1] = mfma16(wfr, x1, u < 3 ? zero4() : aq[pa][1]); }
              if constexpr (m == 1) { ak[pa][0] = mfma16(wfr, x0, u < 3 ? zero4() : ak[pa][0]); ak[pa][1] = mfma16(wfr, x1, u < 3 ? zero4() : ak[pa][1]); }
              if constexpr (m == 2) { av[pa][0] = mfma16(x0, wfr, u < 3 ? zero4() : av[pa][0]); av[pa][1] = mfma16(x1, wfr, u < 3 ? zero4() : av[pa][1]); }
            } else {
              if constexpr (2 * t < 32 - NPQ) { xq[NPQ + 2 * t] = ldx(IC<NPQ + 2 * t>{}, TB); xq[NPQ + 2 * t + 1] = ldx(IC<NPQ + 2 * t + 1>{}, TB); }
              if constexpr (u == 40) {
                const int st = (pa ? LDS_KL : LDS_ST) + w * 128;
                rd0 = lds1f(L, st + t16 * 4); rd1 = lds1f(L, st + 64 + t16 * 4);
                const int a = g4 ? LDS_TAG + 8 : st + 1024 + t16 * 4;
                mf0 = mfrag(L, a); mf1 = mfrag(L, g4 ? a : a + 64);
              }
              if constexpr (t >= 45 && t < 54) epi(t - 45, 0);
              if constexpr (t >= 96) epi(t - 96, 1);
              static_for<16>([&](auto j) { if constexpr (xa_step(j.value) == t) { xa[2 * j.value] = ldx(IC<2 * j.value>{}, TA); xa[2 * j.value + 1] = ldx(IC<2 * j.value + 1>{}, TA); } });
              if constexpr (t == 50) CST();
            }
          });
      epi(6, 1); epi(7, 1); epi(8, 1);
      BAR();
      CST();
    } else {
      COORDS();
      const bool loader = w != 0;
      auto cs_qkv = [&]() {
        const unsigned one = (unsigned)vopq(0x3f803f80);
        const v4u ones = (v4u){one, one, one, one};
        f32x4 a = zero4();
        v4u f[16];
        const int fa = (w - 1) * 1024 + lsw, fb = (w < 3 ? 35 + w : 44) * 1024 + lsw, fs = w < 3 ? 2048 : 1024;
#pragma unroll
        for (int ks = 0; ks < 16; ++ks) asm volatile("ds_read_b128 %0, %1" : "=v"(f[ks]) : "v"(ks < 12 ? fa + ks * 3072 : fb + (ks - 12) * fs) : "memory");
        asm volatile("s_waitcnt lgkmcnt(0)" : "+v"(f[0]), "+v"(f[1]), "+v"(f[2]), "+v"(f[3]), "+v"(f[4]), "+v"(f[5]), "+v"(f[6]), "+v"(f[7]));
        asm volatile("" : "+v"(f[8]), "+v"(f[9]), "+v"(f[10]), "+v"(f[11]), "+v"(f[12]), "+v"(f[13]), "+v"(f[14]), "+v"(f[15]));
#pragma unroll
        for (int ks = 0; ks < 16; ++ks) a = mfma16(f[ks], ones, a);
        if (t16 == 0) {
#pragma unroll
          for (int r = 0; r < 4; ++r) cs_put(L, (w % 3) * 16 + 4 * g4 + r, a[r]);
        }
      };
      const unsigned wob = WO_OFF + n32 * 32768 - LDS_WO;
      if (last && !loader) {
        v4u o[4];
#pragma unroll
        for (int i = 0; i < 4; ++i) o[i] = lds16(L, LDS_QL + i * 1024 + lo);
        const v2u sq = *(LAS v2u*)(L + LDS_QL + 4096 + lane * 8);
#pragma unroll
        for (int i = 0; i < 4; ++i) st16(rs, o[i], lo, lay + L_X + ((16 * sp + 4 * rh + i) * 16 + n32) * 1024);
        st8(rs, sq, hh * 4096 + (g4 & 1) * 128 + t16 * 8, lay + L_X + MB2 + (8 * sp + 2 * rh) * 4096 + n32 * 256);
        PIN();
      }
      HBARS(false);
      if (layer == 0) {
        if (loader) wait_vm<8>();
        HBAR();
        if (loader) cs_qkv();
        HBAR();
      } else {
        if (loader) { ldw(IC<10>{}, wob, 80); wait_vm<13>(); }
        HBAR();
        if (loader) ldw(IC<10>{}, wob, 90);
        HBAR();
        if (loader) ldw(IC<12>{}, wob, 100);
      }
      {
        v4u kv[4];
        auto ldkv = [&]() {
          const unsigned so = lay + ((8 * sp - 5 + w) * 32 + c) * 1024, s2 = w == 1 ? so + 3 * 32768 : NONE;
          kv[0] = ld16(rs, lo, so + L_K); kv[1] = ld16(rs, lo, so + L_V); kv[2] = ld16(rs, lo, s2 + L_K); kv[3] = ld16(rs, lo, s2 + L_V);
          PIN();
          DBGC(0);
        };
        if (!loader) {
          Stats st;
          gu32* const bl = (gu32*)ws + (FL_XA + (sp ? sp - 1 : 0) * 32 + c) * 32;
          unsigned spins = 0, pv = tag;
          if (!last) pv = __hip_atomic_load(bl, RLX_AGENT);
          do { st.load(rs, lane, lay + L_X + MB2 + 8 * sp * 4096); DBGC(0); } while (missing<16>((const v4u*)st.p) && ++spins < DATA_CAP);
          st.all(L, lane, LDS_KL);
          if (!__all(pv == tag)) poll(bl, lane == 0, tmo, tag);
        }
        DRAIN();
        HBARS(false);
        if (loader && sp > 0) {
          unsigned spins = 0;
          do ldkv(); while (missing<4>(kv) && ++spins < DATA_CAP);
          *(LAS v4u*)(L + LDS_KL + (3 + w) * 1024 + lo) = kv[0];
          *(LAS v4u*)(L + LDS_VL + (6 + 2 * w + hh) * VBLK + (lane & 31) * 16) = kv[1];
          if (w == 1) {
            *(LAS v4u*)(L + LDS_KL + 7 * 1024 + lo) = kv[2];
            *(LAS v4u*)(L + LDS_VL + (14 + hh) * VBLK + (lane & 31) * 16) = kv[3];
          }
        }
      }
      if (layer == 0 && !loader) { asm volatile("" ::"v"(touch0)); FLAG(FLAG_G, FL_WR + n32 * 16 + 2 * sp + rh); }
      if (!last) HOLD(1);
      DRAIN();
      HBAR();
      CST(); CST();
      if (loader) {
        if (layer == 0) {
          POLL(FLAG_G, FL_WR + n32 * 16, 16);
          ldw(IC<10>{}, wob, 80); ldw(IC<10>{}, wob, 90); ldw(IC<12>{}, wob, 100);
          ld_gu(0);
          wait_vm<5>();
          if (lane == 0) asm volatile("ds_add_u32 %0, %1" ::"v"(LDS_TAG + 12), "v"(1) : "memory");
        } else ld_gu(0);
        ld_gu(1); ld_gu(2); ldw(IC<6>{}, WGU_OFF + c * 114688, 42);
      }
      if (sp > 0) {
        LAS unsigned* hw = (LAS unsigned*)(L + LDS_TAG + 4);
        CST();
        if (!loader) {
          v4u kv[8];
          unsigned spins = 0;
          do {
#pragma unroll
            for (int t = 0; t < 4; ++t) {
              const unsigned so = lay + ((8 * sp - 8 + t) * 32 + c) * 1024;
              kv[t] = ld16(rs, lo, so + L_K); kv[4 + t] = ld16(rs, lo, so + L_V);
            }
            PIN();
            DBGC(1);
          } while (missing<8>(kv) && ++spins < DATA_CAP);
#pragma unroll
          for (int t = 0; t < 4; ++t) {
            *(LAS v4u*)(L + LDS_KL + t * 1024 + lo) = kv[t];
            *(LAS v4u*)(L + LDS_VL + (2 * t + hh) * VBLK + (lane & 31) * 16) = kv[4 + t];
          }
          asm volatile("s_waitcnt lgkmcnt(0)" ::: "memory");
          __hip_atomic_store(hw, (unsigned)layer + 1u, __ATOMIC_RELAXED, __HIP_MEMORY_SCOPE_WORKGROUP);
        } else {
          unsigned v;
          do {
            __builtin_amdgcn_s_sleep(1);
            asm volatile("ds_read_b32 %0, %1\n\ts_waitcnt lgkmcnt(0)" : "=v"(v) : "v"(LDS_TAG + 4) : "memory");
          } while ((unsigned)__builtin_amdgcn_readfirstlane(v) != (unsigned)layer + 1u);
        }
      } else CST();
      CST();
      if (layer == 0 && !loader) touch0 = __builtin_amdgcn_raw_buffer_load_b32(rs, LAY_OFF + L_O + lane * TSTR + c * 4096 + sp * 128, 0, AUX_SC1);
      SETPRIO(1);
    }
    {
      COORDS();
      const int tl = compute ? 4 + w : w, T = 8 * sp + tl;
      const v4u o = attn_unit(L, lane, tl, sp == 0 ? 8 - tl : 0);
      st16(rs, o, (g4 & 1) * 16384 + hh * 256 + t16 * 16, lay + L_O + T * 32768 + c * 512);
      DRAIN();
      FLAG(flo, FL_O + T * 32 + c);
      CST();
    }
    if (compute) {
      SETPRIO(0);
      v4u fr[176];
      {
        COORDS();
        const int TB = 8 * sp + 4 + 2 * rh + (w >> 1), SB = 2 * TB + (w & 1);
        const v4u rB = ld16(rs, lo, lay + L_X + (SB * 16 + n32) * 1024), rA = ld16(rs, lo, lay + L_X + (SB * 16 + n32) * 1024 - 8 * 16384);
        const unsigned xv = (g4 >> 1) * 1024 + (g4 & 1) * 512 + t16 * 16, sv = g4 ? NONE : t16 * 8;
        const unsigned xo = lay + L_X1 + (TB * 32 + 2 * n32) * 1024 + (w & 1) * 256, so = lay + L_X1 + MB2 + TB * 4096 + n32 * 256 + (w & 1) * 128;
        const int lswo = vopq(lsw + LDS_WO);
        while ((unsigned)__builtin_amdgcn_readfirstlane(__hip_atomic_load((LAS unsigned*)(L + LDS_TAG + 12), __ATOMIC_RELAXED, __HIP_MEMORY_SCOPE_WORKGROUP)) != 3u)
          __builtin_amdgcn_s_sleep(1);
        asm volatile("" ::: "memory");
        v4u of[16];
        unsigned pk;
        f32x4 a0, a1;
        Fin fin;
        pipe<64>(
            [&](auto fc) { return lds16(L, (fc.value & 31) * 1024 + lswo); },
            [&](auto fc, const v4u& wfr, auto pc) {
              constexpr int t = fc.value, part = pc.value, u = t & 31;
              if constexpr (part == 0) {
                if constexpr (u & 1) a1 = mfma16(wfr, of[u >> 1], u == 1 ? zero4() : a1);
                else a0 = mfma16(wfr, of[u >> 1], u == 0 ? zero4() : a0);
              } else {
                if constexpr (t == 31 || t == 63) {
                  asm volatile("s_nop 7" : "+v"(a0), "+v"(a1));
                  static_for<8>([&](auto k) { fin.template chunk<k.value>(a0, a1, t == 31 ? rB : rA, rs, xv, xo - (t == 31 ? 0 : 4 * 32768), rs, sv, so - (t == 31 ? 0 : 4 * 4096)); });
                  CST();
                }
                if constexpr (t == -1 || t == 31) {
                  if constexpr (t == 31) {
                    pk = __hip_atomic_load(fl + (FL_O + (TB - 4) * 32 + (lane & 31)) * 32, RLX_AGENT);
                    DRAIN();
                    FLAG(flo, FL_X1B + TB * 32 + 2 * n32 + (w & 1));
                  }
                  if (t == -1 || !__all(pk == tag)) POLL(flo, FL_O + (TB - (t == 31 ? 4 : 0)) * 32, 32);
                  CST();
                  static_for<16>([&](auto i) { of[i.value] = ld16(rs, lo + (i.value & 3) * 1024, lay + L_O + (SB - (t == 31 ? 8 : 0)) * 16384 + (i.value >> 2) * 4096); });
                }
                if constexpr (t == 55) pk = __hip_atomic_load(fl + (FL_X1B + (8 * sp + 4 + w) * 32 + (lane & 31)) * 32, RLX_AGENT);
                if constexpr (t == 63) {
                  const int T = 8 * sp + 4 + w;
                  if (!__all(pk == tag)) POLL(flo, FL_X1B + T * 32, 32);
                  CST();
                  static_for<NPG>([&](auto i) { fr[i.value] = ld16(rs, lo + (i.value & 3) * 1024, lay + L_X1 + T * 32768 + (i.value >> 2) * 4096); });
                }
              }
            });
      }
      SETPRIO(2);
      BAR();
      {
        COORDS();
        const int TB = 8 * sp + 4 + w;
        const int TD = 8 * sp + 4 + 2 * rh + (w >> 1), SD = 2 * TD + (w & 1);
        const unsigned x1b = lay + L_X1 + TB * 32768, hb = lay + L_H + SD * 57344, xn = last ? NONE : lay + LAY_BYTES + L_X;
        const unsigned hsv = (g4 & 1) * 57344 + hh * 256 + t16 * 16, hso = lay + L_H + TB * 114688;
        const rsrc_t ro = last ? __builtin_amdgcn_make_buffer_rsrc((void*)out, 0, (int)MB2, 0x00020000) : rs;
        const unsigned ob = last ? SD * 16384 + n32 * 64 : xn + (SD * 16 + n32) * 1024;
        const unsigned sb = xn + MB2 + TD * 4096 + n32 * 256 + (w & 1) * 128;
        unsigned ov, sv;
        const int lsw2 = vopq(lsw + 65536), lh = (hh * 16 + t16) * 16, lh2 = vopq(lh + 65536);
        f32x16 acc[2][4];
        f32x4 d0[2], d1[2];
        v4u mf, hv[4], res[2];
        float k1[2], den2[2], t0, t1, p0_, p1_;
        Fin fin;
        auto epi = [&](auto ec) {
          constexpr int job = ec.value >> 6, k = ec.value & 63, j = k >> 4, i = (k >> 2) & 3, uo = j < 3 ? 8 : 4;
          const f32x16& a = acc[job][j];
          if constexpr ((k & 3) == 0)
            asm volatile("v_mul_f32 %0, %2, %4\n\tv_mul_f32 %1, %3, %4\n\tv_exp_f32 %0, %0" : "=&v"(t0), "=&v"(t1) : "v"(a[2 * i]), "v"(a[2 * i + 1]), "v"(k1[job]));
          else if constexpr ((k & 3) == 1)
            asm volatile("v_exp_f32 %0, %0\n\tv_mul_f32 %1, %3, %5\n\tv_mul_f32 %2, %4, %6"
                : "+v"(t1), "=&v"(p0_), "=&v"(p1_) : "v"(a[2 * i]), "v"(a[2 * i + 1]), "v"(a[uo + 2 * i]), "v"(a[uo + 2 * i + 1]));
          else if constexpr ((k & 3) == 2)
            asm volatile("v_fma_f32 %0, %0, %2, %2\n\tv_fma_f32 %1, %1, %2, %2\n\tv_rcp_f32 %0, %0" : "+v"(t0), "+v"(t1) : "v"(den2[job]));
          else {
            unsigned d;
            asm volatile("v_rcp_f32 %2, %2\n\tv_mul_f32 %1, %1, %3\n\tv_mul_f32 %2, %2, %4\n\tv_cvt_pk_bf16_f32 %0, %1, %2" : "=v"(d), "+v"(t0), "+v"(t1) : "v"(p0_), "v"(p1_));
            hv[j][i] = d;
            if constexpr (k == (job ? 31 : NEP - 1)) { st16(rs, hv[0], hsv, hso - job * 4 * 114688 + c * 1024); st16(rs, hv[1], hsv + 512, hso - job * 4 * 114688 + c * 1024); }
            if constexpr (k == NEP - 1) {
              st16(rs, hv[2], hsv, hso - job * 4 * 114688 + 32768 + c * 512);
              st8(rs, (v2u){hv[3][0], hv[3][1]}, hsv - hh * 248, hso - job * 4 * 114688 + 49152 + c * 256);
            }
          }
        };
        auto vload = [&](auto ic) {
          constexpr int id = ic.value;
          if constexpr (id < 64) fr[id] = ld16(rs, lo + (id & 3) * 1024, x1b - (id >> 5) * 4 * 32768 + ((id & 31) >> 2) * 4096);
          else if constexpr (id < 176) {
            constexpr int n = (id - 64) % 56;
            fr[id] = ld16(rs, lo + (n & 3) * 1024, hb - (id >= 120 ? 8 * 57344 : 0) + (n >> 2) * 4096);
          } else if constexpr (id < 208) {
            constexpr int n = id - 176;
            xq[n] = ld16(rs, lo + ((n >> 1) & 3) * 1024, xn + TB * 32768 + (n & 1) * 16384 + (n >> 3) * 4096);
          } else res[id - 224] = ld16(rs, vopq(lo & 0x2f0) + ((lo & 256) << 1) + (lo & 512), lay + L_X1 + ((TD - 4 * (id - 224)) * 32 + 2 * n32) * 1024 + (w & 1) * 256);
        };
        pipe<NST>(
            [&](auto fc) {
              constexpr int t = fc.value;
              if constexpr (t == 32) wait_vm<vm_between(-1, t - LA - 1) + NPG>();
              if constexpr (t == GA + 48) wait_vm<vm_between(ep_step(NEP - 1), t - LA - 1)>();
              if constexpr (t == DB + 14) wait_vm<vm_between(GA + ep_step(31), t - LA - 1)>();
              if constexpr (t == DB + 56) wait_vm<vm_between(GA + ep_step(NEP - 1), t - LA - 1)>();
              if constexpr (t == DA + 42) wait_vm<vm_between(FINB + 7, t - LA - 1)>();
              if constexpr (t < DB) {
                constexpr GS s = gu_step(t % GA);
                if constexpr (s.cs) return csfrag(L, 48 + 32 * s.j + (s.j < 3 ? lane & 31 : t16));
                else {
                  if (s.off % 14336 == 0 && s.off >= (t < GA ? 14336 : 28672)) BARX();
                  return lds16(L, s.off < 65536 ? s.off + (s.j < 3 ? lsw : lh) : s.off - 65536 + (s.j < 3 ? lsw2 : lh2));
                }
              } else {
                constexpr int v = (t - DB) % 112;
                if (v % 14 == 0 && v >= (t < DA ? 0 : 28)) BARX();
                return lds16(L, v < 64 ? v * 1024 + lsw : (v - 64) * 1024 + lsw2);
              }
            },
            [&](auto fc, const v4u& wfr, auto pc) {
              constexpr int t = fc.value, part = pc.value;
              if constexpr (t < 0) return;
              else if constexpr (part == 0) {
                if constexpr (t < DB) {
                  constexpr int job = t / GA, u = t % GA;
                  constexpr GS s = gu_step(u);
                  acc[job][s.j] = mfma(wfr, s.cs ? mf : fr[32 * job + (s.kap & 31)], u < 4 ? splat16(0.f) : acc[job][s.j]);
                } else {
                  constexpr int job = t >= DA, v = (t - DB) % 112;
                  if constexpr (v & 1) d1[job] = mfma16(wfr, fr[64 + 56 * job + (v >> 1)], v == 1 ? zero4() : d1[job]);
                  else d0[job] = mfma16(wfr, fr[64 + 56 * job + (v >> 1)], v == 0 ? zero4() : d0[job]);
                }
              } else {
                if constexpr (t < DB) {
                  constexpr int job = t / GA, u = t % GA;
                  if constexpr (u == 74) {
                    const int a = LDS_ST + w * 128 + (lane & 31) * 4;
                    k1[job] = lds1f(L, a); den2[job] = lds1f(L, a + 512); mf = mfrag(L, hh ? LDS_TAG + 8 : a + 1024);
                  }
                  if constexpr (u == 79) k1[job] *= -LOG2E;
                }
                if constexpr (TAB.ld[t][0] >= 0) vload(IC<TAB.ld[t][0]>{});
                if constexpr (TAB.ld[t][1] >= 0) vload(IC<TAB.ld[t][1]>{});
                if constexpr (TAB.ep[t] >= 0) epi(IC<TAB.ep[t]>{});
                if constexpr (t == FINB - 1) { const int l = lane_id(); ov = l * 16 + ((((l & 15) * 1008) - (l >> 4) * 240) & sopq(-layer)); sv = l >= 16 ? NONE : l * 8; }
                if constexpr (t >= FINB && t < FINB + 8) fin.template chunk<t - FINB>(d0[0], d1[0], res[0], ro, ov, ob, rs, sv, sb);
              }
            });
        CST();
        asm volatile("s_nop 7" : "+v"(d0[1]), "+v"(d1[1]));
        static_for<8>([&](auto k) { fin.template chunk<k.value, false>(d0[1], d1[1], res[1], ro, ov, ob - 8 * 16384, rs, sv, sb - 4 * 4096); });
        if (last) st16(ro, fin.o, ov, ob - 8 * 16384);
        else {
          *(LAS v4u*)(L + LDS_QL + w * 1024 + lo) = fin.o;
          if (lane < 16) *(LAS v2u*)(L + LDS_QL + 4096 + w * 128 + lane * 8) = fin.sq;
          BAR();
        }
      }
    } else {
      COORDS();
      const bool poller = w == 0, loader = w != 0;
      unsigned pv = 0;
#define PEEK(blk, line) pv = __hip_atomic_load((gu32*)(ws + (blk)) + ((line) + (lane & 31)) * 32, RLX_AGENT)
#define GATE(blk, line) do { if (!__all(pv == tag)) POLL(blk, line, 32); } while (0)
      if (layer == 0) {
        if (loader) { ldw(IC<8>{}, WGU_OFF + c * 114688, 48); ld_gu(4); ldw(IC<10>{}, WGU_OFF + c * 114688, 70); }
        f32x4 ca = zero4();
        const unsigned one = (unsigned)vopq((w == 3 || t16 == (g4 & 1)) ? 0x3f803f80 : 0), wb = WGU_OFF + c * 114688;
        for (int q = 0; q < (w < 3 ? 2 : 1); ++q) {
          v4u f[16];
          static_for<16>([&](auto ic) {
            constexpr int i = ic.value;
            f[i] = ld16(rs, w < 3 ? lsw : lo, wb + (w < 3 ? (q ? 56 + 16 * w + i : 7 * (i >> 1) + 3 * (i & 1) + w) : i < 8 ? 7 * i + 6 : 96 + i) * 1024);
          });
          PIN();
          static_for<16>([&](auto i) { ca = mfma16(f[i.value], (v4u){one, one, one, one}, ca); });
        }
        if (t16 < (w < 3 ? 2 : 1)) {
#pragma unroll
          for (int r = 0; r < 4; ++r) cs_put(L, 48 + 32 * w + 16 * t16 + 4 * g4 + r, ca[r]);
        }
      }
      SETPRIO(3);
      if (poller) {
        asm volatile("" ::"v"(touch0));
        Stats st;
        unsigned ev[4];
        const int h2l = lane < 32 ? FL_HA2 + sp * 32 + lane : FL_HB + (sp ? sp - 1 : 0) * 32 + c;
        SETPRIO(1);
        HBAR();
        static_for<7>([&](auto nc) {
          constexpr int n = nc.value + 1;
          if (n <= 2) st.load(rs, lane, lay + L_X1 + MB2 + (8 * sp + 4) * 4096, 8 * n - 8, 8 * n);
          if (n == 3) { FLAG(flo, FL_X1A + sp * 32 + c); st.part1(); }
          if (n == 4) st.part2();
          if (n == 5) st.part3(L, lane);
          if (n == 6) PEEK(flo, FL_X1A + sp * 32);
          if (n == 7) GATE(flo, FL_X1A + sp * 32);
          HBARS(n == 3 || n == 7);
        });
        st.load(rs, lane, lay + L_X1 + MB2 + 8 * sp * 4096);
        st.all(L, lane);
        static_for<6>([&](auto nc) {
          constexpr int n = nc.value + 2;
          if (n == 4) FLAG(flo, FL_HB + sp * 32 + c);
          if (n == 6) PEEK(flo, FL_HB + sp * 32);
          if (n == 7) GATE(flo, FL_HB + sp * 32);
          HBARS(n == 2 || n == 7);
        });
        static_for<8>([&](auto nc) {
          constexpr int n = nc.value;
          if (n == 2) FLAG(flo, FL_HA + sp * 32 + c);
          if (n == 5) { FLAG(flo, FL_HA2 + sp * 32 + c); PEEK(flo, FL_HA + sp * 32); }
          if (n == 6) GATE(flo, FL_HA + sp * 32);
          if (n == 7) pv = __hip_atomic_load(fl + h2l * 32, RLX_AGENT);
          HBARS(n == 0 || n == 3 || n == 6);
        });
        static_for<6>([&](auto nc) {
          constexpr int n = nc.value + 2;
          if (n == 2) { if (!__all(pv == tag)) poll(fl + h2l * 32, true, tmo, tag); }
          if (n == 3) {
#pragma unroll
            for (int q = 0; q < 4; ++q) ev[q] = __builtin_amdgcn_raw_buffer_load_b32(rs, lane * 128, last && blockIdx.x == 0 ? flo + (FL_HB + 64 * q) * 128 : NONE, AUX_SC1);
          }
          if (n == 4 && !last) FLAG(FLAG_LAYER, FL_XB + sp * 32 + c);
          if (n == 5) PEEK(FLAG_LAYER, FL_XB + sp * 32);
          if (n == 6) GATE(FLAG_LAYER, FL_XB + sp * 32);
          if (n == 7) st.load(rs, lane, last ? NONE : LAY_OFF + LAY_BYTES + L_X + MB2 + (8 * sp + 4) * 4096);
          HBARS(n == 2 || n == 6 || n == 7);
        });
        if (!last) { st.all(L, lane); }
        if (last && blockIdx.x == 0) {
          if (!__all(ev[0] == tag && ev[1] == tag && ev[2] == tag && ev[3] == tag))
            for (int q = 0; q < 4; ++q) POLL(flo, FL_HB + 64 * q, 64);
          if (lane == 0) __hip_atomic_store(fg + FL_EP * 32, tag, RLX_AGENT);
        }
      } else {
        const unsigned wq = WQKV_OFF + c * 49152, wb = WGU_OFF + c * 114688;
        DRAIN();
        HBAR();
        static_for<7>([&](auto nc) {
          constexpr int n = nc.value + 1;
          if (n == 7) wait_vm<1>();
          blank(3 * n - 4 + w, last ? NONE : LAY_OFF + LAY_BYTES);
          if (n >= 2 && n <= 4) ldw(IC<9>{}, wb, 62 + 9 * n);
          if (n == 5) { ldw(IC<5>{}, wb, 107); wait_vm<11>(); }
          if (n == 6) wait_vm<8>();
          HBARS(n == 3 || n == 7);
        });
        static_for<6>([&](auto nc) {
          constexpr int n = nc.value + 2;
          if (n == 3) wait_vm<0>();
          if (n == 5 || n == 6) { ld_dn(2 * n - 10); ld_dn(2 * n - 9); } else if (n > 6) ld_dn(n - 3);
          HBARS(n == 2 || n == 7);
        });
        static_for<8>([&](auto nc) {
          constexpr int n = nc.value;
          if (n <= 2) ld_dn(n + 5);
          wait_vm<(n <= 2 ? 25 : 35 - 5 * n)>();
          HBARS(n == 0 || n == 3 || n == 6);
        });
        static_for<6>([&](auto nc) {
          constexpr int n = nc.value + 2;
          if (!last) {
            if (n >= 3 && n <= 5) ldw(IC<14>{}, wq, 14 * (n - 3));
            if (n == 6) { ldw(IC<6>{}, wq, 42); ldw(IC<8>{}, wb, 48); }
            if (n == 7) ldw(IC<14>{}, wb, 56);
          }
          HBARS(n == 2 || n == 6 || n == 7);
        });
        if (!last) { ldw(IC<10>{}, wb, 70); wait_vm<14>(); }
      }
      SETPRIO(3);
      if (!last) {
        HOLD(2);
        HBAR();
      }
      static_for<NPQ>([&](auto i) { undef(xq[i.value]); });
    }
  }
}

}

extern "C" void kernel_launch(void* const* d_in, const int* in_sizes, int n_in, void* d_out, int out_size, void* d_ws, size_t ws_size,
                              hipStream_t stream) {
  (void)in_sizes; (void)n_in; (void)out_size; (void)ws_size;
  (void)hipFuncSetAttribute((const void*)k_main, hipFuncAttributeMaxDynamicSharedMemorySize, LDS_BYTES);
  k_main<<<256, 512, LDS_BYTES, stream>>>((const u16*)d_in[0], (const u16*)d_in[1], (const u16*)d_in[2], (const u16*)d_in[4], (const u16*)d_in[5],
                                          (const u16*)d_in[6], (unsigned char*)d_ws, (u16*)d_out);
}
```

```cpp
#define X_AT 9
#define X_HOLD 500
#define X_XCD 8
#include <hip/hip_runtime.h>
#include <stdint.h>
#include <utility>

#define LAS __attribute__((address_space(3)))
#define GAS __attribute__((address_space(1)))
#define DI __device__ __forceinline__
#define RLX_AGENT __ATOMIC_RELAXED, __HIP_MEMORY_SCOPE_AGENT
typedef unsigned short u16;
typedef unsigned v4u __attribute__((ext_vector_type(4)));
typedef unsigned v2u __attribute__((ext_vector_type(2)));
typedef float f32x2 __attribute__((ext_vector_type(2)));
typedef float f32x4 __attribute__((ext_vector_type(4)));
typedef float f32x16 __attribute__((ext_vector_type(16)));
typedef __bf16 bf16x2 __attribute__((ext_vector_type(2)));
typedef __bf16 bf16x8 __attribute__((ext_vector_type(8)));
typedef GAS unsigned gu32;
typedef GAS unsigned long long gu64;

namespace {

constexpr int D_ = 512, F_ = 1792;
constexpr unsigned MB2 = 2097152u, STB = 262144u;
constexpr unsigned FLAG_LAYER = 1048576u, FLAG_G = 2 * FLAG_LAYER, FLAG_BYTES = FLAG_G + 65536;
constexpr unsigned WQKV_OFF = FLAG_BYTES;
constexpr unsigned WO_OFF = WQKV_OFF + 32 * 49152;
constexpr unsigned WGU_OFF = WO_OFF + 16 * 32768;
constexpr unsigned WDN_OFF = WGU_OFF + 32 * 114688;
constexpr unsigned LAY_OFF = WDN_OFF + 16 * 114688;
constexpr unsigned L_K = 0, L_V = MB2, L_O = 2 * MB2, L_X1 = 3 * MB2, L_X = 4 * MB2 + STB, L_H = 5 * MB2 + 2 * STB;
constexpr unsigned LAY_BYTES = L_H + 128 * 57344;
constexpr unsigned WS_BYTES = LAY_OFF + 2 * LAY_BYTES;
constexpr unsigned NONE = 0x40000000u;
constexpr int FL_KV = 0, FL_O = 2048, FL_X1B = 4096, FL_X1A = 6144, FL_HB = 6400, FL_HA = 6656, FL_XB = 6912, FL_XA = 7168, FL_HA2 = 7424;
constexpr int FL_WR = 0, FL_EP = 256, FL_TMO = 257;
constexpr int VBLK = 528;
constexpr int LDS_KL = 114688, LDS_VL = LDS_KL + 16384, LDS_QL = LDS_VL + 32 * VBLK, LDS_CS = LDS_QL + 8192, LDS_ST = LDS_CS + 1536;
constexpr int LDS_MK = LDS_ST + 1536, LDS_TAG = LDS_MK + 2048;
constexpr int LDS_WO = 81920, LDS_TB = 49152;
constexpr int LDS_BYTES = 163840;
#define TSTR 1048576u
constexpr unsigned SPIN_CAP = 1u << 16;
constexpr int AUX_SC1 = 16;
constexpr float LOG2E = 1.4426950408889634f;

DI f32x16 mfma(v4u a, v4u b, f32x16 c) {
  return __builtin_amdgcn_mfma_f32_32x32x16_bf16(__builtin_bit_cast(bf16x8, a), __builtin_bit_cast(bf16x8, b), c, 0, 0, 0);
}
DI f32x4 mfma16(v4u a, v4u b, f32x4 c) {
  return __builtin_amdgcn_mfma_f32_16x16x32_bf16(__builtin_bit_cast(bf16x8, a), __builtin_bit_cast(bf16x8, b), c, 0, 0, 0);
}
DI unsigned pk2(float lo, float hi) {
  f32x2 p = {lo, hi};
  return __builtin_bit_cast(unsigned, __builtin_convertvector(p, bf16x2));
}
DI float bfr(float x) { return __uint_as_float(pk2(x, 0.f) << 16); }
DI float bflo(unsigned u) { return __uint_as_float(u << 16); }
DI float bfhi(unsigned u) { return __uint_as_float(u & 0xffff0000u); }
DI float amul(float a, float b) { float d; asm("v_mul_f32 %0, %1, %2" : "=v"(d) : "v"(a), "v"(b)); return d; }
DI float afma(float a, float b, float c) { float d; asm("v_fma_f32 %0, %1, %2, %3" : "=v"(d) : "v"(a), "v"(b), "v"(c)); return d; }
DI float aadd(float a, float b) { float d; asm("v_add_f32 %0, %1, %2" : "=v"(d) : "v"(a), "v"(b)); return d; }
DI float asub(float a, float b) { float d; asm("v_sub_f32 %0, %1, %2" : "=v"(d) : "v"(a), "v"(b)); return d; }
DI float xhalf_sum(float v) {
  float t;
  asm("v_mov_b32 %1, %0\n\ts_nop 1\n\tv_permlane32_swap_b32 %0, %1\n\tv_add_f32 %0, %0, %1" : "+v"(v), "=&v"(t));
  return v;
}
DI float xrow_sum(float v) {
  float t;
  asm("v_mov_b32 %1, %0\n\ts_nop 1\n\tv_permlane16_swap_b32 %0, %1\n\tv_add_f32 %0, %0, %1" : "+v"(v), "=&v"(t));
  return v;
}
DI float xhalf_max(float v) {
  auto r = __builtin_amdgcn_permlane32_swap(__float_as_uint(v), __float_as_uint(v), false, false);
  return fmaxf(__uint_as_float(r[0]), __uint_as_float(r[1]));
}
DI void swap16(unsigned& a, unsigned& b) { asm("s_nop 1\n\tv_permlane16_swap_b32 %0, %1" : "+v"(a), "+v"(b)); }
DI void swap32(unsigned& a, unsigned& b) { asm("s_nop 1\n\tv_permlane32_swap_b32 %0, %1" : "+v"(a), "+v"(b)); }
DI f32x16 splat16(float v) {
  f32x16 z;
#pragma unroll
  for (int i = 0; i < 16; ++i) z[i] = v;
  return z;
}
DI f32x4 zero4() { return (f32x4){0.f, 0.f, 0.f, 0.f}; }
DI int vopq(int x) { asm volatile("" : "+v"(x)); return x; }
DI int lane_id() { int l; asm volatile("v_mbcnt_lo_u32_b32 %0, -1, 0\n\tv_mbcnt_hi_u32_b32 %0, -1, %0" : "=v"(l)); return l; }
template <int LN> DI unsigned wrl(unsigned v, unsigned s) { asm volatile("v_writelane_b32 %0, %1, %2" : "+v"(v) : "s"(s), "i"(LN)); return v; }
template <class T> DI void undef(T& x) { asm volatile("" : "=v"(x)); }
DI int sopq(int x) { asm volatile("" : "+s"(x)); return x; }
#define SWZ(l) ((l) ^ (((l) >> 3) & 7))
#define COORDS() \
  const int lane = lane_id(), lo = lane * 16, hh = lane >> 5, t16 = lane & 15, g4 = lane >> 4, lsw = SWZ(lane) * 16; \
  const int w = sopq(w0) & 3, sp = sopq(sp0), c = sopq(c0), n32 = c >> 1, rh = c & 1; \
  const unsigned lay = (unsigned)sopq((int)lay0); \
  const unsigned flo = sopq(layer) * FLAG_LAYER; \
  gu32* const fl = (gu32*)(ws + flo); \
  (void)lo; (void)hh; (void)t16; (void)g4; (void)lsw; (void)n32; (void)rh; (void)lay; (void)sp; (void)c; (void)w; (void)fl;
#define HBARS(c) asm volatile("s_waitcnt lgkmcnt(0)\n\ts_barrier" ::: "memory")
#define CST() do {} while (0)
#define DBGC(k) asm volatile("" ::: "memory")
#define HBAR() asm volatile("s_waitcnt lgkmcnt(0)\n\ts_barrier" ::: "memory")
#define BAR() asm volatile("s_waitcnt lgkmcnt(0)\n\ts_barrier" ::: "memory")
#define BARX() asm volatile("s_barrier" ::: "memory")
#define DRAIN() asm volatile("s_waitcnt vmcnt(0)" ::: "memory")
#define PIN() __builtin_amdgcn_sched_barrier(0)
#define SETPRIO(n) __builtin_amdgcn_s_setprio(n)
template <int N> DI void wait_vm() { static_assert(N >= 0 && N < 64, ""); asm volatile("s_waitcnt vmcnt(%0)" ::"i"(N) : "memory"); }

typedef __amdgpu_buffer_rsrc_t rsrc_t;
DI v4u ld16(rsrc_t rs, unsigned voff, unsigned soff) { return __builtin_amdgcn_raw_buffer_load_b128(rs, voff, soff, AUX_SC1); }
DI f32x2 ld8f(rsrc_t rs, unsigned voff, unsigned soff) { return __builtin_bit_cast(f32x2, __builtin_amdgcn_raw_buffer_load_b64(rs, voff, soff, AUX_SC1)); }
DI void st16(rsrc_t rs, v4u v, unsigned voff, unsigned soff) {
  __builtin_amdgcn_raw_buffer_store_b128(v, rs, voff, soff, AUX_SC1);
  asm volatile("s_nop 1" ::"v"(v));
}
DI void st8(rsrc_t rs, v2u v, unsigned voff, unsigned soff) { __builtin_amdgcn_raw_buffer_store_b64(v, rs, voff, soff, AUX_SC1); }
DI void st4(rsrc_t rs, unsigned v, unsigned voff, unsigned soff) { __builtin_amdgcn_raw_buffer_store_b32(v, rs, voff, soff, AUX_SC1); }
DI v4u lds16(LAS unsigned char* L, int off) { return *(LAS v4u*)(L + off); }
DI f32x4 lds4f(LAS unsigned char* L, int off) { return *(LAS f32x4*)(L + off); }

DI void poll(gu32* word, bool active, gu32* tmo, unsigned tag) {
  for (unsigned spins = 0;;) {
    unsigned v = tag;
    if (active) v = __hip_atomic_load(word, RLX_AGENT);
    if (__all(v == tag)) break;
    ++spins;
    if (spins > SPIN_CAP) { __hip_atomic_store(tmo, tag, RLX_AGENT); break; }
    if ((spins & 255u) == 0u && (unsigned)__builtin_amdgcn_readfirstlane(__hip_atomic_load(tmo, RLX_AGENT)) == tag) break;
  }
  __builtin_amdgcn_fence(__ATOMIC_ACQUIRE, "wavefront");
}

template <int I> DI void dma1(rsrc_t rs, LAS unsigned char* dst, unsigned voff, unsigned soff) {
  __builtin_amdgcn_raw_ptr_buffer_load_lds(rs, (LAS void*)dst, 16, voff, soff, I * 1024, AUX_SC1);
}
template <int V> using IC = std::integral_constant<int, V>;
template <class FN, int... I> DI void static_for_(FN fn, std::integer_sequence<int, I...>) { (fn(IC<I>{}), ...); }
template <int N, class FN> DI void static_for(FN fn) { static_for_(fn, std::make_integer_sequence<int, N>{}); }
template <int N> DI void dma(rsrc_t rs, LAS unsigned char* dst, unsigned lo, unsigned src) {
  static_for<N>([&](auto i) { dma1<i.value & 3>(rs, dst + (i.value >> 2) * 4096, lo, src + (i.value >> 2) * 4096); });
}

struct Stats {
  f32x4 p[16];
  float m0, m1, q0, q1;
  DI void load(rsrc_t rs, int lane, unsigned base, int i0 = 0, int i1 = 16) {
#pragma unroll
    for (int i = i0; i < i1; ++i) p[i] = __builtin_bit_cast(f32x4, ld16(rs, (lane >> 4) * 4096 + (lane & 15) * 16 + i * 256, base));
    PIN();
  }
  DI void part1() {
    m0 = m1 = q0 = q1 = 0.f;
#pragma unroll
    for (int i = 0; i < 16; ++i) { m0 += p[i][0]; q0 += p[i][1]; m1 += p[i][2]; q1 += p[i][3]; }
    asm volatile("" : "+v"(m0), "+v"(m1), "+v"(q0), "+v"(q1));
  }
  DI void part2() {
    const float a0 = m0 * (1.0f / 16.0f), a1 = m1 * (1.0f / 16.0f);
    q0 *= 32.0f; q1 *= 32.0f;
#pragma unroll
    for (int i = 0; i < 16; ++i) { const float d0 = p[i][0] - a0, d1 = p[i][2] - a1; q0 += d0 * d0; q1 += d1 * d1; }
    asm volatile("" : "+v"(q0), "+v"(q1));
  }
  DI void all(LAS unsigned char* L, int lane, int dst = LDS_ST) { part1(); part2(); part3(L, lane, dst); }
  DI void part3(LAS unsigned char* L, int lane, int dst = LDS_ST) {
    const float den0 = bfr(__builtin_amdgcn_sqrtf(bfr(bfr(q0 * (1.0f / (32.0f * D_))) + 1e-5f)));
    const float den1 = bfr(__builtin_amdgcn_sqrtf(bfr(bfr(q1 * (1.0f / (32.0f * D_))) + 1e-5f)));
    *(LAS f32x2*)(L + dst + lane * 8) = (f32x2){__builtin_amdgcn_rcpf(den0), __builtin_amdgcn_rcpf(den1)};
    *(LAS f32x2*)(L + dst + 512 + lane * 8) = (f32x2){den0 * den0, den1 * den1};
    *(LAS v2u*)(L + dst + 1024 + lane * 8) = (v2u){pk2(m0 * (-1.0f / D_), m0 * (-1.0f / D_)), pk2(m1 * (-1.0f / D_), m1 * (-1.0f / D_))};
  }
};
DI float lds1f(LAS unsigned char* L, int off) { return *(LAS float*)(L + off); }
DI v4u mfrag(LAS unsigned char* L, int off) { const unsigned n = *(LAS unsigned*)(L + off); return (v4u){n, n, 0u, 0u}; }

constexpr int LA = 6;
template <int NF, class RD, class STEP>
DI void pipe(RD rd, STEP step) {
  v4u wf[8];
  static_for<LA>([&](auto i) { wf[i.value] = rd(i); });
  step(IC<-1>{}, wf[0], IC<1>{});
  PIN();
  static_for<NF>([&](auto fc) {
    constexpr int f = fc.value;
    if constexpr ((f & 1) == 0 && f + LA <= NF) __builtin_amdgcn_s_waitcnt(0xc07f | ((LA - 2) << 8));
    step(fc, wf[f & 7], IC<0>{});
    if constexpr (f + LA < NF) wf[(f + LA) & 7] = rd(IC<f + LA>{});
    step(fc, wf[f & 7], IC<1>{});
    PIN();
  });
}
DI v4u csfrag(LAS unsigned char* L, int entry) {
  const v2u t = *(LAS v2u*)(L + LDS_CS + entry * 8);
  return (v4u){t.x, t.y, 0u, 0u};
}
DI void cs_put(LAS unsigned char* L, int entry, float cs) {
  const float hi = bfr(cs), mid = bfr(cs - hi), lw = (cs - hi) - mid;
  *(LAS v2u*)(L + LDS_CS + entry * 8) = (v2u){pk2(hi, mid), pk2(lw, 0.f)};
}
DI unsigned mulpk(float a, float s0, float b, float s1) {
  unsigned d; float t;
  asm("v_mul_f32 %0, %2, %3\n\tv_mul_f32 %1, %4, %5\n\tv_cvt_pk_bf16_f32 %0, %0, %1" : "=&v"(d), "=&v"(t) : "v"(a), "v"(s0), "v"(b), "v"(s1));
  return d;
}
DI unsigned nos(unsigned v) { return v < 0xfffffffeu ? v : 0xfffffffeu; }
DI v4u nos4(v4u v) { return (v4u){nos(v[0]), nos(v[1]), nos(v[2]), nos(v[3])}; }
DI unsigned umax3(unsigned a, unsigned b, unsigned c) { const unsigned t = a > b ? a : b; return t > c ? t : c; }
template <int N> DI bool missing(const v4u* f, int step = 1) {
  unsigned m0 = 0, m1 = 0;
#pragma unroll
  for (int i = 0; i < N; ++i) { m0 = umax3(m0, f[i * step][0], f[i * step][1]); m1 = umax3(m1, f[i * step][2], f[i * step][3]); }
  return __builtin_amdgcn_ballot_w64((m0 > m1 ? m0 : m1) == 0xffffffffu) != 0;
}
constexpr unsigned DATA_CAP = 1u << 14;
DI v4u pack8(const float* y) { return (v4u){pk2(y[0], y[1]), pk2(y[2], y[3]), pk2(y[4], y[5]), pk2(y[6], y[7])}; }

struct Fin {
  float y[8], s;
  v4u o;
  v2u sq;
  template <int K, bool ST = true> DI void chunk(const f32x4& a0, const f32x4& a1, const v4u& r, rsrc_t ro, unsigned vo, unsigned so, rsrc_t rs, unsigned vs, unsigned ss) {
    if constexpr (K == 0) { y[0] = aadd(a0[0], bflo(r[0])); y[1] = aadd(a0[1], bfhi(r[0])); y[2] = aadd(a0[2], bflo(r[1])); y[3] = aadd(a0[3], bfhi(r[1])); }
    if constexpr (K == 1) { y[4] = aadd(a1[0], bflo(r[2])); y[5] = aadd(a1[1], bfhi(r[2])); y[6] = aadd(a1[2], bflo(r[3])); y[7] = aadd(a1[3], bfhi(r[3])); }
    if constexpr (K == 2) { o = nos4(pack8(y)); if constexpr (ST) st16(ro, o, vo, so); }
    if constexpr (K == 3) {
#pragma unroll
      for (int m = 0; m < 4; ++m) { y[2 * m] = bflo(o[m]); y[2 * m + 1] = bfhi(o[m]); }
    }
    if constexpr (K == 4) s = xrow_sum(aadd(aadd(aadd(y[0], y[1]), aadd(y[2], y[3])), aadd(aadd(y[4], y[5]), aadd(y[6], y[7]))));
    if constexpr (K == 5) {
      s = xhalf_sum(s);
      const float mu = s * (1.0f / 32.0f);
#pragma unroll
      for (int i = 0; i < 8; ++i) y[i] = asub(y[i], mu);
    }
    if constexpr (K == 6) {
      y[0] = amul(y[0], y[0]); y[1] = amul(y[1], y[1]);
#pragma unroll
      for (int i = 2; i < 8; ++i) y[i & 1] = afma(y[i], y[i], y[i & 1]);
    }
    if constexpr (K == 7) {
      sq = (v2u){nos(__float_as_uint(s)), nos(__float_as_uint(xhalf_sum(xrow_sum(aadd(y[0], y[1])))))};
      if constexpr (ST) st8(rs, sq, vs, ss);
    }
  }
};

constexpr unsigned long long low_mask(int r) {
  unsigned long long m = 0;
  for (int l = 0; l < 64; ++l)
    if ((r & 3) + 8 * (r >> 2) + 4 * (l >> 5) <= (l & 31)) m |= 1ull << l;
  return m;
}
DI float sel(unsigned long long mask, float set, float unset) {
  float d;
  asm("v_cndmask_b32_e64 %0, %1, %2, %3" : "=v"(d) : "v"(unset), "v"(set), "s"(mask));
  return d;
}
DI float max16(float m, const f32x16& s) {
#pragma unroll
  for (int r = 0; r < 16; r += 2) m = __builtin_fmaxf(__builtin_fmaxf(m, s[r]), s[r + 1]);
  return m;
}
DI v4u exp_pack(const f32x16& s, int h) {
  float pr[8];
#pragma unroll
  for (int r = 0; r < 8; ++r) pr[r] = __builtin_amdgcn_exp2f(s[8 * h + r]);
  return pack8(pr);
}
template <bool EXACT>
DI f32x16 attn_pass(LAS unsigned char* L, int lane, int tl, int nv) {
  const int lo = lane * 16;
  const int kb = LDS_KL + tl * 1024 + lo, kf0 = LDS_KL + (tl + 8) * 1024 + (lane >> 5) * 512;
  const v4u q = lds16(L, LDS_QL + tl * 1024 + lo);
  v4u kf[9];
  kf[8] = lds16(L, kb + 8192); kf[0] = lds16(L, nv <= 0 ? kb : kf0);
#pragma unroll
  for (int i = 7; i >= 1; --i) kf[i] = lds16(L, (nv <= i ? kb : kf0 - i * 1024) + i * 1024);
  f32x16 s[9], cm = splat16(0.f);
  if constexpr (EXACT) {
    s[8] = mfma(kf[8], q, splat16(0.f));
    s[0] = mfma(kf[0], q, splat16(0.f));
    s[7] = mfma(kf[7], q, splat16(0.f));
    s[6] = mfma(kf[6], q, splat16(0.f));
    asm volatile("s_nop 7\n\ts_nop 7" : "+v"(s[8]), "+v"(s[0]));
    PIN();
    static_for<16>([&](auto r) { s[8][r.value] = sel(low_mask(r.value), s[8][r.value], s[0][r.value]); });
    float m = max16(s[8][0], s[8]);
    PIN();
    static_for<7>([&](auto ic) {
      constexpr int i = 7 - ic.value;
      if constexpr (i - 2 >= 1) s[i - 2] = mfma(kf[i - 2], q, splat16(0.f));
      m = max16(m, s[i]);
      PIN();
    });
    cm = splat16(-xhalf_max(m));
  }
  const int vb = LDS_VL + 2 * tl * VBLK + ((lane & 16) ? 512 : (lane >> 5) * 256 + (lane & 15) * 16);
  s[8] = mfma(kf[8], q, cm);
  s[0] = mfma(kf[0], q, cm);
  s[7] = mfma(kf[7], q, cm);
  const v4u mk0 = lds16(L, LDS_MK + lo), mk1 = lds16(L, LDS_MK + 1024 + lo);
  v4u va = lds16(L, vb + 16 * VBLK), vc = lds16(L, vb + 17 * VBLK), v0a = lds16(L, vb), v0c = lds16(L, vb + VBLK);
  asm volatile("s_nop 7\n\ts_nop 7" : "+v"(s[8]), "+v"(s[0]));
  PIN();
  f32x16 o;
  {
    static_for<16>([&](auto r) { s[8][r.value] = sel(low_mask(r.value), s[8][r.value], s[0][r.value]); });
    const v4u p0 = exp_pack(s[8], 0), l0 = p0 & mk0;
    o = mfma(va, l0, splat16(0.f));
    o = mfma(v0a, p0 ^ l0, o);
    const v4u p1 = exp_pack(s[8], 1), l1 = p1 & mk1;
    o = mfma(vc, l1, o);
    o = mfma(v0c, p1 ^ l1, o);
    va = lds16(L, vb + 14 * VBLK); vc = lds16(L, vb + 15 * VBLK);
    PIN();
  }
  static_for<7>([&](auto ic) {
    constexpr int i = 7 - ic.value;
    if constexpr (i > 1) s[i - 1] = mfma(kf[i - 1], q, cm);
    PIN();
    o = mfma(va, exp_pack(s[i], 0), o);
    if constexpr (i > 1) va = lds16(L, vb + (2 * i - 2) * VBLK);
    o = mfma(vc, exp_pack(s[i], 1), o);
    if constexpr (i > 1) vc = lds16(L, vb + (2 * i - 1) * VBLK);
    PIN();
  });
  return o;
}
DI v4u attn_unit(LAS unsigned char* L, int lane, int tl, int nv) {
  f32x16 o = attn_pass<false>(L, lane, tl, nv);
  float big = __builtin_fabsf(o[0]);
#pragma unroll
  for (int r = 1; r < 8; ++r) big += __builtin_fabsf(o[r]);
  if (__builtin_expect(!__all(o[8] > 0x1p-30f && o[8] < 0x1p60f && big < 0x1p120f), 0)) {
    asm volatile("" ::: "memory");
    o = attn_pass<true>(L, lane, tl, nv);
  }
  const float rl = __builtin_amdgcn_rcpf(o[8]);
  float y[8];
#pragma unroll
  for (int r = 0; r < 8; ++r) y[r] = o[r] * rl;
  return pack8(y);
}

struct Retile {
  v4u in[8];
  DI void load(const u16* src, int ld, int rs4) {
#pragma unroll
    for (int j = 0; j < 8; ++j) in[j] = *(const v4u*)(src + (size_t)(((j & 3) + rs4 * (j >> 2)) * ld));
    PIN();
  }
  unsigned d[8];
  DI void out(rsrc_t rs) {
#pragma unroll
    for (int i = 0; i < 8; ++i) st16(rs, in[i], d[i], 0);
  }
  DI void lines(LAS unsigned char* t, int lane, int sw, unsigned dst) {
#pragma unroll
    for (int e = 0; e < 8; ++e) {
      v4u o;
#pragma unroll
      for (int m = 0; m < 4; ++m) o[m] = __builtin_amdgcn_perm(in[2 * m + 1][e >> 1], in[2 * m][e >> 1], (e & 1) ? 0x07060302u : 0x05040100u);
      *(LAS v4u*)(t + lane * 128 + (e ^ sw ^ (lane & 7)) * 16) = o;
    }
    asm volatile("s_waitcnt lgkmcnt(0)" ::: "memory");
#pragma unroll
    for (int i = 0; i < 8; ++i) {
      in[i] = *(LAS v4u*)(t + i * 1024 + (lane ^ (lane >> 3)) * 16);
      d[i] = (unsigned)__builtin_amdgcn_ds_bpermute((8 * i + (lane >> 3)) * 4, (int)dst) + (lane & 7) * 16;
    }
    PIN();
  }
  DI void put(LAS unsigned char* L, int pb, int dd, int lp, int dl) {
#pragma unroll
    for (int e = 0; e < 8; ++e) {
      v4u o;
#pragma unroll
      for (int m = 0; m < 4; ++m) o[m] = __builtin_amdgcn_perm(in[2 * m + 1][e >> 1], in[2 * m][e >> 1], (e & 1) ? 0x07060302u : 0x05040100u);
      const int s = lp + (e & 3) + (e >> 2) * dl;
      *(LAS v4u*)(L + pb + (e >> 2) * dd + (s ^ ((s >> 3) & 7)) * 16) = o;
    }
  }
};

struct GS { int cs, j, kap, off; };
constexpr GS gu_step(int u) {
  if (u < 64) { const int k = u >> 2, j = u & 3; return GS{0, j, k, j < 3 ? (7 * (k >> 1) + 3 * (k & 1) + j) * 1024 : (7 * (k >> 1) + 6) * 1024 + (k & 1) * 512}; }
  const int v = u - 64, j = v / 17, i = v % 17;
  return GS{i == 16, j, 16 + i, 57344 + j * 16384 + i * (j < 3 ? 1024 : 512)};
}
constexpr int GA = 132, DB = 264, DA = 376, NST = 488;
constexpr int NPG = 16, NPQ = 12, LDN = 12;
constexpr int ld_step(int id) {
  if (id < 32) return id < NPG ? -1 : 2 * (id - NPG);
  if (id < 64) { const int n = id - 32; return n < 12 ? 103 + (5 * n) / 2 : GA + 10 + 2 * (n - 12); }
  if (id < 176) {
    const int n = id - 64;
    return n < 12 ? GA + 103 + n : n < 26 ? DB - 15 + ((n - 12) * 7) / 4 : n < 56 ? DB + 27 + ((n - 26) * 5) / 3 : n < 84 ? DB + 79 + ((n - 56) * 4) / 3 : DA + 31 + 2 * (n - 84);
  }
  if (id < 208) { const int n = id - 176; return n < NPQ ? DA + 87 + 2 * n : -1; }
  if (id < 224) return -1;
  return id == 224 ? DB + 80 : DA + 72;
}
constexpr int NEP = 56;
constexpr int ep_step(int k) { return 83 + 17 * (k / 16) + k % 16; }
struct Tab { short ld[NST][2]; short ep[NST]; short nvm[NST]; };
constexpr int FINB = DA + 3;
#define Q3T 75
#define XA0 18
constexpr int xa_step(int j) { const int a = XA0 + j, b = j < 12 ? 3 * j + 2 : 34 + j; return a > b ? a : b; }
constexpr Tab make_tab() {
  Tab t{};
  for (int s = 0; s < NST; ++s) { t.ld[s][0] = t.ld[s][1] = t.ep[s] = -1; t.nvm[s] = 0; }
  for (int id = 0; id < 226; ++id) {
    const int s = ld_step(id);
    if (s < 0) continue;
    t.ld[s][t.ld[s][0] >= 0] = (short)id; ++t.nvm[s];
  }
  for (int job = 0; job < 2; ++job)
    for (int k = 0; k < NEP; ++k) {
      const int s = job * GA + ep_step(k);
      t.ep[s] = (short)(job * 64 + k);
      if (k == NEP - 1) t.nvm[s] += 2;
      if (k == (job ? 31 : NEP - 1)) t.nvm[s] += 2;
    }
  ++t.nvm[FINB + 2]; ++t.nvm[FINB + 7];
  return t;
}
constexpr Tab TAB = make_tab();
constexpr int vm_between(int a, int b) {
  int n = 0;
  for (int s = a + 1; s <= b; ++s) n += TAB.nvm[s];
  return n;
}

__global__ void __launch_bounds__(512, 2) k_main(const u16* __restrict__ x, const u16* __restrict__ wqkv, const u16* __restrict__ wo, const u16* __restrict__ wg,
                                                 const u16* __restrict__ wu, const u16* __restrict__ wd, unsigned char* __restrict__ ws,
                                                 u16* __restrict__ out) {
  extern __shared__ __attribute__((aligned(16))) unsigned char smem[];
  LAS unsigned char* L = (LAS unsigned char*)smem;
  const int tid = threadIdx.x, w0 = __builtin_amdgcn_readfirstlane(tid >> 6);
  const int b = blockIdx.x;
  const int sp0 = (b & 6) + ((b >> 3) & 1), c0 = (b >> 4) + 16 * (b & 1);
  const rsrc_t rs = __builtin_amdgcn_make_buffer_rsrc((void*)ws, 0, (int)WS_BYTES, 0x00020000);
  const bool compute = w0 < 4;
  gu32* const fg = (gu32*)(ws + FLAG_G);
  gu32* const tmo = fg + FL_TMO * 32;
  unsigned tag = 0;
  const unsigned long long t_hold = __builtin_amdgcn_s_memrealtime();
#define HOLD(k) do { if (X_AT == (k)) while (__builtin_amdgcn_s_memrealtime() - t_hold < X_HOLD) __builtin_amdgcn_s_sleep(1); } while (0)
  if (((X_XCD >> (b & 7)) & 1) != 0) HOLD(9);
#define FLAG(blk, line) st4(rs, tag, lane ? NONE : 0u, (blk) + (line) * 128)
#define POLL(blk, line, n) poll((gu32*)(ws + (blk)) + ((line) + lane) * 32, lane < (n), tmo, tag)
#define GETTAG() tag = (unsigned)__builtin_amdgcn_readfirstlane(*(LAS unsigned*)(L + LDS_TAG))
  auto blank = [&](int it, unsigned ly) {
    const int lane = lane_id(), sp = sopq(sp0), c = sopq(c0);
    const unsigned o = (unsigned)vopq(-1);
    if (it < 20) st16(rs, (v4u){o, o, o, o}, lane * 16, ly + (it < 16 ? (it & 8 ? L_V : L_K) + ((8 * sp + (it & 7)) * 32 + c) * 1024 : L_X + ((16 * sp + 4 * (c & 1) + (it & 3)) * 16 + (c >> 1)) * 1024));
    else st8(rs, (v2u){o, o}, (lane >> 5) * 4096 + (lane & 31) * 8, ly + L_X + MB2 + (8 * sp + 2 * (c & 1)) * 4096 + (c >> 1) * 256);
  };
  unsigned touch0 = 0;
  v4u xq[32];

  if (compute) {
    const int layer = 0; const unsigned lay0 = LAY_OFF;
    COORDS();
    SETPRIO(2);
    const int r = lane >> 3, j = lane & 7;
    const u16* src = x + (size_t)(256 * sp + 8 * c + r) * D_ + 128 * w + 8 * j;
    v4u a[2] = {*(const v4u*)src, *(const v4u*)(src + 64)};
#pragma unroll
    for (int i = 0; i < 2; ++i) {
      a[i] = nos4(a[i]);
      st16(rs, a[i], (j >> 2) * 1024 + ((j & 3) * 16 + r) * 16, lay + L_X + (16 * sp + n32) * 16384 + (2 * w + i) * 2048 + rh * 128);
      auto quad = [](float v) {
        v += __int_as_float(__builtin_amdgcn_update_dpp(0, __float_as_int(v), 0xb1, 0xf, 0xf, true));
        return v + __int_as_float(__builtin_amdgcn_update_dpp(0, __float_as_int(v), 0x4e, 0xf, 0xf, true));
      };
      float sm = 0.f, q = 0.f;
#pragma unroll
      for (int m = 0; m < 4; ++m) sm += bflo(a[i][m]) + bfhi(a[i][m]);
      sm = quad(sm);
      const float mu = sm * (1.0f / 32.0f);
#pragma unroll
      for (int m = 0; m < 4; ++m) { const float e0 = bflo(a[i][m]) - mu, e1 = bfhi(a[i][m]) - mu; q += e0 * e0 + e1 * e1; }
      q = quad(q);
      st8(rs, (v2u){nos(__float_as_uint(sm)), nos(__float_as_uint(q))}, (j & 3) ? NONE : (j >> 2) * 256 + r * 8,
          lay + L_X + MB2 + (8 * sp + (c >> 2)) * 4096 + (4 * w + 2 * i) * 256 + (c & 3) * 64);
    }
    DRAIN();
    BAR();
    BAR();
    GETTAG();
    const int TB = 8 * sp + 4 + w;
    static_for<NPQ>([&](auto nc) { constexpr int n = nc.value; xq[n] = ld16(rs, lo + ((n >> 1) & 3) * 1024, lay + L_X + TB * 32768 + (n & 1) * 16384 + (n >> 3) * 4096); });
    BARX();
  } else {
    const int layer = 0; const unsigned lay0 = LAY_OFF;
    COORDS();
    SETPRIO(3);
    Retile rt;
    LAS unsigned char* tb = L + LDS_TB + w * 8192;
    const int id = 2 * sp + rh + 16 * (w - 1);
    auto fill0 = [&]() {
#pragma unroll 1
      for (int it = w - 1; it < 16; it += 3) blank(it, lay);
      PIN();
    };
    if (w == 0 || id < 28) {
      const u16* src; int ld, sw; unsigned dst;
      if (w == 0) {
        const int fi = lane >> 3, i8 = lane & 7, F = 6 * sp + (fi < 6 ? fi : 0);
        const int ks = F < 36 ? F / 3 : F < 44 ? (F - 12) >> 1 : F - 32, m = F < 36 ? (F - 3 * ks + 1) % 3 : F < 44 ? 1 + (F & 1) : 0;
        src = wqkv + (size_t)(32 * ks + 8 * (i8 >> 1)) * (3 * D_) + m * D_ + 16 * c + 8 * (i8 & 1);
        ld = 3 * D_; sw = i8; dst = fi < 6 ? WQKV_OFF + c * 49152 + F * 1024 + i8 * 128 : NONE;
      } else {
        const int t = 64 * id + lane, mo = t / 14, ch = t - 14 * mo, mat = mo >> 6, o = mo & 63, c7 = ch >= 7 ? ch - 7 : ch, j = c7 >> 1, kap = o >> 1;
        src = (mat ? wu : wg) + (size_t)(8 * o) * F_ + 112 * n32 + 8 * ch;
        ld = F_;
        const int cell = kap < 16 ? 7 * (kap >> 1) + (j < 3 ? 3 * (kap & 1) + j : 6) : 56 + 16 * j + (j < 3 ? kap - 16 : (kap - 16) >> 1);
        const int rho = j < 3 ? 2 * mat + (c7 & 1) + 4 * (o & 1) : 4 * (kap & 1) + 2 * (o & 1) + mat;
        sw = j < 3 ? rho : 0; dst = WGU_OFF + (2 * n32 + (ch >= 7)) * 114688 + cell * 1024 + rho * 128;
      }
      unsigned ep = 0;
      if (w == 0) ep = __hip_atomic_load(fg + FL_EP * 32, RLX_AGENT);
      rt.load(src, ld, 4);
      if (w) fill0();
      if (w == 0) {
        tag = __builtin_amdgcn_readfirstlane(ep) + 1u;
        if (lane == 0) *(LAS v4u*)(L + LDS_TAG) = (v4u){tag, 0u, 0u, 0u};
        v4u mk[2];
#pragma unroll
        for (int k = 0; k < 8; ++k) {
          const int kq = ((2 * k) & 3) + 8 * ((2 * k) >> 2) + 4 * hh, ql = lane & 31;
          mk[k >> 2][k & 3] = (kq <= ql ? 0xffffu : 0u) | (kq + 1 <= ql ? 0xffff0000u : 0u);
        }
        *(LAS v4u*)(L + LDS_MK + lo) = mk[0];
        *(LAS v4u*)(L + LDS_MK + 1024 + lo) = mk[1];
        if (sp == 0) {
#pragma unroll
          for (int i = 0; i < 9; ++i) *(LAS v4u*)(L + LDS_VL + (i < 8 ? i * 1024 + lo : 8192 + t16 * 16)) = (v4u){0u, 0u, 0u, 0u};
        }
        const unsigned one = (sp == 0 && lane < 16) ? 0u : 0x3f803f80u;
        if (lane < 32) *(LAS v4u*)(L + LDS_VL + lane * VBLK + 512) = (v4u){one, one, one, one};
      }
      rt.lines(tb, lane, sw, dst);
      if (w == 0) rt.out(rs);
    } else if (id < 46) {
      const int slot = (lane >> 2) & 3, m4 = lane & 3, q = 16 * (id < 42 ? id - 28 : id - 42) + 4 * g4 + slot;
      const int cq = q < 128 ? q >> 2 : q < 192 ? (q - 128) >> 1 : q - 192, r = q < 128 ? q & 3 : q < 192 ? 4 + (q & 1) : 6;
      rt.load((id < 42 ? wd + (size_t)(56 * cq + (r < 6 ? 16 * (r >> 1) + 4 * (r & 1) : 48)) * D_ : wo + (size_t)(16 * (q >> 1) + 4 * (q & 1)) * D_) + 32 * n32 + 8 * m4,
              D_, (id < 42 && r == 6) ? 4 : 8);
      fill0();
      rt.put(tb, g4 * 2048, 1024, 4 * m4 + 16 * slot, 0);
      asm volatile("s_waitcnt lgkmcnt(0)" ::: "memory");
#pragma unroll
      for (int i = 0; i < 8; ++i) {
        rt.in[i] = lds16(tb, i * 1024 + lo);
        rt.d[i] = (id < 42 ? WDN_OFF + n32 * 114688 + (id - 28) * 8192 : WO_OFF + n32 * 32768 + (id - 42) * 8192) + i * 1024 + lo;
      }
    } else {
#pragma unroll
      for (int i = 0; i < 8; ++i) { undef(rt.in[i]); rt.d[i] = NONE; }
      fill0();
    }
    if (w == 0) DRAIN();
    HBAR();
    if (w == 0) {
      FLAG(0, FL_XB + sp * 32 + c);
      poll((gu32*)ws + (FL_XB + (lane < 32 ? sp * 32 + lane : (lane & 7) * 32 + c)) * 32, lane < 40, tmo, tag);
    }
    Stats st;
    if (w == 0) {
      st.load(rs, lane, lay + L_X + MB2 + (8 * sp + 4) * 4096);
    }
    HBAR();
    GETTAG();
    if (w) {
      const int l = w - 1;
      dma<4>(rs, L + l * 4096, lo, WQKV_OFF + c * 49152 + l * 4096);
      dma<7>(rs, L + (12 + 7 * l - (l >> 1)) * 1024, lo, WQKV_OFF + c * 49152 + (12 + 7 * l - (l >> 1)) * 1024);
      wait_vm<7>();
    }
    HOLD(0);
    HBAR();
    if (w) { dma<6>(rs, L + (27 + 5 * w) * 1024, lo, WQKV_OFF + c * 49152 + (27 + 5 * w) * 1024);
      rt.out(rs); wait_vm<14>();
    }
    else {
      st.all(L, lane);
      PIN();
      FLAG(0, FL_XA + sp * 32 + c);
      const int i = lane & 31;
      const int line = i < 8 ? FL_KV + (8 * sp + i) * 32 + c : i < 16 ? FL_O + (8 * sp + i - 8) * 32 + c
                     : i < 20 ? FL_X1B + (8 * sp + 4 + 2 * rh + ((i >> 1) & 1)) * 32 + 2 * n32 + (i & 1) : FL_X1A + (i - 20 < 6 ? i - 20 : 5) * 256 + sp * 32 + c;
      touch0 = __builtin_amdgcn_raw_buffer_load_b32(rs, hh * FLAG_LAYER + line * 128, 0, AUX_SC1);
    }
  }

  for (int layer = 0; layer < 2; ++layer) {
    const bool last = layer == 1;
    const unsigned lay0 = LAY_OFF + layer * LAY_BYTES;
    auto ldw = [&](auto nc, unsigned src, int cell) {
      constexpr int N = nc.value, K = (N + 2) / 3;
      const int l = sopq(w0) - 5, c1 = cell + (l * K + K > N ? N - K : l * K);
      dma<K>(rs, L + c1 * 1024, lane_id() * 16, src + c1 * 1024);
    };
    auto ld_gu = [&](int s) { ldw(IC<14>{}, WGU_OFF + sopq(c0) * 114688, 14 * s); };
    auto ld_dn = [&](int s) { ldw(IC<14>{}, WDN_OFF + (sopq(c0) >> 1) * 114688, 14 * s); };
    if (compute) {
      COORDS();
      const int TB = 8 * sp + 4 + w, TA = TB - 4;
      v4u xa[32];
      auto ldx = [&](auto nc, int T) {
        constexpr int n = nc.value;
        return ld16(rs, lo + ((n >> 1) & 3) * 1024, lay + L_X + (2 * T + (n & 1)) * 16384 + (n >> 3) * 4096);
      };
      f32x4 aq[2][2], ak[2][2], av[2][2], r0, r1;
      v4u mf0, mf1; float rd0, rd1;
      unsigned o0, o1, o2, o3;
      const int kpos = (t16 + 16 * hh + 32 * (g4 & 1)) * 16;
      auto epi = [&](int k, int pa) {
        const int tl = pa ? w : 4 + w, st = (pa ? LDS_KL : LDS_ST) + w * 128;
        const unsigned kvb = lay + ((8 * sp + tl) * 32 + c) * 1024;
        if (k == 0) { r0 = lds4f(L, st + g4 * 16); r1 = lds4f(L, st + 64 + g4 * 16); }
        if (k == 2) { o0 = mulpk(ak[pa][0][0], rd0, ak[pa][0][1], rd0); o1 = mulpk(ak[pa][0][2], rd0, ak[pa][0][3], rd0); }
        if (k == 3) { o2 = mulpk(ak[pa][1][0], rd1, ak[pa][1][1], rd1); o3 = mulpk(ak[pa][1][2], rd1, ak[pa][1][3], rd1); }
        if (k == 4) {
          swap32(o0, o2); swap32(o1, o3);
          st16(rs, nos4((v4u){o0, o1, o2, o3}), kpos, kvb + L_K);
          *(LAS v4u*)(L + LDS_KL + (8 + tl) * 1024 + kpos) = (v4u){o0, o1, o2, o3};
        }
        if (k == 5) { o0 = mulpk(av[pa][0][0], r0[0], av[pa][0][1], r0[1]); o1 = mulpk(av[pa][0][2], r0[2], av[pa][0][3], r0[3]); }
        if (k == 6) { o2 = mulpk(av[pa][1][0], r1[0], av[pa][1][1], r1[1]); o3 = mulpk(av[pa][1][2], r1[2], av[pa][1][3], r1[3]); }
        if (k == 7) {
          swap32(o0, o2); swap32(o1, o3);
          st16(rs, nos4((v4u){o0, o1, o2, o3}), lo, kvb + L_V);
          *(LAS v4u*)(L + LDS_VL + (16 + 2 * tl + hh) * VBLK + (lane & 31) * 16) = (v4u){o0, o1, o2, o3};
        }
        if (k == 8) {
          asm volatile("s_nop 7" : "+v"(aq[pa][0]), "+v"(aq[pa][1]));
          const float s0 = rd0 * (0.25f * LOG2E), s1 = rd1 * (0.25f * LOG2E);
          o0 = mulpk(aq[pa][0][0], s0, aq[pa][0][1], s0); o1 = mulpk(aq[pa][0][2], s0, aq[pa][0][3], s0);
          o2 = mulpk(aq[pa][1][0], s1, aq[pa][1][1], s1); o3 = mulpk(aq[pa][1][2], s1, aq[pa][1][3], s1);
          swap32(o0, o2); swap32(o1, o3);
          *(LAS v4u*)(L + LDS_QL + tl * 1024 + kpos) = (v4u){o0, o1, o2, o3};
        }
      };
      pipe<102>(
          [&](auto fc) {
            constexpr int t = fc.value, u = t % 51;
            if constexpr (t == 28) { BARX(); CST(); }
            if constexpr (t == 12 || t == 44 || t == Q3T) BARX();
            if constexpr (u == 44 || u == 45 || u == 50) return csfrag(L, (u == 50 ? 0 : u - 43) * 16 + t16);
            else return lds16(L, (u < 44 ? u : u - 2) * 1024 + lsw);
          },
          [&](auto fc, const v4u& wfr, auto pc) {
            constexpr int t = fc.value, part = pc.value, u = t % 51;
            constexpr int ks = (u < 36 ? u / 3 : u < 44 ? (u - 12) >> 1 : u - 34) & 15, m = u < 36 ? (u % 3 + 1) % 3 : u < 46 ? 1 + (u & 1) : 0;
            constexpr bool pa = t >= 51, cs = u == 44 || u == 45 || u == 50;
            if constexpr (t < 0) return;
            else if constexpr (part == 0) {
              if constexpr (pa && u < 44 && u % 12 == 0) {
                constexpr int n = u / 12 * 8;
                if (__builtin_expect(missing<8>(xa + n), 0)) {
                  unsigned spins = 0;
                  do { static_for<8>([&](auto i) { xa[n + i.value] = ldx(IC<n + i.value>{}, TA); }); DBGC(0); } while (missing<8>(xa + n) && ++spins < DATA_CAP);
                }
                PIN();
              }
              const v4u& x0 = cs ? mf0 : pa ? xa[2 * ks] : xq[2 * ks];
              const v4u& x1 = cs ? mf1 : pa ? xa[2 * ks + 1] : xq[2 * ks + 1];
              if constexpr (m == 0) { aq[pa][0] = mfma16(wfr, x0, u < 3 ? zero4() : aq[pa][0]); aq[pa][1] = mfma16(wfr, x1, u < 3 ? zero4() : aq[pa][1]); }
              if constexpr (m == 1) { ak[pa][0] = mfma16(wfr, x0, u < 3 ? zero4() : ak[pa][0]); ak[pa][1] = mfma16(wfr, x1, u < 3 ? zero4() : ak[pa][1]); }
              if constexpr (m == 2) { av[pa][0] = mfma16(x0, wfr, u < 3 ? zero4() : av[pa][0]); av[pa][1] = mfma16(x1, wfr, u < 3 ? zero4() : av[pa][1]); }
            } else {
              if constexpr (2 * t < 32 - NPQ) { xq[NPQ + 2 * t] = ldx(IC<NPQ + 2 * t>{}, TB); xq[NPQ + 2 * t + 1] = ldx(IC<NPQ + 2 * t + 1>{}, TB); }
              if constexpr (u == 40) {
                const int st = (pa ? LDS_KL : LDS_ST) + w * 128;
                rd0 = lds1f(L, st + t16 * 4); rd1 = lds1f(L, st + 64 + t16 * 4);
                const int a = g4 ? LDS_TAG + 8 : st + 1024 + t16 * 4;
                mf0 = mfrag(L, a); mf1 = mfrag(L, g4 ? a : a + 64);
              }
              if constexpr (t >= 45 && t < 54) epi(t - 45, 0);
              if constexpr (t >= 96) epi(t - 96, 1);
              static_for<16>([&](auto j) { if constexpr (xa_step(j.value) == t) { xa[2 * j.value] = ldx(IC<2 * j.value>{}, TA); xa[2 * j.value + 1] = ldx(IC<2 * j.value + 1>{}, TA); } });
              if constexpr (t == 50) CST();
            }
          });
      epi(6, 1); epi(7, 1); epi(8, 1);
      BAR();
      CST();
    } else {
      COORDS();
      const bool loader = w != 0;
      auto cs_qkv = [&]() {
        const unsigned one = (unsigned)vopq(0x3f803f80);
        const v4u ones = (v4u){one, one, one, one};
        f32x4 a = zero4();
        v4u f[16];
        const int fa = (w - 1) * 1024 + lsw, fb = (w < 3 ? 35 + w : 44) * 1024 + lsw, fs = w < 3 ? 2048 : 1024;
#pragma unroll
        for (int ks = 0; ks < 16; ++ks) asm volatile("ds_read_b128 %0, %1" : "=v"(f[ks]) : "v"(ks < 12 ? fa + ks * 3072 : fb + (ks - 12) * fs) : "memory");
        asm volatile("s_waitcnt lgkmcnt(0)" : "+v"(f[0]), "+v"(f[1]), "+v"(f[2]), "+v"(f[3]), "+v"(f[4]), "+v"(f[5]), "+v"(f[6]), "+v"(f[7]));
        asm volatile("" : "+v"(f[8]), "+v"(f[9]), "+v"(f[10]), "+v"(f[11]), "+v"(f[12]), "+v"(f[13]), "+v"(f[14]), "+v"(f[15]));
#pragma unroll
        for (int ks = 0; ks < 16; ++ks) a = mfma16(f[ks], ones, a);
        if (t16 == 0) {
#pragma unroll
          for (int r = 0; r < 4; ++r) cs_put(L, (w % 3) * 16 + 4 * g4 + r, a[r]);
        }
      };
      const unsigned wob = WO_OFF + n32 * 32768 - LDS_WO;
      if (last && !loader) {
        v4u o[4];
#pragma unroll
        for (int i = 0; i < 4; ++i) o[i] = lds16(L, LDS_QL + i * 1024 + lo);
        const v2u sq = *(LAS v2u*)(L + LDS_QL + 4096 + lane * 8);
#pragma unroll
        for (int i = 0; i < 4; ++i) st16(rs, o[i], lo, lay + L_X + ((16 * sp + 4 * rh + i) * 16 + n32) * 1024);
        st8(rs, sq, hh * 4096 + (g4 & 1) * 128 + t16 * 8, lay + L_X + MB2 + (8 * sp + 2 * rh) * 4096 + n32 * 256);
        PIN();
      }
      HBARS(false);
      if (layer == 0) {
        if (loader) wait_vm<8>();
        HBAR();
        if (loader) cs_qkv();
        HBAR();
      } else {
        if (loader) { ldw(IC<10>{}, wob, 80); wait_vm<13>(); }
        HBAR();
        if (loader) ldw(IC<10>{}, wob, 90);
        HBAR();
        if (loader) ldw(IC<12>{}, wob, 100);
      }
      {
        v4u kv[4];
        auto ldkv = [&]() {
          const unsigned so = lay + ((8 * sp - 5 + w) * 32 + c) * 1024, s2 = w == 1 ? so + 3 * 32768 : NONE;
          kv[0] = ld16(rs, lo, so + L_K); kv[1] = ld16(rs, lo, so + L_V); kv[2] = ld16(rs, lo, s2 + L_K); kv[3] = ld16(rs, lo, s2 + L_V);
          PIN();
          DBGC(0);
        };
        if (!loader) {
          Stats st;
          gu32* const bl = (gu32*)ws + (FL_XA + (sp ? sp - 1 : 0) * 32 + c) * 32;
          unsigned spins = 0, pv = tag;
          if (!last) pv = __hip_atomic_load(bl, RLX_AGENT);
          do { st.load(rs, lane, lay + L_X + MB2 + 8 * sp * 4096); DBGC(0); } while (missing<16>((const v4u*)st.p) && ++spins < DATA_CAP);
          st.all(L, lane, LDS_KL);
          if (!__all(pv == tag)) poll(bl, lane == 0, tmo, tag);
        }
        DRAIN();
        HBARS(false);
        if (loader && sp > 0) {
          unsigned spins = 0;
          do ldkv(); while (missing<4>(kv) && ++spins < DATA_CAP);
          *(LAS v4u*)(L + LDS_KL + (3 + w) * 1024 + lo) = kv[0];
          *(LAS v4u*)(L + LDS_VL + (6 + 2 * w + hh) * VBLK + (lane & 31) * 16) = kv[1];
          if (w == 1) {
            *(LAS v4u*)(L + LDS_KL + 7 * 1024 + lo) = kv[2];
            *(LAS v4u*)(L + LDS_VL + (14 + hh) * VBLK + (lane & 31) * 16) = kv[3];
          }
        }
      }
      if (layer == 0 && !loader) { asm volatile("" ::"v"(touch0)); FLAG(FLAG_G, FL_WR + n32 * 16 + 2 * sp + rh); }
      if (!last) HOLD(1);
      DRAIN();
      HBAR();
      CST(); CST();
      if (loader) {
        if (layer == 0) {
          POLL(FLAG_G, FL_WR + n32 * 16, 16);
          ldw(IC<10>{}, wob, 80); ldw(IC<10>{}, wob, 90); ldw(IC<12>{}, wob, 100);
          ld_gu(0);
          wait_vm<5>();
          if (lane == 0) asm volatile("ds_add_u32 %0, %1" ::"v"(LDS_TAG + 12), "v"(1) : "memory");
        } else ld_gu(0);
        ld_gu(1); ld_gu(2); ldw(IC<6>{}, WGU_OFF + c * 114688, 42);
      }
      if (sp > 0) {
        LAS unsigned* hw = (LAS unsigned*)(L + LDS_TAG + 4);
        CST();
        if (!loader) {
          v4u kv[8];
          unsigned spins = 0;
          do {
#pragma unroll
            for (int t = 0; t < 4; ++t) {
              const unsigned so = lay + ((8 * sp - 8 + t) * 32 + c) * 1024;
              kv[t] = ld16(rs, lo, so + L_K); kv[4 + t] = ld16(rs, lo, so + L_V);
            }
            PIN();
            DBGC(1);
          } while (missing<8>(kv) && ++spins < DATA_CAP);
#pragma unroll
          for (int t = 0; t < 4; ++t) {
            *(LAS v4u*)(L + LDS_KL + t * 1024 + lo) = kv[t];
            *(LAS v4u*)(L + LDS_VL + (2 * t + hh) * VBLK + (lane & 31) * 16) = kv[4 + t];
          }
          asm volatile("s_waitcnt lgkmcnt(0)" ::: "memory");
          __hip_atomic_store(hw, (unsigned)layer + 1u, __ATOMIC_RELAXED, __HIP_MEMORY_SCOPE_WORKGROUP);
        } else {
          unsigned v;
          do {
            __builtin_amdgcn_s_sleep(1);
            asm volatile("ds_read_b32 %0, %1\n\ts_waitcnt lgkmcnt(0)" : "=v"(v) : "v"(LDS_TAG + 4) : "memory");
          } while ((unsigned)__builtin_amdgcn_readfirstlane(v) != (unsigned)layer + 1u);
        }
      } else CST();
      CST();
      if (layer == 0 && !loader) touch0 = __builtin_amdgcn_raw_buffer_load_b32(rs, LAY_OFF + L_O + lane * TSTR + c * 4096 + sp * 128, 0, AUX_SC1);
      SETPRIO(1);
    }
    {
      COORDS();
      const int tl = compute ? 4 + w : w, T = 8 * sp + tl;
      const v4u o = attn_unit(L, lane, tl, sp == 0 ? 8 - tl : 0);
      st16(rs, o, (g4 & 1) * 16384 + hh * 256 + t16 * 16, lay + L_O + T * 32768 + c * 512);
      DRAIN();
      FLAG(flo, FL_O + T * 32 + c);
      CST();
    }
    if (compute) {
      SETPRIO(0);
      v4u fr[176];
      {
        COORDS();
        const int TB = 8 * sp + 4 + 2 * rh + (w >> 1), SB = 2 * TB + (w & 1);
        const v4u rB = ld16(rs, lo, lay + L_X + (SB * 16 + n32) * 1024), rA = ld16(rs, lo, lay + L_X + (SB * 16 + n32) * 1024 - 8 * 16384);
        const unsigned xv = (g4 >> 1) * 1024 + (g4 & 1) * 512 + t16 * 16, sv = g4 ? NONE : t16 * 8;
        const unsigned xo = lay + L_X1 + (TB * 32 + 2 * n32) * 1024 + (w & 1) * 256, so = lay + L_X1 + MB2 + TB * 4096 + n32 * 256 + (w & 1) * 128;
        const int lswo = vopq(lsw + LDS_WO);
        while ((unsigned)__builtin_amdgcn_readfirstlane(__hip_atomic_load((LAS unsigned*)(L + LDS_TAG + 12), __ATOMIC_RELAXED, __HIP_MEMORY_SCOPE_WORKGROUP)) != 3u)
          __builtin_amdgcn_s_sleep(1);
        asm volatile("" ::: "memory");
        v4u of[16];
        unsigned pk;
        f32x4 a0, a1;
        Fin fin;
        pipe<64>(
            [&](auto fc) { return lds16(L, (fc.value & 31) * 1024 + lswo); },
            [&](auto fc, const v4u& wfr, auto pc) {
              constexpr int t = fc.value, part = pc.value, u = t & 31;
              if constexpr (part == 0) {
                if constexpr (u & 1) a1 = mfma16(wfr, of[u >> 1], u == 1 ? zero4() : a1);
                else a0 = mfma16(wfr, of[u >> 1], u == 0 ? zero4() : a0);
              } else {
                if constexpr (t == 31 || t == 63) {
                  asm volatile("s_nop 7" : "+v"(a0), "+v"(a1));
                  static_for<8>([&](auto k) { fin.template chunk<k.value>(a0, a1, t == 31 ? rB : rA, rs, xv, xo - (t == 31 ? 0 : 4 * 32768), rs, sv, so - (t == 31 ? 0 : 4 * 4096)); });
                  CST();
                }
                if constexpr (t == -1 || t == 31) {
                  if constexpr (t == 31) {
                    pk = __hip_atomic_load(fl + (FL_O + (TB - 4) * 32 + (lane & 31)) * 32, RLX_AGENT);
                    DRAIN();
                    FLAG(flo, FL_X1B + TB * 32 + 2 * n32 + (w & 1));
                  }
                  if (t == -1 || !__all(pk == tag)) POLL(flo, FL_O + (TB - (t == 31 ? 4 : 0)) * 32, 32);
                  CST();
                  static_for<16>([&](auto i) { of[i.value] = ld16(rs, lo + (i.value & 3) * 1024, lay + L_O + (SB - (t == 31 ? 8 : 0)) * 16384 + (i.value >> 2) * 4096); });
                }
                if constexpr (t == 55) pk = __hip_atomic_load(fl + (FL_X1B + (8 * sp + 4 + w) * 32 + (lane & 31)) * 32, RLX_AGENT);
                if constexpr (t == 63) {
                  const int T = 8 * sp + 4 + w;
                  if (!__all(pk == tag)) POLL(flo, FL_X1B + T * 32, 32);
                  CST();
                  static_for<NPG>([&](auto i) { fr[i.value] = ld16(rs, lo + (i.value & 3) * 1024, lay + L_X1 + T * 32768 + (i.value >> 2) * 4096); });
                }
              }
            });
      }
      SETPRIO(2);
      BAR();
      {
        COORDS();
        const int TB = 8 * sp + 4 + w;
        const int TD = 8 * sp + 4 + 2 * rh + (w >> 1), SD = 2 * TD + (w & 1);
        const unsigned x1b = lay + L_X1 + TB * 32768, hb = lay + L_H + SD * 57344, xn = last ? NONE : lay + LAY_BYTES + L_X;
        const unsigned hsv = (g4 & 1) * 57344 + hh * 256 + t16 * 16, hso = lay + L_H + TB * 114688;
        const rsrc_t ro = last ? __builtin_amdgcn_make_buffer_rsrc((void*)out, 0, (int)MB2, 0x00020000) : rs;
        const unsigned ob = last ? SD * 16384 + n32 * 64 : xn + (SD * 16 + n32) * 1024;
        const unsigned sb = xn + MB2 + TD * 4096 + n32 * 256 + (w & 1) * 128;
        unsigned ov, sv;
        const int lsw2 = vopq(lsw + 65536), lh = (hh * 16 + t16) * 16, lh2 = vopq(lh + 65536);
        f32x16 acc[2][4];
        f32x4 d0[2], d1[2];
        v4u mf, hv[4], res[2];
        float k1[2], den2[2], t0, t1, p0_, p1_;
        Fin fin;
        auto epi = [&](auto ec) {
          constexpr int job = ec.value >> 6, k = ec.value & 63, j = k >> 4, i = (k >> 2) & 3, uo = j < 3 ? 8 : 4;
          const f32x16& a = acc[job][j];
          if constexpr ((k & 3) == 0)
            asm volatile("v_mul_f32 %0, %2, %4\n\tv_mul_f32 %1, %3, %4\n\tv_exp_f32 %0, %0" : "=&v"(t0), "=&v"(t1) : "v"(a[2 * i]), "v"(a[2 * i + 1]), "v"(k1[job]));
          else if constexpr ((k & 3) == 1)
            asm volatile("v_exp_f32 %0, %0\n\tv_mul_f32 %1, %3, %5\n\tv_mul_f32 %2, %4, %6"
                : "+v"(t1), "=&v"(p0_), "=&v"(p1_) : "v"(a[2 * i]), "v"(a[2 * i + 1]), "v"(a[uo + 2 * i]), "v"(a[uo + 2 * i + 1]));
          else if constexpr ((k & 3) == 2)
            asm volatile("v_fma_f32 %0, %0, %2, %2\n\tv_fma_f32 %1, %1, %2, %2\n\tv_rcp_f32 %0, %0" : "+v"(t0), "+v"(t1) : "v"(den2[job]));
          else {
            unsigned d;
            asm volatile("v_rcp_f32 %2, %2\n\tv_mul_f32 %1, %1, %3\n\tv_mul_f32 %2, %2, %4\n\tv_cvt_pk_bf16_f32 %0, %1, %2" : "=v"(d), "+v"(t0), "+v"(t1) : "v"(p0_), "v"(p1_));
            hv[j][i] = d;
            if constexpr (k == (job ? 31 : NEP - 1)) { st16(rs, hv[0], hsv, hso - job * 4 * 114688 + c * 1024); st16(rs, hv[1], hsv + 512, hso - job * 4 * 114688 + c * 1024); }
            if constexpr (k == NEP - 1) {
              st16(rs, hv[2], hsv, hso - job * 4 * 114688 + 32768 + c * 512);
              st8(rs, (v2u){hv[3][0], hv[3][1]}, hsv - hh * 248, hso - job * 4 * 114688 + 49152 + c * 256);
            }
          }
        };
        auto vload = [&](auto ic) {
          constexpr int id = ic.value;
          if constexpr (id < 64) fr[id] = ld16(rs, lo + (id & 3) * 1024, x1b - (id >> 5) * 4 * 32768 + ((id & 31) >> 2) * 4096);
          else if constexpr (id < 176) {
            constexpr int n = (id - 64) % 56;
            fr[id] = ld16(rs, lo + (n & 3) * 1024, hb - (id >= 120 ? 8 * 57344 : 0) + (n >> 2) * 4096);
          } else if constexpr (id < 208) {
            constexpr int n = id - 176;
            xq[n] = ld16(rs, lo + ((n >> 1) & 3) * 1024, xn + TB * 32768 + (n & 1) * 16384 + (n >> 3) * 4096);
          } else res[id - 224] = ld16(rs, vopq(lo & 0x2f0) + ((lo & 256) << 1) + (lo & 512), lay + L_X1 + ((TD - 4 * (id - 224)) * 32 + 2 * n32) * 1024 + (w & 1) * 256);
        };
        pipe<NST>(
            [&](auto fc) {
              constexpr int t = fc.value;
              if constexpr (t == 32) wait_vm<vm_between(-1, t - LA - 1) + NPG>();
              if constexpr (t == GA + 48) wait_vm<vm_between(ep_step(NEP - 1), t - LA - 1)>();
              if constexpr (t == DB + 14) wait_vm<vm_between(GA + ep_step(31), t - LA - 1)>();
              if constexpr (t == DB + 56) wait_vm<vm_between(GA + ep_step(NEP - 1), t - LA - 1)>();
              if constexpr (t == DA + 42) wait_vm<vm_between(FINB + 7, t - LA - 1)>();
              if constexpr (t < DB) {
                constexpr GS s = gu_step(t % GA);
                if constexpr (s.cs) return csfrag(L, 48 + 32 * s.j + (s.j < 3 ? lane & 31 : t16));
                else {
                  if (s.off % 14336 == 0 && s.off >= (t < GA ? 14336 : 28672)) BARX();
                  return lds16(L, s.off < 65536 ? s.off + (s.j < 3 ? lsw : lh) : s.off - 65536 + (s.j < 3 ? lsw2 : lh2));
                }
              } else {
                constexpr int v = (t - DB) % 112;
                if (v % 14 == 0 && v >= (t < DA ? 0 : 28)) BARX();
                return lds16(L, v < 64 ? v * 1024 + lsw : (v - 64) * 1024 + lsw2);
              }
            },
            [&](auto fc, const v4u& wfr, auto pc) {
              constexpr int t = fc.value, part = pc.value;
              if constexpr (t < 0) return;
              else if constexpr (part == 0) {
                if constexpr (t < DB) {
                  constexpr int job = t / GA, u = t % GA;
                  constexpr GS s = gu_step(u);
                  acc[job][s.j] = mfma(wfr, s.cs ? mf : fr[32 * job + (s.kap & 31)], u < 4 ? splat16(0.f) : acc[job][s.j]);
                } else {
                  constexpr int job = t >= DA, v = (t - DB) % 112;
                  if constexpr (v & 1) d1[job] = mfma16(wfr, fr[64 + 56 * job + (v >> 1)], v == 1 ? zero4() : d1[job]);
                  else d0[job] = mfma16(wfr, fr[64 + 56 * job + (v >> 1)], v == 0 ? zero4() : d0[job]);
                }
              } else {
                if constexpr (t < DB) {
                  constexpr int job = t / GA, u = t % GA;
                  if constexpr (u == 74) {
                    const int a = LDS_ST + w * 128 + (lane & 31) * 4;
                    k1[job] = lds1f(L, a); den2[job] = lds1f(L, a + 512); mf = mfrag(L, hh ? LDS_TAG + 8 : a + 1024);
                  }
                  if constexpr (u == 79) k1[job] *= -LOG2E;
                }
                if constexpr (TAB.ld[t][0] >= 0) vload(IC<TAB.ld[t][0]>{});
                if constexpr (TAB.ld[t][1] >= 0) vload(IC<TAB.ld[t][1]>{});
                if constexpr (TAB.ep[t] >= 0) epi(IC<TAB.ep[t]>{});
                if constexpr (t == FINB - 1) { const int l = lane_id(); ov = l * 16 + ((((l & 15) * 1008) - (l >> 4) * 240) & sopq(-layer)); sv = l >= 16 ? NONE : l * 8; }
                if constexpr (t >= FINB && t < FINB + 8) fin.template chunk<t - FINB>(d0[0], d1[0], res[0], ro, ov, ob, rs, sv, sb);
              }
            });
        CST();
        asm volatile("s_nop 7" : "+v"(d0[1]), "+v"(d1[1]));
        static_for<8>([&](auto k) { fin.template chunk<k.value, false>(d0[1], d1[1], res[1], ro, ov, ob - 8 * 16384, rs, sv, sb - 4 * 4096); });
        if (last) st16(ro, fin.o, ov, ob - 8 * 16384);
        else {
          *(LAS v4u*)(L + LDS_QL + w * 1024 + lo) = fin.o;
          if (lane < 16) *(LAS v2u*)(L + LDS_QL + 4096 + w * 128 + lane * 8) = fin.sq;
          BAR();
        }
      }
    } else {
      COORDS();
      const bool poller = w == 0, loader = w != 0;
      unsigned pv = 0;
#define PEEK(blk, line) pv = __hip_atomic_load((gu32*)(ws + (blk)) + ((line) + (lane & 31)) * 32, RLX_AGENT)
#define GATE(blk, line) do { if (!__all(pv == tag)) POLL(blk, line, 32); } while (0)
      if (layer == 0) {
        if (loader) { ldw(IC<8>{}, WGU_OFF + c * 114688, 48); ld_gu(4); ldw(IC<10>{}, WGU_OFF + c * 114688, 70); }
        f32x4 ca = zero4();
        const unsigned one = (unsigned)vopq((w == 3 || t16 == (g4 & 1)) ? 0x3f803f80 : 0), wb = WGU_OFF + c * 114688;
        for (int q = 0; q < (w < 3 ? 2 : 1); ++q) {
          v4u f[16];
          static_for<16>([&](auto ic) {
            constexpr int i = ic.value;
            f[i] = ld16(rs, w < 3 ? lsw : lo, wb + (w < 3 ? (q ? 56 + 16 * w + i : 7 * (i >> 1) + 3 * (i & 1) + w) : i < 8 ? 7 * i + 6 : 96 + i) * 1024);
          });
          PIN();
          static_for<16>([&](auto i) { ca = mfma16(f[i.value], (v4u){one, one, one, one}, ca); });
        }
        if (t16 < (w < 3 ? 2 : 1)) {
#pragma unroll
          for (int r = 0; r < 4; ++r) cs_put(L, 48 + 32 * w + 16 * t16 + 4 * g4 + r, ca[r]);
        }
      }
      SETPRIO(3);
      if (poller) {
        asm volatile("" ::"v"(touch0));
        Stats st;
        unsigned ev[4];
        const int h2l = lane < 32 ? FL_HA2 + sp * 32 + lane : FL_HB + (sp ? sp - 1 : 0) * 32 + c;
        SETPRIO(1);
        HBAR();
        static_for<7>([&](auto nc) {
          constexpr int n = nc.value + 1;
          if (n <= 2) st.load(rs, lane, lay + L_X1 + MB2 + (8 * sp + 4) * 4096, 8 * n - 8, 8 * n);
          if (n == 3) { FLAG(flo, FL_X1A + sp * 32 + c); st.part1(); }
          if (n == 4) st.part2();
          if (n == 5) st.part3(L, lane);
          if (n == 6) PEEK(flo, FL_X1A + sp * 32);
          if (n == 7) GATE(flo, FL_X1A + sp * 32);
          HBARS(n == 3 || n == 7);
        });
        st.load(rs, lane, lay + L_X1 + MB2 + 8 * sp * 4096);
        st.all(L, lane);
        static_for<6>([&](auto nc) {
          constexpr int n = nc.value + 2;
          if (n == 4) FLAG(flo, FL_HB + sp * 32 + c);
          if (n == 6) PEEK(flo, FL_HB + sp * 32);
          if (n == 7) GATE(flo, FL_HB + sp * 32);
          HBARS(n == 2 || n == 7);
        });
        static_for<8>([&](auto nc) {
          constexpr int n = nc.value;
          if (n == 2) FLAG(flo, FL_HA + sp * 32 + c);
          if (n == 5) { FLAG(flo, FL_HA2 + sp * 32 + c); PEEK(flo, FL_HA + sp * 32); }
          if (n == 6) GATE(flo, FL_HA + sp * 32);
          if (n == 7) pv = __hip_atomic_load(fl + h2l * 32, RLX_AGENT);
          HBARS(n == 0 || n == 3 || n == 6);
        });
        static_for<6>([&](auto nc) {
          constexpr int n = nc.value + 2;
          if (n == 2) { if (!__all(pv == tag)) poll(fl + h2l * 32, true, tmo, tag); }
          if (n == 3) {
#pragma unroll
            for (int q = 0; q < 4; ++q) ev[q] = __builtin_amdgcn_raw_buffer_load_b32(rs, lane * 128, last && blockIdx.x == 0 ? flo + (FL_HB + 64 * q) * 128 : NONE, AUX_SC1);
          }
          if (n == 4 && !last) FLAG(FLAG_LAYER, FL_XB + sp * 32 + c);
          if (n == 5) PEEK(FLAG_LAYER, FL_XB + sp * 32);
          if (n == 6) GATE(FLAG_LAYER, FL_XB + sp * 32);
          if (n == 7) st.load(rs, lane, last ? NONE : LAY_OFF + LAY_BYTES + L_X + MB2 + (8 * sp + 4) * 4096);
          HBARS(n == 2 || n == 6 || n == 7);
        });
        if (!last) { st.all(L, lane); }
        if (last && blockIdx.x == 0) {
          if (!__all(ev[0] == tag && ev[1] == tag && ev[2] == tag && ev[3] == tag))
            for (int q = 0; q < 4; ++q) POLL(flo, FL_HB + 64 * q, 64);
          if (lane == 0) __hip_atomic_store(fg + FL_EP * 32, tag, RLX_AGENT);
        }
      } else {
        const unsigned wq = WQKV_OFF + c * 49152, wb = WGU_OFF + c * 114688;
        DRAIN();
        HBAR();
        static_for<7>([&](auto nc) {
          constexpr int n = nc.value + 1;
          if (n == 7) wait_vm<1>();
          blank(3 * n - 4 + w, last ? NONE : LAY_OFF + LAY_BYTES);
          if (n >= 2 && n <= 4) ldw(IC<9>{}, wb, 62 + 9 * n);
          if (n == 5) { ldw(IC<5>{}, wb, 107); wait_vm<11>(); }
          if (n == 6) wait_vm<8>();
          HBARS(n == 3 || n == 7);
        });
        static_for<6>([&](auto nc) {
          constexpr int n = nc.value + 2;
          if (n == 3) wait_vm<0>();
          if (n == 5 || n == 6) { ld_dn(2 * n - 10); ld_dn(2 * n - 9); } else if (n > 6) ld_dn(n - 3);
          HBARS(n == 2 || n == 7);
        });
        static_for<8>([&](auto nc) {
          constexpr int n = nc.value;
          if (n <= 2) ld_dn(n + 5);
          wait_vm<(n <= 2 ? 25 : 35 - 5 * n)>();
          HBARS(n == 0 || n == 3 || n == 6);
        });
        static_for<6>([&](auto nc) {
          constexpr int n = nc.value + 2;
          if (!last) {
            if (n >= 3 && n <= 5) ldw(IC<14>{}, wq, 14 * (n - 3));
            if (n == 6) { ldw(IC<6>{}, wq, 42); ldw(IC<8>{}, wb, 48); }
            if (n == 7) ldw(IC<14>{}, wb, 56);
          }
          HBARS(n == 2 || n == 6 || n == 7);
        });
        if (!last) { ldw(IC<10>{}, wb, 70); wait_vm<14>(); }
      }
      SETPRIO(3);
      if (!last) {
        HOLD(2);
        HBAR();
      }
      static_for<NPQ>([&](auto i) { undef(xq[i.value]); });
    }
  }
}

}

extern "C" void kernel_launch(void* const* d_in, const int* in_sizes, int n_in, void* d_out, int out_size, void* d_ws, size_t ws_size,
                              hipStream_t stream) {
  (void)in_sizes; (void)n_in; (void)out_size; (void)ws_size;
  (void)hipFuncSetAttribute((const void*)k_main, hipFuncAttributeMaxDynamicSharedMemorySize, LDS_BYTES);
  k_main<<<256, 512, LDS_BYTES, stream>>>((const u16*)d_in[0], (const u16*)d_in[1], (const u16*)d_in[2], (const u16*)d_in[4], (const u16*)d_in[5],
                                          (const u16*)d_in[6], (unsigned char*)d_ws, (u16*)d_out);
}
```

```cpp
#define X_AT 3
#define X_HOLD 6400
#include <hip/hip_runtime.h>
#include <stdint.h>
#include <utility>

#define LAS __attribute__((address_space(3)))
#define GAS __attribute__((address_space(1)))
#define DI __device__ __forceinline__
#define RLX_AGENT __ATOMIC_RELAXED, __HIP_MEMORY_SCOPE_AGENT
typedef unsigned short u16;
typedef unsigned v4u __attribute__((ext_vector_type(4)));
typedef unsigned v2u __attribute__((ext_vector_type(2)));
typedef float f32x2 __attribute__((ext_vector_type(2)));
typedef float f32x4 __attribute__((ext_vector_type(4)));
typedef float f32x16 __attribute__((ext_vector_type(16)));
typedef __bf16 bf16x2 __attribute__((ext_vector_type(2)));
typedef __bf16 bf16x8 __attribute__((ext_vector_type(8)));
typedef GAS unsigned gu32;
typedef GAS unsigned long long gu64;

namespace {

constexpr int D_ = 512, F_ = 1792;
constexpr unsigned MB2 = 2097152u, STB = 262144u;
constexpr unsigned FLAG_LAYER = 1048576u, FLAG_G = 2 * FLAG_LAYER, FLAG_BYTES = FLAG_G + 65536;
constexpr unsigned WQKV_OFF = FLAG_BYTES;
constexpr unsigned WO_OFF = WQKV_OFF + 32 * 49152;
constexpr unsigned WGU_OFF = WO_OFF + 16 * 32768;
constexpr unsigned WDN_OFF = WGU_OFF + 32 * 114688;
constexpr unsigned LAY_OFF = WDN_OFF + 16 * 114688;
constexpr unsigned L_K = 0, L_V = MB2, L_O = 2 * MB2, L_X1 = 3 * MB2, L_X = 4 * MB2 + STB, L_H = 5 * MB2 + 2 * STB;
constexpr unsigned LAY_BYTES = L_H + 128 * 57344;
constexpr unsigned WS_BYTES = LAY_OFF + 2 * LAY_BYTES;
constexpr unsigned NONE = 0x40000000u;
constexpr int FL_KV = 0, FL_O = 2048, FL_X1B = 4096, FL_X1A = 6144, FL_HB = 6400, FL_HA = 6656, FL_XB = 6912, FL_XA = 7168, FL_HA2 = 7424;
constexpr int FL_WR = 0, FL_EP = 256, FL_TMO = 257;
constexpr int VBLK = 528;
constexpr int LDS_KL = 114688, LDS_VL = LDS_KL + 16384, LDS_QL = LDS_VL + 32 * VBLK, LDS_CS = LDS_QL + 8192, LDS_ST = LDS_CS + 1536;
constexpr int LDS_MK = LDS_ST + 1536, LDS_TAG = LDS_MK + 2048;
constexpr int LDS_WO = 81920, LDS_TB = 49152;
constexpr int LDS_BYTES = 163840;
#define TSTR 1048576u
constexpr unsigned SPIN_CAP = 1u << 16;
constexpr int AUX_SC1 = 16;
constexpr float LOG2E = 1.4426950408889634f;

DI f32x16 mfma(v4u a, v4u b, f32x16 c) {
  return __builtin_amdgcn_mfma_f32_32x32x16_bf16(__builtin_bit_cast(bf16x8, a), __builtin_bit_cast(bf16x8, b), c, 0, 0, 0);
}
DI f32x4 mfma16(v4u a, v4u b, f32x4 c) {
  return __builtin_amdgcn_mfma_f32_16x16x32_bf16(__builtin_bit_cast(bf16x8, a), __builtin_bit_cast(bf16x8, b), c, 0, 0, 0);
}
DI unsigned pk2(float lo, float hi) {
  f32x2 p = {lo, hi};
  return __builtin_bit_cast(unsigned, __builtin_convertvector(p, bf16x2));
}
DI float bfr(float x) { return __uint_as_float(pk2(x, 0.f) << 16); }
DI float bflo(unsigned u) { return __uint_as_float(u << 16); }
DI float bfhi(unsigned u) { return __uint_as_float(u & 0xffff0000u); }
DI float amul(float a, float b) { float d; asm("v_mul_f32 %0, %1, %2" : "=v"(d) : "v"(a), "v"(b)); return d; }
DI float afma(float a, float b, float c) { float d; asm("v_fma_f32 %0, %1, %2, %3" : "=v"(d) : "v"(a), "v"(b), "v"(c)); return d; }
DI float aadd(float a, float b) { float d; asm("v_add_f32 %0, %1, %2" : "=v"(d) : "v"(a), "v"(b)); return d; }
DI float asub(float a, float b) { float d; asm("v_sub_f32 %0, %1, %2" : "=v"(d) : "v"(a), "v"(b)); return d; }
DI float xhalf_sum(float v) {
  float t;
  asm("v_mov_b32 %1, %0\n\ts_nop 1\n\tv_permlane32_swap_b32 %0, %1\n\tv_add_f32 %0, %0, %1" : "+v"(v), "=&v"(t));
  return v;
}
DI float xrow_sum(float v) {
  float t;
  asm("v_mov_b32 %1, %0\n\ts_nop 1\n\tv_permlane16_swap_b32 %0, %1\n\tv_add_f32 %0, %0, %1" : "+v"(v), "=&v"(t));
  return v;
}
DI float xhalf_max(float v) {
  auto r = __builtin_amdgcn_permlane32_swap(__float_as_uint(v), __float_as_uint(v), false, false);
  return fmaxf(__uint_as_float(r[0]), __uint_as_float(r[1]));
}
DI void swap16(unsigned& a, unsigned& b) { asm("s_nop 1\n\tv_permlane16_swap_b32 %0, %1" : "+v"(a), "+v"(b)); }
DI void swap32(unsigned& a, unsigned& b) { asm("s_nop 1\n\tv_permlane32_swap_b32 %0, %1" : "+v"(a), "+v"(b)); }
DI f32x16 splat16(float v) {
  f32x16 z;
#pragma unroll
  for (int i = 0; i < 16; ++i) z[i] = v;
  return z;
}
DI f32x4 zero4() { return (f32x4){0.f, 0.f, 0.f, 0.f}; }
DI int vopq(int x) { asm volatile("" : "+v"(x)); return x; }
DI int lane_id() { int l; asm volatile("v_mbcnt_lo_u32_b32 %0, -1, 0\n\tv_mbcnt_hi_u32_b32 %0, -1, %0" : "=v"(l)); return l; }
template <int LN> DI unsigned wrl(unsigned v, unsigned s) { asm volatile("v_writelane_b32 %0, %1, %2" : "+v"(v) : "s"(s), "i"(LN)); return v; }
template <class T> DI void undef(T& x) { asm volatile("" : "=v"(x)); }
DI int sopq(int x) { asm volatile("" : "+s"(x)); return x; }
#define SWZ(l) ((l) ^ (((l) >> 3) & 7))
#define COORDS() \
  const int lane = lane_id(), lo = lane * 16, hh = lane >> 5, t16 = lane & 15, g4 = lane >> 4, lsw = SWZ(lane) * 16; \
  const int w = sopq(w0) & 3, sp = sopq(sp0), c = sopq(c0), n32 = c >> 1, rh = c & 1; \
  const unsigned lay = (unsigned)sopq((int)lay0); \
  const unsigned flo = sopq(layer) * FLAG_LAYER; \
  gu32* const fl = (gu32*)(ws + flo); \
  (void)lo; (void)hh; (void)t16; (void)g4; (void)lsw; (void)n32; (void)rh; (void)lay; (void)sp; (void)c; (void)w; (void)fl;
#define HBARS(c) asm volatile("s_waitcnt lgkmcnt(0)\n\ts_barrier" ::: "memory")
#define CST() do {} while (0)
#define DBGC(k) asm volatile("" ::: "memory")
#define HBAR() asm volatile("s_waitcnt lgkmcnt(0)\n\ts_barrier" ::: "memory")
#define BAR() asm volatile("s_waitcnt lgkmcnt(0)\n\ts_barrier" ::: "memory")
#define BARX() asm volatile("s_barrier" ::: "memory")
#define DRAIN() asm volatile("s_waitcnt vmcnt(0)" ::: "memory")
#define PIN() __builtin_amdgcn_sched_barrier(0)
#define SETPRIO(n) __builtin_amdgcn_s_setprio(n)
template <int N> DI void wait_vm() { static_assert(N >= 0 && N < 64, ""); asm volatile("s_waitcnt vmcnt(%0)" ::"i"(N) : "memory"); }

typedef __amdgpu_buffer_rsrc_t rsrc_t;
DI v4u ld16(rsrc_t rs, unsigned voff, unsigned soff) { return __builtin_amdgcn_raw_buffer_load_b128(rs, voff, soff, AUX_SC1); }
DI f32x2 ld8f(rsrc_t rs, unsigned voff, unsigned soff) { return __builtin_bit_cast(f32x2, __builtin_amdgcn_raw_buffer_load_b64(rs, voff, soff, AUX_SC1)); }
DI void st16(rsrc_t rs, v4u v, unsigned voff, unsigned soff) {
  __builtin_amdgcn_raw_buffer_store_b128(v, rs, voff, soff, AUX_SC1);
  asm volatile("s_nop 1" ::"v"(v));
}
DI void st8(rsrc_t rs, v2u v, unsigned voff, unsigned soff) { __builtin_amdgcn_raw_buffer_store_b64(v, rs, voff, soff, AUX_SC1); }
DI void st4(rsrc_t rs, unsigned v, unsigned voff, unsigned soff) { __builtin_amdgcn_raw_buffer_store_b32(v, rs, voff, soff, AUX_SC1); }
DI v4u lds16(LAS unsigned char* L, int off) { return *(LAS v4u*)(L + off); }
DI f32x4 lds4f(LAS unsigned char* L, int off) { return *(LAS f32x4*)(L + off); }

DI void poll(gu32* word, bool active, gu32* tmo, unsigned tag) {
  for (unsigned spins = 0;;) {
    unsigned v = tag;
    if (active) v = __hip_atomic_load(word, RLX_AGENT);
    if (__all(v == tag)) break;
    ++spins;
    if (spins > SPIN_CAP) { __hip_atomic_store(tmo, tag, RLX_AGENT); break; }
    if ((spins & 255u) == 0u && (unsigned)__builtin_amdgcn_readfirstlane(__hip_atomic_load(tmo, RLX_AGENT)) == tag) break;
  }
  __builtin_amdgcn_fence(__ATOMIC_ACQUIRE, "wavefront");
}

template <int I> DI void dma1(rsrc_t rs, LAS unsigned char* dst, unsigned voff, unsigned soff) {
  __builtin_amdgcn_raw_ptr_buffer_load_lds(rs, (LAS void*)dst, 16, voff, soff, I * 1024, AUX_SC1);
}
template <int V> using IC = std::integral_constant<int, V>;
template <class FN, int... I> DI void static_for_(FN fn, std::integer_sequence<int, I...>) { (fn(IC<I>{}), ...); }
template <int N, class FN> DI void static_for(FN fn) { static_for_(fn, std::make_integer_sequence<int, N>{}); }
template <int N> DI void dma(rsrc_t rs, LAS unsigned char* dst, unsigned lo, unsigned src) {
  static_for<N>([&](auto i) { dma1<i.value & 3>(rs, dst + (i.value >> 2) * 4096, lo, src + (i.value >> 2) * 4096); });
}

struct Stats {
  f32x4 p[16];
  float m0, m1, q0, q1;
  DI void load(rsrc_t rs, int lane, unsigned base, int i0 = 0, int i1 = 16) {
#pragma unroll
    for (int i = i0; i < i1; ++i) p[i] = __builtin_bit_cast(f32x4, ld16(rs, (lane >> 4) * 4096 + (lane & 15) * 16 + i * 256, base));
    PIN();
  }
  DI void part1() {
    m0 = m1 = q0 = q1 = 0.f;
#pragma unroll
    for (int i = 0; i < 16; ++i) { m0 += p[i][0]; q0 += p[i][1]; m1 += p[i][2]; q1 += p[i][3]; }
    asm volatile("" : "+v"(m0), "+v"(m1), "+v"(q0), "+v"(q1));
  }
  DI void part2() {
    const float a0 = m0 * (1.0f / 16.0f), a1 = m1 * (1.0f / 16.0f);
    q0 *= 32.0f; q1 *= 32.0f;
#pragma unroll
    for (int i = 0; i < 16; ++i) { const float d0 = p[i][0] - a0, d1 = p[i][2] - a1; q0 += d0 * d0; q1 += d1 * d1; }
    asm volatile("" : "+v"(q0), "+v"(q1));
  }
  DI void all(LAS unsigned char* L, int lane, int dst = LDS_ST) { part1(); part2(); part3(L, lane, dst); }
  DI void part3(LAS unsigned char* L, int lane, int dst = LDS_ST) {
    const float den0 = bfr(__builtin_amdgcn_sqrtf(bfr(bfr(q0 * (1.0f / (32.0f * D_))) + 1e-5f)));
    const float den1 = bfr(__builtin_amdgcn_sqrtf(bfr(bfr(q1 * (1.0f / (32.0f * D_))) + 1e-5f)));
    *(LAS f32x2*)(L + dst + lane * 8) = (f32x2){__builtin_amdgcn_rcpf(den0), __builtin_amdgcn_rcpf(den1)};
    *(LAS f32x2*)(L + dst + 512 + lane * 8) = (f32x2){den0 * den0, den1 * den1};
    *(LAS v2u*)(L + dst + 1024 + lane * 8) = (v2u){pk2(m0 * (-1.0f / D_), m0 * (-1.0f / D_)), pk2(m1 * (-1.0f / D_), m1 * (-1.0f / D_))};
  }
};
DI float lds1f(LAS unsigned char* L, int off) { return *(LAS float*)(L + off); }
DI v4u mfrag(LAS unsigned char* L, int off) { const unsigned n = *(LAS unsigned*)(L + off); return (v4u){n, n, 0u, 0u}; }

constexpr int LA = 6;
template <int NF, class RD, class STEP>
DI void pipe(RD rd, STEP step) {
  v4u wf[8];
  static_for<LA>([&](auto i) { wf[i.value] = rd(i); });
  step(IC<-1>{}, wf[0], IC<1>{});
  PIN();
  static_for<NF>([&](auto fc) {
    constexpr int f = fc.value;
    if constexpr ((f & 1) == 0 && f + LA <= NF) __builtin_amdgcn_s_waitcnt(0xc07f | ((LA - 2) << 8));
    step(fc, wf[f & 7], IC<0>{});
    if constexpr (f + LA < NF) wf[(f + LA) & 7] = rd(IC<f + LA>{});
    step(fc, wf[f & 7], IC<1>{});
    PIN();
  });
}
DI v4u csfrag(LAS unsigned char* L, int entry) {
  const v2u t = *(LAS v2u*)(L + LDS_CS + entry * 8);
  return (v4u){t.x, t.y, 0u, 0u};
}
DI void cs_put(LAS unsigned char* L, int entry, float cs) {
  const float hi = bfr(cs), mid = bfr(cs - hi), lw = (cs - hi) - mid;
  *(LAS v2u*)(L + LDS_CS + entry * 8) = (v2u){pk2(hi, mid), pk2(lw, 0.f)};
}
DI unsigned mulpk(float a, float s0, float b, float s1) {
  unsigned d; float t;
  asm("v_mul_f32 %0, %2, %3\n\tv_mul_f32 %1, %4, %5\n\tv_cvt_pk_bf16_f32 %0, %0, %1" : "=&v"(d), "=&v"(t) : "v"(a), "v"(s0), "v"(b), "v"(s1));
  return d;
}
DI unsigned nos(unsigned v) { return v < 0xfffffffeu ? v : 0xfffffffeu; }
DI v4u nos4(v4u v) { return (v4u){nos(v[0]), nos(v[1]), nos(v[2]), nos(v[3])}; }
DI unsigned umax3(unsigned a, unsigned b, unsigned c) { const unsigned t = a > b ? a : b; return t > c ? t : c; }
template <int N> DI bool missing(const v4u* f, int step = 1) {
  unsigned m0 = 0, m1 = 0;
#pragma unroll
  for (int i = 0; i < N; ++i) { m0 = umax3(m0, f[i * step][0], f[i * step][1]); m1 = umax3(m1, f[i * step][2], f[i * step][3]); }
  return __builtin_amdgcn_ballot_w64((m0 > m1 ? m0 : m1) == 0xffffffffu) != 0;
}
constexpr unsigned DATA_CAP = 1u << 14;
DI v4u pack8(const float* y) { return (v4u){pk2(y[0], y[1]), pk2(y[2], y[3]), pk2(y[4], y[5]), pk2(y[6], y[7])}; }

struct Fin {
  float y[8], s;
  v4u o;
  v2u sq;
  template <int K, bool ST = true> DI void chunk(const f32x4& a0, const f32x4& a1, const v4u& r, rsrc_t ro, unsigned vo, unsigned so, rsrc_t rs, unsigned vs, unsigned ss) {
    if constexpr (K == 0) { y[0] = aadd(a0[0], bflo(r[0])); y[1] = aadd(a0[1], bfhi(r[0])); y[2] = aadd(a0[2], bflo(r[1])); y[3] = aadd(a0[3], bfhi(r[1])); }
    if constexpr (K == 1) { y[4] = aadd(a1[0], bflo(r[2])); y[5] = aadd(a1[1], bfhi(r[2])); y[6] = aadd(a1[2], bflo(r[3])); y[7] = aadd(a1[3], bfhi(r[3])); }
    if constexpr (K == 2) { o = nos4(pack8(y)); if constexpr (ST) st16(ro, o, vo, so); }
    if constexpr (K == 3) {
#pragma unroll
      for (int m = 0; m < 4; ++m) { y[2 * m] = bflo(o[m]); y[2 * m + 1] = bfhi(o[m]); }
    }
    if constexpr (K == 4) s = xrow_sum(aadd(aadd(aadd(y[0], y[1]), aadd(y[2], y[3])), aadd(aadd(y[4], y[5]), aadd(y[6], y[7]))));
    if constexpr (K == 5) {
      s = xhalf_sum(s);
      const float mu = s * (1.0f / 32.0f);
#pragma unroll
      for (int i = 0; i < 8; ++i) y[i] = asub(y[i], mu);
    }
    if constexpr (K == 6) {
      y[0] = amul(y[0], y[0]); y[1] = amul(y[1], y[1]);
#pragma unroll
      for (int i = 2; i < 8; ++i) y[i & 1] = afma(y[i], y[i], y[i & 1]);
    }
    if constexpr (K == 7) {
      sq = (v2u){nos(__float_as_uint(s)), nos(__float_as_uint(xhalf_sum(xrow_sum(aadd(y[0], y[1])))))};
      if constexpr (ST) st8(rs, sq, vs, ss);
    }
  }
};

constexpr unsigned long long low_mask(int r) {
  unsigned long long m = 0;
  for (int l = 0; l < 64; ++l)
    if ((r & 3) + 8 * (r >> 2) + 4 * (l >> 5) <= (l & 31)) m |= 1ull << l;
  return m;
}
DI float sel(unsigned long long mask, float set, float unset) {
  float d;
  asm("v_cndmask_b32_e64 %0, %1, %2, %3" : "=v"(d) : "v"(unset), "v"(set), "s"(mask));
  return d;
}
DI float max16(float m, const f32x16& s) {
#pragma unroll
  for (int r = 0; r < 16; r += 2) m = __builtin_fmaxf(__builtin_fmaxf(m, s[r]), s[r + 1]);
  return m;
}
DI v4u exp_pack(const f32x16& s, int h) {
  float pr[8];
#pragma unroll
  for (int r = 0; r < 8; ++r) pr[r] = __builtin_amdgcn_exp2f(s[8 * h + r]);
  return pack8(pr);
}
template <bool EXACT>
DI f32x16 attn_pass(LAS unsigned char* L, int lane, int tl, int nv) {
  const int lo = lane * 16;
  const int kb = LDS_KL + tl * 1024 + lo, kf0 = LDS_KL + (tl + 8) * 1024 + (lane >> 5) * 512;
  const v4u q = lds16(L, LDS_QL + tl * 1024 + lo);
  v4u kf[9];
  kf[8] = lds16(L, kb + 8192); kf[0] = lds16(L, nv <= 0 ? kb : kf0);
#pragma unroll
  for (int i = 7; i >= 1; --i) kf[i] = lds16(L, (nv <= i ? kb : kf0 - i * 1024) + i * 1024);
  f32x16 s[9], cm = splat16(0.f);
  if constexpr (EXACT) {
    s[8] = mfma(kf[8], q, splat16(0.f));
    s[0] = mfma(kf[0], q, splat16(0.f));
    s[7] = mfma(kf[7], q, splat16(0.f));
    s[6] = mfma(kf[6], q, splat16(0.f));
    asm volatile("s_nop 7\n\ts_nop 7" : "+v"(s[8]), "+v"(s[0]));
    PIN();
    static_for<16>([&](auto r) { s[8][r.value] = sel(low_mask(r.value), s[8][r.value], s[0][r.value]); });
    float m = max16(s[8][0], s[8]);
    PIN();
    static_for<7>([&](auto ic) {
      constexpr int i = 7 - ic.value;
      if constexpr (i - 2 >= 1) s[i - 2] = mfma(kf[i - 2], q, splat16(0.f));
      m = max16(m, s[i]);
      PIN();
    });
    cm = splat16(-xhalf_max(m));
  }
  const int vb = LDS_VL + 2 * tl * VBLK + ((lane & 16) ? 512 : (lane >> 5) * 256 + (lane & 15) * 16);
  s[8] = mfma(kf[8], q, cm);
  s[0] = mfma(kf[0], q, cm);
  s[7] = mfma(kf[7], q, cm);
  const v4u mk0 = lds16(L, LDS_MK + lo), mk1 = lds16(L, LDS_MK + 1024 + lo);
  v4u va = lds16(L, vb + 16 * VBLK), vc = lds16(L, vb + 17 * VBLK), v0a = lds16(L, vb), v0c = lds16(L, vb + VBLK);
  asm volatile("s_nop 7\n\ts_nop 7" : "+v"(s[8]), "+v"(s[0]));
  PIN();
  f32x16 o;
  {
    static_for<16>([&](auto r) { s[8][r.value] = sel(low_mask(r.value), s[8][r.value], s[0][r.value]); });
    const v4u p0 = exp_pack(s[8], 0), l0 = p0 & mk0;
    o = mfma(va, l0, splat16(0.f));
    o = mfma(v0a, p0 ^ l0, o);
    const v4u p1 = exp_pack(s[8], 1), l1 = p1 & mk1;
    o = mfma(vc, l1, o);
    o = mfma(v0c, p1 ^ l1, o);
    va = lds16(L, vb + 14 * VBLK); vc = lds16(L, vb + 15 * VBLK);
    PIN();
  }
  static_for<7>([&](auto ic) {
    constexpr int i = 7 - ic.value;
    if constexpr (i > 1) s[i - 1] = mfma(kf[i - 1], q, cm);
    PIN();
    o = mfma(va, exp_pack(s[i], 0), o);
    if constexpr (i > 1) va = lds16(L, vb + (2 * i - 2) * VBLK);
    o = mfma(vc, exp_pack(s[i], 1), o);
    if constexpr (i > 1) vc = lds16(L, vb + (2 * i - 1) * VBLK);
    PIN();
  });
  return o;
}
DI v4u attn_unit(LAS unsigned char* L, int lane, int tl, int nv) {
  f32x16 o = attn_pass<false>(L, lane, tl, nv);
  float big = __builtin_fabsf(o[0]);
#pragma unroll
  for (int r = 1; r < 8; ++r) big += __builtin_fabsf(o[r]);
  if (__builtin_expect(!__all(o[8] > 0x1p-30f && o[8] < 0x1p60f && big < 0x1p120f), 0)) {
    asm volatile("" ::: "memory");
    o = attn_pass<true>(L, lane, tl, nv);
  }
  const float rl = __builtin_amdgcn_rcpf(o[8]);
  float y[8];
#pragma unroll
  for (int r = 0; r < 8; ++r) y[r] = o[r] * rl;
  return pack8(y);
}

struct Retile {
  v4u in[8];
  DI void load(const u16* src, int ld, int rs4) {
#pragma unroll
    for (int j = 0; j < 8; ++j) in[j] = *(const v4u*)(src + (size_t)(((j & 3) + rs4 * (j >> 2)) * ld));
    PIN();
  }
  unsigned d[8];
  DI void out(rsrc_t rs) {
#pragma unroll
    for (int i = 0; i < 8; ++i) st16(rs, in[i], d[i], 0);
  }
  DI void lines(LAS unsigned char* t, int lane, int sw, unsigned dst) {
#pragma unroll
    for (int e = 0; e < 8; ++e) {
      v4u o;
#pragma unroll
      for (int m = 0; m < 4; ++m) o[m] = __builtin_amdgcn_perm(in[2 * m + 1][e >> 1], in[2 * m][e >> 1], (e & 1) ? 0x07060302u : 0x05040100u);
      *(LAS v4u*)(t + lane * 128 + (e ^ sw ^ (lane & 7)) * 16) = o;
    }
    asm volatile("s_waitcnt lgkmcnt(0)" ::: "memory");
#pragma unroll
    for (int i = 0; i < 8; ++i) {
      in[i] = *(LAS v4u*)(t + i * 1024 + (lane ^ (lane >> 3)) * 16);
      d[i] = (unsigned)__builtin_amdgcn_ds_bpermute((8 * i + (lane >> 3)) * 4, (int)dst) + (lane & 7) * 16;
    }
    PIN();
  }
  DI void put(LAS unsigned char* L, int pb, int dd, int lp, int dl) {
#pragma unroll
    for (int e = 0; e < 8; ++e) {
      v4u o;
#pragma unroll
      for (int m = 0; m < 4; ++m) o[m] = __builtin_amdgcn_perm(in[2 * m + 1][e >> 1], in[2 * m][e >> 1], (e & 1) ? 0x07060302u : 0x05040100u);
      const int s = lp + (e & 3) + (e >> 2) * dl;
      *(LAS v4u*)(L + pb + (e >> 2) * dd + (s ^ ((s >> 3) & 7)) * 16) = o;
    }
  }
};

struct GS { int cs, j, kap, off; };
constexpr GS gu_step(int u) {
  if (u < 64) { const int k = u >> 2, j = u & 3; return GS{0, j, k, j < 3 ? (7 * (k >> 1) + 3 * (k & 1) + j) * 1024 : (7 * (k >> 1) + 6) * 1024 + (k & 1) * 512}; }
  const int v = u - 64, j = v / 17, i = v % 17;
  return GS{i == 16, j, 16 + i, 57344 + j * 16384 + i * (j < 3 ? 1024 : 512)};
}
constexpr int GA = 132, DB = 264, DA = 376, NST = 488;
constexpr int NPG = 16, NPQ = 12, LDN = 12;
constexpr int ld_step(int id) {
  if (id < 32) return id < NPG ? -1 : 2 * (id - NPG);
  if (id < 64) { const int n = id - 32; return n < 12 ? 103 + (5 * n) / 2 : GA + 10 + 2 * (n - 12); }
  if (id < 176) {
    const int n = id - 64;
    return n < 12 ? GA + 103 + n : n < 26 ? DB - 15 + ((n - 12) * 7) / 4 : n < 56 ? DB + 27 + ((n - 26) * 5) / 3 : n < 84 ? DB + 79 + ((n - 56) * 4) / 3 : DA + 31 + 2 * (n - 84);
  }
  if (id < 208) { const int n = id - 176; return n < NPQ ? DA + 87 + 2 * n : -1; }
  if (id < 224) return -1;
  return id == 224 ? DB + 80 : DA + 72;
}
constexpr int NEP = 56;
constexpr int ep_step(int k) { return 83 + 17 * (k / 16) + k % 16; }
struct Tab { short ld[NST][2]; short ep[NST]; short nvm[NST]; };
constexpr int FINB = DA + 3;
#define Q3T 75
#define XA0 18
constexpr int xa_step(int j) { const int a = XA0 + j, b = j < 12 ? 3 * j + 2 : 34 + j; return a > b ? a : b; }
constexpr Tab make_tab() {
  Tab t{};
  for (int s = 0; s < NST; ++s) { t.ld[s][0] = t.ld[s][1] = t.ep[s] = -1; t.nvm[s] = 0; }
  for (int id = 0; id < 226; ++id) {
    const int s = ld_step(id);
    if (s < 0) continue;
    t.ld[s][t.ld[s][0] >= 0] = (short)id; ++t.nvm[s];
  }
  for (int job = 0; job < 2; ++job)
    for (int k = 0; k < NEP; ++k) {
      const int s = job * GA + ep_step(k);
      t.ep[s] = (short)(job * 64 + k);
      if (k == NEP - 1) t.nvm[s] += 2;
      if (k == (job ? 31 : NEP - 1)) t.nvm[s] += 2;
    }
  ++t.nvm[FINB + 2]; ++t.nvm[FINB + 7];
  return t;
}
constexpr Tab TAB = make_tab();
constexpr int vm_between(int a, int b) {
  int n = 0;
  for (int s = a + 1; s <= b; ++s) n += TAB.nvm[s];
  return n;
}

__global__ void __launch_bounds__(512, 2) k_main(const u16* __restrict__ x, const u16* __restrict__ wqkv, const u16* __restrict__ wo, const u16* __restrict__ wg,
                                                 const u16* __restrict__ wu, const u16* __restrict__ wd, unsigned char* __restrict__ ws,
                                                 u16* __restrict__ out) {
  extern __shared__ __attribute__((aligned(16))) unsigned char smem[];
  LAS unsigned char* L = (LAS unsigned char*)smem;
  const int tid = threadIdx.x, w0 = __builtin_amdgcn_readfirstlane(tid >> 6);
  const int b = blockIdx.x;
  const int sp0 = (b & 6) + ((b >> 3) & 1), c0 = (b >> 4) + 16 * (b & 1);
  const rsrc_t rs = __builtin_amdgcn_make_buffer_rsrc((void*)ws, 0, (int)WS_BYTES, 0x00020000);
  const bool compute = w0 < 4;
  gu32* const fg = (gu32*)(ws + FLAG_G);
  gu32* const tmo = fg + FL_TMO * 32;
  unsigned tag = 0;
  const unsigned long long t_hold = __builtin_amdgcn_s_memrealtime();
#define HOLD(k) do { if (X_AT == (k)) while (__builtin_amdgcn_s_memrealtime() - t_hold < X_HOLD) __builtin_amdgcn_s_sleep(1); } while (0)
#define FLAG(blk, line) st4(rs, tag, lane ? NONE : 0u, (blk) + (line) * 128)
#define POLL(blk, line, n) poll((gu32*)(ws + (blk)) + ((line) + lane) * 32, lane < (n), tmo, tag)
#define GETTAG() tag = (unsigned)__builtin_amdgcn_readfirstlane(*(LAS unsigned*)(L + LDS_TAG))
  auto blank = [&](int it, unsigned ly) {
    const int lane = lane_id(), sp = sopq(sp0), c = sopq(c0);
    const unsigned o = (unsigned)vopq(-1);
    if (it < 20) st16(rs, (v4u){o, o, o, o}, lane * 16, ly + (it < 16 ? (it & 8 ? L_V : L_K) + ((8 * sp + (it & 7)) * 32 + c) * 1024 : L_X + ((16 * sp + 4 * (c & 1) + (it & 3)) * 16 + (c >> 1)) * 1024));
    else st8(rs, (v2u){o, o}, (lane >> 5) * 4096 + (lane & 31) * 8, ly + L_X + MB2 + (8 * sp + 2 * (c & 1)) * 4096 + (c >> 1) * 256);
  };
  unsigned touch0 = 0;
  v4u xq[32];

  if (compute) {
    const int layer = 0; const unsigned lay0 = LAY_OFF;
    COORDS();
    SETPRIO(2);
    const int r = lane >> 3, j = lane & 7;
    const u16* src = x + (size_t)(256 * sp + 8 * c + r) * D_ + 128 * w + 8 * j;
    v4u a[2] = {*(const v4u*)src, *(const v4u*)(src + 64)};
#pragma unroll
    for (int i = 0; i < 2; ++i) {
      a[i] = nos4(a[i]);
      st16(rs, a[i], (j >> 2) * 1024 + ((j & 3) * 16 + r) * 16, lay + L_X + (16 * sp + n32) * 16384 + (2 * w + i) * 2048 + rh * 128);
      auto quad = [](float v) {
        v += __int_as_float(__builtin_amdgcn_update_dpp(0, __float_as_int(v), 0xb1, 0xf, 0xf, true));
        return v + __int_as_float(__builtin_amdgcn_update_dpp(0, __float_as_int(v), 0x4e, 0xf, 0xf, true));
      };
      float sm = 0.f, q = 0.f;
#pragma unroll
      for (int m = 0; m < 4; ++m) sm += bflo(a[i][m]) + bfhi(a[i][m]);
      sm = quad(sm);
      const float mu = sm * (1.0f / 32.0f);
#pragma unroll
      for (int m = 0; m < 4; ++m) { const float e0 = bflo(a[i][m]) - mu, e1 = bfhi(a[i][m]) - mu; q += e0 * e0 + e1 * e1; }
      q = quad(q);
      st8(rs, (v2u){nos(__float_as_uint(sm)), nos(__float_as_uint(q))}, (j & 3) ? NONE : (j >> 2) * 256 + r * 8,
          lay + L_X + MB2 + (8 * sp + (c >> 2)) * 4096 + (4 * w + 2 * i) * 256 + (c & 3) * 64);
    }
    DRAIN();
    BAR();
    BAR();
    GETTAG();
    const int TB = 8 * sp + 4 + w;
    static_for<NPQ>([&](auto nc) { constexpr int n = nc.value; xq[n] = ld16(rs, lo + ((n >> 1) & 3) * 1024, lay + L_X + TB * 32768 + (n & 1) * 16384 + (n >> 3) * 4096); });
    BARX();
  } else {
    const int layer = 0; const unsigned lay0 = LAY_OFF;
    COORDS();
    SETPRIO(3);
    Retile rt;
    LAS unsigned char* tb = L + LDS_TB + w * 8192;
    const int id = 2 * sp + rh + 16 * (w - 1);
    auto fill0 = [&]() {
#pragma unroll 1
      for (int it = w - 1; it < 16; it += 3) blank(it, lay);
      PIN();
    };
    if (w == 0 || id < 28) {
      const u16* src; int ld, sw; unsigned dst;
      if (w == 0) {
        const int fi = lane >> 3, i8 = lane & 7, F = 6 * sp + (fi < 6 ? fi : 0);
        const int ks = F < 36 ? F / 3 : F < 44 ? (F - 12) >> 1 : F - 32, m = F < 36 ? (F - 3 * ks + 1) % 3 : F < 44 ? 1 + (F & 1) : 0;
        src = wqkv + (size_t)(32 * ks + 8 * (i8 >> 1)) * (3 * D_) + m * D_ + 16 * c + 8 * (i8 & 1);
        ld = 3 * D_; sw = i8; dst = fi < 6 ? WQKV_OFF + c * 49152 + F * 1024 + i8 * 128 : NONE;
      } else {
        const int t = 64 * id + lane, mo = t / 14, ch = t - 14 * mo, mat = mo >> 6, o = mo & 63, c7 = ch >= 7 ? ch - 7 : ch, j = c7 >> 1, kap = o >> 1;
        src = (mat ? wu : wg) + (size_t)(8 * o) * F_ + 112 * n32 + 8 * ch;
        ld = F_;
        const int cell = kap < 16 ? 7 * (kap >> 1) + (j < 3 ? 3 * (kap & 1) + j : 6) : 56 + 16 * j + (j < 3 ? kap - 16 : (kap - 16) >> 1);
        const int rho = j < 3 ? 2 * mat + (c7 & 1) + 4 * (o & 1) : 4 * (kap & 1) + 2 * (o & 1) + mat;
        sw = j < 3 ? rho : 0; dst = WGU_OFF + (2 * n32 + (ch >= 7)) * 114688 + cell * 1024 + rho * 128;
      }
      unsigned ep = 0;
      if (w == 0) ep = __hip_atomic_load(fg + FL_EP * 32, RLX_AGENT);
      rt.load(src, ld, 4);
      if (w) fill0();
      if (w == 0) {
        tag = __builtin_amdgcn_readfirstlane(ep) + 1u;
        if (lane == 0) *(LAS v4u*)(L + LDS_TAG) = (v4u){tag, 0u, 0u, 0u};
        v4u mk[2];
#pragma unroll
        for (int k = 0; k < 8; ++k) {
          const int kq = ((2 * k) & 3) + 8 * ((2 * k) >> 2) + 4 * hh, ql = lane & 31;
          mk[k >> 2][k & 3] = (kq <= ql ? 0xffffu : 0u) | (kq + 1 <= ql ? 0xffff0000u : 0u);
        }
        *(LAS v4u*)(L + LDS_MK + lo) = mk[0];
        *(LAS v4u*)(L + LDS_MK + 1024 + lo) = mk[1];
        if (sp == 0) {
#pragma unroll
          for (int i = 0; i < 9; ++i) *(LAS v4u*)(L + LDS_VL + (i < 8 ? i * 1024 + lo : 8192 + t16 * 16)) = (v4u){0u, 0u, 0u, 0u};
        }
        const unsigned one = (sp == 0 && lane < 16) ? 0u : 0x3f803f80u;
        if (lane < 32) *(LAS v4u*)(L + LDS_VL + lane * VBLK + 512) = (v4u){one, one, one, one};
      }
      rt.lines(tb, lane, sw, dst);
      if (w == 0) rt.out(rs);
    } else if (id < 46) {
      const int slot = (lane >> 2) & 3, m4 = lane & 3, q = 16 * (id < 42 ? id - 28 : id - 42) + 4 * g4 + slot;
      const int cq = q < 128 ? q >> 2 : q < 192 ? (q - 128) >> 1 : q - 192, r = q < 128 ? q & 3 : q < 192 ? 4 + (q & 1) : 6;
      rt.load((id < 42 ? wd + (size_t)(56 * cq + (r < 6 ? 16 * (r >> 1) + 4 * (r & 1) : 48)) * D_ : wo + (size_t)(16 * (q >> 1) + 4 * (q & 1)) * D_) + 32 * n32 + 8 * m4,
              D_, (id < 42 && r == 6) ? 4 : 8);
      fill0();
      rt.put(tb, g4 * 2048, 1024, 4 * m4 + 16 * slot, 0);
      asm volatile("s_waitcnt lgkmcnt(0)" ::: "memory");
#pragma unroll
      for (int i = 0; i < 8; ++i) {
        rt.in[i] = lds16(tb, i * 1024 + lo);
        rt.d[i] = (id < 42 ? WDN_OFF + n32 * 114688 + (id - 28) * 8192 : WO_OFF + n32 * 32768 + (id - 42) * 8192) + i * 1024 + lo;
      }
    } else {
#pragma unroll
      for (int i = 0; i < 8; ++i) { undef(rt.in[i]); rt.d[i] = NONE; }
      fill0();
    }
    if (w == 0) DRAIN();
    HBAR();
    if (w == 0) {
      FLAG(0, FL_XB + sp * 32 + c);
      poll((gu32*)ws + (FL_XB + (lane < 32 ? sp * 32 + lane : (lane & 7) * 32 + c)) * 32, lane < 40, tmo, tag);
    }
    Stats st;
    if (w == 0) {
      st.load(rs, lane, lay + L_X + MB2 + (8 * sp + 4) * 4096);
    }
    HBAR();
    GETTAG();
    if (w) {
      const int l = w - 1;
      dma<4>(rs, L + l * 4096, lo, WQKV_OFF + c * 49152 + l * 4096);
      dma<7>(rs, L + (12 + 7 * l - (l >> 1)) * 1024, lo, WQKV_OFF + c * 49152 + (12 + 7 * l - (l >> 1)) * 1024);
      wait_vm<7>();
    }
    HOLD(0);
    HBAR();
    if (w) { dma<6>(rs, L + (27 + 5 * w) * 1024, lo, WQKV_OFF + c * 49152 + (27 + 5 * w) * 1024);
      rt.out(rs); wait_vm<14>();
    }
    else {
      st.all(L, lane);
      PIN();
      FLAG(0, FL_XA + sp * 32 + c);
      const int i = lane & 31;
      const int line = i < 8 ? FL_KV + (8 * sp + i) * 32 + c : i < 16 ? FL_O + (8 * sp + i - 8) * 32 + c
                     : i < 20 ? FL_X1B + (8 * sp + 4 + 2 * rh + ((i >> 1) & 1)) * 32 + 2 * n32 + (i & 1) : FL_X1A + (i - 20 < 6 ? i - 20 : 5) * 256 + sp * 32 + c;
      touch0 = __builtin_amdgcn_raw_buffer_load_b32(rs, hh * FLAG_LAYER + line * 128, 0, AUX_SC1);
    }
  }

  for (int layer = 0; layer < 2; ++layer) {
    const bool last = layer == 1;
    const unsigned lay0 = LAY_OFF + layer * LAY_BYTES;
    auto ldw = [&](auto nc, unsigned src, int cell) {
      constexpr int N = nc.value, K = (N + 2) / 3;
      const int l = sopq(w0) - 5, c1 = cell + (l * K + K > N ? N - K : l * K);
      dma<K>(rs, L + c1 * 1024, lane_id() * 16, src + c1 * 1024);
    };
    auto ld_gu = [&](int s) { ldw(IC<14>{}, WGU_OFF + sopq(c0) * 114688, 14 * s); };
    auto ld_dn = [&](int s) { ldw(IC<14>{}, WDN_OFF + (sopq(c0) >> 1) * 114688, 14 * s); };
    if (compute) {
      COORDS();
      const int TB = 8 * sp + 4 + w, TA = TB - 4;
      v4u xa[32];
      auto ldx = [&](auto nc, int T) {
        constexpr int n = nc.value;
        return ld16(rs, lo + ((n >> 1) & 3) * 1024, lay + L_X + (2 * T + (n & 1)) * 16384 + (n >> 3) * 4096);
      };
      f32x4 aq[2][2], ak[2][2], av[2][2], r0, r1;
      v4u mf0, mf1; float rd0, rd1;
      unsigned o0, o1, o2, o3;
      const int kpos = (t16 + 16 * hh + 32 * (g4 & 1)) * 16;
      auto epi = [&](int k, int pa) {
        const int tl = pa ? w : 4 + w, st = (pa ? LDS_KL : LDS_ST) + w * 128;
        const unsigned kvb = lay + ((8 * sp + tl) * 32 + c) * 1024;
        if (k == 0) { r0 = lds4f(L, st + g4 * 16); r1 = lds4f(L, st + 64 + g4 * 16); }
        if (k == 2) { o0 = mulpk(ak[pa][0][0], rd0, ak[pa][0][1], rd0); o1 = mulpk(ak[pa][0][2], rd0, ak[pa][0][3], rd0); }
        if (k == 3) { o2 = mulpk(ak[pa][1][0], rd1, ak[pa][1][1], rd1); o3 = mulpk(ak[pa][1][2], rd1, ak[pa][1][3], rd1); }
        if (k == 4) {
          swap32(o0, o2); swap32(o1, o3);
          st16(rs, nos4((v4u){o0, o1, o2, o3}), kpos, kvb + L_K);
          *(LAS v4u*)(L + LDS_KL + (8 + tl) * 1024 + kpos) = (v4u){o0, o1, o2, o3};
        }
        if (k == 5) { o0 = mulpk(av[pa][0][0], r0[0], av[pa][0][1], r0[1]); o1 = mulpk(av[pa][0][2], r0[2], av[pa][0][3], r0[3]); }
        if (k == 6) { o2 = mulpk(av[pa][1][0], r1[0], av[pa][1][1], r1[1]); o3 = mulpk(av[pa][1][2], r1[2], av[pa][1][3], r1[3]); }
        if (k == 7) {
          swap32(o0, o2); swap32(o1, o3);
          st16(rs, nos4((v4u){o0, o1, o2, o3}), lo, kvb + L_V);
          *(LAS v4u*)(L + LDS_VL + (16 + 2 * tl + hh) * VBLK + (lane & 31) * 16) = (v4u){o0, o1, o2, o3};
        }
        if (k == 8) {
          asm volatile("s_nop 7" : "+v"(aq[pa][0]), "+v"(aq[pa][1]));
          const float s0 = rd0 * (0.25f * LOG2E), s1 = rd1 * (0.25f * LOG2E);
          o0 = mulpk(aq[pa][0][0], s0, aq[pa][0][1], s0); o1 = mulpk(aq[pa][0][2], s0, aq[pa][0][3], s0);
          o2 = mulpk(aq[pa][1][0], s1, aq[pa][1][1], s1); o3 = mulpk(aq[pa][1][2], s1, aq[pa][1][3], s1);
          swap32(o0, o2); swap32(o1, o3);
          *(LAS v4u*)(L + LDS_QL + tl * 1024 + kpos) = (v4u){o0, o1, o2, o3};
        }
      };
      pipe<102>(
          [&](auto fc) {
            constexpr int t = fc.value, u = t % 51;
            if constexpr (t == 28) { BARX(); CST(); }
            if constexpr (t == 12 || t == 44 || t == Q3T) BARX();
            if constexpr (u == 44 || u == 45 || u == 50) return csfrag(L, (u == 50 ? 0 : u - 43) * 16 + t16);
            else return lds16(L, (u < 44 ? u : u - 2) * 1024 + lsw);
          },
          [&](auto fc, const v4u& wfr, auto pc) {
            constexpr int t = fc.value, part = pc.value, u = t % 51;
            constexpr int ks = (u < 36 ? u / 3 : u < 44 ? (u - 12) >> 1 : u - 34) & 15, m = u < 36 ? (u % 3 + 1) % 3 : u < 46 ? 1 + (u & 1) : 0;
            constexpr bool pa = t >= 51, cs = u == 44 || u == 45 || u == 50;
            if constexpr (t < 0) return;
            else if constexpr (part == 0) {
              if constexpr (pa && u < 44 && u % 12 == 0) {
                constexpr int n = u / 12 * 8;
                if (__builtin_expect(missing<8>(xa + n), 0)) {
                  unsigned spins = 0;
                  do { static_for<8>([&](auto i) { xa[n + i.value] = ldx(IC<n + i.value>{}, TA); }); DBGC(0); } while (missing<8>(xa + n) && ++spins < DATA_CAP);
                }
                PIN();
              }
              const v4u& x0 = cs ? mf0 : pa ? xa[2 * ks] : xq[2 * ks];
              const v4u& x1 = cs ? mf1 : pa ? xa[2 * ks + 1] : xq[2 * ks + 1];
              if constexpr (m == 0) { aq[pa][0] = mfma16(wfr, x0, u < 3 ? zero4() : aq[pa][0]); aq[pa][1] = mfma16(wfr, x1, u < 3 ? zero4() : aq[pa][1]); }
              if constexpr (m == 1) { ak[pa][0] = mfma16(wfr, x0, u < 3 ? zero4() : ak[pa][0]); ak[pa][1] = mfma16(wfr, x1, u < 3 ? zero4() : ak[pa][1]); }
              if constexpr (m == 2) { av[pa][0] = mfma16(x0, wfr, u < 3 ? zero4() : av[pa][0]); av[pa][1] = mfma16(x1, wfr, u < 3 ? zero4() : av[pa][1]); }
            } else {
              if constexpr (2 * t < 32 - NPQ) { xq[NPQ + 2 * t] = ldx(IC<NPQ + 2 * t>{}, TB); xq[NPQ + 2 * t + 1] = ldx(IC<NPQ + 2 * t + 1>{}, TB); }
              if constexpr (u == 40) {
                const int st = (pa ? LDS_KL : LDS_ST) + w * 128;
                rd0 = lds1f(L, st + t16 * 4); rd1 = lds1f(L, st + 64 + t16 * 4);
                const int a = g4 ? LDS_TAG + 8 : st + 1024 + t16 * 4;
                mf0 = mfrag(L, a); mf1 = mfrag(L, g4 ? a : a + 64);
              }
              if constexpr (t >= 45 && t < 54) epi(t - 45, 0);
              if constexpr (t >= 96) epi(t - 96, 1);
              static_for<16>([&](auto j) { if constexpr (xa_step(j.value) == t) { xa[2 * j.value] = ldx(IC<2 * j.value>{}, TA); xa[2 * j.value + 1] = ldx(IC<2 * j.value + 1>{}, TA); } });
              if constexpr (t == 50) CST();
            }
          });
      epi(6, 1); epi(7, 1); epi(8, 1);
      BAR();
      CST();
    } else {
      COORDS();
      const bool loader = w != 0;
      auto cs_qkv = [&]() {
        const unsigned one = (unsigned)vopq(0x3f803f80);
        const v4u ones = (v4u){one, one, one, one};
        f32x4 a = zero4();
        v4u f[16];
        const int fa = (w - 1) * 1024 + lsw, fb = (w < 3 ? 35 + w : 44) * 1024 + lsw, fs = w < 3 ? 2048 : 1024;
#pragma unroll
        for (int ks = 0; ks < 16; ++ks) asm volatile("ds_read_b128 %0, %1" : "=v"(f[ks]) : "v"(ks < 12 ? fa + ks * 3072 : fb + (ks - 12) * fs) : "memory");
        asm volatile("s_waitcnt lgkmcnt(0)" : "+v"(f[0]), "+v"(f[1]), "+v"(f[2]), "+v"(f[3]), "+v"(f[4]), "+v"(f[5]), "+v"(f[6]), "+v"(f[7]));
        asm volatile("" : "+v"(f[8]), "+v"(f[9]), "+v"(f[10]), "+v"(f[11]), "+v"(f[12]), "+v"(f[13]), "+v"(f[14]), "+v"(f[15]));
#pragma unroll
        for (int ks = 0; ks < 16; ++ks) a = mfma16(f[ks], ones, a);
        if (t16 == 0) {
#pragma unroll
          for (int r = 0; r < 4; ++r) cs_put(L, (w % 3) * 16 + 4 * g4 + r, a[r]);
        }
      };
      const unsigned wob = WO_OFF + n32 * 32768 - LDS_WO;
      if (last && !loader) {
        v4u o[4];
#pragma unroll
        for (int i = 0; i < 4; ++i) o[i] = lds16(L, LDS_QL + i * 1024 + lo);
        const v2u sq = *(LAS v2u*)(L + LDS_QL + 4096 + lane * 8);
#pragma unroll
        for (int i = 0; i < 4; ++i) st16(rs, o[i], lo, lay + L_X + ((16 * sp + 4 * rh + i) * 16 + n32) * 1024);
        st8(rs, sq, hh * 4096 + (g4 & 1) * 128 + t16 * 8, lay + L_X + MB2 + (8 * sp + 2 * rh) * 4096 + n32 * 256);
        PIN();
      }
      HBARS(false);
      if (layer == 0) {
        if (loader) wait_vm<8>();
        HBAR();
        if (loader) cs_qkv();
        HBAR();
      } else {
        if (loader) { ldw(IC<10>{}, wob, 80); wait_vm<13>(); }
        HBAR();
        if (loader) ldw(IC<10>{}, wob, 90);
        HBAR();
        if (loader) ldw(IC<12>{}, wob, 100);
      }
      {
        v4u kv[4];
        auto ldkv = [&]() {
          const unsigned so = lay + ((8 * sp - 5 + w) * 32 + c) * 1024, s2 = w == 1 ? so + 3 * 32768 : NONE;
          kv[0] = ld16(rs, lo, so + L_K); kv[1] = ld16(rs, lo, so + L_V); kv[2] = ld16(rs, lo, s2 + L_K); kv[3] = ld16(rs, lo, s2 + L_V);
          PIN();
          DBGC(0);
        };
        if (!loader) {
          Stats st;
          gu32* const bl = (gu32*)ws + (FL_XA + (sp ? sp - 1 : 0) * 32 + c) * 32;
          unsigned spins = 0, pv = tag;
          if (!last) pv = __hip_atomic_load(bl, RLX_AGENT);
          do { st.load(rs, lane, lay + L_X + MB2 + 8 * sp * 4096); DBGC(0); } while (missing<16>((const v4u*)st.p) && ++spins < DATA_CAP);
          st.all(L, lane, LDS_KL);
          if (!__all(pv == tag)) poll(bl, lane == 0, tmo, tag);
        }
        DRAIN();
        HBARS(false);
        if (loader && sp > 0) {
          unsigned spins = 0;
          do ldkv(); while (missing<4>(kv) && ++spins < DATA_CAP);
          *(LAS v4u*)(L + LDS_KL + (3 + w) * 1024 + lo) = kv[0];
          *(LAS v4u*)(L + LDS_VL + (6 + 2 * w + hh) * VBLK + (lane & 31) * 16) = kv[1];
          if (w == 1) {
            *(LAS v4u*)(L + LDS_KL + 7 * 1024 + lo) = kv[2];
            *(LAS v4u*)(L + LDS_VL + (14 + hh) * VBLK + (lane & 31) * 16) = kv[3];
          }
        }
      }
      if (layer == 0 && !loader) { asm volatile("" ::"v"(touch0)); FLAG(FLAG_G, FL_WR + n32 * 16 + 2 * sp + rh); }
      if (!last) HOLD(1);
      DRAIN();
      HBAR();
      CST(); CST();
      if (loader) {
        if (layer == 0) {
          POLL(FLAG_G, FL_WR + n32 * 16, 16);
          ldw(IC<10>{}, wob, 80); ldw(IC<10>{}, wob, 90); ldw(IC<12>{}, wob, 100);
          ld_gu(0);
          wait_vm<5>();
          if (lane == 0) asm volatile("ds_add_u32 %0, %1" ::"v"(LDS_TAG + 12), "v"(1) : "memory");
        } else ld_gu(0);
        ld_gu(1); ld_gu(2); ldw(IC<6>{}, WGU_OFF + c * 114688, 42);
      }
      if (sp > 0) {
        LAS unsigned* hw = (LAS unsigned*)(L + LDS_TAG + 4);
        CST();
        if (!loader) {
          v4u kv[8];
          unsigned spins = 0;
          do {
#pragma unroll
            for (int t = 0; t < 4; ++t) {
              const unsigned so = lay + ((8 * sp - 8 + t) * 32 + c) * 1024;
              kv[t] = ld16(rs, lo, so + L_K); kv[4 + t] = ld16(rs, lo, so + L_V);
            }
            PIN();
            DBGC(1);
          } while (missing<8>(kv) && ++spins < DATA_CAP);
#pragma unroll
          for (int t = 0; t < 4; ++t) {
            *(LAS v4u*)(L + LDS_KL + t * 1024 + lo) = kv[t];
            *(LAS v4u*)(L + LDS_VL + (2 * t + hh) * VBLK + (lane & 31) * 16) = kv[4 + t];
          }
          asm volatile("s_waitcnt lgkmcnt(0)" ::: "memory");
          __hip_atomic_store(hw, (unsigned)layer + 1u, __ATOMIC_RELAXED, __HIP_MEMORY_SCOPE_WORKGROUP);
        } else {
          unsigned v;
          do {
            __builtin_amdgcn_s_sleep(1);
            asm volatile("ds_read_b32 %0, %1\n\ts_waitcnt lgkmcnt(0)" : "=v"(v) : "v"(LDS_TAG + 4) : "memory");
          } while ((unsigned)__builtin_amdgcn_readfirstlane(v) != (unsigned)layer + 1u);
        }
      } else CST();
      CST();
      if (layer == 0 && !loader) touch0 = __builtin_amdgcn_raw_buffer_load_b32(rs, LAY_OFF + L_O + lane * TSTR + c * 4096 + sp * 128, 0, AUX_SC1);
      SETPRIO(1);
    }
    {
      COORDS();
      const int tl = compute ? 4 + w : w, T = 8 * sp + tl;
      const v4u o = attn_unit(L, lane, tl, sp == 0 ? 8 - tl : 0);
      st16(rs, o, (g4 & 1) * 16384 + hh * 256 + t16 * 16, lay + L_O + T * 32768 + c * 512);
      DRAIN();
      FLAG(flo, FL_O + T * 32 + c);
      CST();
    }
    if (compute) {
      SETPRIO(0);
      v4u fr[176];
      {
        COORDS();
        const int TB = 8 * sp + 4 + 2 * rh + (w >> 1), SB = 2 * TB + (w & 1);
        const v4u rB = ld16(rs, lo, lay + L_X + (SB * 16 + n32) * 1024), rA = ld16(rs, lo, lay + L_X + (SB * 16 + n32) * 1024 - 8 * 16384);
        const unsigned xv = (g4 >> 1) * 1024 + (g4 & 1) * 512 + t16 * 16, sv = g4 ? NONE : t16 * 8;
        const unsigned xo = lay + L_X1 + (TB * 32 + 2 * n32) * 1024 + (w & 1) * 256, so = lay + L_X1 + MB2 + TB * 4096 + n32 * 256 + (w & 1) * 128;
        const int lswo = vopq(lsw + LDS_WO);
        while ((unsigned)__builtin_amdgcn_readfirstlane(__hip_atomic_load((LAS unsigned*)(L + LDS_TAG + 12), __ATOMIC_RELAXED, __HIP_MEMORY_SCOPE_WORKGROUP)) != 3u)
          __builtin_amdgcn_s_sleep(1);
        asm volatile("" ::: "memory");
        v4u of[16];
        unsigned pk;
        f32x4 a0, a1;
        Fin fin;
        pipe<64>(
            [&](auto fc) { return lds16(L, (fc.value & 31) * 1024 + lswo); },
            [&](auto fc, const v4u& wfr, auto pc) {
              constexpr int t = fc.value, part = pc.value, u = t & 31;
              if constexpr (part == 0) {
                if constexpr (u & 1) a1 = mfma16(wfr, of[u >> 1], u == 1 ? zero4() : a1);
                else a0 = mfma16(wfr, of[u >> 1], u == 0 ? zero4() : a0);
              } else {
                if constexpr (t == 31 || t == 63) {
                  asm volatile("s_nop 7" : "+v"(a0), "+v"(a1));
                  static_for<8>([&](auto k) { fin.template chunk<k.value>(a0, a1, t == 31 ? rB : rA, rs, xv, xo - (t == 31 ? 0 : 4 * 32768), rs, sv, so - (t == 31 ? 0 : 4 * 4096)); });
                  CST();
                }
                if constexpr (t == -1 || t == 31) {
                  if constexpr (t == 31) {
                    pk = __hip_atomic_load(fl + (FL_O + (TB - 4) * 32 + (lane & 31)) * 32, RLX_AGENT);
                    DRAIN();
                    FLAG(flo, FL_X1B + TB * 32 + 2 * n32 + (w & 1));
                  }
                  if (t == -1 || !__all(pk == tag)) POLL(flo, FL_O + (TB - (t == 31 ? 4 : 0)) * 32, 32);
                  CST();
                  static_for<16>([&](auto i) { of[i.value] = ld16(rs, lo + (i.value & 3) * 1024, lay + L_O + (SB - (t == 31 ? 8 : 0)) * 16384 + (i.value >> 2) * 4096); });
                }
                if constexpr (t == 55) pk = __hip_atomic_load(fl + (FL_X1B + (8 * sp + 4 + w) * 32 + (lane & 31)) * 32, RLX_AGENT);
                if constexpr (t == 63) {
                  const int T = 8 * sp + 4 + w;
                  if (!__all(pk == tag)) POLL(flo, FL_X1B + T * 32, 32);
                  CST();
                  static_for<NPG>([&](auto i) { fr[i.value] = ld16(rs, lo + (i.value & 3) * 1024, lay + L_X1 + T * 32768 + (i.value >> 2) * 4096); });
                }
              }
            });
      }
      SETPRIO(2);
      BAR();
      {
        COORDS();
        const int TB = 8 * sp + 4 + w;
        const int TD = 8 * sp + 4 + 2 * rh + (w >> 1), SD = 2 * TD + (w & 1);
        const unsigned x1b = lay + L_X1 + TB * 32768, hb = lay + L_H + SD * 57344, xn = last ? NONE : lay + LAY_BYTES + L_X;
        const unsigned hsv = (g4 & 1) * 57344 + hh * 256 + t16 * 16, hso = lay + L_H + TB * 114688;
        const rsrc_t ro = last ? __builtin_amdgcn_make_buffer_rsrc((void*)out, 0, (int)MB2, 0x00020000) : rs;
        const unsigned ob = last ? SD * 16384 + n32 * 64 : xn + (SD * 16 + n32) * 1024;
        const unsigned sb = xn + MB2 + TD * 4096 + n32 * 256 + (w & 1) * 128;
        unsigned ov, sv;
        const int lsw2 = vopq(lsw + 65536), lh = (hh * 16 + t16) * 16, lh2 = vopq(lh + 65536);
        f32x16 acc[2][4];
        f32x4 d0[2], d1[2];
        v4u mf, hv[4], res[2];
        float k1[2], den2[2], t0, t1, p0_, p1_;
        Fin fin;
        auto epi = [&](auto ec) {
          constexpr int job = ec.value >> 6, k = ec.value & 63, j = k >> 4, i = (k >> 2) & 3, uo = j < 3 ? 8 : 4;
          const f32x16& a = acc[job][j];
          if constexpr ((k & 3) == 0)
            asm volatile("v_mul_f32 %0, %2, %4\n\tv_mul_f32 %1, %3, %4\n\tv_exp_f32 %0, %0" : "=&v"(t0), "=&v"(t1) : "v"(a[2 * i]), "v"(a[2 * i + 1]), "v"(k1[job]));
          else if constexpr ((k & 3) == 1)
            asm volatile("v_exp_f32 %0, %0\n\tv_mul_f32 %1, %3, %5\n\tv_mul_f32 %2, %4, %6"
                : "+v"(t1), "=&v"(p0_), "=&v"(p1_) : "v"(a[2 * i]), "v"(a[2 * i + 1]), "v"(a[uo + 2 * i]), "v"(a[uo + 2 * i + 1]));
          else if constexpr ((k & 3) == 2)
            asm volatile("v_fma_f32 %0, %0, %2, %2\n\tv_fma_f32 %1, %1, %2, %2\n\tv_rcp_f32 %0, %0" : "+v"(t0), "+v"(t1) : "v"(den2[job]));
          else {
            unsigned d;
            asm volatile("v_rcp_f32 %2, %2\n\tv_mul_f32 %1, %1, %3\n\tv_mul_f32 %2, %2, %4\n\tv_cvt_pk_bf16_f32 %0, %1, %2" : "=v"(d), "+v"(t0), "+v"(t1) : "v"(p0_), "v"(p1_));
            hv[j][i] = d;
            if constexpr (k == (job ? 31 : NEP - 1)) { st16(rs, hv[0], hsv, hso - job * 4 * 114688 + c * 1024); st16(rs, hv[1], hsv + 512, hso - job * 4 * 114688 + c * 1024); }
            if constexpr (k == NEP - 1) {
              st16(rs, hv[2], hsv, hso - job * 4 * 114688 + 32768 + c * 512);
              st8(rs, (v2u){hv[3][0], hv[3][1]}, hsv - hh * 248, hso - job * 4 * 114688 + 49152 + c * 256);
            }
          }
        };
        auto vload = [&](auto ic) {
          constexpr int id = ic.value;
          if constexpr (id < 64) fr[id] = ld16(rs, lo + (id & 3) * 1024, x1b - (id >> 5) * 4 * 32768 + ((id & 31) >> 2) * 4096);
          else if constexpr (id < 176) {
            constexpr int n = (id - 64) % 56;
            fr[id] = ld16(rs, lo + (n & 3) * 1024, hb - (id >= 120 ? 8 * 57344 : 0) + (n >> 2) * 4096);
          } else if constexpr (id < 208) {
            constexpr int n = id - 176;
            xq[n] = ld16(rs, lo + ((n >> 1) & 3) * 1024, xn + TB * 32768 + (n & 1) * 16384 + (n >> 3) * 4096);
          } else res[id - 224] = ld16(rs, vopq(lo & 0x2f0) + ((lo & 256) << 1) + (lo & 512), lay + L_X1 + ((TD - 4 * (id - 224)) * 32 + 2 * n32) * 1024 + (w & 1) * 256);
        };
        pipe<NST>(
            [&](auto fc) {
              constexpr int t = fc.value;
              if constexpr (t == 32) wait_vm<vm_between(-1, t - LA - 1) + NPG>();
              if constexpr (t == GA + 48) wait_vm<vm_between(ep_step(NEP - 1), t - LA - 1)>();
              if constexpr (t == DB + 14) wait_vm<vm_between(GA + ep_step(31), t - LA - 1)>();
              if constexpr (t == DB + 56) wait_vm<vm_between(GA + ep_step(NEP - 1), t - LA - 1)>();
              if constexpr (t == DA + 42) wait_vm<vm_between(FINB + 7, t - LA - 1)>();
              if constexpr (t < DB) {
                constexpr GS s = gu_step(t % GA);
                if constexpr (s.cs) return csfrag(L, 48 + 32 * s.j + (s.j < 3 ? lane & 31 : t16));
                else {
                  if (s.off % 14336 == 0 && s.off >= (t < GA ? 14336 : 28672)) BARX();
                  return lds16(L, s.off < 65536 ? s.off + (s.j < 3 ? lsw : lh) : s.off - 65536 + (s.j < 3 ? lsw2 : lh2));
                }
              } else {
                constexpr int v = (t - DB) % 112;
                if (v % 14 == 0 && v >= (t < DA ? 0 : 28)) BARX();
                return lds16(L, v < 64 ? v * 1024 + lsw : (v - 64) * 1024 + lsw2);
              }
            },
            [&](auto fc, const v4u& wfr, auto pc) {
              constexpr int t = fc.value, part = pc.value;
              if constexpr (t < 0) return;
              else if constexpr (part == 0) {
                if constexpr (t < DB) {
                  constexpr int job = t / GA, u = t % GA;
                  constexpr GS s = gu_step(u);
                  acc[job][s.j] = mfma(wfr, s.cs ? mf : fr[32 * job + (s.kap & 31)], u < 4 ? splat16(0.f) : acc[job][s.j]);
                } else {
                  constexpr int job = t >= DA, v = (t - DB) % 112;
                  if constexpr (v & 1) d1[job] = mfma16(wfr, fr[64 + 56 * job + (v >> 1)], v == 1 ? zero4() : d1[job]);
                  else d0[job] = mfma16(wfr, fr[64 + 56 * job + (v >> 1)], v == 0 ? zero4() : d0[job]);
                }
              } else {
                if constexpr (t < DB) {
                  constexpr int job = t / GA, u = t % GA;
                  if constexpr (u == 74) {
                    const int a = LDS_ST + w * 128 + (lane & 31) * 4;
                    k1[job] = lds1f(L, a); den2[job] = lds1f(L, a + 512); mf = mfrag(L, hh ? LDS_TAG + 8 : a + 1024);
                  }
                  if constexpr (u == 79) k1[job] *= -LOG2E;
                }
                if constexpr (TAB.ld[t][0] >= 0) vload(IC<TAB.ld[t][0]>{});
                if constexpr (TAB.ld[t][1] >= 0) vload(IC<TAB.ld[t][1]>{});
                if constexpr (TAB.ep[t] >= 0) epi(IC<TAB.ep[t]>{});
                if constexpr (t == FINB - 1) { const int l = lane_id(); ov = l * 16 + ((((l & 15) * 1008) - (l >> 4) * 240) & sopq(-layer)); sv = l >= 16 ? NONE : l * 8; }
                if constexpr (t >= FINB && t < FINB + 8) fin.template chunk<t - FINB>(d0[0], d1[0], res[0], ro, ov, ob, rs, sv, sb);
              }
            });
        CST();
        asm volatile("s_nop 7" : "+v"(d0[1]), "+v"(d1[1]));
        static_for<8>([&](auto k) { fin.template chunk<k.value, false>(d0[1], d1[1], res[1], ro, ov, ob - 8 * 16384, rs, sv, sb - 4 * 4096); });
        if (last) st16(ro, fin.o, ov, ob - 8 * 16384);
        else {
          *(LAS v4u*)(L + LDS_QL + w * 1024 + lo) = fin.o;
          if (lane < 16) *(LAS v2u*)(L + LDS_QL + 4096 + w * 128 + lane * 8) = fin.sq;
          BAR();
        }
      }
    } else {
      COORDS();
      const bool poller = w == 0, loader = w != 0;
      unsigned pv = 0;
#define PEEK(blk, line) pv = __hip_atomic_load((gu32*)(ws + (blk)) + ((line) + (lane & 31)) * 32, RLX_AGENT)
#define GATE(blk, line) do { if (!__all(pv == tag)) POLL(blk, line, 32); } while (0)
      if (layer == 0) {
        if (loader) { ldw(IC<8>{}, WGU_OFF + c * 114688, 48); ld_gu(4); ldw(IC<10>{}, WGU_OFF + c * 114688, 70); }
        f32x4 ca = zero4();
        const unsigned one = (unsigned)vopq((w == 3 || t16 == (g4 & 1)) ? 0x3f803f80 : 0), wb = WGU_OFF + c * 114688;
        for (int q = 0; q < (w < 3 ? 2 : 1); ++q) {
          v4u f[16];
          static_for<16>([&](auto ic) {
            constexpr int i = ic.value;
            f[i] = ld16(rs, w < 3 ? lsw : lo, wb + (w < 3 ? (q ? 56 + 16 * w + i : 7 * (i >> 1) + 3 * (i & 1) + w) : i < 8 ? 7 * i + 6 : 96 + i) * 1024);
          });
          PIN();
          static_for<16>([&](auto i) { ca = mfma16(f[i.value], (v4u){one, one, one, one}, ca); });
        }
        if (t16 < (w < 3 ? 2 : 1)) {
#pragma unroll
          for (int r = 0; r < 4; ++r) cs_put(L, 48 + 32 * w + 16 * t16 + 4 * g4 + r, ca[r]);
        }
      }
      SETPRIO(3);
      if (poller) {
        asm volatile("" ::"v"(touch0));
        Stats st;
        unsigned ev[4];
        const int h2l = lane < 32 ? FL_HA2 + sp * 32 + lane : FL_HB + (sp ? sp - 1 : 0) * 32 + c;
        SETPRIO(1);
        if (last) HOLD(4);
        HBAR();
        static_for<7>([&](auto nc) {
          constexpr int n = nc.value + 1;
          if (n <= 2) st.load(rs, lane, lay + L_X1 + MB2 + (8 * sp + 4) * 4096, 8 * n - 8, 8 * n);
          if (n == 3) { FLAG(flo, FL_X1A + sp * 32 + c); st.part1(); }
          if (n == 4) st.part2();
          if (n == 5) st.part3(L, lane);
          if (n == 6) PEEK(flo, FL_X1A + sp * 32);
          if (n == 7) GATE(flo, FL_X1A + sp * 32);
          HBARS(n == 3 || n == 7);
        });
        st.load(rs, lane, lay + L_X1 + MB2 + 8 * sp * 4096);
        st.all(L, lane);
        static_for<6>([&](auto nc) {
          constexpr int n = nc.value + 2;
          if (n == 4) FLAG(flo, FL_HB + sp * 32 + c);
          if (n == 6) PEEK(flo, FL_HB + sp * 32);
          if (n == 7) GATE(flo, FL_HB + sp * 32);
          HBARS(n == 2 || n == 7);
        });
        static_for<8>([&](auto nc) {
          constexpr int n = nc.value;
          if (n == 2) FLAG(flo, FL_HA + sp * 32 + c);
          if (n == 5) { FLAG(flo, FL_HA2 + sp * 32 + c); PEEK(flo, FL_HA + sp * 32); }
          if (n == 6) GATE(flo, FL_HA + sp * 32);
          if (n == 7) pv = __hip_atomic_load(fl + h2l * 32, RLX_AGENT);
          HBARS(n == 0 || n == 3 || n == 6);
        });
        static_for<6>([&](auto nc) {
          constexpr int n = nc.value + 2;
          if (n == 2) { if (!__all(pv == tag)) poll(fl + h2l * 32, true, tmo, tag); }
          if (n == 3) {
#pragma unroll
            for (int q = 0; q < 4; ++q) ev[q] = __builtin_amdgcn_raw_buffer_load_b32(rs, lane * 128, last && blockIdx.x == 0 ? flo + (FL_HB + 64 * q) * 128 : NONE, AUX_SC1);
          }
          if (n == 4 && !last) FLAG(FLAG_LAYER, FL_XB + sp * 32 + c);
          if (n == 5) PEEK(FLAG_LAYER, FL_XB + sp * 32);
          if (n == 6) GATE(FLAG_LAYER, FL_XB + sp * 32);
          if (n == 7) st.load(rs, lane, last ? NONE : LAY_OFF + LAY_BYTES + L_X + MB2 + (8 * sp + 4) * 4096);
          if (n == 7 && last) HOLD(3);
          HBARS(n == 2 || n == 6 || n == 7);
        });
        if (!last) { st.all(L, lane); }
        if (last && blockIdx.x == 0) {
          if (!__all(ev[0] == tag && ev[1] == tag && ev[2] == tag && ev[3] == tag))
            for (int q = 0; q < 4; ++q) POLL(flo, FL_HB + 64 * q, 64);
          if (lane == 0) __hip_atomic_store(fg + FL_EP * 32, tag, RLX_AGENT);
        }
      } else {
        const unsigned wq = WQKV_OFF + c * 49152, wb = WGU_OFF + c * 114688;
        DRAIN();
        HBAR();
        static_for<7>([&](auto nc) {
          constexpr int n = nc.value + 1;
          if (n == 7) wait_vm<1>();
          blank(3 * n - 4 + w, last ? NONE : LAY_OFF + LAY_BYTES);
          if (n >= 2 && n <= 4) ldw(IC<9>{}, wb, 62 + 9 * n);
          if (n == 5) { ldw(IC<5>{}, wb, 107); wait_vm<11>(); }
          if (n == 6) wait_vm<8>();
          HBARS(n == 3 || n == 7);
        });
        static_for<6>([&](auto nc) {
          constexpr int n = nc.value + 2;
          if (n == 3) wait_vm<0>();
          if (n == 5 || n == 6) { ld_dn(2 * n - 10); ld_dn(2 * n - 9); } else if (n > 6) ld_dn(n - 3);
          HBARS(n == 2 || n == 7);
        });
        static_for<8>([&](auto nc) {
          constexpr int n = nc.value;
          if (n <= 2) ld_dn(n + 5);
          wait_vm<(n <= 2 ? 25 : 35 - 5 * n)>();
          HBARS(n == 0 || n == 3 || n == 6);
        });
        static_for<6>([&](auto nc) {
          constexpr int n = nc.value + 2;
          if (!last) {
            if (n >= 3 && n <= 5) ldw(IC<14>{}, wq, 14 * (n - 3));
            if (n == 6) { ldw(IC<6>{}, wq, 42); ldw(IC<8>{}, wb, 48); }
            if (n == 7) ldw(IC<14>{}, wb, 56);
          }
          HBARS(n == 2 || n == 6 || n == 7);
        });
        if (!last) { ldw(IC<10>{}, wb, 70); wait_vm<14>(); }
      }
      SETPRIO(3);
      if (!last) {
        HOLD(2);
        HBAR();
      }
      static_for<NPQ>([&](auto i) { undef(xq[i.value]); });
    }
  }
}

}

extern "C" void kernel_launch(void* const* d_in, const int* in_sizes, int n_in, void* d_out, int out_size, void* d_ws, size_t ws_size,
                              hipStream_t stream) {
  (void)in_sizes; (void)n_in; (void)out_size; (void)ws_size;
  (void)hipFuncSetAttribute((const void*)k_main, hipFuncAttributeMaxDynamicSharedMemorySize, LDS_BYTES);
  k_main<<<256, 512, LDS_BYTES, stream>>>((const u16*)d_in[0], (const u16*)d_in[1], (const u16*)d_in[2], (const u16*)d_in[4], (const u16*)d_in[5],
                                          (const u16*)d_in[6], (unsigned char*)d_ws, (u16*)d_out);
}
```

```cpp
#include <hip/hip_runtime.h>
#include <stdint.h>
#include <utility>

#define LAS __attribute__((address_space(3)))
#define GAS __attribute__((address_space(1)))
#define DI __device__ __forceinline__
#define RLX_AGENT __ATOMIC_RELAXED, __HIP_MEMORY_SCOPE_AGENT
typedef unsigned short u16;
typedef unsigned v4u __attribute__((ext_vector_type(4)));
typedef unsigned v2u __attribute__((ext_vector_type(2)));
typedef float f32x2 __attribute__((ext_vector_type(2)));
typedef float f32x4 __attribute__((ext_vector_type(4)));
typedef float f32x16 __attribute__((ext_vector_type(16)));
typedef __bf16 bf16x2 __attribute__((ext_vector_type(2)));
typedef __bf16 bf16x8 __attribute__((ext_vector_type(8)));
typedef GAS unsigned gu32;
typedef GAS unsigned long long gu64;

namespace {

constexpr int D_ = 512, F_ = 1792;
constexpr unsigned MB2 = 2097152u, STB = 262144u;
constexpr unsigned FLAG_LAYER = 1048576u, FLAG_G = 2 * FLAG_LAYER, FLAG_BYTES = FLAG_G + 65536;
constexpr unsigned WQKV_OFF = FLAG_BYTES;
constexpr unsigned WO_OFF = WQKV_OFF + 32 * 49152;
constexpr unsigned WGU_OFF = WO_OFF + 16 * 32768;
constexpr unsigned WDN_OFF = WGU_OFF + 32 * 114688;
constexpr unsigned LAY_OFF = WDN_OFF + 16 * 114688;
constexpr unsigned L_K = 0, L_V = MB2, L_O = 2 * MB2, L_X1 = 3 * MB2, L_X = 4 * MB2 + STB, L_H = 5 * MB2 + 2 * STB;
constexpr unsigned LAY_BYTES = L_H + 128 * 57344;
constexpr unsigned WS_BYTES = LAY_OFF + 2 * LAY_BYTES;
constexpr unsigned NONE = 0x40000000u;
constexpr int FL_KV = 0, FL_O = 2048, FL_X1B = 4096, FL_X1A = 6144, FL_HB = 6400, FL_HA = 6656, FL_XB = 6912, FL_XA = 7168, FL_HA2 = 7424;
constexpr int FL_WR = 0, FL_EP = 256, FL_TMO = 257;
constexpr int VBLK = 528;
constexpr int LDS_KL = 114688, LDS_VL = LDS_KL + 16384, LDS_QL = LDS_VL + 32 * VBLK, LDS_CS = LDS_QL + 8192, LDS_ST = LDS_CS + 1536;
constexpr int LDS_MK = LDS_ST + 1536, LDS_TAG = LDS_MK + 2048;
constexpr int LDS_WO = 81920, LDS_TB = 49152;
constexpr int LDS_BYTES = 163840;
#define TSTR 1048576u
constexpr int CODE_LINES = 14;
constexpr unsigned SPIN_CAP = 1u << 16;
constexpr int AUX_SC1 = 16;
constexpr float LOG2E = 1.4426950408889634f;

DI f32x16 mfma(v4u a, v4u b, f32x16 c) {
  return __builtin_amdgcn_mfma_f32_32x32x16_bf16(__builtin_bit_cast(bf16x8, a), __builtin_bit_cast(bf16x8, b), c, 0, 0, 0);
}
DI f32x4 mfma16(v4u a, v4u b, f32x4 c) {
  return __builtin_amdgcn_mfma_f32_16x16x32_bf16(__builtin_bit_cast(bf16x8, a), __builtin_bit_cast(bf16x8, b), c, 0, 0, 0);
}
DI unsigned pk2(float lo, float hi) {
  f32x2 p = {lo, hi};
  return __builtin_bit_cast(unsigned, __builtin_convertvector(p, bf16x2));
}
DI float bfr(float x) { return __uint_as_float(pk2(x, 0.f) << 16); }
DI float bflo(unsigned u) { return __uint_as_float(u << 16); }
DI float bfhi(unsigned u) { return __uint_as_float(u & 0xffff0000u); }
DI float amul(float a, float b) { float d; asm("v_mul_f32 %0, %1, %2" : "=v"(d) : "v"(a), "v"(b)); return d; }
DI float afma(float a, float b, float c) { float d; asm("v_fma_f32 %0, %1, %2, %3" : "=v"(d) : "v"(a), "v"(b), "v"(c)); return d; }
DI float aadd(float a, float b) { float d; asm("v_add_f32 %0, %1, %2" : "=v"(d) : "v"(a), "v"(b)); return d; }
DI float asub(float a, float b) { float d; asm("v_sub_f32 %0, %1, %2" : "=v"(d) : "v"(a), "v"(b)); return d; }
DI float xhalf_sum(float v) {
  float t;
  asm("v_mov_b32 %1, %0\n\ts_nop 1\n\tv_permlane32_swap_b32 %0, %1\n\tv_add_f32 %0, %0, %1" : "+v"(v), "=&v"(t));
  return v;
}
DI float xrow_sum(float v) {
  float t;
  asm("v_mov_b32 %1, %0\n\ts_nop 1\n\tv_permlane16_swap_b32 %0, %1\n\tv_add_f32 %0, %0, %1" : "+v"(v), "=&v"(t));
  return v;
}
DI float xhalf_max(float v) {
  auto r = __builtin_amdgcn_permlane32_swap(__float_as_uint(v), __float_as_uint(v), false, false);
  return fmaxf(__uint_as_float(r[0]), __uint_as_float(r[1]));
}
DI void swap16(unsigned& a, unsigned& b) { asm("s_nop 1\n\tv_permlane16_swap_b32 %0, %1" : "+v"(a), "+v"(b)); }
DI void swap32(unsigned& a, unsigned& b) { asm("s_nop 1\n\tv_permlane32_swap_b32 %0, %1" : "+v"(a), "+v"(b)); }
DI f32x16 splat16(float v) {
  f32x16 z;
#pragma unroll
  for (int i = 0; i < 16; ++i) z[i] = v;
  return z;
}
DI f32x4 zero4() { return (f32x4){0.f, 0.f, 0.f, 0.f}; }
DI int vopq(int x) { asm volatile("" : "+v"(x)); return x; }
DI int lane_id() { int l; asm volatile("v_mbcnt_lo_u32_b32 %0, -1, 0\n\tv_mbcnt_hi_u32_b32 %0, -1, %0" : "=v"(l)); return l; }
template <int LN> DI unsigned wrl(unsigned v, unsigned s) { asm volatile("v_writelane_b32 %0, %1, %2" : "+v"(v) : "s"(s), "i"(LN)); return v; }
template <class T> DI void undef(T& x) { asm volatile("" : "=v"(x)); }
DI int sopq(int x) { asm volatile("" : "+s"(x)); return x; }
#define SWZ(l) ((l) ^ (((l) >> 3) & 7))
#define COORDS() \
  const int lane = lane_id(), lo = lane * 16, hh = lane >> 5, t16 = lane & 15, g4 = lane >> 4, lsw = SWZ(lane) * 16; \
  const int w = sopq(w0) & 3, sp = sopq(sp0), c = sopq(c0), n32 = c >> 1, rh = c & 1; \
  const unsigned lay = (unsigned)sopq((int)lay0); \
  const unsigned flo = sopq(layer) * FLAG_LAYER; \
  gu32* const fl = (gu32*)(ws + flo); \
  (void)lo; (void)hh; (void)t16; (void)g4; (void)lsw; (void)n32; (void)rh; (void)lay; (void)sp; (void)c; (void)w; (void)fl;
#define HBARS(c) asm volatile("s_waitcnt lgkmcnt(0)\n\ts_barrier" ::: "memory")
#define CST() do {} while (0)
#define DBGC(k) asm volatile("" ::: "memory")
#define HBAR() asm volatile("s_waitcnt lgkmcnt(0)\n\ts_barrier" ::: "memory")
#define BAR() asm volatile("s_waitcnt lgkmcnt(0)\n\ts_barrier" ::: "memory")
#define BARX() asm volatile("s_barrier" ::: "memory")
#define DRAIN() asm volatile("s_waitcnt vmcnt(0)" ::: "memory")
#define PIN() __builtin_amdgcn_sched_barrier(0)
#define SETPRIO(n) __builtin_amdgcn_s_setprio(n)
template <int N> DI void wait_vm() { static_assert(N >= 0 && N < 64, ""); asm volatile("s_waitcnt vmcnt(%0)" ::"i"(N) : "memory"); }

typedef __amdgpu_buffer_rsrc_t rsrc_t;
DI v4u ld16(rsrc_t rs, unsigned voff, unsigned soff) { return __builtin_amdgcn_raw_buffer_load_b128(rs, voff, soff, AUX_SC1); }
DI f32x2 ld8f(rsrc_t rs, unsigned voff, unsigned soff) { return __builtin_bit_cast(f32x2, __builtin_amdgcn_raw_buffer_load_b64(rs, voff, soff, AUX_SC1)); }
DI void st16(rsrc_t rs, v4u v, unsigned voff, unsigned soff) {
  __builtin_amdgcn_raw_buffer_store_b128(v, rs, voff, soff, AUX_SC1);
  asm volatile("s_nop 1" ::"v"(v));
}
DI void st8(rsrc_t rs, v2u v, unsigned voff, unsigned soff) { __builtin_amdgcn_raw_buffer_store_b64(v, rs, voff, soff, AUX_SC1); }
DI void st4(rsrc_t rs, unsigned v, unsigned voff, unsigned soff) { __builtin_amdgcn_raw_buffer_store_b32(v, rs, voff, soff, AUX_SC1); }
DI v4u lds16(LAS unsigned char* L, int off) { return *(LAS v4u*)(L + off); }
DI f32x4 lds4f(LAS unsigned char* L, int off) { return *(LAS f32x4*)(L + off); }

DI void poll(gu32* word, bool active, gu32* tmo, unsigned tag) {
  for (unsigned spins = 0;;) {
    unsigned v = tag;
    if (active) v = __hip_atomic_load(word, RLX_AGENT);
    if (__all(v == tag)) break;
    ++spins;
    if (spins > SPIN_CAP) { __hip_atomic_store(tmo, tag, RLX_AGENT); break; }
    if ((spins & 255u) == 0u && (unsigned)__builtin_amdgcn_readfirstlane(__hip_atomic_load(tmo, RLX_AGENT)) == tag) break;
  }
  __builtin_amdgcn_fence(__ATOMIC_ACQUIRE, "wavefront");
}

template <int I> DI void dma1(rsrc_t rs, LAS unsigned char* dst, unsigned voff, unsigned soff) {
  __builtin_amdgcn_raw_ptr_buffer_load_lds(rs, (LAS void*)dst, 16, voff, soff, I * 1024, AUX_SC1);
}
template <int V> using IC = std::integral_constant<int, V>;
template <class FN, int... I> DI void static_for_(FN fn, std::integer_sequence<int, I...>) { (fn(IC<I>{}), ...); }
template <int N, class FN> DI void static_for(FN fn) { static_for_(fn, std::make_integer_sequence<int, N>{}); }
template <int N> DI void dma(rsrc_t rs, LAS unsigned char* dst, unsigned lo, unsigned src) {
  static_for<N>([&](auto i) { dma1<i.value & 3>(rs, dst + (i.value >> 2) * 4096, lo, src + (i.value >> 2) * 4096); });
}

struct Stats {
  f32x4 p[16];
  float m0, m1, q0, q1;
  DI void load(rsrc_t rs, int lane, unsigned base, int i0 = 0, int i1 = 16) {
#pragma unroll
    for (int i = i0; i < i1; ++i) p[i] = __builtin_bit_cast(f32x4, ld16(rs, (lane >> 4) * 4096 + (lane & 15) * 16 + i * 256, base));
    PIN();
  }
  DI void part1() {
    m0 = m1 = q0 = q1 = 0.f;
#pragma unroll
    for (int i = 0; i < 16; ++i) { m0 += p[i][0]; q0 += p[i][1]; m1 += p[i][2]; q1 += p[i][3]; }
    asm volatile("" : "+v"(m0), "+v"(m1), "+v"(q0), "+v"(q1));
  }
  DI void part2() {
    const float a0 = m0 * (1.0f / 16.0f), a1 = m1 * (1.0f / 16.0f);
    q0 *= 32.0f; q1 *= 32.0f;
#pragma unroll
    for (int i = 0; i < 16; ++i) { const float d0 = p[i][0] - a0, d1 = p[i][2] - a1; q0 += d0 * d0; q1 += d1 * d1; }
    asm volatile("" : "+v"(q0), "+v"(q1));
  }
  DI void all(LAS unsigned char* L, int lane, int dst = LDS_ST) { part1(); part2(); part3(L, lane, dst); }
  DI void part3(LAS unsigned char* L, int lane, int dst = LDS_ST) {
    const float den0 = bfr(__builtin_amdgcn_sqrtf(bfr(bfr(q0 * (1.0f / (32.0f * D_))) + 1e-5f)));
    const float den1 = bfr(__builtin_amdgcn_sqrtf(bfr(bfr(q1 * (1.0f / (32.0f * D_))) + 1e-5f)));
    *(LAS f32x2*)(L + dst + lane * 8) = (f32x2){__builtin_amdgcn_rcpf(den0), __builtin_amdgcn_rcpf(den1)};
    *(LAS f32x2*)(L + dst + 512 + lane * 8) = (f32x2){den0 * den0, den1 * den1};
    *(LAS v2u*)(L + dst + 1024 + lane * 8) = (v2u){pk2(m0 * (-1.0f / D_), m0 * (-1.0f / D_)), pk2(m1 * (-1.0f / D_), m1 * (-1.0f / D_))};
  }
};
DI float lds1f(LAS unsigned char* L, int off) { return *(LAS float*)(L + off); }
DI v4u mfrag(LAS unsigned char* L, int off) { const unsigned n = *(LAS unsigned*)(L + off); return (v4u){n, n, 0u, 0u}; }

constexpr int LA = 6;
template <int NF, class RD, class STEP>
DI void pipe(RD rd, STEP step) {
  v4u wf[8];
  static_for<LA>([&](auto i) { wf[i.value] = rd(i); });
  step(IC<-1>{}, wf[0], IC<1>{});
  PIN();
  static_for<NF>([&](auto fc) {
    constexpr int f = fc.value;
    if constexpr ((f & 1) == 0 && f + LA <= NF) __builtin_amdgcn_s_waitcnt(0xc07f | ((LA - 2) << 8));
    step(fc, wf[f & 7], IC<0>{});
    if constexpr (f + LA < NF) wf[(f + LA) & 7] = rd(IC<f + LA>{});
    step(fc, wf[f & 7], IC<1>{});
    PIN();
  });
}
DI v4u csfrag(LAS unsigned char* L, int entry) {
  const v2u t = *(LAS v2u*)(L + LDS_CS + entry * 8);
  return (v4u){t.x, t.y, 0u, 0u};
}
DI void cs_put(LAS unsigned char* L, int entry, float cs) {
  const float hi = bfr(cs), mid = bfr(cs - hi), lw = (cs - hi) - mid;
  *(LAS v2u*)(L + LDS_CS + entry * 8) = (v2u){pk2(hi, mid), pk2(lw, 0.f)};
}
DI unsigned mulpk(float a, float s0, float b, float s1) {
  unsigned d; float t;
  asm("v_mul_f32 %0, %2, %3\n\tv_mul_f32 %1, %4, %5\n\tv_cvt_pk_bf16_f32 %0, %0, %1" : "=&v"(d), "=&v"(t) : "v"(a), "v"(s0), "v"(b), "v"(s1));
  return d;
}
DI unsigned nos(unsigned v) { return v < 0xfffffffeu ? v : 0xfffffffeu; }
DI v4u nos4(v4u v) { return (v4u){nos(v[0]), nos(v[1]), nos(v[2]), nos(v[3])}; }
DI unsigned umax3(unsigned a, unsigned b, unsigned c) { const unsigned t = a > b ? a : b; return t > c ? t : c; }
template <int N> DI bool missing(const v4u* f, int step = 1) {
  unsigned m0 = 0, m1 = 0;
#pragma unroll
  for (int i = 0; i < N; ++i) { m0 = umax3(m0, f[i * step][0], f[i * step][1]); m1 = umax3(m1, f[i * step][2], f[i * step][3]); }
  return __builtin_amdgcn_ballot_w64((m0 > m1 ? m0 : m1) == 0xffffffffu) != 0;
}
constexpr unsigned DATA_CAP = 1u << 14;
DI v4u pack8(const float* y) { return (v4u){pk2(y[0], y[1]), pk2(y[2], y[3]), pk2(y[4], y[5]), pk2(y[6], y[7])}; }

struct Fin {
  float y[8], s;
  v4u o;
  v2u sq;
  template <int K, bool ST = true> DI void chunk(const f32x4& a0, const f32x4& a1, const v4u& r, rsrc_t ro, unsigned vo, unsigned so, rsrc_t rs, unsigned vs, unsigned ss) {
    if constexpr (K == 0) { y[0] = aadd(a0[0], bflo(r[0])); y[1] = aadd(a0[1], bfhi(r[0])); y[2] = aadd(a0[2], bflo(r[1])); y[3] = aadd(a0[3], bfhi(r[1])); }
    if constexpr (K == 1) { y[4] = aadd(a1[0], bflo(r[2])); y[5] = aadd(a1[1], bfhi(r[2])); y[6] = aadd(a1[2], bflo(r[3])); y[7] = aadd(a1[3], bfhi(r[3])); }
    if constexpr (K == 2) { o = nos4(pack8(y)); if constexpr (ST) st16(ro, o, vo, so); }
    if constexpr (K == 3) {
#pragma unroll
      for (int m = 0; m < 4; ++m) { y[2 * m] = bflo(o[m]); y[2 * m + 1] = bfhi(o[m]); }
    }
    if constexpr (K == 4) s = xrow_sum(aadd(aadd(aadd(y[0], y[1]), aadd(y[2], y[3])), aadd(aadd(y[4], y[5]), aadd(y[6], y[7]))));
    if constexpr (K == 5) {
      s = xhalf_sum(s);
      const float mu = s * (1.0f / 32.0f);
#pragma unroll
      for (int i = 0; i < 8; ++i) y[i] = asub(y[i], mu);
    }
    if constexpr (K == 6) {
      y[0] = amul(y[0], y[0]); y[1] = amul(y[1], y[1]);
#pragma unroll
      for (int i = 2; i < 8; ++i) y[i & 1] = afma(y[i], y[i], y[i & 1]);
    }
    if constexpr (K == 7) {
      sq = (v2u){nos(__float_as_uint(s)), nos(__float_as_uint(xhalf_sum(xrow_sum(aadd(y[0], y[1])))))};
      if constexpr (ST) st8(rs, sq, vs, ss);
    }
  }
};

constexpr unsigned long long low_mask(int r) {
  unsigned long long m = 0;
  for (int l = 0; l < 64; ++l)
    if ((r & 3) + 8 * (r >> 2) + 4 * (l >> 5) <= (l & 31)) m |= 1ull << l;
  return m;
}
DI float sel(unsigned long long mask, float set, float unset) {
  float d;
  asm("v_cndmask_b32_e64 %0, %1, %2, %3" : "=v"(d) : "v"(unset), "v"(set), "s"(mask));
  return d;
}
DI float max16(float m, const f32x16& s) {
#pragma unroll
  for (int r = 0; r < 16; r += 2) m = __builtin_fmaxf(__builtin_fmaxf(m, s[r]), s[r + 1]);
  return m;
}
DI v4u exp_pack(const f32x16& s, int h) {
  float pr[8];
#pragma unroll
  for (int r = 0; r < 8; ++r) pr[r] = __builtin_amdgcn_exp2f(s[8 * h + r]);
  return pack8(pr);
}
template <bool EXACT>
DI f32x16 attn_pass(LAS unsigned char* L, int lane, int tl, int nv) {
  const int lo = lane * 16;
  const int kb = LDS_KL + tl * 1024 + lo, kf0 = LDS_KL + (tl + 8) * 1024 + (lane >> 5) * 512;
  const v4u q = lds16(L, LDS_QL + tl * 1024 + lo);
  v4u kf[9];
  kf[8] = lds16(L, kb + 8192); kf[0] = lds16(L, nv <= 0 ? kb : kf0);
#pragma unroll
  for (int i = 7; i >= 1; --i) kf[i] = lds16(L, (nv <= i ? kb : kf0 - i * 1024) + i * 1024);
  f32x16 s[9], cm = splat16(0.f);
  if constexpr (EXACT) {
    s[8] = mfma(kf[8], q, splat16(0.f));
    s[0] = mfma(kf[0], q, splat16(0.f));
    s[7] = mfma(kf[7], q, splat16(0.f));
    s[6] = mfma(kf[6], q, splat16(0.f));
    asm volatile("s_nop 7\n\ts_nop 7" : "+v"(s[8]), "+v"(s[0]));
    PIN();
    static_for<16>([&](auto r) { s[8][r.value] = sel(low_mask(r.value), s[8][r.value], s[0][r.value]); });
    float m = max16(s[8][0], s[8]);
    PIN();
    static_for<7>([&](auto ic) {
      constexpr int i = 7 - ic.value;
      if constexpr (i - 2 >= 1) s[i - 2] = mfma(kf[i - 2], q, splat16(0.f));
      m = max16(m, s[i]);
      PIN();
    });
    cm = splat16(-xhalf_max(m));
  }
  const int vb = LDS_VL + 2 * tl * VBLK + ((lane & 16) ? 512 : (lane >> 5) * 256 + (lane & 15) * 16);
  s[8] = mfma(kf[8], q, cm);
  s[0] = mfma(kf[0], q, cm);
  s[7] = mfma(kf[7], q, cm);
  const v4u mk0 = lds16(L, LDS_MK + lo), mk1 = lds16(L, LDS_MK + 1024 + lo);
  v4u va = lds16(L, vb + 16 * VBLK), vc = lds16(L, vb + 17 * VBLK), v0a = lds16(L, vb), v0c = lds16(L, vb + VBLK);
  asm volatile("s_nop 7\n\ts_nop 7" : "+v"(s[8]), "+v"(s[0]));
  PIN();
  f32x16 o;
  {
    static_for<16>([&](auto r) { s[8][r.value] = sel(low_mask(r.value), s[8][r.value], s[0][r.value]); });
    const v4u p0 = exp_pack(s[8], 0), l0 = p0 & mk0;
    o = mfma(va, l0, splat16(0.f));
    o = mfma(v0a, p0 ^ l0, o);
    const v4u p1 = exp_pack(s[8], 1), l1 = p1 & mk1;
    o = mfma(vc, l1, o);
    o = mfma(v0c, p1 ^ l1, o);
    va = lds16(L, vb + 14 * VBLK); vc = lds16(L, vb + 15 * VBLK);
    PIN();
  }
  static_for<7>([&](auto ic) {
    constexpr int i = 7 - ic.value;
    if constexpr (i > 1) s[i - 1] = mfma(kf[i - 1], q, cm);
    PIN();
    o = mfma(va, exp_pack(s[i], 0), o);
    if constexpr (i > 1) va = lds16(L, vb + (2 * i - 2) * VBLK);
    o = mfma(vc, exp_pack(s[i], 1), o);
    if constexpr (i > 1) vc = lds16(L, vb + (2 * i - 1) * VBLK);
    PIN();
  });
  return o;
}
DI v4u attn_unit(LAS unsigned char* L, int lane, int tl, int nv) {
  f32x16 o = attn_pass<false>(L, lane, tl, nv);
  float big = __builtin_fabsf(o[0]);
#pragma unroll
  for (int r = 1; r < 8; ++r) big += __builtin_fabsf(o[r]);
  if (__builtin_expect(!__all(o[8] > 0x1p-30f && o[8] < 0x1p60f && big < 0x1p120f), 0)) {
    asm volatile("" ::: "memory");
    o = attn_pass<true>(L, lane, tl, nv);
  }
  const float rl = __builtin_amdgcn_rcpf(o[8]);
  float y[8];
#pragma unroll
  for (int r = 0; r < 8; ++r) y[r] = o[r] * rl;
  return pack8(y);
}

struct Retile {
  v4u in[8];
  DI void load(const u16* src, int ld, int rs4) {
#pragma unroll
    for (int j = 0; j < 8; ++j) in[j] = *(const v4u*)(src + (size_t)(((j & 3) + rs4 * (j >> 2)) * ld));
    PIN();
  }
  unsigned d[8];
  DI void out(rsrc_t rs) {
#pragma unroll
    for (int i = 0; i < 8; ++i) st16(rs, in[i], d[i], 0);
  }
  DI void lines(LAS unsigned char* t, int lane, int sw, unsigned dst) {
#pragma unroll
    for (int e = 0; e < 8; ++e) {
      v4u o;
#pragma unroll
      for (int m = 0; m < 4; ++m) o[m] = __builtin_amdgcn_perm(in[2 * m + 1][e >> 1], in[2 * m][e >> 1], (e & 1) ? 0x07060302u : 0x05040100u);
      *(LAS v4u*)(t + lane * 128 + (e ^ sw ^ (lane & 7)) * 16) = o;
    }
    asm volatile("s_waitcnt lgkmcnt(0)" ::: "memory");
#pragma unroll
    for (int i = 0; i < 8; ++i) {
      in[i] = *(LAS v4u*)(t + i * 1024 + (lane ^ (lane >> 3)) * 16);
      d[i] = (unsigned)__builtin_amdgcn_ds_bpermute((8 * i + (lane >> 3)) * 4, (int)dst) + (lane & 7) * 16;
    }
    PIN();
  }
  DI void put(LAS unsigned char* L, int pb, int dd, int lp, int dl) {
#pragma unroll
    for (int e = 0; e < 8; ++e) {
      v4u o;
#pragma unroll
      for (int m = 0; m < 4; ++m) o[m] = __builtin_amdgcn_perm(in[2 * m + 1][e >> 1], in[2 * m][e >> 1], (e & 1) ? 0x07060302u : 0x05040100u);
      const int s = lp + (e & 3) + (e >> 2) * dl;
      *(LAS v4u*)(L + pb + (e >> 2) * dd + (s ^ ((s >> 3) & 7)) * 16) = o;
    }
  }
};

struct GS { int cs, j, kap, off; };
constexpr GS gu_step(int u) {
  if (u < 64) { const int k = u >> 2, j = u & 3; return GS{0, j, k, j < 3 ? (7 * (k >> 1) + 3 * (k & 1) + j) * 1024 : (7 * (k >> 1) + 6) * 1024 + (k & 1) * 512}; }
  const int v = u - 64, j = v / 17, i = v % 17;
  return GS{i == 16, j, 16 + i, 57344 + j * 16384 + i * (j < 3 ? 1024 : 512)};
}
constexpr int GA = 132, DB = 264, DA = 376, NST = 488;
constexpr int NPG = 16, NPQ = 12, LDN = 12;
constexpr int ld_step(int id) {
  if (id < 32) return id < NPG ? -1 : 2 * (id - NPG);
  if (id < 64) { const int n = id - 32; return n < 12 ? 103 + (5 * n) / 2 : GA + 10 + 2 * (n - 12); }
  if (id < 176) {
    const int n = id - 64;
    return n < 12 ? GA + 103 + n : n < 26 ? DB - 15 + ((n - 12) * 7) / 4 : n < 56 ? DB + 27 + ((n - 26) * 5) / 3 : n < 84 ? DB + 79 + ((n - 56) * 4) / 3 : DA + 31 + 2 * (n - 84);
  }
  if (id < 208) { const int n = id - 176; return n < NPQ ? DA + 87 + 2 * n : -1; }
  if (id < 224) return -1;
  return id == 224 ? DB + 80 : DA + 72;
}
constexpr int NEP = 56;
constexpr int ep_step(int k) { return 83 + 17 * (k / 16) + k % 16; }
struct Tab { short ld[NST][2]; short ep[NST]; short nvm[NST]; };
constexpr int FINB = DA + 3;
#define Q3T 75
#define XA0 18
constexpr int xa_step(int j) { const int a = XA0 + j, b = j < 12 ? 3 * j + 2 : 34 + j; return a > b ? a : b; }
constexpr Tab make_tab() {
  Tab t{};
  for (int s = 0; s < NST; ++s) { t.ld[s][0] = t.ld[s][1] = t.ep[s] = -1; t.nvm[s] = 0; }
  for (int id = 0; id < 226; ++id) {
    const int s = ld_step(id);
    if (s < 0) continue;
    t.ld[s][t.ld[s][0] >= 0] = (short)id; ++t.nvm[s];
  }
  for (int job = 0; job < 2; ++job)
    for (int k = 0; k < NEP; ++k) {
      const int s = job * GA + ep_step(k);
      t.ep[s] = (short)(job * 64 + k);
      if (k == NEP - 1) t.nvm[s] += 2;
      if (k == (job ? 31 : NEP - 1)) t.nvm[s] += 2;
    }
  ++t.nvm[FINB + 2]; ++t.nvm[FINB + 7];
  return t;
}
constexpr Tab TAB = make_tab();
constexpr int vm_between(int a, int b) {
  int n = 0;
  for (int s = a + 1; s <= b; ++s) n += TAB.nvm[s];
  return n;
}

__global__ void __launch_bounds__(512, 2) k_main(const u16* __restrict__ x, const u16* __restrict__ wqkv, const u16* __restrict__ wo, const u16* __restrict__ wg,
                                                 const u16* __restrict__ wu, const u16* __restrict__ wd, unsigned char* __restrict__ ws,
                                                 u16* __restrict__ out) {
  extern __shared__ __attribute__((aligned(16))) unsigned char smem[];
  LAS unsigned char* L = (LAS unsigned char*)smem;
  const int tid = threadIdx.x, w0 = __builtin_amdgcn_readfirstlane(tid >> 6);
  const int b = blockIdx.x;
  unsigned touch0 = 0;
  {
    unsigned long long pc0;
    asm volatile("s_getpc_b64 %0" : "=s"(pc0));
    const rsrc_t rc = __builtin_amdgcn_make_buffer_rsrc((void*)(pc0 & ~127ull), 0, 32 * CODE_LINES * 128, 0x00020000);
    if (w0 == 3) touch0 = __builtin_amdgcn_raw_buffer_load_b32(rc, (tid & 63) < CODE_LINES ? (tid & 63) * 128 : NONE, (b >> 3) * CODE_LINES * 128, 0);
    PIN();
  }
  const int sp0 = (b & 6) + ((b >> 3) & 1), c0 = (b >> 4) + 16 * (b & 1);
  const rsrc_t rs = __builtin_amdgcn_make_buffer_rsrc((void*)ws, 0, (int)WS_BYTES, 0x00020000);
  const bool compute = w0 < 4;
  gu32* const fg = (gu32*)(ws + FLAG_G);
  gu32* const tmo = fg + FL_TMO * 32;
  unsigned tag = 0;
#define HOLD(k) do {} while (0)
#define FLAG(blk, line) st4(rs, tag, lane ? NONE : 0u, (blk) + (line) * 128)
#define POLL(blk, line, n) poll((gu32*)(ws + (blk)) + ((line) + lane) * 32, lane < (n), tmo, tag)
#define GETTAG() tag = (unsigned)__builtin_amdgcn_readfirstlane(*(LAS unsigned*)(L + LDS_TAG))
  auto blank = [&](int it, unsigned ly) {
    const int lane = lane_id(), sp = sopq(sp0), c = sopq(c0);
    const unsigned o = (unsigned)vopq(-1);
    if (it < 20) st16(rs, (v4u){o, o, o, o}, lane * 16, ly + (it < 16 ? (it & 8 ? L_V : L_K) + ((8 * sp + (it & 7)) * 32 + c) * 1024 : L_X + ((16 * sp + 4 * (c & 1) + (it & 3)) * 16 + (c >> 1)) * 1024));
    else st8(rs, (v2u){o, o}, (lane >> 5) * 4096 + (lane & 31) * 8, ly + L_X + MB2 + (8 * sp + 2 * (c & 1)) * 4096 + (c >> 1) * 256);
  };
  v4u xq[32];

  if (compute) {
    const int layer = 0; const unsigned lay0 = LAY_OFF;
    COORDS();
    SETPRIO(2);
    const int r = lane >> 3, j = lane & 7;
    const u16* src = x + (size_t)(256 * sp + 8 * c + r) * D_ + 128 * w + 8 * j;
    v4u a[2] = {*(const v4u*)src, *(const v4u*)(src + 64)};
#pragma unroll
    for (int i = 0; i < 2; ++i) {
      a[i] = nos4(a[i]);
      st16(rs, a[i], (j >> 2) * 1024 + ((j & 3) * 16 + r) * 16, lay + L_X + (16 * sp + n32) * 16384 + (2 * w + i) * 2048 + rh * 128);
      auto quad = [](float v) {
        v += __int_as_float(__builtin_amdgcn_update_dpp(0, __float_as_int(v), 0xb1, 0xf, 0xf, true));
        return v + __int_as_float(__builtin_amdgcn_update_dpp(0, __float_as_int(v), 0x4e, 0xf, 0xf, true));
      };
      float sm = 0.f, q = 0.f;
#pragma unroll
      for (int m = 0; m < 4; ++m) sm += bflo(a[i][m]) + bfhi(a[i][m]);
      sm = quad(sm);
      const float mu = sm * (1.0f / 32.0f);
#pragma unroll
      for (int m = 0; m < 4; ++m) { const float e0 = bflo(a[i][m]) - mu, e1 = bfhi(a[i][m]) - mu; q += e0 * e0 + e1 * e1; }
      q = quad(q);
      st8(rs, (v2u){nos(__float_as_uint(sm)), nos(__float_as_uint(q))}, (j & 3) ? NONE : (j >> 2) * 256 + r * 8,
          lay + L_X + MB2 + (8 * sp + (c >> 2)) * 4096 + (4 * w + 2 * i) * 256 + (c & 3) * 64);
    }
    DRAIN();
    asm volatile("" ::"v"(touch0));
    BAR();
    BAR();
    GETTAG();
    const int TB = 8 * sp + 4 + w;
    static_for<NPQ>([&](auto nc) { constexpr int n = nc.value; xq[n] = ld16(rs, lo + ((n >> 1) & 3) * 1024, lay + L_X + TB * 32768 + (n & 1) * 16384 + (n >> 3) * 4096); });
    BARX();
  } else {
    const int layer = 0; const unsigned lay0 = LAY_OFF;
    COORDS();
    SETPRIO(3);
    Retile rt;
    LAS unsigned char* tb = L + LDS_TB + w * 8192;
    const int id = 2 * sp + rh + 16 * (w - 1);
    auto fill0 = [&]() {
#pragma unroll 1
      for (int it = w - 1; it < 16; it += 3) blank(it, lay);
      PIN();
    };
    if (w == 0 || id < 28) {
      const u16* src; int ld, sw; unsigned dst;
      if (w == 0) {
        const int fi = lane >> 3, i8 = lane & 7, F = 6 * sp + (fi < 6 ? fi : 0);
        const int ks = F < 36 ? F / 3 : F < 44 ? (F - 12) >> 1 : F - 32, m = F < 36 ? (F - 3 * ks + 1) % 3 : F < 44 ? 1 + (F & 1) : 0;
        src = wqkv + (size_t)(32 * ks + 8 * (i8 >> 1)) * (3 * D_) + m * D_ + 16 * c + 8 * (i8 & 1);
        ld = 3 * D_; sw = i8; dst = fi < 6 ? WQKV_OFF + c * 49152 + F * 1024 + i8 * 128 : NONE;
      } else {
        const int t = 64 * id + lane, mo = t / 14, ch = t - 14 * mo, mat = mo >> 6, o = mo & 63, c7 = ch >= 7 ? ch - 7 : ch, j = c7 >> 1, kap = o >> 1;
        src = (mat ? wu : wg) + (size_t)(8 * o) * F_ + 112 * n32 + 8 * ch;
        ld = F_;
        const int cell = kap < 16 ? 7 * (kap >> 1) + (j < 3 ? 3 * (kap & 1) + j : 6) : 56 + 16 * j + (j < 3 ? kap - 16 : (kap - 16) >> 1);
        const int rho = j < 3 ? 2 * mat + (c7 & 1) + 4 * (o & 1) : 4 * (kap & 1) + 2 * (o & 1) + mat;
        sw = j < 3 ? rho : 0; dst = WGU_OFF + (2 * n32 + (ch >= 7)) * 114688 + cell * 1024 + rho * 128;
      }
      unsigned ep = 0;
      if (w == 0) ep = __hip_atomic_load(fg + FL_EP * 32, RLX_AGENT);
      rt.load(src, ld, 4);
      if (w) fill0();
      if (w == 0) {
        tag = __builtin_amdgcn_readfirstlane(ep) + 1u;
        if (lane < 2) *(LAS v4u*)(L + LDS_TAG + lo) = (v4u){lane ? 0u : tag, 0u, 0u, 0u};
        v4u mk[2];
#pragma unroll
        for (int k = 0; k < 8; ++k) {
          const int kq = ((2 * k) & 3) + 8 * ((2 * k) >> 2) + 4 * hh, ql = lane & 31;
          mk[k >> 2][k & 3] = (kq <= ql ? 0xffffu : 0u) | (kq + 1 <= ql ? 0xffff0000u : 0u);
        }
        *(LAS v4u*)(L + LDS_MK + lo) = mk[0];
        *(LAS v4u*)(L + LDS_MK + 1024 + lo) = mk[1];
        if (sp == 0) {
#pragma unroll
          for (int i = 0; i < 9; ++i) *(LAS v4u*)(L + LDS_VL + (i < 8 ? i * 1024 + lo : 8192 + t16 * 16)) = (v4u){0u, 0u, 0u, 0u};
        }
        const unsigned one = (sp == 0 && lane < 16) ? 0u : 0x3f803f80u;
        if (lane < 32) *(LAS v4u*)(L + LDS_VL + lane * VBLK + 512) = (v4u){one, one, one, one};
      }
      rt.lines(tb, lane, sw, dst);
      if (w == 0) rt.out(rs);
    } else if (id < 46) {
      const int slot = (lane >> 2) & 3, m4 = lane & 3, q = 16 * (id < 42 ? id - 28 : id - 42) + 4 * g4 + slot;
      const int cq = q < 128 ? q >> 2 : q < 192 ? (q - 128) >> 1 : q - 192, r = q < 128 ? q & 3 : q < 192 ? 4 + (q & 1) : 6;
      rt.load((id < 42 ? wd + (size_t)(56 * cq + (r < 6 ? 16 * (r >> 1) + 4 * (r & 1) : 48)) * D_ : wo + (size_t)(16 * (q >> 1) + 4 * (q & 1)) * D_) + 32 * n32 + 8 * m4,
              D_, (id < 42 && r == 6) ? 4 : 8);
      fill0();
      rt.put(tb, g4 * 2048, 1024, 4 * m4 + 16 * slot, 0);
      asm volatile("s_waitcnt lgkmcnt(0)" ::: "memory");
#pragma unroll
      for (int i = 0; i < 8; ++i) {
        rt.in[i] = lds16(tb, i * 1024 + lo);
        rt.d[i] = (id < 42 ? WDN_OFF + n32 * 114688 + (id - 28) * 8192 : WO_OFF + n32 * 32768 + (id - 42) * 8192) + i * 1024 + lo;
      }
    } else {
#pragma unroll
      for (int i = 0; i < 8; ++i) { undef(rt.in[i]); rt.d[i] = NONE; }
      fill0();
    }
    if (w == 0) DRAIN();
    HBAR();
    if (w == 0) {
      FLAG(0, FL_XB + sp * 32 + c);
      poll((gu32*)ws + (FL_XB + (lane < 32 ? sp * 32 + lane : (lane & 7) * 32 + c)) * 32, lane < 40, tmo, tag);
    }
    Stats st;
    if (w == 0) {
      st.load(rs, lane, lay + L_X + MB2 + (8 * sp + 4) * 4096);
    }
    HBAR();
    GETTAG();
    if (w) {
      const int l = w - 1;
      dma<4>(rs, L + l * 4096, lo, WQKV_OFF + c * 49152 + l * 4096);
      dma<7>(rs, L + (12 + 7 * l - (l >> 1)) * 1024, lo, WQKV_OFF + c * 49152 + (12 + 7 * l - (l >> 1)) * 1024);
      wait_vm<7>();
    }
    HOLD(0);
    HBAR();
    if (w) { dma<6>(rs, L + (27 + 5 * w) * 1024, lo, WQKV_OFF + c * 49152 + (27 + 5 * w) * 1024);
      rt.out(rs); wait_vm<14>();
    }
    else {
      st.all(L, lane);
      PIN();
      FLAG(0, FL_XA + sp * 32 + c);
      const int i = lane & 31;
      const int line = i < 8 ? FL_KV + (8 * sp + i) * 32 + c : i < 16 ? FL_O + (8 * sp + i - 8) * 32 + c
                     : i < 20 ? FL_X1B + (8 * sp + 4 + 2 * rh + ((i >> 1) & 1)) * 32 + 2 * n32 + (i & 1) : FL_X1A + (i - 20 < 6 ? i - 20 : 5) * 256 + sp * 32 + c;
      touch0 = __builtin_amdgcn_raw_buffer_load_b32(rs, hh * FLAG_LAYER + line * 128, 0, AUX_SC1);
    }
  }

  for (int layer = 0; layer < 2; ++layer) {
    const bool last = layer == 1;
    const unsigned lay0 = LAY_OFF + layer * LAY_BYTES;
    auto ldw = [&](auto nc, unsigned src, int cell) {
      constexpr int N = nc.value, K = (N + 2) / 3;
      const int l = sopq(w0) - 5, c1 = cell + (l * K + K > N ? N - K : l * K);
      dma<K>(rs, L + c1 * 1024, lane_id() * 16, src + c1 * 1024);
    };
    auto ld_gu = [&](int s) { ldw(IC<14>{}, WGU_OFF + sopq(c0) * 114688, 14 * s); };
    auto ld_dn = [&](int s) { ldw(IC<14>{}, WDN_OFF + (sopq(c0) >> 1) * 114688, 14 * s); };
    if (compute) {
      COORDS();
      const int TB = 8 * sp + 4 + w, TA = TB - 4;
      v4u xa[32];
      auto ldx = [&](auto nc, int T) {
        constexpr int n = nc.value;
        return ld16(rs, lo + ((n >> 1) & 3) * 1024, lay + L_X + (2 * T + (n & 1)) * 16384 + (n >> 3) * 4096);
      };
      f32x4 aq[2][2], ak[2][2], av[2][2], r0, r1;
      v4u mf0, mf1; float rd0, rd1;
      unsigned o0, o1, o2, o3;
      const int kpos = (t16 + 16 * hh + 32 * (g4 & 1)) * 16;
      auto epi = [&](int k, int pa) {
        const int tl = pa ? w : 4 + w, st = (pa ? LDS_KL : LDS_ST) + w * 128;
        const unsigned kvb = lay + ((8 * sp + tl) * 32 + c) * 1024;
        if (k == 0) { r0 = lds4f(L, st + g4 * 16); r1 = lds4f(L, st + 64 + g4 * 16); }
        if (k == 2) { o0 = mulpk(ak[pa][0][0], rd0, ak[pa][0][1], rd0); o1 = mulpk(ak[pa][0][2], rd0, ak[pa][0][3], rd0); }
        if (k == 3) { o2 = mulpk(ak[pa][1][0], rd1, ak[pa][1][1], rd1); o3 = mulpk(ak[pa][1][2], rd1, ak[pa][1][3], rd1); }
        if (k == 4) {
          swap32(o0, o2); swap32(o1, o3);
          st16(rs, nos4((v4u){o0, o1, o2, o3}), kpos, kvb + L_K);
          *(LAS v4u*)(L + LDS_KL + (8 + tl) * 1024 + kpos) = (v4u){o0, o1, o2, o3};
        }
        if (k == 5) { o0 = mulpk(av[pa][0][0], r0[0], av[pa][0][1], r0[1]); o1 = mulpk(av[pa][0][2], r0[2], av[pa][0][3], r0[3]); }
        if (k == 6) { o2 = mulpk(av[pa][1][0], r1[0], av[pa][1][1], r1[1]); o3 = mulpk(av[pa][1][2], r1[2], av[pa][1][3], r1[3]); }
        if (k == 7) {
          swap32(o0, o2); swap32(o1, o3);
          st16(rs, nos4((v4u){o0, o1, o2, o3}), lo, kvb + L_V);
          *(LAS v4u*)(L + LDS_VL + (16 + 2 * tl + hh) * VBLK + (lane & 31) * 16) = (v4u){o0, o1, o2, o3};
        }
        if (k == 8) {
          asm volatile("s_nop 7" : "+v"(aq[pa][0]), "+v"(aq[pa][1]));
          const float s0 = rd0 * (0.25f * LOG2E), s1 = rd1 * (0.25f * LOG2E);
          o0 = mulpk(aq[pa][0][0], s0, aq[pa][0][1], s0); o1 = mulpk(aq[pa][0][2], s0, aq[pa][0][3], s0);
          o2 = mulpk(aq[pa][1][0], s1, aq[pa][1][1], s1); o3 = mulpk(aq[pa][1][2], s1, aq[pa][1][3], s1);
          swap32(o0, o2); swap32(o1, o3);
          *(LAS v4u*)(L + LDS_QL + tl * 1024 + kpos) = (v4u){o0, o1, o2, o3};
        }
      };
      pipe<102>(
          [&](auto fc) {
            constexpr int t = fc.value, u = t % 51;
            if constexpr (t == 28) { BARX(); CST(); }
            if constexpr (t == 12 || t == 44 || t == Q3T) BARX();
            if constexpr (u == 44 || u == 45 || u == 50) return csfrag(L, (u == 50 ? 0 : u - 43) * 16 + t16);
            else return lds16(L, (u < 44 ? u : u - 2) * 1024 + lsw);
          },
          [&](auto fc, const v4u& wfr, auto pc) {
            constexpr int t = fc.value, part = pc.value, u = t % 51;
            constexpr int ks = (u < 36 ? u / 3 : u < 44 ? (u - 12) >> 1 : u - 34) & 15, m = u < 36 ? (u % 3 + 1) % 3 : u < 46 ? 1 + (u & 1) : 0;
            constexpr bool pa = t >= 51, cs = u == 44 || u == 45 || u == 50;
            if constexpr (t < 0) return;
            else if constexpr (part == 0) {
              if constexpr (pa && u < 44 && u % 12 == 0) {
                constexpr int n = u / 12 * 8;
                if (__builtin_expect(missing<8>(xa + n), 0)) {
                  unsigned spins = 0;
                  do { static_for<8>([&](auto i) { xa[n + i.value] = ldx(IC<n + i.value>{}, TA); }); DBGC(0); } while (missing<8>(xa + n) && ++spins < DATA_CAP);
                }
                PIN();
              }
              const v4u& x0 = cs ? mf0 : pa ? xa[2 * ks] : xq[2 * ks];
              const v4u& x1 = cs ? mf1 : pa ? xa[2 * ks + 1] : xq[2 * ks + 1];
              if constexpr (m == 0) { aq[pa][0] = mfma16(wfr, x0, u < 3 ? zero4() : aq[pa][0]); aq[pa][1] = mfma16(wfr, x1, u < 3 ? zero4() : aq[pa][1]); }
              if constexpr (m == 1) { ak[pa][0] = mfma16(wfr, x0, u < 3 ? zero4() : ak[pa][0]); ak[pa][1] = mfma16(wfr, x1, u < 3 ? zero4() : ak[pa][1]); }
              if constexpr (m == 2) { av[pa][0] = mfma16(x0, wfr, u < 3 ? zero4() : av[pa][0]); av[pa][1] = mfma16(x1, wfr, u < 3 ? zero4() : av[pa][1]); }
            } else {
              if constexpr (2 * t < 32 - NPQ) { xq[NPQ + 2 * t] = ldx(IC<NPQ + 2 * t>{}, TB); xq[NPQ + 2 * t + 1] = ldx(IC<NPQ + 2 * t + 1>{}, TB); }
              if constexpr (u == 40) {
                const int st = (pa ? LDS_KL : LDS_ST) + w * 128;
                rd0 = lds1f(L, st + t16 * 4); rd1 = lds1f(L, st + 64 + t16 * 4);
                const int a = g4 ? LDS_TAG + 8 : st + 1024 + t16 * 4;
                mf0 = mfrag(L, a); mf1 = mfrag(L, g4 ? a : a + 64);
              }
              if constexpr (t >= 45 && t < 54) epi(t - 45, 0);
              if constexpr (t >= 96) epi(t - 96, 1);
              static_for<16>([&](auto j) { if constexpr (xa_step(j.value) == t) { xa[2 * j.value] = ldx(IC<2 * j.value>{}, TA); xa[2 * j.value + 1] = ldx(IC<2 * j.value + 1>{}, TA); } });
              if constexpr (t == 50) CST();
            }
          });
      epi(6, 1); epi(7, 1); epi(8, 1);
      BAR();
      CST();
    } else {
      COORDS();
      const bool loader = w != 0;
      auto cs_qkv = [&]() {
        const unsigned one = (unsigned)vopq(0x3f803f80);
        const v4u ones = (v4u){one, one, one, one};
        f32x4 a = zero4();
        v4u f[16];
        const int fa = (w - 1) * 1024 + lsw, fb = (w < 3 ? 35 + w : 44) * 1024 + lsw, fs = w < 3 ? 2048 : 1024;
#pragma unroll
        for (int ks = 0; ks < 16; ++ks) asm volatile("ds_read_b128 %0, %1" : "=v"(f[ks]) : "v"(ks < 12 ? fa + ks * 3072 : fb + (ks - 12) * fs) : "memory");
        asm volatile("s_waitcnt lgkmcnt(0)" : "+v"(f[0]), "+v"(f[1]), "+v"(f[2]), "+v"(f[3]), "+v"(f[4]), "+v"(f[5]), "+v"(f[6]), "+v"(f[7]));
        asm volatile("" : "+v"(f[8]), "+v"(f[9]), "+v"(f[10]), "+v"(f[11]), "+v"(f[12]), "+v"(f[13]), "+v"(f[14]), "+v"(f[15]));
#pragma unroll
        for (int ks = 0; ks < 16; ++ks) a = mfma16(f[ks], ones, a);
        if (t16 == 0) {
#pragma unroll
          for (int r = 0; r < 4; ++r) cs_put(L, (w % 3) * 16 + 4 * g4 + r, a[r]);
        }
      };
      const unsigned wob = WO_OFF + n32 * 32768 - LDS_WO;
      if (last && !loader) {
        v4u o[4];
#pragma unroll
        for (int i = 0; i < 4; ++i) o[i] = lds16(L, LDS_QL + i * 1024 + lo);
        const v2u sq = *(LAS v2u*)(L + LDS_QL + 4096 + lane * 8);
#pragma unroll
        for (int i = 0; i < 4; ++i) st16(rs, o[i], lo, lay + L_X + ((16 * sp + 4 * rh + i) * 16 + n32) * 1024);
        st8(rs, sq, hh * 4096 + (g4 & 1) * 128 + t16 * 8, lay + L_X + MB2 + (8 * sp + 2 * rh) * 4096 + n32 * 256);
        PIN();
      }
      HBARS(false);
      if (layer == 0) {
        if (loader) wait_vm<8>();
        HBAR();
        if (loader) { cs_qkv(); DRAIN(); }
        HBAR();
        if (!loader) FLAG(FLAG_G, FL_WR + n32 * 16 + 2 * sp + rh);
      } else {
        if (loader) { ldw(IC<10>{}, wob, 80); wait_vm<13>(); }
        HBAR();
        if (loader) ldw(IC<10>{}, wob, 90);
        HBAR();
        if (loader) ldw(IC<12>{}, wob, 100);
      }
      {
        v4u kv[4];
        auto ldkv = [&]() {
          const unsigned so = lay + ((8 * sp - 5 + w) * 32 + c) * 1024, s2 = w == 1 ? so + 3 * 32768 : NONE;
          kv[0] = ld16(rs, lo, so + L_K); kv[1] = ld16(rs, lo, so + L_V); kv[2] = ld16(rs, lo, s2 + L_K); kv[3] = ld16(rs, lo, s2 + L_V);
          PIN();
          DBGC(0);
        };
        if (!loader) {
          Stats st;
          gu32* const bl = lane < 16 ? fg + (FL_WR + n32 * 16 + lane) * 32 : (gu32*)ws + (FL_XA + (sp ? sp - 1 : 0) * 32 + c) * 32;
          unsigned spins = 0, pv = tag;
          do { st.load(rs, lane, lay + L_X + MB2 + 8 * sp * 4096); DBGC(0); } while (missing<16>((const v4u*)st.p) && ++spins < DATA_CAP);
          if (!last) pv = __hip_atomic_load(bl, RLX_AGENT);
          st.all(L, lane, LDS_KL);
          if (!__all(pv == tag)) poll(bl, lane <= 16, tmo, tag);
          asm volatile("" ::"v"(touch0));
        }
        HBARS(false);
        if (loader && sp > 0) ldkv();
        if (loader && !last) { ldw(IC<10>{}, wob, 80); ldw(IC<10>{}, wob, 90); ldw(IC<12>{}, wob, 100); }
        if (loader && sp > 0) {
          for (unsigned spins = 0; missing<4>(kv) && spins < DATA_CAP; ++spins) ldkv();
          *(LAS v4u*)(L + LDS_KL + (3 + w) * 1024 + lo) = kv[0];
          *(LAS v4u*)(L + LDS_VL + (6 + 2 * w + hh) * VBLK + (lane & 31) * 16) = kv[1];
          if (w == 1) {
            *(LAS v4u*)(L + LDS_KL + 7 * 1024 + lo) = kv[2];
            *(LAS v4u*)(L + LDS_VL + (14 + hh) * VBLK + (lane & 31) * 16) = kv[3];
          }
        }
      }
      if (!last) HOLD(1);
      HBAR();
      CST(); CST();
      if (loader) {
        ld_gu(0); ld_gu(1); ld_gu(2); ldw(IC<6>{}, WGU_OFF + c * 114688, 42);
        if (!last) { ldw(IC<8>{}, WGU_OFF + c * 114688, 48); ld_gu(4); ldw(IC<10>{}, WGU_OFF + c * 114688, 70); wait_vm<29>(); }
        else wait_vm<17>();
        if (lane == 0) asm volatile("ds_add_u32 %0, %1" ::"v"(LDS_TAG + 12), "v"(1) : "memory");
      }
      if (sp > 0) {
        LAS unsigned* hw = (LAS unsigned*)(L + LDS_TAG + 4);
        CST();
        if (!loader) {
          v4u kv[8];
          unsigned spins = 0;
          do {
#pragma unroll
            for (int t = 0; t < 4; ++t) {
              const unsigned so = lay + ((8 * sp - 8 + t) * 32 + c) * 1024;
              kv[t] = ld16(rs, lo, so + L_K); kv[4 + t] = ld16(rs, lo, so + L_V);
            }
            PIN();
            DBGC(1);
          } while (missing<8>(kv) && ++spins < DATA_CAP);
#pragma unroll
          for (int t = 0; t < 4; ++t) {
            *(LAS v4u*)(L + LDS_KL + t * 1024 + lo) = kv[t];
            *(LAS v4u*)(L + LDS_VL + (2 * t + hh) * VBLK + (lane & 31) * 16) = kv[4 + t];
          }
          asm volatile("s_waitcnt lgkmcnt(0)" ::: "memory");
          __hip_atomic_store(hw, (unsigned)layer + 1u, __ATOMIC_RELAXED, __HIP_MEMORY_SCOPE_WORKGROUP);
        } else {
          unsigned v;
          do {
            __builtin_amdgcn_s_sleep(1);
            asm volatile("ds_read_b32 %0, %1\n\ts_waitcnt lgkmcnt(0)" : "=v"(v) : "v"(LDS_TAG + 4) : "memory");
          } while ((unsigned)__builtin_amdgcn_readfirstlane(v) != (unsigned)layer + 1u);
        }
      } else CST();
      CST();
      if (layer == 0 && !loader) touch0 = __builtin_amdgcn_raw_buffer_load_b32(rs, LAY_OFF + L_O + lane * TSTR + c * 4096 + sp * 128, 0, AUX_SC1);
      SETPRIO(1);
    }
    {
      COORDS();
      const int tl = compute ? 4 + w : w, T = 8 * sp + tl;
      const v4u o = attn_unit(L, lane, tl, sp == 0 ? 8 - tl : 0);
      st16(rs, o, (g4 & 1) * 16384 + hh * 256 + t16 * 16, lay + L_O + T * 32768 + c * 512);
      DRAIN();
      FLAG(flo, FL_O + T * 32 + c);
      CST();
    }
    if (compute) {
      SETPRIO(0);
      v4u fr[176];
      {
        COORDS();
        const int TB = 8 * sp + 4 + 2 * rh + (w >> 1), SB = 2 * TB + (w & 1);
        const v4u rB = ld16(rs, lo, lay + L_X + (SB * 16 + n32) * 1024), rA = ld16(rs, lo, lay + L_X + (SB * 16 + n32) * 1024 - 8 * 16384);
        const unsigned xv = (g4 >> 1) * 1024 + (g4 & 1) * 512 + t16 * 16, sv = g4 ? NONE : t16 * 8;
        const unsigned xo = lay + L_X1 + (TB * 32 + 2 * n32) * 1024 + (w & 1) * 256, so = lay + L_X1 + MB2 + TB * 4096 + n32 * 256 + (w & 1) * 128;
        const int lswo = vopq(lsw + LDS_WO);
        while ((unsigned)__builtin_amdgcn_readfirstlane(__hip_atomic_load((LAS unsigned*)(L + LDS_TAG + 12), __ATOMIC_RELAXED, __HIP_MEMORY_SCOPE_WORKGROUP)) != 3u * layer + 3u)
          __builtin_amdgcn_s_sleep(1);
        asm volatile("" ::: "memory");
        v4u of[16];
        unsigned pk;
        f32x4 a0, a1;
        Fin fin;
        pipe<64>(
            [&](auto fc) { return lds16(L, (fc.value & 31) * 1024 + lswo); },
            [&](auto fc, const v4u& wfr, auto pc) {
              constexpr int t = fc.value, part = pc.value, u = t & 31;
              if constexpr (part == 0) {
                if constexpr (u & 1) a1 = mfma16(wfr, of[u >> 1], u == 1 ? zero4() : a1);
                else a0 = mfma16(wfr, of[u >> 1], u == 0 ? zero4() : a0);
              } else {
                if constexpr (t == 31 || t == 63) {
                  asm volatile("s_nop 7" : "+v"(a0), "+v"(a1));
                  static_for<8>([&](auto k) { fin.template chunk<k.value>(a0, a1, t == 31 ? rB : rA, rs, xv, xo - (t == 31 ? 0 : 4 * 32768), rs, sv, so - (t == 31 ? 0 : 4 * 4096)); });
                  CST();
                }
                if constexpr (t == -1 || t == 31) {
                  if constexpr (t == 31) {
                    pk = __hip_atomic_load(fl + (FL_O + (TB - 4) * 32 + (lane & 31)) * 32, RLX_AGENT);
                    DRAIN();
                    FLAG(flo, FL_X1B + TB * 32 + 2 * n32 + (w & 1));
                  }
                  if (t == -1 || !__all(pk == tag)) POLL(flo, FL_O + (TB - (t == 31 ? 4 : 0)) * 32, 32);
                  CST();
                  static_for<16>([&](auto i) { of[i.value] = ld16(rs, lo + (i.value & 3) * 1024, lay + L_O + (SB - (t == 31 ? 8 : 0)) * 16384 + (i.value >> 2) * 4096); });
                }
                if constexpr (t == 55) pk = __hip_atomic_load(fl + (FL_X1B + (8 * sp + 4 + w) * 32 + (lane & 31)) * 32, RLX_AGENT);
                if constexpr (t == 63) {
                  const int T = 8 * sp + 4 + w;
                  if (!__all(pk == tag)) POLL(flo, FL_X1B + T * 32, 32);
                  CST();
                  static_for<NPG>([&](auto i) { fr[i.value] = ld16(rs, lo + (i.value & 3) * 1024, lay + L_X1 + T * 32768 + (i.value >> 2) * 4096); });
                }
              }
            });
      }
      SETPRIO(2);
      BAR();
      {
        COORDS();
        const int TB = 8 * sp + 4 + w;
        const int TD = 8 * sp + 4 + 2 * rh + (w >> 1), SD = 2 * TD + (w & 1);
        const unsigned x1b = lay + L_X1 + TB * 32768, hb = lay + L_H + SD * 57344, xn = last ? NONE : lay + LAY_BYTES + L_X;
        const unsigned hsv = (g4 & 1) * 57344 + hh * 256 + t16 * 16, hso = lay + L_H + TB * 114688;
        const rsrc_t ro = last ? __builtin_amdgcn_make_buffer_rsrc((void*)out, 0, (int)MB2, 0x00020000) : rs;
        const unsigned ob = last ? SD * 16384 + n32 * 64 : xn + (SD * 16 + n32) * 1024;
        const unsigned sb = xn + MB2 + TD * 4096 + n32 * 256 + (w & 1) * 128;
        unsigned ov, sv;
        const int lsw2 = vopq(lsw + 65536), lh = (hh * 16 + t16) * 16, lh2 = vopq(lh + 65536);
        f32x16 acc[2][4];
        f32x4 d0[2], d1[2];
        v4u mf, hv[4], res[2];
        float k1[2], den2[2], t0, t1, p0_, p1_;
        Fin fin;
        auto epi = [&](auto ec) {
          constexpr int job = ec.value >> 6, k = ec.value & 63, j = k >> 4, i = (k >> 2) & 3, uo = j < 3 ? 8 : 4;
          const f32x16& a = acc[job][j];
          if constexpr ((k & 3) == 0)
            asm volatile("v_mul_f32 %0, %2, %4\n\tv_mul_f32 %1, %3, %4\n\tv_exp_f32 %0, %0" : "=&v"(t0), "=&v"(t1) : "v"(a[2 * i]), "v"(a[2 * i + 1]), "v"(k1[job]));
          else if constexpr ((k & 3) == 1)
            asm volatile("v_exp_f32 %0, %0\n\tv_mul_f32 %1, %3, %5\n\tv_mul_f32 %2, %4, %6"
                : "+v"(t1), "=&v"(p0_), "=&v"(p1_) : "v"(a[2 * i]), "v"(a[2 * i + 1]), "v"(a[uo + 2 * i]), "v"(a[uo + 2 * i + 1]));
          else if constexpr ((k & 3) == 2)
            asm volatile("v_fma_f32 %0, %0, %2, %2\n\tv_fma_f32 %1, %1, %2, %2\n\tv_rcp_f32 %0, %0" : "+v"(t0), "+v"(t1) : "v"(den2[job]));
          else {
            unsigned d;
            asm volatile("v_rcp_f32 %2, %2\n\tv_mul_f32 %1, %1, %3\n\tv_mul_f32 %2, %2, %4\n\tv_cvt_pk_bf16_f32 %0, %1, %2" : "=v"(d), "+v"(t0), "+v"(t1) : "v"(p0_), "v"(p1_));
            hv[j][i] = d;
            if constexpr (k == (job ? 31 : NEP - 1)) { st16(rs, hv[0], hsv, hso - job * 4 * 114688 + c * 1024); st16(rs, hv[1], hsv + 512, hso - job * 4 * 114688 + c * 1024); }
            if constexpr (k == NEP - 1) {
              st16(rs, hv[2], hsv, hso - job * 4 * 114688 + 32768 + c * 512);
              st8(rs, (v2u){hv[3][0], hv[3][1]}, hsv - hh * 248, hso - job * 4 * 114688 + 49152 + c * 256);
            }
          }
        };
        auto vload = [&](auto ic) {
          constexpr int id = ic.value;
          if constexpr (id < 64) fr[id] = ld16(rs, lo + (id & 3) * 1024, x1b - (id >> 5) * 4 * 32768 + ((id & 31) >> 2) * 4096);
          else if constexpr (id < 176) {
            constexpr int n = (id - 64) % 56;
            fr[id] = ld16(rs, lo + (n & 3) * 1024, hb - (id >= 120 ? 8 * 57344 : 0) + (n >> 2) * 4096);
          } else if constexpr (id < 208) {
            constexpr int n = id - 176;
            xq[n] = ld16(rs, lo + ((n >> 1) & 3) * 1024, xn + TB * 32768 + (n & 1) * 16384 + (n >> 3) * 4096);
          } else res[id - 224] = ld16(rs, vopq(lo & 0x2f0) + ((lo & 256) << 1) + (lo & 512), lay + L_X1 + ((TD - 4 * (id - 224)) * 32 + 2 * n32) * 1024 + (w & 1) * 256);
        };
        pipe<NST>(
            [&](auto fc) {
              constexpr int t = fc.value;
              if constexpr (t == 32) wait_vm<vm_between(-1, t - LA - 1) + NPG>();
              if constexpr (t == GA + 48) wait_vm<vm_between(ep_step(NEP - 1), t - LA - 1)>();
              if constexpr (t == DB + 14) wait_vm<vm_between(GA + ep_step(31), t - LA - 1)>();
              if constexpr (t == DB + 56) wait_vm<vm_between(GA + ep_step(NEP - 1), t - LA - 1)>();
              if constexpr (t == DA + 42) wait_vm<vm_between(FINB + 7, t - LA - 1)>();
              if constexpr (t < DB) {
                constexpr GS s = gu_step(t % GA);
                if constexpr (s.cs) return csfrag(L, 48 + 32 * s.j + (s.j < 3 ? lane & 31 : t16));
                else {
                  if (s.off % 14336 == 0 && s.off >= (t < GA ? 14336 : 28672)) BARX();
                  return lds16(L, s.off < 65536 ? s.off + (s.j < 3 ? lsw : lh) : s.off - 65536 + (s.j < 3 ? lsw2 : lh2));
                }
              } else {
                constexpr int v = (t - DB) % 112;
                if (v % 14 == 0 && v >= (t < DA ? 0 : 28)) BARX();
                return lds16(L, v < 64 ? v * 1024 + lsw : (v - 64) * 1024 + lsw2);
              }
            },
            [&](auto fc, const v4u& wfr, auto pc) {
              constexpr int t = fc.value, part = pc.value;
              if constexpr (t < 0) return;
              else if constexpr (part == 0) {
                if constexpr (t < DB) {
                  constexpr int job = t / GA, u = t % GA;
                  constexpr GS s = gu_step(u);
                  acc[job][s.j] = mfma(wfr, s.cs ? mf : fr[32 * job + (s.kap & 31)], u < 4 ? splat16(0.f) : acc[job][s.j]);
                } else {
                  constexpr int job = t >= DA, v = (t - DB) % 112;
                  if constexpr (v & 1) d1[job] = mfma16(wfr, fr[64 + 56 * job + (v >> 1)], v == 1 ? zero4() : d1[job]);
                  else d0[job] = mfma16(wfr, fr[64 + 56 * job + (v >> 1)], v == 0 ? zero4() : d0[job]);
                }
              } else {
                if constexpr (t < DB) {
                  constexpr int job = t / GA, u = t % GA;
                  if constexpr (u == 74) {
                    const int a = LDS_ST + w * 128 + (lane & 31) * 4;
                    k1[job] = lds1f(L, a); den2[job] = lds1f(L, a + 512); mf = mfrag(L, hh ? LDS_TAG + 8 : a + 1024);
                  }
                  if constexpr (u == 79) k1[job] *= -LOG2E;
                }
                if constexpr (TAB.ld[t][0] >= 0) vload(IC<TAB.ld[t][0]>{});
                if constexpr (TAB.ld[t][1] >= 0) vload(IC<TAB.ld[t][1]>{});
                if constexpr (TAB.ep[t] >= 0) epi(IC<TAB.ep[t]>{});
                if constexpr (t == FINB - 1) { const int l = lane_id(); ov = l * 16 + ((((l & 15) * 1008) - (l >> 4) * 240) & sopq(-layer)); sv = l >= 16 ? NONE : l * 8; }
                if constexpr (t >= FINB && t < FINB + 8) fin.template chunk<t - FINB>(d0[0], d1[0], res[0], ro, ov, ob, rs, sv, sb);
              }
            });
        CST();
        asm volatile("s_nop 7" : "+v"(d0[1]), "+v"(d1[1]));
        static_for<8>([&](auto k) { fin.template chunk<k.value, false>(d0[1], d1[1], res[1], ro, ov, ob - 8 * 16384, rs, sv, sb - 4 * 4096); });
        if (last) st16(ro, fin.o, ov, ob - 8 * 16384);
        else {
          *(LAS v4u*)(L + LDS_QL + w * 1024 + lo) = fin.o;
          if (lane < 16) *(LAS v2u*)(L + LDS_QL + 4096 + w * 128 + lane * 8) = fin.sq;
          BAR();
        }
      }
    } else {
      COORDS();
      const bool poller = w == 0, loader = w != 0;
      unsigned pv = 0;
#define PEEK(blk, line) pv = __hip_atomic_load((gu32*)(ws + (blk)) + ((line) + (lane & 31)) * 32, RLX_AGENT)
#define GATE(blk, line) do { if (!__all(pv == tag)) POLL(blk, line, 32); } while (0)
      f32x4 ca = zero4();
      auto cs_gu = [&](int cell, int n2, bool put) {
        const unsigned one = (unsigned)vopq((w == 3 || t16 == (g4 & 1)) ? 0x3f803f80 : 0);
        const int a = cell * 1024 + (w < 3 ? lsw : lo), d2 = n2 == 1 ? 2048 : 7168;
        v4u f[8];
#pragma unroll
        for (int i = 0; i < 8; ++i) asm volatile("ds_read_b128 %0, %1" : "=v"(f[i]) : "v"(a + (i >> 1) * d2 + (i & 1) * n2 * 1024) : "memory");
        asm volatile("s_waitcnt lgkmcnt(0)" : "+v"(f[0]), "+v"(f[1]), "+v"(f[2]), "+v"(f[3]), "+v"(f[4]), "+v"(f[5]), "+v"(f[6]), "+v"(f[7]));
#pragma unroll
        for (int i = 0; i < 8; ++i) ca = mfma16(f[i], (v4u){one, one, one, one}, ca);
        if (put && t16 < (w < 3 ? 2 : 1)) {
#pragma unroll
          for (int r = 0; r < 4; ++r) cs_put(L, 48 + 32 * w + 16 * t16 + 4 * g4 + r, ca[r]);
        }
      };
      if (!last) {
        unsigned v;
        if (loader && lane == 0) asm volatile("ds_add_u32 %0, %1" ::"v"(LDS_TAG + 16), "v"(1) : "memory");
        do asm volatile("ds_read_b32 %0, %1\n\ts_waitcnt lgkmcnt(0)" : "=v"(v) : "v"(LDS_TAG + 16) : "memory");
        while ((unsigned)__builtin_amdgcn_readfirstlane(v) != 3u);
        for (int q = 0; q < 4 - w; ++q) {
          if (w == 3) cs_gu(6, 28, false);
          else cs_gu(q < 2 ? 28 * q + w : 40 + 16 * w + 8 * q, q < 2 ? 3 : 1, q == 3);
        }
      }
      SETPRIO(3);
      if (poller) {
        asm volatile("" ::"v"(touch0));
        Stats st;
        unsigned ev[4];
        const int h2l = lane < 32 ? FL_HA2 + sp * 32 + lane : FL_HB + (sp ? sp - 1 : 0) * 32 + c;
        SETPRIO(1);
        if (last) HOLD(4);
        HBAR();
        static_for<7>([&](auto nc) {
          constexpr int n = nc.value + 1;
          if (n <= 2) st.load(rs, lane, lay + L_X1 + MB2 + (8 * sp + 4) * 4096, 8 * n - 8, 8 * n);
          if (n == 3) { FLAG(flo, FL_X1A + sp * 32 + c); st.part1(); }
          if (n == 4) st.part2();
          if (n == 5) st.part3(L, lane);
          if (n == 6) PEEK(flo, FL_X1A + sp * 32);
          if (n == 7) GATE(flo, FL_X1A + sp * 32);
          HBARS(n == 3 || n == 7);
        });
        st.load(rs, lane, lay + L_X1 + MB2 + 8 * sp * 4096);
        st.all(L, lane);
        static_for<6>([&](auto nc) {
          constexpr int n = nc.value + 2;
          if (n == 4) FLAG(flo, FL_HB + sp * 32 + c);
          if (n == 6) PEEK(flo, FL_HB + sp * 32);
          if (n == 7) GATE(flo, FL_HB + sp * 32);
          HBARS(n == 2 || n == 7);
        });
        static_for<8>([&](auto nc) {
          constexpr int n = nc.value;
          if (n == 2) FLAG(flo, FL_HA + sp * 32 + c);
          if (n == 5) { FLAG(flo, FL_HA2 + sp * 32 + c); PEEK(flo, FL_HA + sp * 32); }
          if (n == 6) GATE(flo, FL_HA + sp * 32);
          if (n == 7) pv = __hip_atomic_load(fl + h2l * 32, RLX_AGENT);
          HBARS(n == 0 || n == 3 || n == 6);
        });
        static_for<6>([&](auto nc) {
          constexpr int n = nc.value + 2;
          if (n == 2) { if (!__all(pv == tag)) poll(fl + h2l * 32, true, tmo, tag); }
          if (n == 3) {
#pragma unroll
            for (int q = 0; q < 4; ++q) ev[q] = __builtin_amdgcn_raw_buffer_load_b32(rs, lane * 128, last && blockIdx.x == 0 ? flo + (FL_HB + 64 * q) * 128 : NONE, AUX_SC1);
          }
          if (n == 4 && !last) FLAG(FLAG_LAYER, FL_XB + sp * 32 + c);
          if (n == 5) PEEK(FLAG_LAYER, FL_XB + sp * 32);
          if (n == 6) GATE(FLAG_LAYER, FL_XB + sp * 32);
          if (n == 7) st.load(rs, lane, last ? NONE : LAY_OFF + LAY_BYTES + L_X + MB2 + (8 * sp + 4) * 4096);
          if (n == 7 && last) HOLD(3);
          HBARS(n == 2 || n == 6 || n == 7);
        });
        if (!last) { st.all(L, lane); }
        if (last && blockIdx.x == 0) {
          if (!__all(ev[0] == tag && ev[1] == tag && ev[2] == tag && ev[3] == tag))
            for (int q = 0; q < 4; ++q) POLL(flo, FL_HB + 64 * q, 64);
          if (lane == 0) __hip_atomic_store(fg + FL_EP * 32, tag, RLX_AGENT);
        }
      } else {
        const unsigned wq = WQKV_OFF + c * 49152, wb = WGU_OFF + c * 114688;
        DRAIN();
        HBAR();
        static_for<7>([&](auto nc) {
          constexpr int n = nc.value + 1;
          blank(3 * n - 4 + w, last ? NONE : LAY_OFF + LAY_BYTES);
          if (n <= 3) ldw(IC<9>{}, wb, 71 + 9 * n);
          if (n == 4) { ldw(IC<5>{}, wb, 107); wait_vm<11>(); }
          if (n == 5) { if (!last && w == 1) cs_gu(80, 1, true); wait_vm<8>(); }
          if (n == 6) { if (!last && w == 2) cs_gu(88, 1, false); wait_vm<2>(); }
          if (n == 7 && !last && w >= 2) cs_gu(80 + 8 * w, 1, true);
          HBARS(n == 3 || n == 7);
        });
        static_for<6>([&](auto nc) {
          constexpr int n = nc.value + 2;
          if (n == 3) wait_vm<0>();
          if (n == 5 || n == 6) { ld_dn(2 * n - 10); ld_dn(2 * n - 9); } else if (n > 6) ld_dn(n - 3);
          HBARS(n == 2 || n == 7);
        });
        static_for<8>([&](auto nc) {
          constexpr int n = nc.value;
          if (n <= 2) ld_dn(n + 5);
          wait_vm<(n <= 2 ? 25 : 35 - 5 * n)>();
          HBARS(n == 0 || n == 3 || n == 6);
        });
        static_for<6>([&](auto nc) {
          constexpr int n = nc.value + 2;
          if (!last) {
            if (n >= 3 && n <= 5) ldw(IC<14>{}, wq, 14 * (n - 3));
            if (n == 6) { ldw(IC<6>{}, wq, 42); ldw(IC<8>{}, wb, 48); }
            if (n == 7) ldw(IC<14>{}, wb, 56);
          }
          HBARS(n == 2 || n == 6 || n == 7);
        });
        if (!last) { ldw(IC<10>{}, wb, 70); wait_vm<14>(); }
      }
      SETPRIO(3);
      if (!last) {
        HOLD(2);
        HBAR();
      }
      static_for<NPQ>([&](auto i) { undef(xq[i.value]); });
    }
  }
}

}

extern "C" void kernel_launch(void* const* d_in, const int* in_sizes, int n_in, void* d_out, int out_size, void* d_ws, size_t ws_size,
                              hipStream_t stream) {
  (void)in_sizes; (void)n_in; (void)out_size; (void)ws_size;
  (void)hipFuncSetAttribute((const void*)k_main, hipFuncAttributeMaxDynamicSharedMemorySize, LDS_BYTES);
  k_main<<<256, 512, LDS_BYTES, stream>>>((const u16*)d_in[0], (const u16*)d_in[1], (const u16*)d_in[2], (const u16*)d_in[4], (const u16*)d_in[5],
                                          (const u16*)d_in[6], (unsigned char*)d_ws, (u16*)d_out);
}
```

```cpp
#include <hip/hip_runtime.h>
#include <stdint.h>
#include <utility>

#define LAS __attribute__((address_space(3)))
#define GAS __attribute__((address_space(1)))
#define DI __device__ __forceinline__
#define RLX_AGENT __ATOMIC_RELAXED, __HIP_MEMORY_SCOPE_AGENT
typedef unsigned short u16;
typedef unsigned v4u __attribute__((ext_vector_type(4)));
typedef unsigned v2u __attribute__((ext_vector_type(2)));
typedef float f32x2 __attribute__((ext_vector_type(2)));
typedef float f32x4 __attribute__((ext_vector_type(4)));
typedef float f32x16 __attribute__((ext_vector_type(16)));
typedef __bf16 bf16x2 __attribute__((ext_vector_type(2)));
typedef __bf16 bf16x8 __attribute__((ext_vector_type(8)));
typedef GAS unsigned gu32;
typedef GAS unsigned long long gu64;

namespace {

constexpr int D_ = 512, F_ = 1792;
constexpr unsigned MB2 = 2097152u, STB = 262144u;
constexpr unsigned FLAG_LAYER = 1048576u, FLAG_G = 2 * FLAG_LAYER, FLAG_BYTES = FLAG_G + 65536;
constexpr unsigned WQKV_OFF = FLAG_BYTES;
constexpr unsigned WO_OFF = WQKV_OFF + 32 * 49152;
constexpr unsigned WGU_OFF = WO_OFF + 16 * 32768;
constexpr unsigned WDN_OFF = WGU_OFF + 32 * 114688;
constexpr unsigned LAY_OFF = WDN_OFF + 16 * 114688;
constexpr unsigned L_K = 0, L_V = MB2, L_O = 2 * MB2, L_X1 = 3 * MB2, L_X = 4 * MB2 + STB, L_H = 5 * MB2 + 2 * STB;
constexpr unsigned LAY_BYTES = L_H + 128 * 57344;
constexpr unsigned WS_BYTES = LAY_OFF + 2 * LAY_BYTES;
constexpr unsigned NONE = 0x40000000u;
constexpr int FL_KV = 0, FL_O = 2048, FL_X1B = 4096, FL_X1A = 6144, FL_HB = 6400, FL_HA = 6656, FL_XB = 6912, FL_XA = 7168, FL_HA2 = 7424;
constexpr int FL_WR = 0, FL_EP = 256, FL_TMO = 257;
constexpr int VBLK = 528;
constexpr int LDS_KL = 114688, LDS_VL = LDS_KL + 16384, LDS_QL = LDS_VL + 32 * VBLK, LDS_CS = LDS_QL + 8192, LDS_ST = LDS_CS + 1536;
constexpr int LDS_MK = LDS_ST + 1536, LDS_TAG = LDS_MK + 2048;
constexpr int LDS_WO = 81920, LDS_TB = 49152;
constexpr int LDS_BYTES = 163840;
#define TSTR 1048576u
constexpr int CODE_LINES = 15;
constexpr unsigned SPIN_CAP = 1u << 16;
constexpr int AUX_SC1 = 16;
constexpr float LOG2E = 1.4426950408889634f;

DI f32x16 mfma(v4u a, v4u b, f32x16 c) {
  return __builtin_amdgcn_mfma_f32_32x32x16_bf16(__builtin_bit_cast(bf16x8, a), __builtin_bit_cast(bf16x8, b), c, 0, 0, 0);
}
DI f32x4 mfma16(v4u a, v4u b, f32x4 c) {
  return __builtin_amdgcn_mfma_f32_16x16x32_bf16(__builtin_bit_cast(bf16x8, a), __builtin_bit_cast(bf16x8, b), c, 0, 0, 0);
}
DI unsigned pk2(float lo, float hi) {
  f32x2 p = {lo, hi};
  return __builtin_bit_cast(unsigned, __builtin_convertvector(p, bf16x2));
}
DI float bfr(float x) { return __uint_as_float(pk2(x, 0.f) << 16); }
DI float bflo(unsigned u) { return __uint_as_float(u << 16); }
DI float bfhi(unsigned u) { return __uint_as_float(u & 0xffff0000u); }
DI float amul(float a, float b) { float d; asm("v_mul_f32 %0, %1, %2" : "=v"(d) : "v"(a), "v"(b)); return d; }
DI float afma(float a, float b, float c) { float d; asm("v_fma_f32 %0, %1, %2, %3" : "=v"(d) : "v"(a), "v"(b), "v"(c)); return d; }
DI float aadd(float a, float b) { float d; asm("v_add_f32 %0, %1, %2" : "=v"(d) : "v"(a), "v"(b)); return d; }
DI float asub(float a, float b) { float d; asm("v_sub_f32 %0, %1, %2" : "=v"(d) : "v"(a), "v"(b)); return d; }
DI float xhalf_sum(float v) {
  float t;
  asm("v_mov_b32 %1, %0\n\ts_nop 1\n\tv_permlane32_swap_b32 %0, %1\n\tv_add_f32 %0, %0, %1" : "+v"(v), "=&v"(t));
  return v;
}
DI float xrow_sum(float v) {
  float t;
  asm("v_mov_b32 %1, %0\n\ts_nop 1\n\tv_permlane16_swap_b32 %0, %1\n\tv_add_f32 %0, %0, %1" : "+v"(v), "=&v"(t));
  return v;
}
DI float xhalf_max(float v) {
  auto r = __builtin_amdgcn_permlane32_swap(__float_as_uint(v), __float_as_uint(v), false, false);
  return fmaxf(__uint_as_float(r[0]), __uint_as_float(r[1]));
}
DI void swap16(unsigned& a, unsigned& b) { asm("s_nop 1\n\tv_permlane16_swap_b32 %0, %1" : "+v"(a), "+v"(b)); }
DI void swap32(unsigned& a, unsigned& b) { asm("s_nop 1\n\tv_permlane32_swap_b32 %0, %1" : "+v"(a), "+v"(b)); }
DI f32x16 splat16(float v) {
  f32x16 z;
#pragma unroll
  for (int i = 0; i < 16; ++i) z[i] = v;
  return z;
}
DI f32x4 zero4() { return (f32x4){0.f, 0.f, 0.f, 0.f}; }
DI int vopq(int x) { asm volatile("" : "+v"(x)); return x; }
DI int lane_id() { int l; asm volatile("v_mbcnt_lo_u32_b32 %0, -1, 0\n\tv_mbcnt_hi_u32_b32 %0, -1, %0" : "=v"(l)); return l; }
template <int LN> DI unsigned wrl(unsigned v, unsigned s) { asm volatile("v_writelane_b32 %0, %1, %2" : "+v"(v) : "s"(s), "i"(LN)); return v; }
template <class T> DI void undef(T& x) { asm volatile("" : "=v"(x)); }
DI int sopq(int x) { asm volatile("" : "+s"(x)); return x; }
#define SWZ(l) ((l) ^ (((l) >> 3) & 7))
#define COORDS() \
  const int lane = lane_id(), lo = vopq(lane * 16), hh = lane >> 5, t16 = lane & 15, g4 = lane >> 4, lsw = SWZ(lane) * 16; \
  const int w = sopq(w0) & 3, sp = sopq(sp0), c = sopq(c0), n32 = c >> 1, rh = c & 1; \
  const unsigned lay = (unsigned)sopq((int)lay0); \
  const unsigned flo = sopq(layer) * FLAG_LAYER; \
  gu32* const fl = (gu32*)(ws + flo); \
  (void)lo; (void)hh; (void)t16; (void)g4; (void)lsw; (void)n32; (void)rh; (void)lay; (void)sp; (void)c; (void)w; (void)fl;
#define HBARS(c) asm volatile("s_waitcnt lgkmcnt(0)\n\ts_barrier" ::: "memory")
#define CST() do {} while (0)
#define DBGC(k) asm volatile("" ::: "memory")
#define HBAR() asm volatile("s_waitcnt lgkmcnt(0)\n\ts_barrier" ::: "memory")
#define BAR() asm volatile("s_waitcnt lgkmcnt(0)\n\ts_barrier" ::: "memory")
#define BARX() asm volatile("s_barrier" ::: "memory")
#define DRAIN() asm volatile("s_waitcnt vmcnt(0)" ::: "memory")
#define PIN() __builtin_amdgcn_sched_barrier(0)
#define SETPRIO(n) __builtin_amdgcn_s_setprio(n)
template <int N> DI void wait_vm() { static_assert(N >= 0 && N < 64, ""); asm volatile("s_waitcnt vmcnt(%0)" ::"i"(N) : "memory"); }

typedef __amdgpu_buffer_rsrc_t rsrc_t;
DI v4u ld16(rsrc_t rs, unsigned voff, unsigned soff) { return __builtin_amdgcn_raw_buffer_load_b128(rs, voff, soff, AUX_SC1); }
DI f32x2 ld8f(rsrc_t rs, unsigned voff, unsigned soff) { return __builtin_bit_cast(f32x2, __builtin_amdgcn_raw_buffer_load_b64(rs, voff, soff, AUX_SC1)); }
DI void st16(rsrc_t rs, v4u v, unsigned voff, unsigned soff) {
  __builtin_amdgcn_raw_buffer_store_b128(v, rs, voff, soff, AUX_SC1);
  asm volatile("s_nop 1" ::"v"(v));
}
DI void st8(rsrc_t rs, v2u v, unsigned voff, unsigned soff) { __builtin_amdgcn_raw_buffer_store_b64(v, rs, voff, soff, AUX_SC1); }
DI void st4(rsrc_t rs, unsigned v, unsigned voff, unsigned soff) { __builtin_amdgcn_raw_buffer_store_b32(v, rs, voff, soff, AUX_SC1); }
DI v4u lds16(LAS unsigned char* L, int off) { return *(LAS v4u*)(L + off); }
DI f32x4 lds4f(LAS unsigned char* L, int off) { return *(LAS f32x4*)(L + off); }

DI void poll(gu32* word, bool active, gu32* tmo, unsigned tag) {
  for (unsigned spins = 0;;) {
    unsigned v = tag;
    if (active) v = __hip_atomic_load(word, RLX_AGENT);
    if (__all(v == tag)) break;
    ++spins;
    if (spins > SPIN_CAP) { __hip_atomic_store(tmo, tag, RLX_AGENT); break; }
    if ((spins & 255u) == 0u && (unsigned)__builtin_amdgcn_readfirstlane(__hip_atomic_load(tmo, RLX_AGENT)) == tag) break;
  }
  __builtin_amdgcn_fence(__ATOMIC_ACQUIRE, "wavefront");
}

template <int I> DI void dma1(rsrc_t rs, LAS unsigned char* dst, unsigned voff, unsigned soff) {
  __builtin_amdgcn_raw_ptr_buffer_load_lds(rs, (LAS void*)dst, 16, voff, soff, I * 1024, AUX_SC1);
}
template <int V> using IC = std::integral_constant<int, V>;
template <class FN, int... I> DI void static_for_(FN fn, std::integer_sequence<int, I...>) { (fn(IC<I>{}), ...); }
template <int N, class FN> DI void static_for(FN fn) { static_for_(fn, std::make_integer_sequence<int, N>{}); }
template <int N> DI void dma(rsrc_t rs, LAS unsigned char* dst, unsigned lo, unsigned src) {
  static_for<N>([&](auto i) { dma1<i.value & 3>(rs, dst + (i.value >> 2) * 4096, lo, src + (i.value >> 2) * 4096); });
}

struct Stats {
  f32x4 p[16];
  float m0, m1, q0, q1;
  DI void load(rsrc_t rs, int lane, unsigned base, int i0 = 0, int i1 = 16) {
#pragma unroll
    for (int i = i0; i < i1; ++i) p[i] = __builtin_bit_cast(f32x4, ld16(rs, (lane >> 4) * 4096 + (lane & 15) * 16 + i * 256, base));
    PIN();
  }
  DI void part1() {
    m0 = m1 = q0 = q1 = 0.f;
#pragma unroll
    for (int i = 0; i < 16; ++i) { m0 += p[i][0]; q0 += p[i][1]; m1 += p[i][2]; q1 += p[i][3]; }
    asm volatile("" : "+v"(m0), "+v"(m1), "+v"(q0), "+v"(q1));
  }
  DI void part2() {
    const float a0 = m0 * (1.0f / 16.0f), a1 = m1 * (1.0f / 16.0f);
    q0 *= 32.0f; q1 *= 32.0f;
#pragma unroll
    for (int i = 0; i < 16; ++i) { const float d0 = p[i][0] - a0, d1 = p[i][2] - a1; q0 += d0 * d0; q1 += d1 * d1; }
    asm volatile("" : "+v"(q0), "+v"(q1));
  }
  DI void all(LAS unsigned char* L, int lane, int dst = LDS_ST) { part1(); part2(); part3(L, lane, dst); }
  DI void part3(LAS unsigned char* L, int lane, int dst = LDS_ST) {
    const float den0 = bfr(__builtin_amdgcn_sqrtf(bfr(bfr(q0 * (1.0f / (32.0f * D_))) + 1e-5f)));
    const float den1 = bfr(__builtin_amdgcn_sqrtf(bfr(bfr(q1 * (1.0f / (32.0f * D_))) + 1e-5f)));
    *(LAS f32x2*)(L + dst + lane * 8) = (f32x2){__builtin_amdgcn_rcpf(den0), __builtin_amdgcn_rcpf(den1)};
    *(LAS f32x2*)(L + dst + 512 + lane * 8) = (f32x2){den0 * den0, den1 * den1};
    *(LAS v2u*)(L + dst + 1024 + lane * 8) = (v2u){pk2(m0 * (-1.0f / D_), m0 * (-1.0f / D_)), pk2(m1 * (-1.0f / D_), m1 * (-1.0f / D_))};
  }
};
DI float lds1f(LAS unsigned char* L, int off) { return *(LAS float*)(L + off); }
DI v4u mfrag(LAS unsigned char* L, int off) { const unsigned n = *(LAS unsigned*)(L + off); return (v4u){n, n, 0u, 0u}; }

constexpr int LA = 6;
template <int NF, class RD, class STEP>
DI void pipe(RD rd, STEP step) {
  v4u wf[8];
  static_for<LA>([&](auto i) { wf[i.value] = rd(i); });
  step(IC<-1>{}, wf[0], IC<1>{});
  PIN();
  static_for<NF>([&](auto fc) {
    constexpr int f = fc.value;
    if constexpr ((f & 1) == 0 && f + LA <= NF) __builtin_amdgcn_s_waitcnt(0xc07f | ((LA - 2) << 8));
    step(fc, wf[f & 7], IC<0>{});
    if constexpr (f + LA < NF) wf[(f + LA) & 7] = rd(IC<f + LA>{});
    step(fc, wf[f & 7], IC<1>{});
    PIN();
  });
}
DI v4u csfrag(LAS unsigned char* L, int entry) {
  const v2u t = *(LAS v2u*)(L + LDS_CS + entry * 8);
  return (v4u){t.x, t.y, 0u, 0u};
}
DI void cs_put(LAS unsigned char* L, int entry, float cs) {
  const float hi = bfr(cs), mid = bfr(cs - hi), lw = (cs - hi) - mid;
  *(LAS v2u*)(L + LDS_CS + entry * 8) = (v2u){pk2(hi, mid), pk2(lw, 0.f)};
}
DI unsigned mulpk(float a, float s0, float b, float s1) {
  unsigned d; float t;
  asm("v_mul_f32 %0, %2, %3\n\tv_mul_f32 %1, %4, %5\n\tv_cvt_pk_bf16_f32 %0, %0, %1" : "=&v"(d), "=&v"(t) : "v"(a), "v"(s0), "v"(b), "v"(s1));
  return d;
}
DI unsigned nos(unsigned v) { return v < 0xfffffffeu ? v : 0xfffffffeu; }
DI v4u nos4(v4u v) { return (v4u){nos(v[0]), nos(v[1]), nos(v[2]), nos(v[3])}; }
DI unsigned umax3(unsigned a, unsigned b, unsigned c) { const unsigned t = a > b ? a : b; return t > c ? t : c; }
template <int N> DI bool missing(const v4u* f, int step = 1) {
  unsigned m0 = 0, m1 = 0;
#pragma unroll
  for (int i = 0; i < N; ++i) { m0 = umax3(m0, f[i * step][0], f[i * step][1]); m1 = umax3(m1, f[i * step][2], f[i * step][3]); }
  return __builtin_amdgcn_ballot_w64((m0 > m1 ? m0 : m1) == 0xffffffffu) != 0;
}
constexpr unsigned DATA_CAP = 1u << 14;
DI v4u pack8(const float* y) { return (v4u){pk2(y[0], y[1]), pk2(y[2], y[3]), pk2(y[4], y[5]), pk2(y[6], y[7])}; }

struct Fin {
  float y[8], s;
  v4u o;
  v2u sq;
  template <int K, bool ST = true> DI void chunk(const f32x4& a0, const f32x4& a1, const v4u& r, rsrc_t ro, unsigned vo, unsigned so, rsrc_t rs, unsigned vs, unsigned ss) {
    if constexpr (K == 0) { y[0] = aadd(a0[0], bflo(r[0])); y[1] = aadd(a0[1], bfhi(r[0])); y[2] = aadd(a0[2], bflo(r[1])); y[3] = aadd(a0[3], bfhi(r[1])); }
    if constexpr (K == 1) { y[4] = aadd(a1[0], bflo(r[2])); y[5] = aadd(a1[1], bfhi(r[2])); y[6] = aadd(a1[2], bflo(r[3])); y[7] = aadd(a1[3], bfhi(r[3])); }
    if constexpr (K == 2) { o = nos4(pack8(y)); if constexpr (ST) st16(ro, o, vo, so); }
    if constexpr (K == 3) {
#pragma unroll
      for (int m = 0; m < 4; ++m) { y[2 * m] = bflo(o[m]); y[2 * m + 1] = bfhi(o[m]); }
    }
    if constexpr (K == 4) s = xrow_sum(aadd(aadd(aadd(y[0], y[1]), aadd(y[2], y[3])), aadd(aadd(y[4], y[5]), aadd(y[6], y[7]))));
    if constexpr (K == 5) {
      s = xhalf_sum(s);
      const float mu = s * (1.0f / 32.0f);
#pragma unroll
      for (int i = 0; i < 8; ++i) y[i] = asub(y[i], mu);
    }
    if constexpr (K == 6) {
      y[0] = amul(y[0], y[0]); y[1] = amul(y[1], y[1]);
#pragma unroll
      for (int i = 2; i < 8; ++i) y[i & 1] = afma(y[i], y[i], y[i & 1]);
    }
    if constexpr (K == 7) {
      sq = (v2u){nos(__float_as_uint(s)), nos(__float_as_uint(xhalf_sum(xrow_sum(aadd(y[0], y[1])))))};
      if constexpr (ST) st8(rs, sq, vs, ss);
    }
  }
};

constexpr unsigned long long low_mask(int r) {
  unsigned long long m = 0;
  for (int l = 0; l < 64; ++l)
    if ((r & 3) + 8 * (r >> 2) + 4 * (l >> 5) <= (l & 31)) m |= 1ull << l;
  return m;
}
DI float sel(unsigned long long mask, float set, float unset) {
  float d;
  asm("v_cndmask_b32_e64 %0, %1, %2, %3" : "=v"(d) : "v"(unset), "v"(set), "s"(mask));
  return d;
}
DI float max16(float m, const f32x16& s) {
#pragma unroll
  for (int r = 0; r < 16; r += 2) m = __builtin_fmaxf(__builtin_fmaxf(m, s[r]), s[r + 1]);
  return m;
}
DI v4u exp_pack(const f32x16& s, int h) {
  float pr[8];
#pragma unroll
  for (int r = 0; r < 8; ++r) pr[r] = __builtin_amdgcn_exp2f(s[8 * h + r]);
  return pack8(pr);
}
template <bool EXACT>
DI f32x16 attn_pass(LAS unsigned char* L, int lane, int tl, int nv) {
  const int lo = lane * 16;
  const int kb = LDS_KL + tl * 1024 + lo, kf0 = LDS_KL + (tl + 8) * 1024 + (lane >> 5) * 512;
  const v4u q = lds16(L, LDS_QL + tl * 1024 + lo);
  v4u kf[9];
  kf[8] = lds16(L, kb + 8192); kf[0] = lds16(L, nv <= 0 ? kb : kf0);
#pragma unroll
  for (int i = 7; i >= 1; --i) kf[i] = lds16(L, (nv <= i ? kb : kf0 - i * 1024) + i * 1024);
  f32x16 s[9], cm = splat16(0.f);
  if constexpr (EXACT) {
    s[8] = mfma(kf[8], q, splat16(0.f));
    s[0] = mfma(kf[0], q, splat16(0.f));
    s[7] = mfma(kf[7], q, splat16(0.f));
    s[6] = mfma(kf[6], q, splat16(0.f));
    asm volatile("s_nop 7\n\ts_nop 7" : "+v"(s[8]), "+v"(s[0]));
    PIN();
    static_for<16>([&](auto r) { s[8][r.value] = sel(low_mask(r.value), s[8][r.value], s[0][r.value]); });
    float m = max16(s[8][0], s[8]);
    PIN();
    static_for<7>([&](auto ic) {
      constexpr int i = 7 - ic.value;
      if constexpr (i - 2 >= 1) s[i - 2] = mfma(kf[i - 2], q, splat16(0.f));
      m = max16(m, s[i]);
      PIN();
    });
    cm = splat16(-xhalf_max(m));
  }
  const int vb = LDS_VL + 2 * tl * VBLK + ((lane & 16) ? 512 : (lane >> 5) * 256 + (lane & 15) * 16);
  s[8] = mfma(kf[8], q, cm);
  s[0] = mfma(kf[0], q, cm);
  s[7] = mfma(kf[7], q, cm);
  const v4u mk0 = lds16(L, LDS_MK + lo), mk1 = lds16(L, LDS_MK + 1024 + lo);
  v4u va = lds16(L, vb + 16 * VBLK), vc = lds16(L, vb + 17 * VBLK), v0a = lds16(L, vb), v0c = lds16(L, vb + VBLK);
  asm volatile("s_nop 7\n\ts_nop 7" : "+v"(s[8]), "+v"(s[0]));
  PIN();
  f32x16 o;
  {
    static_for<16>([&](auto r) { s[8][r.value] = sel(low_mask(r.value), s[8][r.value], s[0][r.value]); });
    const v4u p0 = exp_pack(s[8], 0), l0 = p0 & mk0;
    o = mfma(va, l0, splat16(0.f));
    o = mfma(v0a, p0 ^ l0, o);
    const v4u p1 = exp_pack(s[8], 1), l1 = p1 & mk1;
    o = mfma(vc, l1, o);
    o = mfma(v0c, p1 ^ l1, o);
    va = lds16(L, vb + 14 * VBLK); vc = lds16(L, vb + 15 * VBLK);
    PIN();
  }
  static_for<7>([&](auto ic) {
    constexpr int i = 7 - ic.value;
    if constexpr (i > 1) s[i - 1] = mfma(kf[i - 1], q, cm);
    PIN();
    o = mfma(va, exp_pack(s[i], 0), o);
    if constexpr (i > 1) va = lds16(L, vb + (2 * i - 2) * VBLK);
    o = mfma(vc, exp_pack(s[i], 1), o);
    if constexpr (i > 1) vc = lds16(L, vb + (2 * i - 1) * VBLK);
    PIN();
  });
  return o;
}
DI v4u attn_unit(LAS unsigned char* L, int lane, int tl, int nv) {
  f32x16 o = attn_pass<false>(L, lane, tl, nv);
  float big = __builtin_fabsf(o[0]);
#pragma unroll
  for (int r = 1; r < 8; ++r) big += __builtin_fabsf(o[r]);
  if (__builtin_expect(!__all(o[8] > 0x1p-30f && o[8] < 0x1p60f && big < 0x1p120f), 0)) {
    asm volatile("" ::: "memory");
    o = attn_pass<true>(L, lane, tl, nv);
  }
  const float rl = __builtin_amdgcn_rcpf(o[8]);
  float y[8];
#pragma unroll
  for (int r = 0; r < 8; ++r) y[r] = o[r] * rl;
  return pack8(y);
}

struct Retile {
  v4u in[8];
  DI void load(const u16* src, int ld, int rs4) {
#pragma unroll
    for (int j = 0; j < 8; ++j) in[j] = *(const v4u*)(src + (size_t)(((j & 3) + rs4 * (j >> 2)) * ld));
    PIN();
  }
  unsigned d[8];
  DI void out(rsrc_t rs) {
#pragma unroll
    for (int i = 0; i < 8; ++i) st16(rs, in[i], d[i], 0);
  }
  DI void lines(LAS unsigned char* t, int lane, int sw, unsigned dst) {
#pragma unroll
    for (int e = 0; e < 8; ++e) {
      v4u o;
#pragma unroll
      for (int m = 0; m < 4; ++m) o[m] = __builtin_amdgcn_perm(in[2 * m + 1][e >> 1], in[2 * m][e >> 1], (e & 1) ? 0x07060302u : 0x05040100u);
      *(LAS v4u*)(t + lane * 128 + (e ^ sw ^ (lane & 7)) * 16) = o;
    }
    asm volatile("s_waitcnt lgkmcnt(0)" ::: "memory");
#pragma unroll
    for (int i = 0; i < 8; ++i) {
      in[i] = *(LAS v4u*)(t + i * 1024 + (lane ^ (lane >> 3)) * 16);
      d[i] = (unsigned)__builtin_amdgcn_ds_bpermute((8 * i + (lane >> 3)) * 4, (int)dst) + (lane & 7) * 16;
    }
    PIN();
  }
  DI void put(LAS unsigned char* L, int pb, int dd, int lp, int dl) {
#pragma unroll
    for (int e = 0; e < 8; ++e) {
      v4u o;
#pragma unroll
      for (int m = 0; m < 4; ++m) o[m] = __builtin_amdgcn_perm(in[2 * m + 1][e >> 1], in[2 * m][e >> 1], (e & 1) ? 0x07060302u : 0x05040100u);
      const int s = lp + (e & 3) + (e >> 2) * dl;
      *(LAS v4u*)(L + pb + (e >> 2) * dd + (s ^ ((s >> 3) & 7)) * 16) = o;
    }
  }
};

struct GS { int cs, j, kap, off; };
constexpr GS gu_step(int u) {
  if (u < 64) { const int k = u >> 2, j = u & 3; return GS{0, j, k, j < 3 ? (7 * (k >> 1) + 3 * (k & 1) + j) * 1024 : (7 * (k >> 1) + 6) * 1024 + (k & 1) * 512}; }
  const int v = u - 64, j = v / 17, i = v % 17;
  return GS{i == 16, j, 16 + i, 57344 + j * 16384 + i * (j < 3 ? 1024 : 512)};
}
constexpr int GA = 132, DB = 264, DA = 376, NST = 488;
constexpr int NPG = 16, NPQ = 12, LDN = 12;
constexpr int ld_step(int id) {
  if (id < 32) return id < NPG ? -1 : 2 * (id - NPG);
  if (id < 64) { const int n = id - 32; return n < 12 ? 103 + (5 * n) / 2 : GA + 10 + 2 * (n - 12); }
  if (id < 176) {
    const int n = id - 64;
    return n < 12 ? GA + 103 + n : n < 26 ? DB - 15 + ((n - 12) * 7) / 4 : n < 56 ? DB + 27 + ((n - 26) * 5) / 3 : n < 84 ? DB + 79 + ((n - 56) * 4) / 3 : DA + 31 + 2 * (n - 84);
  }
  if (id < 208) { const int n = id - 176; return n < NPQ ? DA + 87 + 2 * n : -1; }
  if (id < 224) return -1;
  return id == 224 ? DB + 80 : DA + 72;
}
constexpr int NEP = 56;
constexpr int ep_step(int k) { return 83 + 17 * (k / 16) + k % 16; }
struct Tab { short ld[NST][2]; short ep[NST]; short nvm[NST]; };
constexpr int FINB = DA + 3;
#define Q3T 75
#define XA0 18
constexpr int xa_step(int j) { const int a = XA0 + j, b = j < 12 ? 3 * j + 2 : 34 + j; return a > b ? a : b; }
constexpr Tab make_tab() {
  Tab t{};
  for (int s = 0; s < NST; ++s) { t.ld[s][0] = t.ld[s][1] = t.ep[s] = -1; t.nvm[s] = 0; }
  for (int id = 0; id < 226; ++id) {
    const int s = ld_step(id);
    if (s < 0) continue;
    t.ld[s][t.ld[s][0] >= 0] = (short)id; ++t.nvm[s];
  }
  for (int job = 0; job < 2; ++job)
    for (int k = 0; k < NEP; ++k) {
      const int s = job * GA + ep_step(k);
      t.ep[s] = (short)(job * 64 + k);
      if (k == NEP - 1) t.nvm[s] += 2;
      if (k == (job ? 31 : NEP - 1)) t.nvm[s] += 2;
    }
  ++t.nvm[FINB + 2]; ++t.nvm[FINB + 7];
  return t;
}
constexpr Tab TAB = make_tab();
constexpr int vm_between(int a, int b) {
  int n = 0;
  for (int s = a + 1; s <= b; ++s) n += TAB.nvm[s];
  return n;
}

__global__ void __launch_bounds__(512, 2) k_main(const u16* __restrict__ x, const u16* __restrict__ wqkv, const u16* __restrict__ wo, const u16* __restrict__ wg,
                                                 const u16* __restrict__ wu, const u16* __restrict__ wd, unsigned char* __restrict__ ws,
                                                 u16* __restrict__ out) {
  extern __shared__ __attribute__((aligned(16))) unsigned char smem[];
  LAS unsigned char* L = (LAS unsigned char*)smem;
  const int tid = threadIdx.x, w0 = __builtin_amdgcn_readfirstlane(tid >> 6);
  const int b = blockIdx.x;
  unsigned touch0 = 0;
  {
    unsigned long long pc0;
    asm volatile("s_getpc_b64 %0" : "=s"(pc0));
    const rsrc_t rc = __builtin_amdgcn_make_buffer_rsrc((void*)(pc0 & ~127ull), 0, 32 * CODE_LINES * 128, 0x00020000);
    if (w0 == 3) touch0 = __builtin_amdgcn_raw_buffer_load_b32(rc, (tid & 63) < CODE_LINES ? (tid & 63) * 128 : NONE, (b >> 3) * CODE_LINES * 128, 0);
    PIN();
  }
  const int sp0 = (b & 6) + ((b >> 3) & 1), c0 = (b >> 4) + 16 * (b & 1);
  const rsrc_t rs = __builtin_amdgcn_make_buffer_rsrc((void*)ws, 0, (int)WS_BYTES, 0x00020000);
  const bool compute = w0 < 4;
  gu32* const fg = (gu32*)(ws + FLAG_G);
  gu32* const tmo = fg + FL_TMO * 32;
  unsigned tag = 0;
#define HOLD(k) do {} while (0)
#define FLAG(blk, line) st4(rs, tag, lane ? NONE : 0u, (blk) + (line) * 128)
#define POLL(blk, line, n) poll((gu32*)(ws + (blk)) + ((line) + lane) * 32, lane < (n), tmo, tag)
#define GETTAG() tag = (unsigned)__builtin_amdgcn_readfirstlane(*(LAS unsigned*)(L + LDS_TAG))
  auto blank = [&](int it, unsigned ly) {
    const int lane = lane_id(), sp = sopq(sp0), c = sopq(c0), w = it & 3;
    const unsigned o = (unsigned)vopq(-1);
    if (it < 16) st16(rs, (v4u){o, o, o, o}, lane * 16, ly + (it & 8 ? L_V : L_K) + ((8 * sp + (it & 7)) * 32 + c) * 1024);
    else if (it < 24) st16(rs, (v4u){o, o, o, o}, ((lane >> 4) & 1) * 16384 + (lane >> 5) * 256 + (lane & 15) * 16, ly + L_O + (8 * sp + (it & 7)) * 32768 + c * 512);
    else if (it < 28) st16(rs, (v4u){o, o, o, o}, (lane >> 4) * 512 + (lane & 15) * 16, ly + L_X1 + ((8 * sp + 4 + 2 * (c & 1) + (w >> 1)) * 32 + (c & ~1)) * 1024 + (w & 1) * 256);
    else if (it > 28 && it < 33) st16(rs, (v4u){o, o, o, o}, lane * 16, ly + L_X + ((16 * sp + 4 * (c & 1) + w) * 16 + (c >> 1)) * 1024);
    else st8(rs, (v2u){o, o}, (lane >> 5) * 4096 + (lane & 31) * 8, ly + (it == 28 ? L_X1 + 4 * 4096 : L_X) + MB2 + (8 * sp + 2 * (c & 1)) * 4096 + (c >> 1) * 256);
  };
  v4u xq[32];

  if (compute) {
    const int layer = 0; const unsigned lay0 = LAY_OFF;
    COORDS();
    SETPRIO(2);
    const int r = lane >> 3, j = lane & 7;
    const u16* src = x + (size_t)(256 * sp + 8 * c + r) * D_ + 128 * w + 8 * j;
    v4u a[2] = {*(const v4u*)src, *(const v4u*)(src + 64)};
#pragma unroll
    for (int i = 0; i < 2; ++i) {
      a[i] = nos4(a[i]);
      st16(rs, a[i], (j >> 2) * 1024 + ((j & 3) * 16 + r) * 16, lay + L_X + (16 * sp + n32) * 16384 + (2 * w + i) * 2048 + rh * 128);
      auto quad = [](float v) {
        v += __int_as_float(__builtin_amdgcn_update_dpp(0, __float_as_int(v), 0xb1, 0xf, 0xf, true));
        return v + __int_as_float(__builtin_amdgcn_update_dpp(0, __float_as_int(v), 0x4e, 0xf, 0xf, true));
      };
      float sm = 0.f, q = 0.f;
#pragma unroll
      for (int m = 0; m < 4; ++m) sm += bflo(a[i][m]) + bfhi(a[i][m]);
      sm = quad(sm);
      const float mu = sm * (1.0f / 32.0f);
#pragma unroll
      for (int m = 0; m < 4; ++m) { const float e0 = bflo(a[i][m]) - mu, e1 = bfhi(a[i][m]) - mu; q += e0 * e0 + e1 * e1; }
      q = quad(q);
      st8(rs, (v2u){nos(__float_as_uint(sm)), nos(__float_as_uint(q))}, (j & 3) ? NONE : (j >> 2) * 256 + r * 8,
          lay + L_X + MB2 + (8 * sp + (c >> 2)) * 4096 + (4 * w + 2 * i) * 256 + (c & 3) * 64);
    }
    DRAIN();
    asm volatile("" ::"v"(touch0));
    BAR();
    BAR();
    GETTAG();
    const int TB = 8 * sp + 4 + w;
    static_for<NPQ>([&](auto nc) { constexpr int n = nc.value; xq[n] = ld16(rs, lo + ((n >> 1) & 3) * 1024, lay + L_X + TB * 32768 + (n & 1) * 16384 + (n >> 3) * 4096); });
    BARX();
  } else {
    const int layer = 0; const unsigned lay0 = LAY_OFF;
    COORDS();
    SETPRIO(3);
    Retile rt;
    LAS unsigned char* tb = L + LDS_TB + w * 8192;
    const int id = 2 * sp + rh + 16 * (w - 1);
    auto fill0 = [&]() {
#pragma unroll 1
      for (int it = w - 1; it < 16; it += 3) blank(it, lay);
      PIN();
    };
    if (w == 0 || id < 28) {
      const u16* src; int ld, sw; unsigned dst;
      if (w == 0) {
        const int fi = lane >> 3, i8 = lane & 7, F = 6 * sp + (fi < 6 ? fi : 0);
        const int ks = F < 36 ? F / 3 : F < 44 ? (F - 12) >> 1 : F - 32, m = F < 36 ? (F - 3 * ks + 1) % 3 : F < 44 ? 1 + (F & 1) : 0;
        src = wqkv + (size_t)(32 * ks + 8 * (i8 >> 1)) * (3 * D_) + m * D_ + 16 * c + 8 * (i8 & 1);
        ld = 3 * D_; sw = i8; dst = fi < 6 ? WQKV_OFF + c * 49152 + F * 1024 + i8 * 128 : NONE;
      } else {
        const int t = 64 * id + lane, mo = t / 14, ch = t - 14 * mo, mat = mo >> 6, o = mo & 63, c7 = ch >= 7 ? ch - 7 : ch, j = c7 >> 1, kap = o >> 1;
        src = (mat ? wu : wg) + (size_t)(8 * o) * F_ + 112 * n32 + 8 * ch;
        ld = F_;
        const int cell = kap < 16 ? 7 * (kap >> 1) + (j < 3 ? 3 * (kap & 1) + j : 6) : 56 + 16 * j + (j < 3 ? kap - 16 : (kap - 16) >> 1);
        const int rho = j < 3 ? 2 * mat + (c7 & 1) + 4 * (o & 1) : 4 * (kap & 1) + 2 * (o & 1) + mat;
        sw = j < 3 ? rho : 0; dst = WGU_OFF + (2 * n32 + (ch >= 7)) * 114688 + cell * 1024 + rho * 128;
      }
      unsigned ep = 0;
      if (w == 0) ep = __hip_atomic_load(fg + FL_EP * 32, RLX_AGENT);
      rt.load(src, ld, 4);
      if (w) fill0();
      if (w == 0) {
        tag = __builtin_amdgcn_readfirstlane(ep) + 1u;
        if (lane < 2) *(LAS v4u*)(L + LDS_TAG + lo) = (v4u){lane ? 0u : tag, 0u, 0u, 0u};
        v4u mk[2];
#pragma unroll
        for (int k = 0; k < 8; ++k) {
          const int kq = ((2 * k) & 3) + 8 * ((2 * k) >> 2) + 4 * hh, ql = lane & 31;
          mk[k >> 2][k & 3] = (kq <= ql ? 0xffffu : 0u) | (kq + 1 <= ql ? 0xffff0000u : 0u);
        }
        *(LAS v4u*)(L + LDS_MK + lo) = mk[0];
        *(LAS v4u*)(L + LDS_MK + 1024 + lo) = mk[1];
        if (sp == 0) {
#pragma unroll
          for (int i = 0; i < 9; ++i) *(LAS v4u*)(L + LDS_VL + (i < 8 ? i * 1024 + lo : 8192 + t16 * 16)) = (v4u){0u, 0u, 0u, 0u};
        }
        const unsigned one = (sp == 0 && lane < 16) ? 0u : 0x3f803f80u;
        if (lane < 32) *(LAS v4u*)(L + LDS_VL + lane * VBLK + 512) = (v4u){one, one, one, one};
      }
      rt.lines(tb, lane, sw, dst);
      if (w == 0) rt.out(rs);
    } else if (id < 46) {
      const int slot = (lane >> 2) & 3, m4 = lane & 3, q = 16 * (id < 42 ? id - 28 : id - 42) + 4 * g4 + slot;
      const int cq = q < 128 ? q >> 2 : q < 192 ? (q - 128) >> 1 : q - 192, r = q < 128 ? q & 3 : q < 192 ? 4 + (q & 1) : 6;
      rt.load((id < 42 ? wd + (size_t)(56 * cq + (r < 6 ? 16 * (r >> 1) + 4 * (r & 1) : 48)) * D_ : wo + (size_t)(16 * (q >> 1) + 4 * (q & 1)) * D_) + 32 * n32 + 8 * m4,
              D_, (id < 42 && r == 6) ? 4 : 8);
      fill0();
      rt.put(tb, g4 * 2048, 1024, 4 * m4 + 16 * slot, 0);
      asm volatile("s_waitcnt lgkmcnt(0)" ::: "memory");
#pragma unroll
      for (int i = 0; i < 8; ++i) {
        rt.in[i] = lds16(tb, i * 1024 + lo);
        rt.d[i] = (id < 42 ? WDN_OFF + n32 * 114688 + (id - 28) * 8192 : WO_OFF + n32 * 32768 + (id - 42) * 8192) + i * 1024 + lo;
      }
    } else {
#pragma unroll
      for (int i = 0; i < 8; ++i) { undef(rt.in[i]); rt.d[i] = NONE; }
      fill0();
    }
    if (w == 0) DRAIN();
    HBAR();
    if (w == 0) {
      FLAG(0, FL_XB + sp * 32 + c);
      poll((gu32*)ws + (FL_XB + (lane < 32 ? sp * 32 + lane : (lane & 7) * 32 + c)) * 32, lane < 40, tmo, tag);
    }
    Stats st;
    if (w == 0) {
      st.load(rs, lane, lay + L_X + MB2 + (8 * sp + 4) * 4096);
    }
    HBAR();
    GETTAG();
    if (w) {
      const int l = w - 1;
      dma<4>(rs, L + l * 4096, lo, WQKV_OFF + c * 49152 + l * 4096);
      dma<7>(rs, L + (12 + 7 * l - (l >> 1)) * 1024, lo, WQKV_OFF + c * 49152 + (12 + 7 * l - (l >> 1)) * 1024);
      wait_vm<7>();
    }
    HOLD(0);
    HBAR();
    if (w) { dma<6>(rs, L + (27 + 5 * w) * 1024, lo, WQKV_OFF + c * 49152 + (27 + 5 * w) * 1024);
      rt.out(rs); wait_vm<14>();
    }
    else {
      st.all(L, lane);
      PIN();
      FLAG(0, FL_XA + sp * 32 + c);
      const int i = lane & 31;
      const int line = i < 8 ? FL_KV + (8 * sp + i) * 32 + c : i < 16 ? FL_O + (8 * sp + i - 8) * 32 + c
                     : i < 20 ? FL_X1B + (8 * sp + 4 + 2 * rh + ((i >> 1) & 1)) * 32 + 2 * n32 + (i & 1) : FL_X1A + (i - 20 < 6 ? i - 20 : 5) * 256 + sp * 32 + c;
      touch0 = __builtin_amdgcn_raw_buffer_load_b32(rs, hh * FLAG_LAYER + line * 128, 0, AUX_SC1);
    }
  }

  for (int layer = 0; layer < 2; ++layer) {
    const bool last = layer == 1;
    const unsigned lay0 = LAY_OFF + layer * LAY_BYTES;
    auto ldw = [&](auto nc, unsigned src, int cell) {
      constexpr int N = nc.value, K = (N + 2) / 3;
      const int l = sopq(w0) - 5, c1 = cell + (l * K + K > N ? N - K : l * K);
      dma<K>(rs, L + c1 * 1024, lane_id() * 16, src + c1 * 1024);
    };
    auto ld_gu = [&](int s) { ldw(IC<14>{}, WGU_OFF + sopq(c0) * 114688, 14 * s); };
    auto ld_dn = [&](int s) { ldw(IC<14>{}, WDN_OFF + (sopq(c0) >> 1) * 114688, 14 * s); };
    if (compute) {
      COORDS();
      const int TB = 8 * sp + 4 + w, TA = TB - 4;
      v4u xa[32];
      auto ldx = [&](auto nc, int T) {
        constexpr int n = nc.value;
        return ld16(rs, lo + ((n >> 1) & 3) * 1024, lay + L_X + (2 * T + (n & 1)) * 16384 + (n >> 3) * 4096);
      };
      f32x4 aq[2][2], ak[2][2], av[2][2], r0, r1;
      v4u mf0, mf1; float rd0, rd1;
      unsigned o0, o1, o2, o3;
      const int kpos = (t16 + 16 * hh + 32 * (g4 & 1)) * 16;
      auto epi = [&](int k, int pa) {
        const int tl = pa ? w : 4 + w, st = (pa ? LDS_KL : LDS_ST) + w * 128;
        const unsigned kvb = lay + ((8 * sp + tl) * 32 + c) * 1024;
        if (k == 0) { r0 = lds4f(L, st + g4 * 16); r1 = lds4f(L, st + 64 + g4 * 16); }
        if (k == 2) { o0 = mulpk(ak[pa][0][0], rd0, ak[pa][0][1], rd0); o1 = mulpk(ak[pa][0][2], rd0, ak[pa][0][3], rd0); }
        if (k == 3) { o2 = mulpk(ak[pa][1][0], rd1, ak[pa][1][1], rd1); o3 = mulpk(ak[pa][1][2], rd1, ak[pa][1][3], rd1); }
        if (k == 4) {
          swap32(o0, o2); swap32(o1, o3);
          st16(rs, nos4((v4u){o0, o1, o2, o3}), kpos, kvb + L_K);
          *(LAS v4u*)(L + LDS_KL + (8 + tl) * 1024 + kpos) = (v4u){o0, o1, o2, o3};
        }
        if (k == 5) { o0 = mulpk(av[pa][0][0], r0[0], av[pa][0][1], r0[1]); o1 = mulpk(av[pa][0][2], r0[2], av[pa][0][3], r0[3]); }
        if (k == 6) { o2 = mulpk(av[pa][1][0], r1[0], av[pa][1][1], r1[1]); o3 = mulpk(av[pa][1][2], r1[2], av[pa][1][3], r1[3]); }
        if (k == 7) {
          swap32(o0, o2); swap32(o1, o3);
          st16(rs, nos4((v4u){o0, o1, o2, o3}), lo, kvb + L_V);
          *(LAS v4u*)(L + LDS_VL + (16 + 2 * tl + hh) * VBLK + (lane & 31) * 16) = (v4u){o0, o1, o2, o3};
        }
        if (k == 8) {
          asm volatile("s_nop 7" : "+v"(aq[pa][0]), "+v"(aq[pa][1]));
          const float s0 = rd0 * (0.25f * LOG2E), s1 = rd1 * (0.25f * LOG2E);
          o0 = mulpk(aq[pa][0][0], s0, aq[pa][0][1], s0); o1 = mulpk(aq[pa][0][2], s0, aq[pa][0][3], s0);
          o2 = mulpk(aq[pa][1][0], s1, aq[pa][1][1], s1); o3 = mulpk(aq[pa][1][2], s1, aq[pa][1][3], s1);
          swap32(o0, o2); swap32(o1, o3);
          *(LAS v4u*)(L + LDS_QL + tl * 1024 + kpos) = (v4u){o0, o1, o2, o3};
        }
      };
      pipe<102>(
          [&](auto fc) {
            constexpr int t = fc.value, u = t % 51;
            if constexpr (t == 28) { BARX(); CST(); }
            if constexpr (t == 12 || t == 44 || t == Q3T) BARX();
            if constexpr (u == 44 || u == 45 || u == 50) return csfrag(L, (u == 50 ? 0 : u - 43) * 16 + t16);
            else return lds16(L, (u < 44 ? u : u - 2) * 1024 + lsw);
          },
          [&](auto fc, const v4u& wfr, auto pc) {
            constexpr int t = fc.value, part = pc.value, u = t % 51;
            constexpr int ks = (u < 36 ? u / 3 : u < 44 ? (u - 12) >> 1 : u - 34) & 15, m = u < 36 ? (u % 3 + 1) % 3 : u < 46 ? 1 + (u & 1) : 0;
            constexpr bool pa = t >= 51, cs = u == 44 || u == 45 || u == 50;
            if constexpr (t < 0) return;
            else if constexpr (part == 0) {
              if constexpr (pa && u < 44 && u % 12 == 0) {
                constexpr int n = u / 12 * 8;
                if (__builtin_expect(missing<8>(xa + n), 0)) {
                  unsigned spins = 0;
                  do { static_for<8>([&](auto i) { xa[n + i.value] = ldx(IC<n + i.value>{}, TA); }); DBGC(0); } while (missing<8>(xa + n) && ++spins < DATA_CAP);
                }
                PIN();
              }
              const v4u& x0 = cs ? mf0 : pa ? xa[2 * ks] : xq[2 * ks];
              const v4u& x1 = cs ? mf1 : pa ? xa[2 * ks + 1] : xq[2 * ks + 1];
              if constexpr (m == 0) { aq[pa][0] = mfma16(wfr, x0, u < 3 ? zero4() : aq[pa][0]); aq[pa][1] = mfma16(wfr, x1, u < 3 ? zero4() : aq[pa][1]); }
              if constexpr (m == 1) { ak[pa][0] = mfma16(wfr, x0, u < 3 ? zero4() : ak[pa][0]); ak[pa][1] = mfma16(wfr, x1, u < 3 ? zero4() : ak[pa][1]); }
              if constexpr (m == 2) { av[pa][0] = mfma16(x0, wfr, u < 3 ? zero4() : av[pa][0]); av[pa][1] = mfma16(x1, wfr, u < 3 ? zero4() : av[pa][1]); }
            } else {
              if constexpr (2 * t < 32 - NPQ) { xq[NPQ + 2 * t] = ldx(IC<NPQ + 2 * t>{}, TB); xq[NPQ + 2 * t + 1] = ldx(IC<NPQ + 2 * t + 1>{}, TB); }
              if constexpr (u == 40) {
                const int st = (pa ? LDS_KL : LDS_ST) + w * 128;
                rd0 = lds1f(L, st + t16 * 4); rd1 = lds1f(L, st + 64 + t16 * 4);
                const int a = g4 ? LDS_TAG + 8 : st + 1024 + t16 * 4;
                mf0 = mfrag(L, a); mf1 = mfrag(L, g4 ? a : a + 64);
              }
              if constexpr (t >= 45 && t < 54) epi(t - 45, 0);
              if constexpr (t >= 96) epi(t - 96, 1);
              static_for<16>([&](auto j) { if constexpr (xa_step(j.value) == t) { xa[2 * j.value] = ldx(IC<2 * j.value>{}, TA); xa[2 * j.value + 1] = ldx(IC<2 * j.value + 1>{}, TA); } });
              if constexpr (t == 50) CST();
            }
          });
      epi(6, 1); epi(7, 1); epi(8, 1);
      BAR();
      CST();
    } else {
      COORDS();
      const bool loader = w != 0;
      auto cs_qkv = [&]() {
        const unsigned one = (unsigned)vopq(0x3f803f80);
        const v4u ones = (v4u){one, one, one, one};
        f32x4 a = zero4();
        v4u f[16];
        const int fa = (w - 1) * 1024 + lsw, fb = (w < 3 ? 35 + w : 44) * 1024 + lsw, fs = w < 3 ? 2048 : 1024;
#pragma unroll
        for (int ks = 0; ks < 16; ++ks) asm volatile("ds_read_b128 %0, %1" : "=v"(f[ks]) : "v"(ks < 12 ? fa + ks * 3072 : fb + (ks - 12) * fs) : "memory");
        asm volatile("s_waitcnt lgkmcnt(0)" : "+v"(f[0]), "+v"(f[1]), "+v"(f[2]), "+v"(f[3]), "+v"(f[4]), "+v"(f[5]), "+v"(f[6]), "+v"(f[7]));
        asm volatile("" : "+v"(f[8]), "+v"(f[9]), "+v"(f[10]), "+v"(f[11]), "+v"(f[12]), "+v"(f[13]), "+v"(f[14]), "+v"(f[15]));
#pragma unroll
        for (int ks = 0; ks < 16; ++ks) a = mfma16(f[ks], ones, a);
        if (t16 == 0) {
#pragma unroll
          for (int r = 0; r < 4; ++r) cs_put(L, (w % 3) * 16 + 4 * g4 + r, a[r]);
        }
      };
      const unsigned wob = WO_OFF + n32 * 32768 - LDS_WO;
      if (last && !loader) {
        v4u o[4];
#pragma unroll
        for (int i = 0; i < 4; ++i) o[i] = lds16(L, LDS_QL + i * 1024 + lo);
        const v2u sq = *(LAS v2u*)(L + LDS_QL + 4096 + lane * 8);
#pragma unroll
        for (int i = 0; i < 4; ++i) st16(rs, o[i], lo, lay + L_X + ((16 * sp + 4 * rh + i) * 16 + n32) * 1024);
        st8(rs, sq, hh * 4096 + (g4 & 1) * 128 + t16 * 8, lay + L_X + MB2 + (8 * sp + 2 * rh) * 4096 + n32 * 256);
        PIN();
      }
      HBARS(false);
      if (layer == 0) {
        if (loader) wait_vm<8>();
        HBAR();
        if (loader) { cs_qkv(); DRAIN(); }
        HBAR();
        if (!loader) FLAG(FLAG_G, FL_WR + n32 * 16 + 2 * sp + rh);
      } else {
        if (loader) { ldw(IC<10>{}, wob, 80); wait_vm<13>(); }
        HBAR();
        if (loader) ldw(IC<10>{}, wob, 90);
        HBAR();
        if (loader) ldw(IC<12>{}, wob, 100);
      }
      {
        v4u kv[4];
        auto ldkv = [&]() {
          const unsigned so = lay + ((8 * sp - 5 + w) * 32 + c) * 1024, s2 = w == 1 ? so + 3 * 32768 : NONE;
          kv[0] = ld16(rs, lo, so + L_K); kv[1] = ld16(rs, lo, so + L_V); kv[2] = ld16(rs, lo, s2 + L_K); kv[3] = ld16(rs, lo, s2 + L_V);
          PIN();
          DBGC(0);
        };
        if (!loader) {
          Stats st;
          gu32* const bl = lane < 16 ? fg + (FL_WR + n32 * 16 + lane) * 32 : (gu32*)ws + (FL_XA + (sp ? sp - 1 : 0) * 32 + c) * 32;
          unsigned spins = 0, pv = tag;
          do { st.load(rs, lane, lay + L_X + MB2 + 8 * sp * 4096); DBGC(0); } while (missing<16>((const v4u*)st.p) && ++spins < DATA_CAP);
          if (!last) pv = __hip_atomic_load(bl, RLX_AGENT);
          st.all(L, lane, LDS_KL);
          if (!__all(pv == tag)) poll(bl, lane <= 16, tmo, tag);
          asm volatile("" ::"v"(touch0));
        }
        HBARS(false);
        if (loader && sp > 0) ldkv();
        if (loader && !last) { ldw(IC<10>{}, wob, 80); ldw(IC<10>{}, wob, 90); ldw(IC<12>{}, wob, 100); }
        if (loader && sp > 0) {
          for (unsigned spins = 0; missing<4>(kv) && spins < DATA_CAP; ++spins) ldkv();
          *(LAS v4u*)(L + LDS_KL + (3 + w) * 1024 + lo) = kv[0];
          *(LAS v4u*)(L + LDS_VL + (6 + 2 * w + hh) * VBLK + (lane & 31) * 16) = kv[1];
          if (w == 1) {
            *(LAS v4u*)(L + LDS_KL + 7 * 1024 + lo) = kv[2];
            *(LAS v4u*)(L + LDS_VL + (14 + hh) * VBLK + (lane & 31) * 16) = kv[3];
          }
        }
      }
      if (!last) HOLD(1);
      HBAR();
      CST(); CST();
      if (loader) {
        ld_gu(0); ld_gu(1); ld_gu(2); ldw(IC<6>{}, WGU_OFF + c * 114688, 42);
        if (!last) { ldw(IC<8>{}, WGU_OFF + c * 114688, 48); ld_gu(4); ldw(IC<10>{}, WGU_OFF + c * 114688, 70); wait_vm<29>(); }
        else wait_vm<17>();
        if (lane == 0) asm volatile("ds_add_u32 %0, %1" ::"v"(LDS_TAG + 12), "v"(1) : "memory");
      }
      if (sp > 0) {
        LAS unsigned* hw = (LAS unsigned*)(L + LDS_TAG + 4);
        CST();
        if (!loader) {
          v4u kv[8];
          unsigned spins = 0;
          do {
#pragma unroll
            for (int t = 0; t < 4; ++t) {
              const unsigned so = lay + ((8 * sp - 8 + t) * 32 + c) * 1024;
              kv[t] = ld16(rs, lo, so + L_K); kv[4 + t] = ld16(rs, lo, so + L_V);
            }
            PIN();
            DBGC(1);
          } while (missing<8>(kv) && ++spins < DATA_CAP);
#pragma unroll
          for (int t = 0; t < 4; ++t) {
            *(LAS v4u*)(L + LDS_KL + t * 1024 + lo) = kv[t];
            *(LAS v4u*)(L + LDS_VL + (2 * t + hh) * VBLK + (lane & 31) * 16) = kv[4 + t];
          }
          asm volatile("s_waitcnt lgkmcnt(0)" ::: "memory");
          __hip_atomic_store(hw, (unsigned)layer + 1u, __ATOMIC_RELAXED, __HIP_MEMORY_SCOPE_WORKGROUP);
        } else {
          unsigned v;
          do {
            __builtin_amdgcn_s_sleep(1);
            asm volatile("ds_read_b32 %0, %1\n\ts_waitcnt lgkmcnt(0)" : "=v"(v) : "v"(LDS_TAG + 4) : "memory");
          } while ((unsigned)__builtin_amdgcn_readfirstlane(v) != (unsigned)layer + 1u);
        }
      } else CST();
      CST();
      if (layer == 0 && !loader) touch0 = __builtin_amdgcn_raw_buffer_load_b32(rs, LAY_OFF + L_O + lane * TSTR + c * 4096 + sp * 128, 0, AUX_SC1);
      SETPRIO(1);
    }
    {
      COORDS();
      const int tl = compute ? 4 + w : w, T = 8 * sp + tl;
      const v4u o = attn_unit(L, lane, tl, sp == 0 ? 8 - tl : 0);
      st16(rs, nos4(o), (g4 & 1) * 16384 + hh * 256 + t16 * 16, lay + L_O + T * 32768 + c * 512);
      if (!last) { DRAIN(); FLAG(flo, FL_O + T * 32 + c); }
      CST();
    }
    if (compute) {
      SETPRIO(0);
      v4u fr[176];
      {
        COORDS();
        const int TB = 8 * sp + 4 + 2 * rh + (w >> 1), SB = 2 * TB + (w & 1);
        const v4u rB = ld16(rs, lo, lay + L_X + (SB * 16 + n32) * 1024), rA = ld16(rs, lo, lay + L_X + (SB * 16 + n32) * 1024 - 8 * 16384);
        const unsigned xv = (g4 >> 1) * 1024 + (g4 & 1) * 512 + t16 * 16, sv = g4 ? NONE : t16 * 8;
        const unsigned xo = lay + L_X1 + (TB * 32 + 2 * n32) * 1024 + (w & 1) * 256, so = lay + L_X1 + MB2 + TB * 4096 + n32 * 256 + (w & 1) * 128;
        const int lswo = vopq(lsw + LDS_WO);
        while ((unsigned)__builtin_amdgcn_readfirstlane(__hip_atomic_load((LAS unsigned*)(L + LDS_TAG + 12), __ATOMIC_RELAXED, __HIP_MEMORY_SCOPE_WORKGROUP)) != 3u * layer + 3u)
          __builtin_amdgcn_s_sleep(1);
        asm volatile("" ::: "memory");
        v4u of[32];
        unsigned pk;
        f32x4 a0, a1;
        Fin fin;
        auto ldo = [&](auto i) { return ld16(rs, lo + (i.value & 3) * 1024, lay + L_O + (SB - 8 * (i.value >> 4)) * 16384 + ((i.value >> 2) & 3) * 4096); };
        pipe<64>(
            [&](auto fc) { return lds16(L, (fc.value & 31) * 1024 + lswo); },
            [&](auto fc, const v4u& wfr, auto pc) {
              constexpr int t = fc.value, part = pc.value, u = t & 31;
              if constexpr (part == 0) {
                if constexpr (u % 16 == 0) {
                  if (__builtin_expect(missing<8>(of + t / 2), 0)) {
                    unsigned spins = 0;
                    do { static_for<16 - u / 2>([&](auto i) { of[t / 2 + i.value] = ldo(IC<t / 2 + i.value>{}); }); DBGC(1 + (t >> 4)); } while (missing<8>(of + t / 2) && ++spins < DATA_CAP);
                  }
                  if constexpr (u == 0) CST();
                  PIN();
                }
                if constexpr (u & 1) a1 = mfma16(wfr, of[t >> 1], u == 1 ? zero4() : a1);
                else a0 = mfma16(wfr, of[t >> 1], u == 0 ? zero4() : a0);
              } else {
                if constexpr (t == -1) {
                  if (!last) POLL(flo, FL_O + TB * 32, 32);
                  static_for<16>([&](auto i) { of[i.value] = ldo(i); });
                }
                if constexpr (t == 55) { pk = tag; if (!last) pk = __hip_atomic_load(fl + (FL_X1B + (8 * sp + 4 + w) * 32 + (lane & 31)) * 32, RLX_AGENT); }
                if constexpr (t == 63) { if (!__all(pk == tag)) POLL(flo, FL_X1B + (8 * sp + 4 + w) * 32, 32); }
                if constexpr (t == 63) static_for<NPG>([&](auto i) { fr[i.value] = ld16(rs, lo + (i.value & 3) * 1024, lay + L_X1 + (8 * sp + 4 + w) * 32768 + (i.value >> 2) * 4096); });
                if constexpr (t == 31 || t == 63) {
                  asm volatile("s_nop 7" : "+v"(a0), "+v"(a1));
                  static_for<8>([&](auto k) { fin.template chunk<k.value>(a0, a1, t == 31 ? rB : rA, rs, xv, xo - (t == 31 ? 0 : 4 * 32768), rs, sv, so - (t == 31 ? 0 : 4 * 4096)); });
                  CST();
                }
                if constexpr (t == 31) {
                  if (!last) {
                    pk = __hip_atomic_load(fl + (FL_O + (TB - 4) * 32 + (lane & 31)) * 32, RLX_AGENT);
                    DRAIN();
                    FLAG(flo, FL_X1B + TB * 32 + 2 * n32 + (w & 1));
                    if (!__all(pk == tag)) POLL(flo, FL_O + (TB - 4) * 32, 32);
                  }
                  static_for<16>([&](auto i) { of[16 + i.value] = ldo(IC<16 + i.value>{}); });
                }
              }
            });
        CST();
      }
      SETPRIO(2);
      BAR();
      {
        COORDS();
        const int TB = 8 * sp + 4 + w;
        const int TD = 8 * sp + 4 + 2 * rh + (w >> 1), SD = 2 * TD + (w & 1);
        const unsigned x1b = lay + L_X1 + TB * 32768, hb = lay + L_H + SD * 57344, xn = last ? NONE : lay + LAY_BYTES + L_X;
        const unsigned hsv = (g4 & 1) * 57344 + hh * 256 + t16 * 16, hso = lay + L_H + TB * 114688;
        const rsrc_t ro = last ? __builtin_amdgcn_make_buffer_rsrc((void*)out, 0, (int)MB2, 0x00020000) : rs;
        const unsigned ob = last ? SD * 16384 + n32 * 64 : xn + (SD * 16 + n32) * 1024;
        const unsigned sb = xn + MB2 + TD * 4096 + n32 * 256 + (w & 1) * 128;
        unsigned ov, sv;
        const int lsw2 = vopq(lsw + 65536), lh = (hh * 16 + t16) * 16, lh2 = vopq(lh + 65536);
        f32x16 acc[2][4];
        f32x4 d0[2], d1[2];
        v4u mf, hv[4], res[2];
        float k1[2], den2[2], t0, t1, p0_, p1_;
        unsigned cm0, cm1;
        Fin fin;
        auto epi = [&](auto ec) {
          constexpr int job = ec.value >> 6, k = ec.value & 63, j = k >> 4, i = (k >> 2) & 3, uo = j < 3 ? 8 : 4;
          const f32x16& a = acc[job][j];
          if constexpr ((k & 3) == 0)
            asm volatile("v_mul_f32 %0, %2, %4\n\tv_mul_f32 %1, %3, %4\n\tv_exp_f32 %0, %0" : "=&v"(t0), "=&v"(t1) : "v"(a[2 * i]), "v"(a[2 * i + 1]), "v"(k1[job]));
          else if constexpr ((k & 3) == 1)
            asm volatile("v_exp_f32 %0, %0\n\tv_mul_f32 %1, %3, %5\n\tv_mul_f32 %2, %4, %6"
                : "+v"(t1), "=&v"(p0_), "=&v"(p1_) : "v"(a[2 * i]), "v"(a[2 * i + 1]), "v"(a[uo + 2 * i]), "v"(a[uo + 2 * i + 1]));
          else if constexpr ((k & 3) == 2)
            asm volatile("v_fma_f32 %0, %0, %2, %2\n\tv_fma_f32 %1, %1, %2, %2\n\tv_rcp_f32 %0, %0" : "+v"(t0), "+v"(t1) : "v"(den2[job]));
          else {
            unsigned d;
            asm volatile("v_rcp_f32 %2, %2\n\tv_mul_f32 %1, %1, %3\n\tv_mul_f32 %2, %2, %4\n\tv_cvt_pk_bf16_f32 %0, %1, %2" : "=v"(d), "+v"(t0), "+v"(t1) : "v"(p0_), "v"(p1_));
            hv[j][i] = d;
            if constexpr (k == (job ? 31 : NEP - 1)) { st16(rs, hv[0], hsv, hso - job * 4 * 114688 + c * 1024); st16(rs, hv[1], hsv + 512, hso - job * 4 * 114688 + c * 1024); }
            if constexpr (k == NEP - 1) {
              st16(rs, hv[2], hsv, hso - job * 4 * 114688 + 32768 + c * 512);
              st8(rs, (v2u){hv[3][0], hv[3][1]}, hh ? hsv - 248 : hsv, hso - job * 4 * 114688 + 49152 + c * 256);
            }
          }
        };
        auto vload = [&](auto ic) {
          constexpr int id = ic.value;
          if constexpr (id < 64) fr[id] = ld16(rs, lo + (id & 3) * 1024, x1b - (id >> 5) * 4 * 32768 + ((id & 31) >> 2) * 4096);
          else if constexpr (id < 176) {
            constexpr int n = (id - 64) % 56;
            fr[id] = ld16(rs, lo + (n & 3) * 1024, hb - (id >= 120 ? 8 * 57344 : 0) + (n >> 2) * 4096);
          } else if constexpr (id < 208) {
            constexpr int n = id - 176;
            xq[n] = ld16(rs, lo + ((n >> 1) & 3) * 1024, xn + TB * 32768 + (n & 1) * 16384 + (n >> 3) * 4096);
          } else res[id - 224] = ld16(rs, vopq(lo & 0x2f0) + ((lo & 256) << 1) + (lo & 512), lay + L_X1 + ((TD - 4 * (id - 224)) * 32 + 2 * n32) * 1024 + (w & 1) * 256);
        };
        pipe<NST>(
            [&](auto fc) {
              constexpr int t = fc.value;
              if constexpr (t == 32) wait_vm<vm_between(-1, t - LA - 1)>();
              if constexpr (t == GA + 48) wait_vm<vm_between(ep_step(NEP - 1), t - LA - 1)>();
              if constexpr (t == DB + 14) wait_vm<vm_between(GA + ep_step(31), t - LA - 1)>();
              if constexpr (t == DB + 56) wait_vm<vm_between(GA + ep_step(NEP - 1), t - LA - 1)>();
              if constexpr (t == DA + 42) wait_vm<vm_between(FINB + 7, t - LA - 1)>();
              if constexpr (t < DB) {
                constexpr GS s = gu_step(t % GA);
                if constexpr (s.cs) return csfrag(L, 48 + 32 * s.j + (s.j < 3 ? lane & 31 : t16));
                else {
                  if (s.off % 14336 == 0 && s.off >= (t < GA ? 14336 : 28672)) BARX();
                  return lds16(L, s.off < 65536 ? s.off + (s.j < 3 ? lsw : lh) : s.off - 65536 + (s.j < 3 ? lsw2 : lh2));
                }
              } else {
                constexpr int v = (t - DB) % 112;
                if (v % 14 == 0 && v >= (t < DA ? 0 : 28)) BARX();
                return lds16(L, v < 64 ? v * 1024 + lsw : (v - 64) * 1024 + lsw2);
              }
            },
            [&](auto fc, const v4u& wfr, auto pc) {
              constexpr int t = fc.value, part = pc.value;
              if constexpr (t < 0) return;
              else if constexpr (part == 0) {
                if constexpr (t < DB) {
                  constexpr int job = t / GA, u = t % GA;
                  constexpr GS s = gu_step(u);
                  if constexpr (t == 0 || t == 16 || t == 32 || t == 64) {
                    constexpr int n0 = t < 64 ? t / 4 : 16, nn = t == 0 || t == 16 ? 4 : t == 32 ? 8 : 16;
                    if (__builtin_expect(t ? __builtin_amdgcn_ballot_w64((cm0 > cm1 ? cm0 : cm1) == 0xffffffffu) != 0 : missing<nn>(fr + n0), 0)) {
                      unsigned spins = 0;
                      do { static_for<(t < 64 ? 16 : 32) - n0>([&](auto i) { vload(IC<n0 + i.value>{}); }); DBGC(t < 64 ? 5 : 6); } while (missing<nn>(fr + n0) && ++spins < DATA_CAP);
                    }
                    PIN();
                  }
                  acc[job][s.j] = mfma(wfr, s.cs ? mf : fr[32 * job + (s.kap & 31)], u < 4 ? splat16(0.f) : acc[job][s.j]);
                } else {
                  constexpr int job = t >= DA, v = (t - DB) % 112;
                  if constexpr (v & 1) d1[job] = mfma16(wfr, fr[64 + 56 * job + (v >> 1)], v == 1 ? zero4() : d1[job]);
                  else d0[job] = mfma16(wfr, fr[64 + 56 * job + (v >> 1)], v == 0 ? zero4() : d0[job]);
                }
              } else {
                if constexpr (t < DB) {
                  constexpr int job = t / GA, u = t % GA;
                  if constexpr (u == 74) {
                    const int a = LDS_ST + w * 128 + (lane & 31) * 4;
                    k1[job] = lds1f(L, a); den2[job] = lds1f(L, a + 512); mf = mfrag(L, hh ? LDS_TAG + 8 : a + 1024);
                  }
                  if constexpr (u == 79) k1[job] *= -LOG2E;
                  if constexpr ((t >= 12 && t < 16) || (t >= 24 && t < 32) || (t >= 48 && t < 64)) {
                    constexpr int n = t < 16 ? t - 8 : t < 32 ? t - 16 : t - 32;
                    constexpr bool first = t == 12 || t == 24 || t == 48;
                    cm0 = umax3(first ? 0u : cm0, fr[n][0], fr[n][1]); cm1 = umax3(first ? 0u : cm1, fr[n][2], fr[n][3]);
                    asm volatile("" : "+v"(cm0), "+v"(cm1));
                  }
                }
                if constexpr (TAB.ld[t][0] >= 0) vload(IC<TAB.ld[t][0]>{});
                if constexpr (TAB.ld[t][1] >= 0) vload(IC<TAB.ld[t][1]>{});
                if constexpr (TAB.ep[t] >= 0) epi(IC<TAB.ep[t]>{});
                if constexpr (t == FINB - 1) { const int l = lane_id(); ov = l * 16 + ((((l & 15) * 1008) - (l >> 4) * 240) & sopq(-layer)); sv = l >= 16 ? NONE : l * 8; }
                if constexpr (t >= FINB && t < FINB + 8) fin.template chunk<t - FINB>(d0[0], d1[0], res[0], ro, ov, ob, rs, sv, sb);
              }
            });
        CST();
        asm volatile("s_nop 7" : "+v"(d0[1]), "+v"(d1[1]));
        static_for<8>([&](auto k) { fin.template chunk<k.value, false>(d0[1], d1[1], res[1], ro, ov, ob - 8 * 16384, rs, sv, sb - 4 * 4096); });
        if (last) st16(ro, fin.o, ov, ob - 8 * 16384);
        else {
          *(LAS v4u*)(L + LDS_QL + w * 1024 + lo) = fin.o;
          if (lane < 16) *(LAS v2u*)(L + LDS_QL + 4096 + w * 128 + lane * 8) = fin.sq;
          BAR();
        }
      }
    } else {
      COORDS();
      const bool poller = w == 0, loader = w != 0;
      unsigned pv = 0;
#define PEEK(blk, line) pv = __hip_atomic_load((gu32*)(ws + (blk)) + ((line) + (lane & 31)) * 32, RLX_AGENT)
#define GATE(blk, line) do { if (!__all(pv == tag)) POLL(blk, line, 32); } while (0)
      f32x4 ca = zero4();
      auto cs_gu = [&](int cell, int n2, bool put) {
        const unsigned one = (unsigned)vopq((w == 3 || t16 == (g4 & 1)) ? 0x3f803f80 : 0);
        const int a = cell * 1024 + (w < 3 ? lsw : lo), d2 = n2 == 1 ? 2048 : 7168;
        v4u f[8];
#pragma unroll
        for (int i = 0; i < 8; ++i) asm volatile("ds_read_b128 %0, %1" : "=v"(f[i]) : "v"(a + (i >> 1) * d2 + (i & 1) * n2 * 1024) : "memory");
        asm volatile("s_waitcnt lgkmcnt(0)" : "+v"(f[0]), "+v"(f[1]), "+v"(f[2]), "+v"(f[3]), "+v"(f[4]), "+v"(f[5]), "+v"(f[6]), "+v"(f[7]));
#pragma unroll
        for (int i = 0; i < 8; ++i) ca = mfma16(f[i], (v4u){one, one, one, one}, ca);
        if (put && t16 < (w < 3 ? 2 : 1)) {
#pragma unroll
          for (int r = 0; r < 4; ++r) cs_put(L, 48 + 32 * w + 16 * t16 + 4 * g4 + r, ca[r]);
        }
      };
      if (!last) {
        unsigned v;
        if (loader && lane == 0) asm volatile("ds_add_u32 %0, %1" ::"v"(LDS_TAG + 16), "v"(1) : "memory");
        do asm volatile("ds_read_b32 %0, %1\n\ts_waitcnt lgkmcnt(0)" : "=v"(v) : "v"(LDS_TAG + 16) : "memory");
        while ((unsigned)__builtin_amdgcn_readfirstlane(v) != 3u);
        for (int q = 0; q < 4 - w; ++q) {
          if (w == 3) cs_gu(6, 28, false);
          else cs_gu(q < 2 ? 28 * q + w : 40 + 16 * w + 8 * q, q < 2 ? 3 : 1, q == 3);
        }
      }
      SETPRIO(3);
      if (poller) {
        asm volatile("" ::"v"(touch0));
        Stats st;
        unsigned ev[4];
        const int h2l = lane < 32 ? FL_HA2 + sp * 32 + lane : FL_HB + (sp ? sp - 1 : 0) * 32 + c;
        SETPRIO(1);
        if (last) HOLD(4);
        HBAR();
        static_for<7>([&](auto nc) {
          constexpr int n = nc.value + 1;
          if (n <= 2) st.load(rs, lane, lay + L_X1 + MB2 + (8 * sp + 4) * 4096, 8 * n - 8, 8 * n);
          if (n == 3) { FLAG(flo, FL_X1A + sp * 32 + c); st.part1(); }
          if (n == 4) {
            if (missing<16>((const v4u*)st.p)) {
              unsigned spins = 0;
              do { st.load(rs, lane, lay + L_X1 + MB2 + (8 * sp + 4) * 4096); DBGC(2); } while (missing<16>((const v4u*)st.p) && ++spins < DATA_CAP);
              st.part1();
            }
            st.part2();
          }
          if (n == 5) st.part3(L, lane);
          if (n == 6) PEEK(flo, FL_X1A + sp * 32);
          if (n == 7) GATE(flo, FL_X1A + sp * 32);
          HBARS(n == 3 || n == 7);
        });
        st.load(rs, lane, lay + L_X1 + MB2 + 8 * sp * 4096);
        st.all(L, lane);
        static_for<6>([&](auto nc) {
          constexpr int n = nc.value + 2;
          if (n == 4) FLAG(flo, FL_HB + sp * 32 + c);
          if (n == 6) PEEK(flo, FL_HB + sp * 32);
          if (n == 7) GATE(flo, FL_HB + sp * 32);
          HBARS(n == 2 || n == 7);
        });
        static_for<8>([&](auto nc) {
          constexpr int n = nc.value;
          if (n == 2) FLAG(flo, FL_HA + sp * 32 + c);
          if (n == 5) { FLAG(flo, FL_HA2 + sp * 32 + c); PEEK(flo, FL_HA + sp * 32); }
          if (n == 6) GATE(flo, FL_HA + sp * 32);
          if (n == 7) pv = __hip_atomic_load(fl + h2l * 32, RLX_AGENT);
          HBARS(n == 0 || n == 3 || n == 6);
        });
        static_for<6>([&](auto nc) {
          constexpr int n = nc.value + 2;
          if (n == 2) { if (!__all(pv == tag)) poll(fl + h2l * 32, true, tmo, tag); }
          if (n == 3) {
#pragma unroll
            for (int q = 0; q < 4; ++q) ev[q] = __builtin_amdgcn_raw_buffer_load_b32(rs, lane * 128, last && blockIdx.x == 0 ? flo + (FL_HB + 64 * q) * 128 : NONE, AUX_SC1);
          }
          if (n == 4 && !last) FLAG(FLAG_LAYER, FL_XB + sp * 32 + c);
          if (n == 5) PEEK(FLAG_LAYER, FL_XB + sp * 32);
          if (n == 6) GATE(FLAG_LAYER, FL_XB + sp * 32);
          if (n == 7) st.load(rs, lane, last ? NONE : LAY_OFF + LAY_BYTES + L_X + MB2 + (8 * sp + 4) * 4096);
          if (n == 7 && last) HOLD(3);
          HBARS(n == 2 || n == 6 || n == 7);
        });
        if (!last) { st.all(L, lane); }
        if (last && blockIdx.x == 0) {
          if (!__all(ev[0] == tag && ev[1] == tag && ev[2] == tag && ev[3] == tag))
            for (int q = 0; q < 4; ++q) POLL(flo, FL_HB + 64 * q, 64);
          if (lane == 0) __hip_atomic_store(fg + FL_EP * 32, tag, RLX_AGENT);
        }
      } else {
        const unsigned wq = WQKV_OFF + c * 49152, wb = WGU_OFF + c * 114688;
        DRAIN();
        HBAR();
        static_for<7>([&](auto nc) {
          constexpr int n = nc.value + 1;
          if (n < 7) { blank(6 * n - 7 + w, last ? NONE : LAY_OFF + LAY_BYTES); blank(6 * n - 4 + w, last || 6 * n - 4 + w > 33 ? NONE : LAY_OFF + LAY_BYTES); }
          if (n <= 3) ldw(IC<9>{}, wb, 71 + 9 * n);
          if (n == 4) { ldw(IC<5>{}, wb, 107); wait_vm<14>(); }
          if (n == 5) { if (!last && w == 1) cs_gu(80, 1, true); wait_vm<11>(); }
          if (n == 6) { if (!last && w == 2) cs_gu(88, 1, false); wait_vm<4>(); }
          if (n == 7 && !last && w >= 2) cs_gu(80 + 8 * w, 1, true);
          HBARS(n == 3 || n == 7);
        });
        static_for<6>([&](auto nc) {
          constexpr int n = nc.value + 2;
          if (n == 3) wait_vm<0>();
          if (n == 5 || n == 6) { ld_dn(2 * n - 10); ld_dn(2 * n - 9); } else if (n > 6) ld_dn(n - 3);
          HBARS(n == 2 || n == 7);
        });
        static_for<8>([&](auto nc) {
          constexpr int n = nc.value;
          if (n <= 2) ld_dn(n + 5);
          wait_vm<(n <= 2 ? 25 : 35 - 5 * n)>();
          HBARS(n == 0 || n == 3 || n == 6);
        });
        static_for<6>([&](auto nc) {
          constexpr int n = nc.value + 2;
          if (!last) {
            if (n >= 3 && n <= 5) ldw(IC<14>{}, wq, 14 * (n - 3));
            if (n == 6) { ldw(IC<6>{}, wq, 42); ldw(IC<8>{}, wb, 48); }
            if (n == 7) ldw(IC<14>{}, wb, 56);
          }
          HBARS(n == 2 || n == 6 || n == 7);
        });
        if (!last) { ldw(IC<10>{}, wb, 70); wait_vm<14>(); }
      }
      SETPRIO(3);
      if (!last) {
        HOLD(2);
        HBAR();
      }
      static_for<NPQ>([&](auto i) { undef(xq[i.value]); });
    }
  }
}

}

extern "C" void kernel_launch(void* const* d_in, const int* in_sizes, int n_in, void* d_out, int out_size, void* d_ws, size_t ws_size,
                              hipStream_t stream) {
  (void)in_sizes; (void)n_in; (void)out_size; (void)ws_size;
  (void)hipFuncSetAttribute((const void*)k_main, hipFuncAttributeMaxDynamicSharedMemorySize, LDS_BYTES);
  k_main<<<256, 512, LDS_BYTES, stream>>>((const u16*)d_in[0], (const u16*)d_in[1], (const u16*)d_in[2], (const u16*)d_in[4], (const u16*)d_in[5],
                                          (const u16*)d_in[6], (unsigned char*)d_ws, (u16*)d_out);
}
```

```cpp
#include <hip/hip_runtime.h>
#include <stdint.h>
#include <utility>

#define LAS __attribute__((address_space(3)))
#define GAS __attribute__((address_space(1)))
#define DI __device__ __forceinline__
#define RLX_AGENT __ATOMIC_RELAXED, __HIP_MEMORY_SCOPE_AGENT
typedef unsigned short u16;
typedef unsigned v4u __attribute__((ext_vector_type(4)));
typedef unsigned v2u __attribute__((ext_vector_type(2)));
typedef float f32x2 __attribute__((ext_vector_type(2)));
typedef float f32x4 __attribute__((ext_vector_type(4)));
typedef float f32x16 __attribute__((ext_vector_type(16)));
typedef __bf16 bf16x2 __attribute__((ext_vector_type(2)));
typedef __bf16 bf16x8 __attribute__((ext_vector_type(8)));
typedef GAS unsigned gu32;
typedef GAS unsigned long long gu64;

namespace {

constexpr int D_ = 512, F_ = 1792;
constexpr unsigned MB2 = 2097152u, STB = 262144u;
constexpr unsigned FLAG_LAYER = 1048576u, FLAG_G = 2 * FLAG_LAYER, FLAG_BYTES = FLAG_G + 65536;
constexpr unsigned WQKV_OFF = FLAG_BYTES;
constexpr unsigned WO_OFF = WQKV_OFF + 32 * 49152;
constexpr unsigned WGU_OFF = WO_OFF + 16 * 32768;
constexpr unsigned WDN_OFF = WGU_OFF + 32 * 114688;
constexpr unsigned LAY_OFF = WDN_OFF + 16 * 114688;
constexpr unsigned L_K = 0, L_V = MB2, L_O = 2 * MB2, L_X1 = 3 * MB2, L_X = 4 * MB2 + STB, L_H = 5 * MB2 + 2 * STB;
constexpr unsigned LAY_BYTES = L_H + 128 * 57344;
constexpr unsigned WS_BYTES = LAY_OFF + 2 * LAY_BYTES;
constexpr unsigned NONE = 0x40000000u;
constexpr int FL_KV = 0, FL_O = 2048, FL_X1B = 4096, FL_X1A = 6144, FL_HB = 6400, FL_HA = 6656, FL_XB = 6912, FL_XA = 7168, FL_HA2 = 7424;
constexpr int FL_WR = 0, FL_EP = 256, FL_TMO = 257;
constexpr int VBLK = 528;
constexpr int LDS_KL = 114688, LDS_VL = LDS_KL + 16384, LDS_QL = LDS_VL + 32 * VBLK, LDS_CS = LDS_QL + 8192, LDS_ST = LDS_CS + 1536;
constexpr int LDS_MK = LDS_ST + 1536, LDS_TAG = LDS_MK + 2048;
constexpr int LDS_WO = 81920, LDS_TB = 49152;
constexpr int LDS_BYTES = 163840;
#define TSTR 1048576u
constexpr int CODE_LINES = 15;
constexpr unsigned SPIN_CAP = 1u << 16;
constexpr int AUX_SC1 = 16;
constexpr float LOG2E = 1.4426950408889634f;

DI f32x16 mfma(v4u a, v4u b, f32x16 c) {
  return __builtin_amdgcn_mfma_f32_32x32x16_bf16(__builtin_bit_cast(bf16x8, a), __builtin_bit_cast(bf16x8, b), c, 0, 0, 0);
}
DI f32x4 mfma16(v4u a, v4u b, f32x4 c) {
  return __builtin_amdgcn_mfma_f32_16x16x32_bf16(__builtin_bit_cast(bf16x8, a), __builtin_bit_cast(bf16x8, b), c, 0, 0, 0);
}
DI unsigned pk2(float lo, float hi) {
  f32x2 p = {lo, hi};
  return __builtin_bit_cast(unsigned, __builtin_convertvector(p, bf16x2));
}
DI float bfr(float x) { return __uint_as_float(pk2(x, 0.f) << 16); }
DI float bflo(unsigned u) { return __uint_as_float(u << 16); }
DI float bfhi(unsigned u) { return __uint_as_float(u & 0xffff0000u); }
DI float amul(float a, float b) { float d; asm("v_mul_f32 %0, %1, %2" : "=v"(d) : "v"(a), "v"(b)); return d; }
DI float afma(float a, float b, float c) { float d; asm("v_fma_f32 %0, %1, %2, %3" : "=v"(d) : "v"(a), "v"(b), "v"(c)); return d; }
DI float aadd(float a, float b) { float d; asm("v_add_f32 %0, %1, %2" : "=v"(d) : "v"(a), "v"(b)); return d; }
DI float asub(float a, float b) { float d; asm("v_sub_f32 %0, %1, %2" : "=v"(d) : "v"(a), "v"(b)); return d; }
DI float xhalf_sum(float v) {
  float t;
  asm("v_mov_b32 %1, %0\n\ts_nop 1\n\tv_permlane32_swap_b32 %0, %1\n\tv_add_f32 %0, %0, %1" : "+v"(v), "=&v"(t));
  return v;
}
DI float xrow_sum(float v) {
  float t;
  asm("v_mov_b32 %1, %0\n\ts_nop 1\n\tv_permlane16_swap_b32 %0, %1\n\tv_add_f32 %0, %0, %1" : "+v"(v), "=&v"(t));
  return v;
}
DI float xhalf_max(float v) {
  auto r = __builtin_amdgcn_permlane32_swap(__float_as_uint(v), __float_as_uint(v), false, false);
  return fmaxf(__uint_as_float(r[0]), __uint_as_float(r[1]));
}
DI void swap16(unsigned& a, unsigned& b) { asm("s_nop 1\n\tv_permlane16_swap_b32 %0, %1" : "+v"(a), "+v"(b)); }
DI void swap32(unsigned& a, unsigned& b) { asm("s_nop 1\n\tv_permlane32_swap_b32 %0, %1" : "+v"(a), "+v"(b)); }
DI f32x16 splat16(float v) {
  f32x16 z;
#pragma unroll
  for (int i = 0; i < 16; ++i) z[i] = v;
  return z;
}
DI f32x4 zero4() { return (f32x4){0.f, 0.f, 0.f, 0.f}; }
DI int vopq(int x) { asm volatile("" : "+v"(x)); return x; }
DI int lane_id() { int l; asm volatile("v_mbcnt_lo_u32_b32 %0, -1, 0\n\tv_mbcnt_hi_u32_b32 %0, -1, %0" : "=v"(l)); return l; }
template <int LN> DI unsigned wrl(unsigned v, unsigned s) { asm volatile("v_writelane_b32 %0, %1, %2" : "+v"(v) : "s"(s), "i"(LN)); return v; }
template <class T> DI void undef(T& x) { asm volatile("" : "=v"(x)); }
DI int sopq(int x) { asm volatile("" : "+s"(x)); return x; }
#define SWZ(l) ((l) ^ (((l) >> 3) & 7))
#define COORDS() \
  const int lane = lane_id(), lo = vopq(lane * 16), hh = lane >> 5, t16 = lane & 15, g4 = lane >> 4, lsw = SWZ(lane) * 16; \
  const int w = sopq(w0) & 3, sp = sopq(sp0), c = sopq(c0), n32 = c >> 1, rh = c & 1; \
  const unsigned lay = (unsigned)sopq((int)lay0); \
  const unsigned flo = sopq(layer) * FLAG_LAYER; \
  gu32* const fl = (gu32*)(ws + flo); \
  (void)lo; (void)hh; (void)t16; (void)g4; (void)lsw; (void)n32; (void)rh; (void)lay; (void)sp; (void)c; (void)w; (void)fl;
#define HBARS(c) asm volatile("s_waitcnt lgkmcnt(0)\n\ts_barrier" ::: "memory")
#define CST() do {} while (0)
#define DBGC(k) asm volatile("" ::: "memory")
#define HBAR() asm volatile("s_waitcnt lgkmcnt(0)\n\ts_barrier" ::: "memory")
#define BAR() asm volatile("s_waitcnt lgkmcnt(0)\n\ts_barrier" ::: "memory")
#define BARX() asm volatile("s_barrier" ::: "memory")
#define DRAIN() asm volatile("s_waitcnt vmcnt(0)" ::: "memory")
#define PIN() __builtin_amdgcn_sched_barrier(0)
#define SETPRIO(n) __builtin_amdgcn_s_setprio(n)
template <int N> DI void wait_vm() { static_assert(N >= 0 && N < 64, ""); asm volatile("s_waitcnt vmcnt(%0)" ::"i"(N) : "memory"); }

typedef __amdgpu_buffer_rsrc_t rsrc_t;
DI v4u ld16(rsrc_t rs, unsigned voff, unsigned soff) { return __builtin_amdgcn_raw_buffer_load_b128(rs, voff, soff, AUX_SC1); }
DI f32x2 ld8f(rsrc_t rs, unsigned voff, unsigned soff) { return __builtin_bit_cast(f32x2, __builtin_amdgcn_raw_buffer_load_b64(rs, voff, soff, AUX_SC1)); }
DI void st16(rsrc_t rs, v4u v, unsigned voff, unsigned soff) {
  __builtin_amdgcn_raw_buffer_store_b128(v, rs, voff, soff, AUX_SC1);
  asm volatile("s_nop 1" ::"v"(v));
}
DI void st8(rsrc_t rs, v2u v, unsigned voff, unsigned soff) { __builtin_amdgcn_raw_buffer_store_b64(v, rs, voff, soff, AUX_SC1); }
DI void st4(rsrc_t rs, unsigned v, unsigned voff, unsigned soff) { __builtin_amdgcn_raw_buffer_store_b32(v, rs, voff, soff, AUX_SC1); }
DI v4u lds16(LAS unsigned char* L, int off) { return *(LAS v4u*)(L + off); }
DI f32x4 lds4f(LAS unsigned char* L, int off) { return *(LAS f32x4*)(L + off); }

DI void poll(gu32* word, bool active, gu32* tmo, unsigned tag) {
  for (unsigned spins = 0;;) {
    unsigned v = tag;
    if (active) v = __hip_atomic_load(word, RLX_AGENT);
    if (__all(v == tag)) break;
    ++spins;
    if (spins > SPIN_CAP) { __hip_atomic_store(tmo, tag, RLX_AGENT); break; }
    if ((spins & 255u) == 0u && (unsigned)__builtin_amdgcn_readfirstlane(__hip_atomic_load(tmo, RLX_AGENT)) == tag) break;
  }
  __builtin_amdgcn_fence(__ATOMIC_ACQUIRE, "wavefront");
}

template <int I> DI void dma1(rsrc_t rs, LAS unsigned char* dst, unsigned voff, unsigned soff) {
  __builtin_amdgcn_raw_ptr_buffer_load_lds(rs, (LAS void*)dst, 16, voff, soff, I * 1024, AUX_SC1);
}
template <int V> using IC = std::integral_constant<int, V>;
template <class FN, int... I> DI void static_for_(FN fn, std::integer_sequence<int, I...>) { (fn(IC<I>{}), ...); }
template <int N, class FN> DI void static_for(FN fn) { static_for_(fn, std::make_integer_sequence<int, N>{}); }
template <int N> DI void dma(rsrc_t rs, LAS unsigned char* dst, unsigned lo, unsigned src) {
  static_for<N>([&](auto i) { dma1<i.value & 3>(rs, dst + (i.value >> 2) * 4096, lo, src + (i.value >> 2) * 4096); });
}

struct Stats {
  f32x4 p[16];
  float m0, m1, q0, q1;
  DI void load(rsrc_t rs, int lane, unsigned base, int i0 = 0, int i1 = 16) {
#pragma unroll
    for (int i = i0; i < i1; ++i) p[i] = __builtin_bit_cast(f32x4, ld16(rs, (lane >> 4) * 4096 + (lane & 15) * 16 + i * 256, base));
    PIN();
  }
  DI void part1() {
    m0 = m1 = q0 = q1 = 0.f;
#pragma unroll
    for (int i = 0; i < 16; ++i) { m0 += p[i][0]; q0 += p[i][1]; m1 += p[i][2]; q1 += p[i][3]; }
    asm volatile("" : "+v"(m0), "+v"(m1), "+v"(q0), "+v"(q1));
  }
  DI void part2() {
    const float a0 = m0 * (1.0f / 16.0f), a1 = m1 * (1.0f / 16.0f);
    q0 *= 32.0f; q1 *= 32.0f;
#pragma unroll
    for (int i = 0; i < 16; ++i) { const float d0 = p[i][0] - a0, d1 = p[i][2] - a1; q0 += d0 * d0; q1 += d1 * d1; }
    asm volatile("" : "+v"(q0), "+v"(q1));
  }
  DI void all(LAS unsigned char* L, int lane, int dst = LDS_ST) { part1(); part2(); part3(L, lane, dst); }
  DI void part3(LAS unsigned char* L, int lane, int dst = LDS_ST) {
    const float den0 = bfr(__builtin_amdgcn_sqrtf(bfr(bfr(q0 * (1.0f / (32.0f * D_))) + 1e-5f)));
    const float den1 = bfr(__builtin_amdgcn_sqrtf(bfr(bfr(q1 * (1.0f / (32.0f * D_))) + 1e-5f)));
    *(LAS f32x2*)(L + dst + lane * 8) = (f32x2){__builtin_amdgcn_rcpf(den0), __builtin_amdgcn_rcpf(den1)};
    *(LAS f32x2*)(L + dst + 512 + lane * 8) = (f32x2){den0 * den0, den1 * den1};
    *(LAS v2u*)(L + dst + 1024 + lane * 8) = (v2u){pk2(m0 * (-1.0f / D_), m0 * (-1.0f / D_)), pk2(m1 * (-1.0f / D_), m1 * (-1.0f / D_))};
  }
};
DI float lds1f(LAS unsigned char* L, int off) { return *(LAS float*)(L + off); }
DI v4u mfrag(LAS unsigned char* L, int off) { const unsigned n = *(LAS unsigned*)(L + off); return (v4u){n, n, 0u, 0u}; }

constexpr int LA = 6;
template <int NF, class RD, class STEP>
DI void pipe(RD rd, STEP step) {
  v4u wf[8];
  static_for<LA>([&](auto i) { wf[i.value] = rd(i); });
  step(IC<-1>{}, wf[0], IC<1>{});
  PIN();
  static_for<NF>([&](auto fc) {
    constexpr int f = fc.value;
    if constexpr ((f & 1) == 0 && f + LA <= NF) __builtin_amdgcn_s_waitcnt(0xc07f | ((LA - 2) << 8));
    step(fc, wf[f & 7], IC<0>{});
    if constexpr (f + LA < NF) wf[(f + LA) & 7] = rd(IC<f + LA>{});
    step(fc, wf[f & 7], IC<1>{});
    PIN();
  });
}
DI v4u csfrag(LAS unsigned char* L, int entry) {
  const v2u t = *(LAS v2u*)(L + LDS_CS + entry * 8);
  return (v4u){t.x, t.y, 0u, 0u};
}
DI void cs_put(LAS unsigned char* L, int entry, float cs) {
  const float hi = bfr(cs), mid = bfr(cs - hi), lw = (cs - hi) - mid;
  *(LAS v2u*)(L + LDS_CS + entry * 8) = (v2u){pk2(hi, mid), pk2(lw, 0.f)};
}
DI unsigned mulpk(float a, float s0, float b, float s1) {
  unsigned d; float t;
  asm("v_mul_f32 %0, %2, %3\n\tv_mul_f32 %1, %4, %5\n\tv_cvt_pk_bf16_f32 %0, %0, %1" : "=&v"(d), "=&v"(t) : "v"(a), "v"(s0), "v"(b), "v"(s1));
  return d;
}
DI unsigned nos(unsigned v) { return v < 0xfffffffeu ? v : 0xfffffffeu; }
DI v4u nos4(v4u v) { return (v4u){nos(v[0]), nos(v[1]), nos(v[2]), nos(v[3])}; }
DI unsigned umax3(unsigned a, unsigned b, unsigned c) { const unsigned t = a > b ? a : b; return t > c ? t : c; }
template <int N> DI bool missing(const v4u* f, int step = 1) {
  unsigned m0 = 0, m1 = 0;
#pragma unroll
  for (int i = 0; i < N; ++i) { m0 = umax3(m0, f[i * step][0], f[i * step][1]); m1 = umax3(m1, f[i * step][2], f[i * step][3]); }
  return __builtin_amdgcn_ballot_w64((m0 > m1 ? m0 : m1) == 0xffffffffu) != 0;
}
constexpr unsigned DATA_CAP = 1u << 14;
DI v4u pack8(const float* y) { return (v4u){pk2(y[0], y[1]), pk2(y[2], y[3]), pk2(y[4], y[5]), pk2(y[6], y[7])}; }

struct Fin {
  float y[8], s;
  v4u o;
  v2u sq;
  template <int K, bool ST = true> DI void chunk(const f32x4& a0, const f32x4& a1, const v4u& r, rsrc_t ro, unsigned vo, unsigned so, rsrc_t rs, unsigned vs, unsigned ss) {
    if constexpr (K == 0) { y[0] = aadd(a0[0], bflo(r[0])); y[1] = aadd(a0[1], bfhi(r[0])); y[2] = aadd(a0[2], bflo(r[1])); y[3] = aadd(a0[3], bfhi(r[1])); }
    if constexpr (K == 1) { y[4] = aadd(a1[0], bflo(r[2])); y[5] = aadd(a1[1], bfhi(r[2])); y[6] = aadd(a1[2], bflo(r[3])); y[7] = aadd(a1[3], bfhi(r[3])); }
    if constexpr (K == 2) { o = nos4(pack8(y)); if constexpr (ST) st16(ro, o, vo, so); }
    if constexpr (K == 3) {
#pragma unroll
      for (int m = 0; m < 4; ++m) { y[2 * m] = bflo(o[m]); y[2 * m + 1] = bfhi(o[m]); }
    }
    if constexpr (K == 4) s = xrow_sum(aadd(aadd(aadd(y[0], y[1]), aadd(y[2], y[3])), aadd(aadd(y[4], y[5]), aadd(y[6], y[7]))));
    if constexpr (K == 5) {
      s = xhalf_sum(s);
      const float mu = s * (1.0f / 32.0f);
#pragma unroll
      for (int i = 0; i < 8; ++i) y[i] = asub(y[i], mu);
    }
    if constexpr (K == 6) {
      y[0] = amul(y[0], y[0]); y[1] = amul(y[1], y[1]);
#pragma unroll
      for (int i = 2; i < 8; ++i) y[i & 1] = afma(y[i], y[i], y[i & 1]);
    }
    if constexpr (K == 7) {
      sq = (v2u){nos(__float_as_uint(s)), nos(__float_as_uint(xhalf_sum(xrow_sum(aadd(y[0], y[1])))))};
      if constexpr (ST) st8(rs, sq, vs, ss);
    }
  }
};

constexpr unsigned long long low_mask(int r) {
  unsigned long long m = 0;
  for (int l = 0; l < 64; ++l)
    if ((r & 3) + 8 * (r >> 2) + 4 * (l >> 5) <= (l & 31)) m |= 1ull << l;
  return m;
}
DI float sel(unsigned long long mask, float set, float unset) {
  float d;
  asm("v_cndmask_b32_e64 %0, %1, %2, %3" : "=v"(d) : "v"(unset), "v"(set), "s"(mask));
  return d;
}
DI float max16(float m, const f32x16& s) {
#pragma unroll
  for (int r = 0; r < 16; r += 2) m = __builtin_fmaxf(__builtin_fmaxf(m, s[r]), s[r + 1]);
  return m;
}
DI v4u exp_pack(const f32x16& s, int h) {
  float pr[8];
#pragma unroll
  for (int r = 0; r < 8; ++r) pr[r] = __builtin_amdgcn_exp2f(s[8 * h + r]);
  return pack8(pr);
}
template <bool EXACT>
DI f32x16 attn_pass(LAS unsigned char* L, int lane, int tl, int nv) {
  const int lo = lane * 16;
  const int kb = LDS_KL + tl * 1024 + lo, kf0 = LDS_KL + (tl + 8) * 1024 + (lane >> 5) * 512;
  const v4u q = lds16(L, LDS_QL + tl * 1024 + lo);
  v4u kf[9];
  kf[8] = lds16(L, kb + 8192); kf[0] = lds16(L, nv <= 0 ? kb : kf0);
#pragma unroll
  for (int i = 7; i >= 1; --i) kf[i] = lds16(L, (nv <= i ? kb : kf0 - i * 1024) + i * 1024);
  f32x16 s[9], cm = splat16(0.f);
  if constexpr (EXACT) {
    s[8] = mfma(kf[8], q, splat16(0.f));
    s[0] = mfma(kf[0], q, splat16(0.f));
    s[7] = mfma(kf[7], q, splat16(0.f));
    s[6] = mfma(kf[6], q, splat16(0.f));
    asm volatile("s_nop 7\n\ts_nop 7" : "+v"(s[8]), "+v"(s[0]));
    PIN();
    static_for<16>([&](auto r) { s[8][r.value] = sel(low_mask(r.value), s[8][r.value], s[0][r.value]); });
    float m = max16(s[8][0], s[8]);
    PIN();
    static_for<7>([&](auto ic) {
      constexpr int i = 7 - ic.value;
      if constexpr (i - 2 >= 1) s[i - 2] = mfma(kf[i - 2], q, splat16(0.f));
      m = max16(m, s[i]);
      PIN();
    });
    cm = splat16(-xhalf_max(m));
  }
  const int vb = LDS_VL + 2 * tl * VBLK + ((lane & 16) ? 512 : (lane >> 5) * 256 + (lane & 15) * 16);
  s[8] = mfma(kf[8], q, cm);
  s[0] = mfma(kf[0], q, cm);
  s[7] = mfma(kf[7], q, cm);
  const v4u mk0 = lds16(L, LDS_MK + lo), mk1 = lds16(L, LDS_MK + 1024 + lo);
  v4u va = lds16(L, vb + 16 * VBLK), vc = lds16(L, vb + 17 * VBLK), v0a = lds16(L, vb), v0c = lds16(L, vb + VBLK);
  asm volatile("s_nop 7\n\ts_nop 7" : "+v"(s[8]), "+v"(s[0]));
  PIN();
  f32x16 o;
  {
    static_for<16>([&](auto r) { s[8][r.value] = sel(low_mask(r.value), s[8][r.value], s[0][r.value]); });
    const v4u p0 = exp_pack(s[8], 0), l0 = p0 & mk0;
    o = mfma(va, l0, splat16(0.f));
    o = mfma(v0a, p0 ^ l0, o);
    const v4u p1 = exp_pack(s[8], 1), l1 = p1 & mk1;
    o = mfma(vc, l1, o);
    o = mfma(v0c, p1 ^ l1, o);
    va = lds16(L, vb + 14 * VBLK); vc = lds16(L, vb + 15 * VBLK);
    PIN();
  }
  static_for<7>([&](auto ic) {
    constexpr int i = 7 - ic.value;
    if constexpr (i > 1) s[i - 1] = mfma(kf[i - 1], q, cm);
    PIN();
    o = mfma(va, exp_pack(s[i], 0), o);
    if constexpr (i > 1) va = lds16(L, vb + (2 * i - 2) * VBLK);
    o = mfma(vc, exp_pack(s[i], 1), o);
    if constexpr (i > 1) vc = lds16(L, vb + (2 * i - 1) * VBLK);
    PIN();
  });
  return o;
}
DI v4u attn_unit(LAS unsigned char* L, int lane, int tl, int nv) {
  f32x16 o = attn_pass<false>(L, lane, tl, nv);
  float big = __builtin_fabsf(o[0]);
#pragma unroll
  for (int r = 1; r < 8; ++r) big += __builtin_fabsf(o[r]);
  if (__builtin_expect(!__all(o[8] > 0x1p-30f && o[8] < 0x1p60f && big < 0x1p120f), 0)) {
    asm volatile("" ::: "memory");
    o = attn_pass<true>(L, lane, tl, nv);
  }
  const float rl = __builtin_amdgcn_rcpf(o[8]);
  float y[8];
#pragma unroll
  for (int r = 0; r < 8; ++r) y[r] = o[r] * rl;
  return pack8(y);
}

struct Retile {
  v4u in[8];
  DI void load(const u16* m, int elems, int el, int ld, int rs4) {
    const rsrc_t r = __builtin_amdgcn_make_buffer_rsrc((void*)m, 0, elems * 2, 0x00020000);
    const int e4 = el + rs4 * ld;
#pragma unroll
    for (int j = 0; j < 8; ++j) in[j] = __builtin_amdgcn_raw_buffer_load_b128(r, (j < 4 ? el : e4) * 2, (j & 3) * ld * 2, 0);
    PIN();
  }
  unsigned d[8];
  DI void out(rsrc_t rs) {
#pragma unroll
    for (int i = 0; i < 8; ++i) st16(rs, in[i], d[i], 0);
  }
  DI void lines(LAS unsigned char* t, int lane, int sw, unsigned dst) {
#pragma unroll
    for (int e = 0; e < 8; ++e) {
      v4u o;
#pragma unroll
      for (int m = 0; m < 4; ++m) o[m] = __builtin_amdgcn_perm(in[2 * m + 1][e >> 1], in[2 * m][e >> 1], (e & 1) ? 0x07060302u : 0x05040100u);
      *(LAS v4u*)(t + lane * 128 + (e ^ sw ^ (lane & 7)) * 16) = o;
    }
    asm volatile("s_waitcnt lgkmcnt(0)" ::: "memory");
#pragma unroll
    for (int i = 0; i < 8; ++i) {
      in[i] = *(LAS v4u*)(t + i * 1024 + (lane ^ (lane >> 3)) * 16);
      d[i] = (unsigned)__builtin_amdgcn_ds_bpermute((8 * i + (lane >> 3)) * 4, (int)dst) + (lane & 7) * 16;
    }
    PIN();
  }
  DI void put(LAS unsigned char* L, int pb, int dd, int lp, int dl) {
#pragma unroll
    for (int e = 0; e < 8; ++e) {
      v4u o;
#pragma unroll
      for (int m = 0; m < 4; ++m) o[m] = __builtin_amdgcn_perm(in[2 * m + 1][e >> 1], in[2 * m][e >> 1], (e & 1) ? 0x07060302u : 0x05040100u);
      const int s = lp + (e & 3) + (e >> 2) * dl;
      *(LAS v4u*)(L + pb + (e >> 2) * dd + (s ^ ((s >> 3) & 7)) * 16) = o;
    }
  }
};

struct GS { int cs, j, kap, off; };
constexpr GS gu_step(int u) {
  if (u < 64) { const int k = u >> 2, j = u & 3; return GS{0, j, k, j < 3 ? (7 * (k >> 1) + 3 * (k & 1) + j) * 1024 : (7 * (k >> 1) + 6) * 1024 + (k & 1) * 512}; }
  const int v = u - 64, j = v / 17, i = v % 17;
  return GS{i == 16, j, 16 + i, 57344 + j * 16384 + i * (j < 3 ? 1024 : 512)};
}
constexpr int GA = 132, DB = 264, DA = 376, NST = 488;
#define NPQ_ 12
#define NPG_ 16
#define APRIO 1
constexpr int NPG = NPG_, NPQ = NPQ_;
constexpr int ld_step(int id) {
  if (id < 32) return id < NPG ? -1 : 2 * (id - NPG);
  if (id < 64) { const int n = id - 32; return n < 12 ? 103 + (5 * n) / 2 : GA + 10 + 2 * (n - 12); }
  if (id < 176) {
    const int n = id - 64;
    return n < 12 ? GA + 103 + n : n < 26 ? DB - 15 + ((n - 12) * 7) / 4 : n < 56 ? DB + 27 + ((n - 26) * 5) / 3 : n < 84 ? DB + 79 + ((n - 56) * 4) / 3 : DA + 31 + 2 * (n - 84);
  }
  if (id < 208) { const int n = id - 176; return n < NPQ ? DA + 87 + (24 * n) / NPQ : -1; }
  if (id < 224) return -1;
  return id == 224 ? DB + 80 : DA + 72;
}
constexpr int NEP = 56;
constexpr int ep_step(int k) { return 83 + 17 * (k / 16) + k % 16; }
struct Tab { short ld[NST][2]; short ep[NST]; short nvm[NST]; };
constexpr int FINB = DA + 3;
#define SL0 20
#define Q3T 75
#define XAE 0
#define XA0 18
constexpr int xa_step(int j) { const int a = XA0 + j, b = XAE == 0 ? (j < 12 ? 3 * j + 2 : 34 + j) : j < XAE ? 0 : j - XAE < 12 ? 3 * (j - XAE) + 2 : 34 + j - XAE; return a > b ? a : b; }
constexpr Tab make_tab() {
  Tab t{};
  for (int s = 0; s < NST; ++s) { t.ld[s][0] = t.ld[s][1] = t.ep[s] = -1; t.nvm[s] = 0; }
  for (int id = 0; id < 226; ++id) {
    const int s = ld_step(id);
    if (s < 0) continue;
    t.ld[s][t.ld[s][0] >= 0] = (short)id; ++t.nvm[s];
  }
  for (int job = 0; job < 2; ++job)
    for (int k = 0; k < NEP; ++k) {
      const int s = job * GA + ep_step(k);
      t.ep[s] = (short)(job * 64 + k);
      if (k == NEP - 1) t.nvm[s] += 2;
      if (k == (job ? 31 : NEP - 1)) t.nvm[s] += 2;
    }
  ++t.nvm[FINB + 2]; ++t.nvm[FINB + 7];
  return t;
}
constexpr Tab TAB = make_tab();
constexpr int vm_between(int a, int b) {
  int n = 0;
  for (int s = a + 1; s <= b; ++s) n += TAB.nvm[s];
  return n;
}

__global__ void __launch_bounds__(512, 2) k_main(const u16* __restrict__ x, const u16* __restrict__ wqkv, const u16* __restrict__ wo, const u16* __restrict__ wg,
                                                 const u16* __restrict__ wu, const u16* __restrict__ wd, unsigned char* __restrict__ ws,
                                                 u16* __restrict__ out) {
  extern __shared__ __attribute__((aligned(16))) unsigned char smem[];
  LAS unsigned char* L = (LAS unsigned char*)smem;
  const int tid = threadIdx.x, w0 = __builtin_amdgcn_readfirstlane(tid >> 6);
  const int b = blockIdx.x;
  unsigned touch0 = 0;
  {
    unsigned long long pc0;
    asm volatile("s_getpc_b64 %0" : "=s"(pc0));
    const rsrc_t rc = __builtin_amdgcn_make_buffer_rsrc((void*)(pc0 & ~127ull), 0, 32 * CODE_LINES * 128, 0x00020000);
    if (w0 == 3) touch0 = __builtin_amdgcn_raw_buffer_load_b32(rc, (tid & 63) < CODE_LINES ? (tid & 63) * 128 : NONE, (b >> 3) * CODE_LINES * 128, 0);
    PIN();
  }
  const int sp0 = (b & 6) + ((b >> 3) & 1), c0 = (b >> 4) + 16 * (b & 1);
  const rsrc_t rs = __builtin_amdgcn_make_buffer_rsrc((void*)ws, 0, (int)WS_BYTES, 0x00020000);
  const bool compute = w0 < 4;
  gu32* const fg = (gu32*)(ws + FLAG_G);
  gu32* const tmo = fg + FL_TMO * 32;
  unsigned tag = 0;
#define HOLD(k) do {} while (0)
#define FLAG(blk, line) st4(rs, tag, lane ? NONE : 0u, (blk) + (line) * 128)
#define POLL(blk, line, n) poll((gu32*)(ws + (blk)) + ((line) + lane) * 32, lane < (n), tmo, tag)
#define GETTAG() tag = (unsigned)__builtin_amdgcn_readfirstlane(*(LAS unsigned*)(L + LDS_TAG))
  auto blank = [&](int it, unsigned ly) {
    const int lane = lane_id(), sp = sopq(sp0), c = sopq(c0), w = it & 3;
    const unsigned o = (unsigned)vopq(-1);
    if (it < 16) st16(rs, (v4u){o, o, o, o}, lane * 16, ly + (it & 8 ? L_V : L_K) + ((8 * sp + (it & 7)) * 32 + c) * 1024);
    else if (it < 24) st16(rs, (v4u){o, o, o, o}, ((lane >> 4) & 1) * 16384 + (lane >> 5) * 256 + (lane & 15) * 16, ly + L_O + (8 * sp + (it & 7)) * 32768 + c * 512);
    else if (it < 28) st16(rs, (v4u){o, o, o, o}, (lane >> 4) * 512 + (lane & 15) * 16, ly + L_X1 + ((8 * sp + 4 + 2 * (c & 1) + (w >> 1)) * 32 + (c & ~1)) * 1024 + (w & 1) * 256);
    else if (it > 28 && it < 33) st16(rs, (v4u){o, o, o, o}, lane * 16, ly + L_X + ((16 * sp + 4 * (c & 1) + w) * 16 + (c >> 1)) * 1024);
    else st8(rs, (v2u){o, o}, (lane >> 5) * 4096 + (lane & 31) * 8, ly + (it == 28 ? L_X1 + 4 * 4096 : L_X) + MB2 + (8 * sp + 2 * (c & 1)) * 4096 + (c >> 1) * 256);
  };
  v4u xq[32];

  if (compute) {
    const int layer = 0; const unsigned lay0 = LAY_OFF;
    COORDS();
    SETPRIO(2);
    const int r = lane >> 3, j = lane & 7;
    const u16* src = x + (size_t)(256 * sp + 8 * c + r) * D_ + 128 * w + 8 * j;
    v4u a[2] = {*(const v4u*)src, *(const v4u*)(src + 64)};
    if (w < 3) touch0 = __builtin_amdgcn_raw_buffer_load_b32(rs, lane ? NONE : 0u, w == 0 ? lay + L_X + (16 * sp + n32) * 16384 : w == 1 ? lay + L_X + MB2 + 8 * sp * 4096 : (FL_XB + sp * 32 + c) * 128, AUX_SC1);
    PIN();
#pragma unroll
    for (int i = 0; i < 2; ++i) {
      a[i] = nos4(a[i]);
      st16(rs, a[i], (j >> 2) * 1024 + ((j & 3) * 16 + r) * 16, lay + L_X + (16 * sp + n32) * 16384 + (2 * w + i) * 2048 + rh * 128);
      auto quad = [](float v) {
        v += __int_as_float(__builtin_amdgcn_update_dpp(0, __float_as_int(v), 0xb1, 0xf, 0xf, true));
        return v + __int_as_float(__builtin_amdgcn_update_dpp(0, __float_as_int(v), 0x4e, 0xf, 0xf, true));
      };
      float sm = 0.f, q = 0.f;
#pragma unroll
      for (int m = 0; m < 4; ++m) sm += bflo(a[i][m]) + bfhi(a[i][m]);
      sm = quad(sm);
      const float mu = sm * (1.0f / 32.0f);
#pragma unroll
      for (int m = 0; m < 4; ++m) { const float e0 = bflo(a[i][m]) - mu, e1 = bfhi(a[i][m]) - mu; q += e0 * e0 + e1 * e1; }
      q = quad(q);
      st8(rs, (v2u){nos(__float_as_uint(sm)), nos(__float_as_uint(q))}, (j & 3) ? NONE : (j >> 2) * 256 + r * 8,
          lay + L_X + MB2 + (8 * sp + (c >> 2)) * 4096 + (4 * w + 2 * i) * 256 + (c & 3) * 64);
    }
    DRAIN();
    asm volatile("" ::"v"(touch0));
    BAR();
    BAR();
    GETTAG();
    const int TB = 8 * sp + 4 + w;
    static_for<NPQ>([&](auto nc) { constexpr int n = nc.value; xq[n] = ld16(rs, lo + ((n >> 1) & 3) * 1024, lay + L_X + TB * 32768 + (n & 1) * 16384 + (n >> 3) * 4096); });
    BARX();
  } else {
    const int layer = 0; const unsigned lay0 = LAY_OFF;
    COORDS();
    SETPRIO(3);
    Retile rt;
    LAS unsigned char* tb = L + LDS_TB + w * 8192;
    Stats st;
    if (w == 0) {
      const int fi = lane >> 3, i8 = lane & 7, F = sopq(6 * sp) + (fi < 6 ? fi : 0);
      const int ks = F < 36 ? F / 3 : F < 44 ? (F - 12) >> 1 : F - 32, m = F < 36 ? (F - 3 * ks + 1) % 3 : F < 44 ? 1 + (F & 1) : 0;
      rt.load(wqkv, 3 * D_ * D_, __mul24(32 * ks + 8 * (i8 >> 1), 3 * D_) + m * D_ + 16 * c + 8 * (i8 & 1), 3 * D_, 4);
      const unsigned ep = __hip_atomic_load(fg + FL_EP * 32, RLX_AGENT);
      v4u mk[2];
#pragma unroll
      for (int k = 0; k < 8; ++k) {
        const int kq = ((2 * k) & 3) + 8 * ((2 * k) >> 2) + 4 * hh, ql = lane & 31;
        mk[k >> 2][k & 3] = (kq <= ql ? 0xffffu : 0u) | (kq + 1 <= ql ? 0xffff0000u : 0u);
      }
      *(LAS v4u*)(L + LDS_MK + lo) = mk[0];
      *(LAS v4u*)(L + LDS_MK + 1024 + lo) = mk[1];
      if (sp == 0) {
#pragma unroll
        for (int i = 0; i < 9; ++i) *(LAS v4u*)(L + LDS_VL + (i < 8 ? i * 1024 + lo : 8192 + t16 * 16)) = (v4u){0u, 0u, 0u, 0u};
      }
      const unsigned one = (sp == 0 && lane < 16) ? 0u : 0x3f803f80u;
      if (lane < 32) *(LAS v4u*)(L + LDS_VL + lane * VBLK + 512) = (v4u){one, one, one, one};
      PIN();
      rt.lines(tb, lane, i8, fi < 6 ? WQKV_OFF + c * 49152 + F * 1024 + i8 * 128 : NONE);
      rt.out(rs);
      tag = __builtin_amdgcn_readfirstlane(ep) + 1u;
      if (lane < 2) *(LAS v4u*)(L + LDS_TAG + lo) = (v4u){lane ? 0u : tag, 0u, 0u, 0u};
      DRAIN();
      HBAR();
      FLAG(0, FL_XB + sp * 32 + c);
      poll((gu32*)ws + (FL_XB + (lane < 32 ? sp * 32 + lane : (lane & 7) * 32 + c)) * 32, lane < 40, tmo, tag);
      st.load(rs, lane, lay + L_X + MB2 + (8 * sp + 4) * 4096);
    } else {
      const int id = 2 * sp + rh + 16 * (w - 1);
      const int t = 64 * id + lane, mo = t / 14, ch = t - __mul24(14, mo), mat = mo >> 6, o = mo & 63, c7 = ch >= 7 ? ch - 7 : ch, j = c7 >> 1, kap = o >> 1;
      const int slot = (lane >> 2) & 3, m4 = lane & 3, q = 16 * (id < 42 ? id - 28 : (id - 42) & 3) + 4 * g4 + slot;
      const int cq = q < 128 ? q >> 2 : q < 192 ? (q - 128) >> 1 : q - 192, r = q < 128 ? q & 3 : q < 192 ? 4 + (q & 1) : 6;
      __builtin_amdgcn_s_sleep(SL0);
      rt.load(id < 14 ? wg : id < 28 ? wu : id < 42 ? wd : wo, id < 42 ? D_ * F_ : D_ * D_,
              id < 28 ? 8 * o * F_ + 112 * n32 + 8 * ch : (id < 42 ? __mul24(56, cq) + (r < 6 ? 16 * (r >> 1) + 4 * (r & 1) : 48) : 16 * (q >> 1) + 4 * (q & 1)) * D_ + 32 * n32 + 8 * m4,
              id < 28 ? F_ : D_, (id < 28 || (id < 42 && r == 6)) ? 4 : 8);
      HBAR();
      if (id < 28) {
        const int cell = kap < 16 ? 7 * (kap >> 1) + (j < 3 ? 3 * (kap & 1) + j : 6) : 56 + 16 * j + (j < 3 ? kap - 16 : (kap - 16) >> 1);
        const int rho = j < 3 ? 2 * mat + (c7 & 1) + 4 * (o & 1) : 4 * (kap & 1) + 2 * (o & 1) + mat;
        rt.lines(tb, lane, j < 3 ? rho : 0, WGU_OFF + (2 * n32 + (ch >= 7)) * 114688 + cell * 1024 + rho * 128);
      } else {
        rt.put(tb, g4 * 2048, 1024, 4 * m4 + 16 * slot, 0);
        asm volatile("s_waitcnt lgkmcnt(0)" ::: "memory");
#pragma unroll
        for (int i = 0; i < 8; ++i) {
          rt.in[i] = lds16(tb, i * 1024 + lo);
          rt.d[i] = (id < 42 ? WDN_OFF + n32 * 114688 + (id - 28) * 8192 : id < 46 ? WO_OFF + n32 * 32768 + (id - 42) * 8192 : NONE) + i * 1024 + lo;
        }
      }
#pragma unroll 1
      for (int it = w - 1; it < 16; it += 3) blank(it, lay);
    }
    HBAR();
    GETTAG();
    if (w) {
      const int l = w - 1;
      dma<4>(rs, L + l * 4096, lo, WQKV_OFF + c * 49152 + l * 4096);
      dma<7>(rs, L + (12 + 7 * l - (l >> 1)) * 1024, lo, WQKV_OFF + c * 49152 + (12 + 7 * l - (l >> 1)) * 1024);
      wait_vm<7>();
    }
    HOLD(0);
    HBAR();
    if (w) { dma<6>(rs, L + (27 + 5 * w) * 1024, lo, WQKV_OFF + c * 49152 + (27 + 5 * w) * 1024);
      rt.out(rs); wait_vm<14>();
    }
    else {
      st.all(L, lane);
      PIN();
      FLAG(0, FL_XA + sp * 32 + c);
      const int i = lane & 31;
      const int line = i < 8 ? FL_KV + (8 * sp + i) * 32 + c : i < 16 ? FL_O + (8 * sp + i - 8) * 32 + c
                     : i < 20 ? FL_X1B + (8 * sp + 4 + 2 * rh + ((i >> 1) & 1)) * 32 + 2 * n32 + (i & 1) : FL_X1A + (i - 20 < 6 ? i - 20 : 5) * 256 + sp * 32 + c;
      touch0 = __builtin_amdgcn_raw_buffer_load_b32(rs, hh * FLAG_LAYER + line * 128, 0, AUX_SC1);
    }
  }

  for (int layer = 0; layer < 2; ++layer) {
    const bool last = layer == 1;
    const unsigned lay0 = LAY_OFF + layer * LAY_BYTES;
    auto ldw = [&](auto nc, unsigned src, int cell) {
      constexpr int N = nc.value, K = (N + 2) / 3;
      const int l = sopq(w0) - 5, c1 = cell + (l * K + K > N ? N - K : l * K);
      dma<K>(rs, L + c1 * 1024, lane_id() * 16, src + c1 * 1024);
    };
    auto ld_gu = [&](int s) { ldw(IC<14>{}, WGU_OFF + sopq(c0) * 114688, 14 * s); };
    auto ld_dn = [&](int s) { ldw(IC<14>{}, WDN_OFF + (sopq(c0) >> 1) * 114688, 14 * s); };
    if (compute) {
      COORDS();
      const int TB = 8 * sp + 4 + w, TA = TB - 4;
      v4u xa[32];
      auto ldx = [&](auto nc, int T) {
        constexpr int n = nc.value;
        return ld16(rs, lo + ((n >> 1) & 3) * 1024, lay + L_X + (2 * T + (n & 1)) * 16384 + (n >> 3) * 4096);
      };
      f32x4 aq[2][2], ak[2][2], av[2][2], r0, r1;
      v4u mf0, mf1; float rd0, rd1;
      unsigned o0, o1, o2, o3;
      const int kpos = (t16 + 16 * hh + 32 * (g4 & 1)) * 16;
      auto epi = [&](int k, int pa) {
        const int tl = pa ? w : 4 + w, st = (pa ? LDS_KL : LDS_ST) + w * 128;
        const unsigned kvb = lay + ((8 * sp + tl) * 32 + c) * 1024;
        if (k == 0) { r0 = lds4f(L, st + g4 * 16); r1 = lds4f(L, st + 64 + g4 * 16); }
        if (k == 2) { o0 = mulpk(ak[pa][0][0], rd0, ak[pa][0][1], rd0); o1 = mulpk(ak[pa][0][2], rd0, ak[pa][0][3], rd0); }
        if (k == 3) { o2 = mulpk(ak[pa][1][0], rd1, ak[pa][1][1], rd1); o3 = mulpk(ak[pa][1][2], rd1, ak[pa][1][3], rd1); }
        if (k == 4) {
          swap32(o0, o2); swap32(o1, o3);
          st16(rs, nos4((v4u){o0, o1, o2, o3}), kpos, kvb + L_K);
          *(LAS v4u*)(L + LDS_KL + (8 + tl) * 1024 + kpos) = (v4u){o0, o1, o2, o3};
        }
        if (k == 5) { o0 = mulpk(av[pa][0][0], r0[0], av[pa][0][1], r0[1]); o1 = mulpk(av[pa][0][2], r0[2], av[pa][0][3], r0[3]); }
        if (k == 6) { o2 = mulpk(av[pa][1][0], r1[0], av[pa][1][1], r1[1]); o3 = mulpk(av[pa][1][2], r1[2], av[pa][1][3], r1[3]); }
        if (k == 7) {
          swap32(o0, o2); swap32(o1, o3);
          st16(rs, nos4((v4u){o0, o1, o2, o3}), lo, kvb + L_V);
          *(LAS v4u*)(L + LDS_VL + (16 + 2 * tl + hh) * VBLK + (lane & 31) * 16) = (v4u){o0, o1, o2, o3};
        }
        if (k == 8) {
          asm volatile("s_nop 7" : "+v"(aq[pa][0]), "+v"(aq[pa][1]));
          const float s0 = rd0 * (0.25f * LOG2E), s1 = rd1 * (0.25f * LOG2E);
          o0 = mulpk(aq[pa][0][0], s0, aq[pa][0][1], s0); o1 = mulpk(aq[pa][0][2], s0, aq[pa][0][3], s0);
          o2 = mulpk(aq[pa][1][0], s1, aq[pa][1][1], s1); o3 = mulpk(aq[pa][1][2], s1, aq[pa][1][3], s1);
          swap32(o0, o2); swap32(o1, o3);
          *(LAS v4u*)(L + LDS_QL + tl * 1024 + kpos) = (v4u){o0, o1, o2, o3};
        }
      };
      pipe<102>(
          [&](auto fc) {
            constexpr int t = fc.value, u = t % 51;
            if constexpr (t == 28) { BARX(); CST(); }
            if constexpr (t == 12 || t == 44 || t == Q3T) BARX();
            if constexpr (u == 44 || u == 45 || u == 50) return csfrag(L, (u == 50 ? 0 : u - 43) * 16 + t16);
            else return lds16(L, (u < 44 ? u : u - 2) * 1024 + lsw);
          },
          [&](auto fc, const v4u& wfr, auto pc) {
            constexpr int t = fc.value, part = pc.value, u = t % 51;
            constexpr int ks = (u < 36 ? u / 3 : u < 44 ? (u - 12) >> 1 : u - 34) & 15, m = u < 36 ? (u % 3 + 1) % 3 : u < 46 ? 1 + (u & 1) : 0;
            constexpr bool pa = t >= 51, cs = u == 44 || u == 45 || u == 50;
            if constexpr (t < 0) return;
            else if constexpr (part == 0) {
              if constexpr (pa && u < 44 && u % 12 == 0) {
                constexpr int n = u / 12 * 8;
                if (__builtin_expect(missing<8>(xa + n), 0)) {
                  unsigned spins = 0;
                  do { static_for<8>([&](auto i) { xa[n + i.value] = ldx(IC<n + i.value>{}, TA); }); DBGC(0); } while (missing<8>(xa + n) && ++spins < DATA_CAP);
                }
                PIN();
              }
              const v4u& x0 = cs ? mf0 : pa ? xa[2 * ks] : xq[2 * ks];
              const v4u& x1 = cs ? mf1 : pa ? xa[2 * ks + 1] : xq[2 * ks + 1];
              if constexpr (m == 0) { aq[pa][0] = mfma16(wfr, x0, u < 3 ? zero4() : aq[pa][0]); aq[pa][1] = mfma16(wfr, x1, u < 3 ? zero4() : aq[pa][1]); }
              if constexpr (m == 1) { ak[pa][0] = mfma16(wfr, x0, u < 3 ? zero4() : ak[pa][0]); ak[pa][1] = mfma16(wfr, x1, u < 3 ? zero4() : ak[pa][1]); }
              if constexpr (m == 2) { av[pa][0] = mfma16(x0, wfr, u < 3 ? zero4() : av[pa][0]); av[pa][1] = mfma16(x1, wfr, u < 3 ? zero4() : av[pa][1]); }
            } else {
              if constexpr (2 * t < 32 - NPQ) { xq[NPQ + 2 * t] = ldx(IC<NPQ + 2 * t>{}, TB); xq[NPQ + 2 * t + 1] = ldx(IC<NPQ + 2 * t + 1>{}, TB); }
              if constexpr (u == 40) {
                const int st = (pa ? LDS_KL : LDS_ST) + w * 128;
                rd0 = lds1f(L, st + t16 * 4); rd1 = lds1f(L, st + 64 + t16 * 4);
                const int a = g4 ? LDS_TAG + 8 : st + 1024 + t16 * 4;
                mf0 = mfrag(L, a); mf1 = mfrag(L, g4 ? a : a + 64);
              }
              if constexpr (t >= 45 && t < 54) epi(t - 45, 0);
              if constexpr (t >= 96) epi(t - 96, 1);
              static_for<16>([&](auto j) { if constexpr (xa_step(j.value) == t) { xa[2 * j.value] = ldx(IC<2 * j.value>{}, TA); xa[2 * j.value + 1] = ldx(IC<2 * j.value + 1>{}, TA); } });
              if constexpr (t == 50) CST();
            }
          });
      epi(6, 1); epi(7, 1); epi(8, 1);
      BAR();
      CST();
    } else {
      COORDS();
      const bool loader = w != 0;
      auto cs_qkv = [&]() {
        const unsigned one = (unsigned)vopq(0x3f803f80);
        const v4u ones = (v4u){one, one, one, one};
        f32x4 a = zero4();
        v4u f[16];
        const int fa = (w - 1) * 1024 + lsw, fb = (w < 3 ? 35 + w : 44) * 1024 + lsw, fs = w < 3 ? 2048 : 1024;
#pragma unroll
        for (int ks = 0; ks < 16; ++ks) asm volatile("ds_read_b128 %0, %1" : "=v"(f[ks]) : "v"(ks < 12 ? fa + ks * 3072 : fb + (ks - 12) * fs) : "memory");
        asm volatile("s_waitcnt lgkmcnt(0)" : "+v"(f[0]), "+v"(f[1]), "+v"(f[2]), "+v"(f[3]), "+v"(f[4]), "+v"(f[5]), "+v"(f[6]), "+v"(f[7]));
        asm volatile("" : "+v"(f[8]), "+v"(f[9]), "+v"(f[10]), "+v"(f[11]), "+v"(f[12]), "+v"(f[13]), "+v"(f[14]), "+v"(f[15]));
#pragma unroll
        for (int ks = 0; ks < 16; ++ks) a = mfma16(f[ks], ones, a);
        if (t16 == 0) {
#pragma unroll
          for (int r = 0; r < 4; ++r) cs_put(L, (w % 3) * 16 + 4 * g4 + r, a[r]);
        }
      };
      const unsigned wob = WO_OFF + n32 * 32768 - LDS_WO;
      if (last && !loader) {
        v4u o[4];
#pragma unroll
        for (int i = 0; i < 4; ++i) o[i] = lds16(L, LDS_QL + i * 1024 + lo);
        const v2u sq = *(LAS v2u*)(L + LDS_QL + 4096 + lane * 8);
#pragma unroll
        for (int i = 0; i < 4; ++i) st16(rs, o[i], lo, lay + L_X + ((16 * sp + 4 * rh + i) * 16 + n32) * 1024);
        st8(rs, sq, hh * 4096 + (g4 & 1) * 128 + t16 * 8, lay + L_X + MB2 + (8 * sp + 2 * rh) * 4096 + n32 * 256);
        PIN();
      }
      HBARS(false);
      if (layer == 0) {
        if (loader) wait_vm<8>();
        HBAR();
        if (loader) { cs_qkv(); DRAIN(); }
        HBAR();
        if (!loader) FLAG(FLAG_G, FL_WR + n32 * 16 + 2 * sp + rh);
      } else {
        if (loader) { ldw(IC<10>{}, wob, 80); wait_vm<13>(); }
        HBAR();
        if (loader) ldw(IC<10>{}, wob, 90);
        HBAR();
        if (loader) ldw(IC<12>{}, wob, 100);
      }
      {
        v4u kv[4];
        auto ldkv = [&]() {
          const unsigned so = lay + ((8 * sp - 5 + w) * 32 + c) * 1024, s2 = w == 1 ? so + 3 * 32768 : NONE;
          kv[0] = ld16(rs, lo, so + L_K); kv[1] = ld16(rs, lo, so + L_V); kv[2] = ld16(rs, lo, s2 + L_K); kv[3] = ld16(rs, lo, s2 + L_V);
          PIN();
          DBGC(0);
        };
        if (!loader) {
          Stats st;
          gu32* const bl = lane < 16 ? fg + (FL_WR + n32 * 16 + lane) * 32 : (gu32*)ws + (FL_XA + (sp ? sp - 1 : 0) * 32 + c) * 32;
          unsigned spins = 0, pv = tag;
          do { st.load(rs, lane, lay + L_X + MB2 + 8 * sp * 4096); DBGC(0); } while (missing<16>((const v4u*)st.p) && ++spins < DATA_CAP);
          if (!last) pv = __hip_atomic_load(bl, RLX_AGENT);
          st.all(L, lane, LDS_KL);
          if (!__all(pv == tag)) poll(bl, lane <= 16, tmo, tag);
          asm volatile("" ::"v"(touch0));
        }
        HBARS(false);
        if (loader && sp > 0) ldkv();
        if (loader && !last) { ldw(IC<10>{}, wob, 80); ldw(IC<10>{}, wob, 90); ldw(IC<12>{}, wob, 100); }
        if (loader && sp > 0) {
          for (unsigned spins = 0; missing<4>(kv) && spins < DATA_CAP; ++spins) ldkv();
          *(LAS v4u*)(L + LDS_KL + (3 + w) * 1024 + lo) = kv[0];
          *(LAS v4u*)(L + LDS_VL + (6 + 2 * w + hh) * VBLK + (lane & 31) * 16) = kv[1];
          if (w == 1) {
            *(LAS v4u*)(L + LDS_KL + 7 * 1024 + lo) = kv[2];
            *(LAS v4u*)(L + LDS_VL + (14 + hh) * VBLK + (lane & 31) * 16) = kv[3];
          }
        }
      }
      if (!last) HOLD(1);
      HBAR();
      CST(); CST();
      if (loader) {
        ld_gu(0); ld_gu(1); ld_gu(2); ldw(IC<6>{}, WGU_OFF + c * 114688, 42);
        if (!last) { ldw(IC<8>{}, WGU_OFF + c * 114688, 48); ld_gu(4); ldw(IC<10>{}, WGU_OFF + c * 114688, 70); wait_vm<29>(); }
        else wait_vm<17>();
        if (lane == 0) asm volatile("ds_add_u32 %0, %1" ::"v"(LDS_TAG + 12), "v"(1) : "memory");
      }
      if (sp > 0) {
        LAS unsigned* hw = (LAS unsigned*)(L + LDS_TAG + 4);
        CST();
        if (!loader) {
          v4u kv[8];
          unsigned spins = 0;
          do {
#pragma unroll
            for (int t = 0; t < 4; ++t) {
              const unsigned so = lay + ((8 * sp - 8 + t) * 32 + c) * 1024;
              kv[t] = ld16(rs, lo, so + L_K); kv[4 + t] = ld16(rs, lo, so + L_V);
            }
            PIN();
            DBGC(1);
          } while (missing<8>(kv) && ++spins < DATA_CAP);
#pragma unroll
          for (int t = 0; t < 4; ++t) {
            *(LAS v4u*)(L + LDS_KL + t * 1024 + lo) = kv[t];
            *(LAS v4u*)(L + LDS_VL + (2 * t + hh) * VBLK + (lane & 31) * 16) = kv[4 + t];
          }
          asm volatile("s_waitcnt lgkmcnt(0)" ::: "memory");
          __hip_atomic_store(hw, (unsigned)layer + 1u, __ATOMIC_RELAXED, __HIP_MEMORY_SCOPE_WORKGROUP);
        } else {
          unsigned v;
          do {
            __builtin_amdgcn_s_sleep(1);
            asm volatile("ds_read_b32 %0, %1\n\ts_waitcnt lgkmcnt(0)" : "=v"(v) : "v"(LDS_TAG + 4) : "memory");
          } while ((unsigned)__builtin_amdgcn_readfirstlane(v) != (unsigned)layer + 1u);
        }
      } else CST();
      CST();
      if (layer == 0 && !loader) touch0 = __builtin_amdgcn_raw_buffer_load_b32(rs, LAY_OFF + L_O + lane * TSTR + c * 4096 + sp * 128, 0, AUX_SC1);
      SETPRIO(APRIO);
    }
    {
      COORDS();
      const int tl = compute ? 4 + w : w, T = 8 * sp + tl;
      const v4u o = attn_unit(L, lane, tl, sp == 0 ? 8 - tl : 0);
      st16(rs, nos4(o), (g4 & 1) * 16384 + hh * 256 + t16 * 16, lay + L_O + T * 32768 + c * 512);
      if (!last) { DRAIN(); FLAG(flo, FL_O + T * 32 + c); }
      CST();
    }
    if (compute) {
      SETPRIO(0);
      v4u fr[176];
      {
        COORDS();
        const int TB = 8 * sp + 4 + 2 * rh + (w >> 1), SB = 2 * TB + (w & 1);
        const v4u rB = ld16(rs, lo, lay + L_X + (SB * 16 + n32) * 1024), rA = ld16(rs, lo, lay + L_X + (SB * 16 + n32) * 1024 - 8 * 16384);
        const unsigned xv = (g4 >> 1) * 1024 + (g4 & 1) * 512 + t16 * 16, sv = g4 ? NONE : t16 * 8;
        const unsigned xo = lay + L_X1 + (TB * 32 + 2 * n32) * 1024 + (w & 1) * 256, so = lay + L_X1 + MB2 + TB * 4096 + n32 * 256 + (w & 1) * 128;
        const int lswo = vopq(lsw + LDS_WO);
        while ((unsigned)__builtin_amdgcn_readfirstlane(__hip_atomic_load((LAS unsigned*)(L + LDS_TAG + 12), __ATOMIC_RELAXED, __HIP_MEMORY_SCOPE_WORKGROUP)) != 3u * layer + 3u)
          __builtin_amdgcn_s_sleep(1);
        asm volatile("" ::: "memory");
        v4u of[32];
        unsigned pk;
        f32x4 a0, a1;
        Fin fin;
        auto ldo = [&](auto i) { return ld16(rs, lo + (i.value & 3) * 1024, lay + L_O + (SB - 8 * (i.value >> 4)) * 16384 + ((i.value >> 2) & 3) * 4096); };
        pipe<64>(
            [&](auto fc) { return lds16(L, (fc.value & 31) * 1024 + lswo); },
            [&](auto fc, const v4u& wfr, auto pc) {
              constexpr int t = fc.value, part = pc.value, u = t & 31;
              if constexpr (part == 0) {
                if constexpr (u % 16 == 0) {
                  if (__builtin_expect(missing<8>(of + t / 2), 0)) {
                    unsigned spins = 0;
                    do { static_for<16 - u / 2>([&](auto i) { of[t / 2 + i.value] = ldo(IC<t / 2 + i.value>{}); }); DBGC(1 + (t >> 4)); } while (missing<8>(of + t / 2) && ++spins < DATA_CAP);
                  }
                  if constexpr (u == 0) CST();
                  PIN();
                }
                if constexpr (u & 1) a1 = mfma16(wfr, of[t >> 1], u == 1 ? zero4() : a1);
                else a0 = mfma16(wfr, of[t >> 1], u == 0 ? zero4() : a0);
              } else {
                if constexpr (t == -1) {
                  if (!last) POLL(flo, FL_O + TB * 32, 32);
                  static_for<16>([&](auto i) { of[i.value] = ldo(i); });
                }
                if constexpr (t == 55) { pk = tag; if (!last) pk = __hip_atomic_load(fl + (FL_X1B + (8 * sp + 4 + w) * 32 + (lane & 31)) * 32, RLX_AGENT); }
                if constexpr (t == 63) { if (!__all(pk == tag)) POLL(flo, FL_X1B + (8 * sp + 4 + w) * 32, 32); }
                if constexpr (t == 63) static_for<NPG>([&](auto i) { fr[i.value] = ld16(rs, lo + (i.value & 3) * 1024, lay + L_X1 + (8 * sp + 4 + w) * 32768 + (i.value >> 2) * 4096); });
                if constexpr (t == 31 || t == 63) {
                  asm volatile("s_nop 7" : "+v"(a0), "+v"(a1));
                  static_for<8>([&](auto k) { fin.template chunk<k.value>(a0, a1, t == 31 ? rB : rA, rs, xv, xo - (t == 31 ? 0 : 4 * 32768), rs, sv, so - (t == 31 ? 0 : 4 * 4096)); });
                  CST();
                }
                if constexpr (t == 31) {
                  if (!last) {
                    pk = __hip_atomic_load(fl + (FL_O + (TB - 4) * 32 + (lane & 31)) * 32, RLX_AGENT);
                    DRAIN();
                    FLAG(flo, FL_X1B + TB * 32 + 2 * n32 + (w & 1));
                    if (!__all(pk == tag)) POLL(flo, FL_O + (TB - 4) * 32, 32);
                  }
                  static_for<16>([&](auto i) { of[16 + i.value] = ldo(IC<16 + i.value>{}); });
                }
              }
            });
        CST();
      }
      SETPRIO(2);
      BAR();
      {
        COORDS();
        const int TB = 8 * sp + 4 + w;
        const int TD = 8 * sp + 4 + 2 * rh + (w >> 1), SD = 2 * TD + (w & 1);
        const unsigned x1b = lay + L_X1 + TB * 32768, hb = lay + L_H + SD * 57344, xn = last ? NONE : lay + LAY_BYTES + L_X;
        const unsigned hsv = (g4 & 1) * 57344 + hh * 256 + t16 * 16, hso = lay + L_H + TB * 114688;
        const rsrc_t ro = last ? __builtin_amdgcn_make_buffer_rsrc((void*)out, 0, (int)MB2, 0x00020000) : rs;
        const unsigned ob = last ? SD * 16384 + n32 * 64 : xn + (SD * 16 + n32) * 1024;
        const unsigned sb = xn + MB2 + TD * 4096 + n32 * 256 + (w & 1) * 128;
        unsigned ov, sv;
        const int lsw2 = vopq(lsw + 65536), lh = (hh * 16 + t16) * 16, lh2 = vopq(lh + 65536);
        f32x16 acc[2][4];
        f32x4 d0[2], d1[2];
        v4u mf, hv[4], res[2];
        float k1[2], den2[2], t0, t1, p0_, p1_;
        unsigned cm0, cm1;
        Fin fin;
        auto epi = [&](auto ec) {
          constexpr int job = ec.value >> 6, k = ec.value & 63, j = k >> 4, i = (k >> 2) & 3, uo = j < 3 ? 8 : 4;
          const f32x16& a = acc[job][j];
          if constexpr ((k & 3) == 0)
            asm volatile("v_mul_f32 %0, %2, %4\n\tv_mul_f32 %1, %3, %4\n\tv_exp_f32 %0, %0" : "=&v"(t0), "=&v"(t1) : "v"(a[2 * i]), "v"(a[2 * i + 1]), "v"(k1[job]));
          else if constexpr ((k & 3) == 1)
            asm volatile("v_exp_f32 %0, %0\n\tv_mul_f32 %1, %3, %5\n\tv_mul_f32 %2, %4, %6"
                : "+v"(t1), "=&v"(p0_), "=&v"(p1_) : "v"(a[2 * i]), "v"(a[2 * i + 1]), "v"(a[uo + 2 * i]), "v"(a[uo + 2 * i + 1]));
          else if constexpr ((k & 3) == 2)
            asm volatile("v_fma_f32 %0, %0, %2, %2\n\tv_fma_f32 %1, %1, %2, %2\n\tv_rcp_f32 %0, %0" : "+v"(t0), "+v"(t1) : "v"(den2[job]));
          else {
            unsigned d;
            asm volatile("v_rcp_f32 %2, %2\n\tv_mul_f32 %1, %1, %3\n\tv_mul_f32 %2, %2, %4\n\tv_cvt_pk_bf16_f32 %0, %1, %2" : "=v"(d), "+v"(t0), "+v"(t1) : "v"(p0_), "v"(p1_));
            hv[j][i] = d;
            if constexpr (k == (job ? 31 : NEP - 1)) { st16(rs, hv[0], hsv, hso - job * 4 * 114688 + c * 1024); st16(rs, hv[1], hsv + 512, hso - job * 4 * 114688 + c * 1024); }
            if constexpr (k == NEP - 1) {
              st16(rs, hv[2], hsv, hso - job * 4 * 114688 + 32768 + c * 512);
              st8(rs, (v2u){hv[3][0], hv[3][1]}, hh ? hsv - 248 : hsv, hso - job * 4 * 114688 + 49152 + c * 256);
            }
          }
        };
        auto vload = [&](auto ic) {
          constexpr int id = ic.value;
          if constexpr (id < 64) fr[id] = ld16(rs, lo + (id & 3) * 1024, x1b - (id >> 5) * 4 * 32768 + ((id & 31) >> 2) * 4096);
          else if constexpr (id < 176) {
            constexpr int n = (id - 64) % 56;
            fr[id] = ld16(rs, lo + (n & 3) * 1024, hb - (id >= 120 ? 8 * 57344 : 0) + (n >> 2) * 4096);
          } else if constexpr (id < 208) {
            constexpr int n = id - 176;
            xq[n] = ld16(rs, lo + ((n >> 1) & 3) * 1024, xn + TB * 32768 + (n & 1) * 16384 + (n >> 3) * 4096);
          } else res[id - 224] = ld16(rs, vopq(lo & 0x2f0) + ((lo & 256) << 1) + (lo & 512), lay + L_X1 + ((TD - 4 * (id - 224)) * 32 + 2 * n32) * 1024 + (w & 1) * 256);
        };
        pipe<NST>(
            [&](auto fc) {
              constexpr int t = fc.value;
              if constexpr (t == 32) wait_vm<vm_between(-1, t - LA - 1)>();
              if constexpr (t == GA + 48) wait_vm<vm_between(ep_step(NEP - 1), t - LA - 1)>();
              if constexpr (t == DB + 14) wait_vm<vm_between(GA + ep_step(31), t - LA - 1)>();
              if constexpr (t == DB + 56) wait_vm<vm_between(GA + ep_step(NEP - 1), t - LA - 1)>();
              if constexpr (t == DA + 42) wait_vm<vm_between(FINB + 7, t - LA - 1)>();
              if constexpr (t < DB) {
                constexpr GS s = gu_step(t % GA);
                if constexpr (s.cs) return csfrag(L, 48 + 32 * s.j + (s.j < 3 ? lane & 31 : t16));
                else {
                  if (s.off % 14336 == 0 && s.off >= (t < GA ? 14336 : 28672)) BARX();
                  return lds16(L, s.off < 65536 ? s.off + (s.j < 3 ? lsw : lh) : s.off - 65536 + (s.j < 3 ? lsw2 : lh2));
                }
              } else {
                constexpr int v = (t - DB) % 112;
                if (v % 14 == 0 && v >= (t < DA ? 0 : 28)) BARX();
                return lds16(L, v < 64 ? v * 1024 + lsw : (v - 64) * 1024 + lsw2);
              }
            },
            [&](auto fc, const v4u& wfr, auto pc) {
              constexpr int t = fc.value, part = pc.value;
              if constexpr (t < 0) return;
              else if constexpr (part == 0) {
                if constexpr (t < DB) {
                  constexpr int job = t / GA, u = t % GA;
                  constexpr GS s = gu_step(u);
                  if constexpr (t == 0 || t == 16 || t == 32 || t == 64) {
                    constexpr int n0 = t < 64 ? t / 4 : 16, nn = t == 0 || t == 16 ? 4 : t == 32 ? 8 : 16;
                    if (__builtin_expect(t ? __builtin_amdgcn_ballot_w64((cm0 > cm1 ? cm0 : cm1) == 0xffffffffu) != 0 : missing<nn>(fr + n0), 0)) {
                      unsigned spins = 0;
                      do { static_for<(t < 64 ? 16 : 32) - n0>([&](auto i) { vload(IC<n0 + i.value>{}); }); DBGC(t < 64 ? 5 : 6); } while (missing<nn>(fr + n0) && ++spins < DATA_CAP);
                    }
                    PIN();
                  }
                  acc[job][s.j] = mfma(wfr, s.cs ? mf : fr[32 * job + (s.kap & 31)], u < 4 ? splat16(0.f) : acc[job][s.j]);
                } else {
                  constexpr int job = t >= DA, v = (t - DB) % 112;
                  if constexpr (v & 1) d1[job] = mfma16(wfr, fr[64 + 56 * job + (v >> 1)], v == 1 ? zero4() : d1[job]);
                  else d0[job] = mfma16(wfr, fr[64 + 56 * job + (v >> 1)], v == 0 ? zero4() : d0[job]);
                }
              } else {
                if constexpr (t < DB) {
                  constexpr int job = t / GA, u = t % GA;
                  if constexpr (u == 74) {
                    const int a = LDS_ST + w * 128 + (lane & 31) * 4;
                    k1[job] = lds1f(L, a); den2[job] = lds1f(L, a + 512); mf = mfrag(L, hh ? LDS_TAG + 8 : a + 1024);
                  }
                  if constexpr (u == 79) k1[job] *= -LOG2E;
                  if constexpr ((t >= 12 && t < 16) || (t >= 24 && t < 32) || (t >= 48 && t < 64)) {
                    constexpr int n = t < 16 ? t - 8 : t < 32 ? t - 16 : t - 32;
                    constexpr bool first = t == 12 || t == 24 || t == 48;
                    cm0 = umax3(first ? 0u : cm0, fr[n][0], fr[n][1]); cm1 = umax3(first ? 0u : cm1, fr[n][2], fr[n][3]);
                    asm volatile("" : "+v"(cm0), "+v"(cm1));
                  }
                }
                if constexpr (TAB.ld[t][0] >= 0) vload(IC<TAB.ld[t][0]>{});
                if constexpr (TAB.ld[t][1] >= 0) vload(IC<TAB.ld[t][1]>{});
                if constexpr (TAB.ep[t] >= 0) epi(IC<TAB.ep[t]>{});
                if constexpr (t == FINB - 1) { const int l = lane_id(); ov = l * 16 + ((((l & 15) * 1008) - (l >> 4) * 240) & sopq(-layer)); sv = l >= 16 ? NONE : l * 8; }
                if constexpr (t >= FINB && t < FINB + 8) fin.template chunk<t - FINB>(d0[0], d1[0], res[0], ro, ov, ob, rs, sv, sb);
              }
            });
        CST();
        asm volatile("s_nop 7" : "+v"(d0[1]), "+v"(d1[1]));
        static_for<8>([&](auto k) { fin.template chunk<k.value, false>(d0[1], d1[1], res[1], ro, ov, ob - 8 * 16384, rs, sv, sb - 4 * 4096); });
        if (last) st16(ro, fin.o, ov, ob - 8 * 16384);
        else {
          *(LAS v4u*)(L + LDS_QL + w * 1024 + lo) = fin.o;
          if (lane < 16) *(LAS v2u*)(L + LDS_QL + 4096 + w * 128 + lane * 8) = fin.sq;
          BAR();
        }
      }
    } else {
      COORDS();
      const bool poller = w == 0, loader = w != 0;
      unsigned pv = 0;
#define PEEK(blk, line) pv = __hip_atomic_load((gu32*)(ws + (blk)) + ((line) + (lane & 31)) * 32, RLX_AGENT)
#define GATE(blk, line) do { if (!__all(pv == tag)) POLL(blk, line, 32); } while (0)
      f32x4 ca = zero4();
      auto cs_gu = [&](int cell, int n2, bool put) {
        const unsigned one = (unsigned)vopq((w == 3 || t16 == (g4 & 1)) ? 0x3f803f80 : 0);
        const int a = cell * 1024 + (w < 3 ? lsw : lo), d2 = n2 == 1 ? 2048 : 7168;
        v4u f[8];
#pragma unroll
        for (int i = 0; i < 8; ++i) asm volatile("ds_read_b128 %0, %1" : "=v"(f[i]) : "v"(a + (i >> 1) * d2 + (i & 1) * n2 * 1024) : "memory");
        asm volatile("s_waitcnt lgkmcnt(0)" : "+v"(f[0]), "+v"(f[1]), "+v"(f[2]), "+v"(f[3]), "+v"(f[4]), "+v"(f[5]), "+v"(f[6]), "+v"(f[7]));
#pragma unroll
        for (int i = 0; i < 8; ++i) ca = mfma16(f[i], (v4u){one, one, one, one}, ca);
        if (put && t16 < (w < 3 ? 2 : 1)) {
#pragma unroll
          for (int r = 0; r < 4; ++r) cs_put(L, 48 + 32 * w + 16 * t16 + 4 * g4 + r, ca[r]);
        }
      };
      if (!last) {
        unsigned v;
        if (loader && lane == 0) asm volatile("ds_add_u32 %0, %1" ::"v"(LDS_TAG + 16), "v"(1) : "memory");
        do asm volatile("ds_read_b32 %0, %1\n\ts_waitcnt lgkmcnt(0)" : "=v"(v) : "v"(LDS_TAG + 16) : "memory");
        while ((unsigned)__builtin_amdgcn_readfirstlane(v) != 3u);
        for (int q = 0; q < 4 - w; ++q) {
          if (w == 3) cs_gu(6, 28, false);
          else cs_gu(q < 2 ? 28 * q + w : 40 + 16 * w + 8 * q, q < 2 ? 3 : 1, q == 3);
        }
      }
      SETPRIO(3);
      if (poller) {
        asm volatile("" ::"v"(touch0));
        Stats st;
        unsigned ev[4];
        const int h2l = lane < 32 ? FL_HA2 + sp * 32 + lane : FL_HB + (sp ? sp - 1 : 0) * 32 + c;
        SETPRIO(1);
        if (last) HOLD(4);
        HBAR();
        static_for<7>([&](auto nc) {
          constexpr int n = nc.value + 1;
          if (n <= 2) st.load(rs, lane, lay + L_X1 + MB2 + (8 * sp + 4) * 4096, 8 * n - 8, 8 * n);
          if (n == 3) { FLAG(flo, FL_X1A + sp * 32 + c); st.part1(); }
          if (n == 4) {
            if (missing<16>((const v4u*)st.p)) {
              unsigned spins = 0;
              do { st.load(rs, lane, lay + L_X1 + MB2 + (8 * sp + 4) * 4096); DBGC(2); } while (missing<16>((const v4u*)st.p) && ++spins < DATA_CAP);
              st.part1();
            }
            st.part2();
          }
          if (n == 5) st.part3(L, lane);
          if (n == 6) PEEK(flo, FL_X1A + sp * 32);
          if (n == 7) GATE(flo, FL_X1A + sp * 32);
          HBARS(n == 3 || n == 7);
        });
        st.load(rs, lane, lay + L_X1 + MB2 + 8 * sp * 4096);
        st.all(L, lane);
        static_for<6>([&](auto nc) {
          constexpr int n = nc.value + 2;
          if (n == 4) FLAG(flo, FL_HB + sp * 32 + c);
          if (n == 6) PEEK(flo, FL_HB + sp * 32);
          if (n == 7) GATE(flo, FL_HB + sp * 32);
          HBARS(n == 2 || n == 7);
        });
        static_for<8>([&](auto nc) {
          constexpr int n = nc.value;
          if (n == 2) FLAG(flo, FL_HA + sp * 32 + c);
          if (n == 5) { FLAG(flo, FL_HA2 + sp * 32 + c); PEEK(flo, FL_HA + sp * 32); }
          if (n == 6) GATE(flo, FL_HA + sp * 32);
          if (n == 7) pv = __hip_atomic_load(fl + h2l * 32, RLX_AGENT);
          HBARS(n == 0 || n == 3 || n == 6);
        });
        static_for<6>([&](auto nc) {
          constexpr int n = nc.value + 2;
          if (n == 2) { if (!__all(pv == tag)) poll(fl + h2l * 32, true, tmo, tag); }
          if (n == 3) {
#pragma unroll
            for (int q = 0; q < 4; ++q) ev[q] = __builtin_amdgcn_raw_buffer_load_b32(rs, lane * 128, last && blockIdx.x == 0 ? flo + (FL_HB + 64 * q) * 128 : NONE, AUX_SC1);
          }
          if (n == 4 && !last) FLAG(FLAG_LAYER, FL_XB + sp * 32 + c);
          if (n == 5) PEEK(FLAG_LAYER, FL_XB + sp * 32);
          if (n == 6) GATE(FLAG_LAYER, FL_XB + sp * 32);
          if (n == 7) st.load(rs, lane, last ? NONE : LAY_OFF + LAY_BYTES + L_X + MB2 + (8 * sp + 4) * 4096);
          if (n == 7 && last) HOLD(3);
          HBARS(n == 2 || n == 6 || n == 7);
        });
        if (!last) { st.all(L, lane); }
        if (last && blockIdx.x == 0) {
          if (!__all(ev[0] == tag && ev[1] == tag && ev[2] == tag && ev[3] == tag))
            for (int q = 0; q < 4; ++q) POLL(flo, FL_HB + 64 * q, 64);
          if (lane == 0) __hip_atomic_store(fg + FL_EP * 32, tag, RLX_AGENT);
        }
      } else {
        const unsigned wq = WQKV_OFF + c * 49152, wb = WGU_OFF + c * 114688;
        DRAIN();
        HBAR();
        static_for<7>([&](auto nc) {
          constexpr int n = nc.value + 1;
          if (n < 7) { blank(6 * n - 7 + w, last ? NONE : LAY_OFF + LAY_BYTES); blank(6 * n - 4 + w, last || 6 * n - 4 + w > 33 ? NONE : LAY_OFF + LAY_BYTES); }
          if (n <= 3) ldw(IC<9>{}, wb, 71 + 9 * n);
          if (n == 4) { ldw(IC<5>{}, wb, 107); wait_vm<14>(); }
          if (n == 5) { if (!last && w == 1) cs_gu(80, 1, true); wait_vm<11>(); }
          if (n == 6) { if (!last && w == 2) cs_gu(88, 1, false); wait_vm<4>(); }
          if (n == 7 && !last && w >= 2) cs_gu(80 + 8 * w, 1, true);
          HBARS(n == 3 || n == 7);
        });
        static_for<6>([&](auto nc) {
          constexpr int n = nc.value + 2;
          if (n == 3) wait_vm<0>();
          if (n == 5 || n == 6) { ld_dn(2 * n - 10); ld_dn(2 * n - 9); } else if (n > 6) ld_dn(n - 3);
          HBARS(n == 2 || n == 7);
        });
        static_for<8>([&](auto nc) {
          constexpr int n = nc.value;
          if (n <= 2) ld_dn(n + 5);
          wait_vm<(n <= 2 ? 25 : 35 - 5 * n)>();
          HBARS(n == 0 || n == 3 || n == 6);
        });
        static_for<6>([&](auto nc) {
          constexpr int n = nc.value + 2;
          if (!last) {
            if (n >= 3 && n <= 5) ldw(IC<14>{}, wq, 14 * (n - 3));
            if (n == 6) { ldw(IC<6>{}, wq, 42); ldw(IC<8>{}, wb, 48); }
            if (n == 7) ldw(IC<14>{}, wb, 56);
          }
          HBARS(n == 2 || n == 6 || n == 7);
        });
        if (!last) { ldw(IC<10>{}, wb, 70); wait_vm<14>(); }
      }
      SETPRIO(3);
      if (!last) {
        HOLD(2);
        HBAR();
      }
      static_for<NPQ>([&](auto i) { undef(xq[i.value]); });
    }
  }
}

}

extern "C" void kernel_launch(void* const* d_in, const int* in_sizes, int n_in, void* d_out, int out_size, void* d_ws, size_t ws_size,
                              hipStream_t stream) {
  (void)in_sizes; (void)n_in; (void)out_size; (void)ws_size;
  (void)hipFuncSetAttribute((const void*)k_main, hipFuncAttributeMaxDynamicSharedMemorySize, LDS_BYTES);
  k_main<<<256, 512, LDS_BYTES, stream>>>((const u16*)d_in[0], (const u16*)d_in[1], (const u16*)d_in[2], (const u16*)d_in[4], (const u16*)d_in[5],
                                          (const u16*)d_in[6], (unsigned char*)d_ws, (u16*)d_out);
}
```

```cpp
#include <hip/hip_runtime.h>
#include <stdint.h>
#include <utility>
#define V2

#define LAS __attribute__((address_space(3)))
#define GAS __attribute__((address_space(1)))
#define DI __device__ __forceinline__
#define RLX_AGENT __ATOMIC_RELAXED, __HIP_MEMORY_SCOPE_AGENT
typedef unsigned short u16;
typedef unsigned v4u __attribute__((ext_vector_type(4)));
typedef unsigned v2u __attribute__((ext_vector_type(2)));
typedef float f32x2 __attribute__((ext_vector_type(2)));
typedef float f32x4 __attribute__((ext_vector_type(4)));
typedef float f32x16 __attribute__((ext_vector_type(16)));
typedef __bf16 bf16x2 __attribute__((ext_vector_type(2)));
typedef __bf16 bf16x8 __attribute__((ext_vector_type(8)));
typedef GAS unsigned gu32;
typedef GAS unsigned long long gu64;

namespace {

constexpr int D_ = 512, F_ = 1792;
constexpr unsigned MB2 = 2097152u, STB = 262144u;
constexpr unsigned FLAG_LAYER = 1048576u, FLAG_G = 2 * FLAG_LAYER, FLAG_BYTES = FLAG_G + 65536;
constexpr unsigned WQKV_OFF = FLAG_BYTES;
constexpr unsigned WO_OFF = WQKV_OFF + 32 * 49152;
constexpr unsigned WGU_OFF = WO_OFF + 16 * 32768;
constexpr unsigned WDN_OFF = WGU_OFF + 32 * 114688;
constexpr unsigned LAY_OFF = WDN_OFF + 16 * 114688;
constexpr unsigned L_K = 0, L_V = MB2, L_O = 2 * MB2, L_X1 = 3 * MB2, L_X = 4 * MB2 + STB, L_H = 5 * MB2 + 2 * STB;
constexpr unsigned L_P = L_H + 128 * 57344;
constexpr unsigned LAY_BYTES = L_P + 2 * MB2;
constexpr unsigned WS_BYTES = LAY_OFF + 2 * LAY_BYTES;
constexpr unsigned NONE = 0x40000000u;
constexpr int FL_KV = 0, FL_O = 2048, FL_X1B = 4096, FL_X1A = 6144, FL_HB = 6400, FL_HA = 6656, FL_XB = 6912, FL_XA = 7168, FL_HA2 = 7424;
constexpr int FL_WR = 0, FL_EP = 256, FL_TMO = 257;
constexpr int VBLK = 528;
constexpr int LDS_KL = 114688, LDS_VL = LDS_KL + 16384, LDS_QL = LDS_VL + 32 * VBLK, LDS_CS = LDS_QL + 8192, LDS_ST = LDS_CS + 1536;
constexpr int LDS_MK = LDS_ST + 1536, LDS_TAG = LDS_MK + 2048;
constexpr int LDS_WO = 81920, LDS_TB = 49152;
constexpr int LDS_BYTES = 163840;
#define TSTR 1048576u
#define CODE_BYTES_ 74240
constexpr int CODE_BYTES = CODE_BYTES_, CODE_LINES = (CODE_BYTES + 4095) / 4096;
constexpr unsigned SPIN_CAP = 1u << 16;
constexpr int AUX_SC1 = 16;
constexpr float LOG2E = 1.4426950408889634f;

DI f32x16 mfma(v4u a, v4u b, f32x16 c) {
  return __builtin_amdgcn_mfma_f32_32x32x16_bf16(__builtin_bit_cast(bf16x8, a), __builtin_bit_cast(bf16x8, b), c, 0, 0, 0);
}
DI f32x4 mfma16(v4u a, v4u b, f32x4 c) {
  return __builtin_amdgcn_mfma_f32_16x16x32_bf16(__builtin_bit_cast(bf16x8, a), __builtin_bit_cast(bf16x8, b), c, 0, 0, 0);
}
DI unsigned pk2(float lo, float hi) {
  f32x2 p = {lo, hi};
  return __builtin_bit_cast(unsigned, __builtin_convertvector(p, bf16x2));
}
DI float bfr(float x) { return __uint_as_float(pk2(x, 0.f) << 16); }
DI float bflo(unsigned u) { return __uint_as_float(u << 16); }
DI float bfhi(unsigned u) { return __uint_as_float(u & 0xffff0000u); }
DI float amul(float a, float b) { float d; asm("v_mul_f32 %0, %1, %2" : "=v"(d) : "v"(a), "v"(b)); return d; }
DI float afma(float a, float b, float c) { float d; asm("v_fma_f32 %0, %1, %2, %3" : "=v"(d) : "v"(a), "v"(b), "v"(c)); return d; }
DI float aadd(float a, float b) { float d; asm("v_add_f32 %0, %1, %2" : "=v"(d) : "v"(a), "v"(b)); return d; }
DI float asub(float a, float b) { float d; asm("v_sub_f32 %0, %1, %2" : "=v"(d) : "v"(a), "v"(b)); return d; }
DI float xhalf_sum(float v) {
  float t;
  asm("v_mov_b32 %1, %0\n\ts_nop 1\n\tv_permlane32_swap_b32 %0, %1\n\tv_add_f32 %0, %0, %1" : "+v"(v), "=&v"(t));
  return v;
}
DI float xrow_sum(float v) {
  float t;
  asm("v_mov_b32 %1, %0\n\ts_nop 1\n\tv_permlane16_swap_b32 %0, %1\n\tv_add_f32 %0, %0, %1" : "+v"(v), "=&v"(t));
  return v;
}
DI float xhalf_max(float v) {
  auto r = __builtin_amdgcn_permlane32_swap(__float_as_uint(v), __float_as_uint(v), false, false);
  return fmaxf(__uint_as_float(r[0]), __uint_as_float(r[1]));
}
DI void swap16(unsigned& a, unsigned& b) { asm("s_nop 1\n\tv_permlane16_swap_b32 %0, %1" : "+v"(a), "+v"(b)); }
DI void swap32(unsigned& a, unsigned& b) { asm("s_nop 1\n\tv_permlane32_swap_b32 %0, %1" : "+v"(a), "+v"(b)); }
DI f32x16 splat16(float v) {
  f32x16 z;
#pragma unroll
  for (int i = 0; i < 16; ++i) z[i] = v;
  return z;
}
DI f32x4 zero4() { return (f32x4){0.f, 0.f, 0.f, 0.f}; }
DI int vopq(int x) { asm volatile("" : "+v"(x)); return x; }
DI int lane_id() { int l; asm volatile("v_mbcnt_lo_u32_b32 %0, -1, 0\n\tv_mbcnt_hi_u32_b32 %0, -1, %0" : "=v"(l)); return l; }
template <int LN> DI unsigned wrl(unsigned v, unsigned s) { asm volatile("v_writelane_b32 %0, %1, %2" : "+v"(v) : "s"(s), "i"(LN)); return v; }
template <class T> DI void undef(T& x) { asm volatile("" : "=v"(x)); }
DI int sopq(int x) { asm volatile("" : "+s"(x)); return x; }
#define SWZ(l) ((l) ^ (((l) >> 3) & 7))
#define COORDS() \
  const int lane = lane_id(), lo = vopq(lane * 16), hh = lane >> 5, t16 = lane & 15, g4 = lane >> 4, lsw = SWZ(lane) * 16; \
  const int w = sopq(w0) & 3, sp = sopq(sp0), c = sopq(c0), n32 = c >> 1, rh = c & 1; \
  const unsigned lay = (unsigned)sopq((int)lay0); \
  const unsigned flo = sopq(layer) * FLAG_LAYER; \
  gu32* const fl = (gu32*)(ws + flo); \
  (void)lo; (void)hh; (void)t16; (void)g4; (void)lsw; (void)n32; (void)rh; (void)lay; (void)sp; (void)c; (void)w; (void)fl;
#define HBARS(c) asm volatile("s_waitcnt lgkmcnt(0)\n\ts_barrier" ::: "memory")
#define CST() do {} while (0)
#define DBGC(k) asm volatile("" ::: "memory")
#define HBAR() asm volatile("s_waitcnt lgkmcnt(0)\n\ts_barrier" ::: "memory")
#define TR() do {} while (0)
#define BAR() asm volatile("s_waitcnt lgkmcnt(0)\n\ts_barrier" ::: "memory")
#define BARX() asm volatile("s_barrier" ::: "memory")
#define DRAIN() asm volatile("s_waitcnt vmcnt(0)" ::: "memory")
#define SETTLE() __builtin_amdgcn_s_waitcnt(0x0f70)
#define PIN() __builtin_amdgcn_sched_barrier(0)
#define SETPRIO(n) __builtin_amdgcn_s_setprio(n)
template <int N> DI void wait_vm() { static_assert(N >= 0 && N < 64, ""); asm volatile("s_waitcnt vmcnt(%0)" ::"i"(N) : "memory"); }

typedef __amdgpu_buffer_rsrc_t rsrc_t;
DI v4u ld16(rsrc_t rs, unsigned voff, unsigned soff) { return __builtin_amdgcn_raw_buffer_load_b128(rs, voff, soff, AUX_SC1); }
DI f32x2 ld8f(rsrc_t rs, unsigned voff, unsigned soff) { return __builtin_bit_cast(f32x2, __builtin_amdgcn_raw_buffer_load_b64(rs, voff, soff, AUX_SC1)); }
DI void st16(rsrc_t rs, v4u v, unsigned voff, unsigned soff) {
  __builtin_amdgcn_raw_buffer_store_b128(v, rs, voff, soff, AUX_SC1);
  asm volatile("s_nop 1" ::"v"(v));
}
DI void st8(rsrc_t rs, v2u v, unsigned voff, unsigned soff) { __builtin_amdgcn_raw_buffer_store_b64(v, rs, voff, soff, AUX_SC1); }
DI void st4(rsrc_t rs, unsigned v, unsigned voff, unsigned soff) { __builtin_amdgcn_raw_buffer_store_b32(v, rs, voff, soff, AUX_SC1); }
DI v4u lds16(LAS unsigned char* L, int off) { return *(LAS v4u*)(L + off); }
DI f32x4 lds4f(LAS unsigned char* L, int off) { return *(LAS f32x4*)(L + off); }

DI void poll(gu32* word, bool active, gu32* tmo, unsigned tag) {
  for (unsigned spins = 0;;) {
    unsigned v = tag;
    if (active) v = __hip_atomic_load(word, RLX_AGENT);
    if (__all(v == tag)) break;
    ++spins;
    if (spins > SPIN_CAP) { __hip_atomic_store(tmo, tag, RLX_AGENT); break; }
    if ((spins & 255u) == 0u && (unsigned)__builtin_amdgcn_readfirstlane(__hip_atomic_load(tmo, RLX_AGENT)) == tag) break;
  }
  __builtin_amdgcn_fence(__ATOMIC_ACQUIRE, "wavefront");
}

template <int I> DI void dma1(rsrc_t rs, LAS unsigned char* dst, unsigned voff, unsigned soff) {
  __builtin_amdgcn_raw_ptr_buffer_load_lds(rs, (LAS void*)dst, 16, voff, soff, I * 1024, AUX_SC1);
}
template <int V> using IC = std::integral_constant<int, V>;
template <class FN, int... I> DI void static_for_(FN fn, std::integer_sequence<int, I...>) { (fn(IC<I>{}), ...); }
template <int N, class FN> DI void static_for(FN fn) { static_for_(fn, std::make_integer_sequence<int, N>{}); }
template <int N> DI void dma(rsrc_t rs, LAS unsigned char* dst, unsigned lo, unsigned src) {
  static_for<N>([&](auto i) { dma1<i.value & 3>(rs, dst + (i.value >> 2) * 4096, lo, src + (i.value >> 2) * 4096); });
}

struct Stats {
  f32x4 p[16];
  float m0, m1, q0, q1;
  DI void load(rsrc_t rs, int lane, unsigned base, int i0 = 0, int i1 = 16) {
#pragma unroll
    for (int i = i0; i < i1; ++i) p[i] = __builtin_bit_cast(f32x4, ld16(rs, (lane >> 4) * 4096 + (lane & 15) * 16 + i * 256, base));
    PIN();
  }
  DI void part1() {
    m0 = m1 = q0 = q1 = 0.f;
#pragma unroll
    for (int i = 0; i < 16; ++i) { m0 += p[i][0]; q0 += p[i][1]; m1 += p[i][2]; q1 += p[i][3]; }
    asm volatile("" : "+v"(m0), "+v"(m1), "+v"(q0), "+v"(q1));
  }
  DI void part2() {
    const float a0 = m0 * (1.0f / 16.0f), a1 = m1 * (1.0f / 16.0f);
    q0 *= 32.0f; q1 *= 32.0f;
#pragma unroll
    for (int i = 0; i < 16; ++i) { const float d0 = p[i][0] - a0, d1 = p[i][2] - a1; q0 += d0 * d0; q1 += d1 * d1; }
    asm volatile("" : "+v"(q0), "+v"(q1));
  }
  DI void all(LAS unsigned char* L, int lane, int dst = LDS_ST) { part1(); part2(); part3(L, lane, dst); }
  DI void part3(LAS unsigned char* L, int lane, int dst = LDS_ST) {
    const float den0 = bfr(__builtin_amdgcn_sqrtf(bfr(bfr(q0 * (1.0f / (32.0f * D_))) + 1e-5f)));
    const float den1 = bfr(__builtin_amdgcn_sqrtf(bfr(bfr(q1 * (1.0f / (32.0f * D_))) + 1e-5f)));
    *(LAS f32x2*)(L + dst + lane * 8) = (f32x2){__builtin_amdgcn_rcpf(den0), __builtin_amdgcn_rcpf(den1)};
    *(LAS f32x2*)(L + dst + 512 + lane * 8) = (f32x2){den0 * den0, den1 * den1};
    *(LAS v2u*)(L + dst + 1024 + lane * 8) = (v2u){pk2(m0 * (-1.0f / D_), m0 * (-1.0f / D_)), pk2(m1 * (-1.0f / D_), m1 * (-1.0f / D_))};
  }
};
DI float lds1f(LAS unsigned char* L, int off) { return *(LAS float*)(L + off); }
DI v4u mfrag(LAS unsigned char* L, int off) { const unsigned n = *(LAS unsigned*)(L + off); return (v4u){n, n, 0u, 0u}; }

constexpr int LA = 6;
template <int NF, class RD, class STEP>
DI void pipe(RD rd, STEP step) {
  v4u wf[8];
  static_for<LA>([&](auto i) { wf[i.value] = rd(i); });
  step(IC<-1>{}, wf[0], IC<1>{});
  PIN();
  static_for<NF>([&](auto fc) {
    constexpr int f = fc.value;
    if constexpr ((f & 1) == 0 && f + LA <= NF) __builtin_amdgcn_s_waitcnt(0xc07f | ((LA - 2) << 8));
    step(fc, wf[f & 7], IC<0>{});
    if constexpr (f + LA < NF) wf[(f + LA) & 7] = rd(IC<f + LA>{});
    step(fc, wf[f & 7], IC<1>{});
    PIN();
  });
}
DI v4u csfrag(LAS unsigned char* L, int entry) {
  const v2u t = *(LAS v2u*)(L + LDS_CS + entry * 8);
  return (v4u){t.x, t.y, 0u, 0u};
}
DI void cs_put(LAS unsigned char* L, int entry, float cs) {
  const float hi = bfr(cs), mid = bfr(cs - hi), lw = (cs - hi) - mid;
  *(LAS v2u*)(L + LDS_CS + entry * 8) = (v2u){pk2(hi, mid), pk2(lw, 0.f)};
}
DI unsigned mulpk(float a, float s0, float b, float s1) {
  unsigned d; float t;
  asm("v_mul_f32 %0, %2, %3\n\tv_mul_f32 %1, %4, %5\n\tv_cvt_pk_bf16_f32 %0, %0, %1" : "=&v"(d), "=&v"(t) : "v"(a), "v"(s0), "v"(b), "v"(s1));
  return d;
}
DI unsigned nos(unsigned v) { return v < 0xfffffffeu ? v : 0xfffffffeu; }
DI v4u nos4(v4u v) { return (v4u){nos(v[0]), nos(v[1]), nos(v[2]), nos(v[3])}; }
DI unsigned umax3(unsigned a, unsigned b, unsigned c) { const unsigned t = a > b ? a : b; return t > c ? t : c; }
template <int N> DI bool missing(const v4u* f, int step = 1) {
  unsigned m0 = 0, m1 = 0;
#pragma unroll
  for (int i = 0; i < N; ++i) { m0 = umax3(m0, f[i * step][0], f[i * step][1]); m1 = umax3(m1, f[i * step][2], f[i * step][3]); }
  return __builtin_amdgcn_ballot_w64((m0 > m1 ? m0 : m1) == 0xffffffffu) != 0;
}
constexpr unsigned DATA_CAP = 1u << 14;
DI v4u pk24(const f32x4& a, unsigned tag) {
  const v4u u = __builtin_bit_cast(v4u, a);
  return (v4u){__builtin_amdgcn_perm(u[0], u[3], 0x07060501u), __builtin_amdgcn_perm(u[1], u[3], 0x07060502u), __builtin_amdgcn_perm(u[2], u[3], 0x07060503u), tag};
}
DI f32x4 un24(const v4u& u) {
  return __builtin_bit_cast(f32x4, (v4u){u[0], u[1], u[2], __builtin_amdgcn_perm(u[2], __builtin_amdgcn_perm(u[1], u[0], 0x0c0c0400u), 0x0401000cu)});
}
DI v4u pack8(const float* y) { return (v4u){pk2(y[0], y[1]), pk2(y[2], y[3]), pk2(y[4], y[5]), pk2(y[6], y[7])}; }

struct Fin {
  float y[8], s;
  v4u o;
  v2u sq;
  template <int K, bool ST = true> DI void chunk(const f32x4& a0, const f32x4& a1, const v4u& r, rsrc_t ro, unsigned vo, unsigned so, rsrc_t rs, unsigned vs, unsigned ss) {
    if constexpr (K == 0) { y[0] = aadd(a0[0], bflo(r[0])); y[1] = aadd(a0[1], bfhi(r[0])); y[2] = aadd(a0[2], bflo(r[1])); y[3] = aadd(a0[3], bfhi(r[1])); }
    if constexpr (K == 1) { y[4] = aadd(a1[0], bflo(r[2])); y[5] = aadd(a1[1], bfhi(r[2])); y[6] = aadd(a1[2], bflo(r[3])); y[7] = aadd(a1[3], bfhi(r[3])); }
    if constexpr (K == 2) { o = nos4(pack8(y)); if constexpr (ST) st16(ro, o, vo, so); }
    if constexpr (K == 3) {
#pragma unroll
      for (int m = 0; m < 4; ++m) { y[2 * m] = bflo(o[m]); y[2 * m + 1] = bfhi(o[m]); }
    }
    if constexpr (K == 4) s = xrow_sum(aadd(aadd(aadd(y[0], y[1]), aadd(y[2], y[3])), aadd(aadd(y[4], y[5]), aadd(y[6], y[7]))));
    if constexpr (K == 5) {
      s = xhalf_sum(s);
      const float mu = s * (1.0f / 32.0f);
#pragma unroll
      for (int i = 0; i < 8; ++i) y[i] = asub(y[i], mu);
    }
    if constexpr (K == 6) {
      y[0] = amul(y[0], y[0]); y[1] = amul(y[1], y[1]);
#pragma unroll
      for (int i = 2; i < 8; ++i) y[i & 1] = afma(y[i], y[i], y[i & 1]);
    }
    if constexpr (K == 7) {
      sq = (v2u){nos(__float_as_uint(s)), nos(__float_as_uint(xhalf_sum(xrow_sum(aadd(y[0], y[1])))))};
      if constexpr (ST) st8(rs, sq, vs, ss);
    }
  }
};

constexpr unsigned long long low_mask(int r) {
  unsigned long long m = 0;
  for (int l = 0; l < 64; ++l)
    if ((r & 3) + 8 * (r >> 2) + 4 * (l >> 5) <= (l & 31)) m |= 1ull << l;
  return m;
}
DI float sel(unsigned long long mask, float set, float unset) {
  float d;
  asm("v_cndmask_b32_e64 %0, %1, %2, %3" : "=v"(d) : "v"(unset), "v"(set), "s"(mask));
  return d;
}
DI float max16(float m, const f32x16& s) {
#pragma unroll
  for (int r = 0; r < 16; r += 2) m = __builtin_fmaxf(__builtin_fmaxf(m, s[r]), s[r + 1]);
  return m;
}
DI v4u exp_pack(const f32x16& s, int h) {
  float pr[8];
#pragma unroll
  for (int r = 0; r < 8; ++r) pr[r] = __builtin_amdgcn_exp2f(s[8 * h + r]);
  return pack8(pr);
}
template <bool EXACT>
DI f32x16 attn_pass(LAS unsigned char* L, int lane, int tl, int nv) {
  const int lo = lane * 16;
  const int kb = LDS_KL + tl * 1024 + lo, kf0 = LDS_KL + (tl + 8) * 1024 + (lane >> 5) * 512;
  const v4u q = lds16(L, LDS_QL + tl * 1024 + lo);
  v4u kf[9];
  kf[8] = lds16(L, kb + 8192); kf[0] = lds16(L, nv <= 0 ? kb : kf0);
#pragma unroll
  for (int i = 7; i >= 1; --i) kf[i] = lds16(L, (nv <= i ? kb : kf0 - i * 1024) + i * 1024);
  f32x16 s[9], cm = splat16(0.f);
  if constexpr (EXACT) {
    s[8] = mfma(kf[8], q, splat16(0.f));
    s[0] = mfma(kf[0], q, splat16(0.f));
    s[7] = mfma(kf[7], q, splat16(0.f));
    s[6] = mfma(kf[6], q, splat16(0.f));
    asm volatile("s_nop 7\n\ts_nop 7" : "+v"(s[8]), "+v"(s[0]));
    PIN();
    static_for<16>([&](auto r) { s[8][r.value] = sel(low_mask(r.value), s[8][r.value], s[0][r.value]); });
    float m = max16(s[8][0], s[8]);
    PIN();
    static_for<7>([&](auto ic) {
      constexpr int i = 7 - ic.value;
      if constexpr (i - 2 >= 1) s[i - 2] = mfma(kf[i - 2], q, splat16(0.f));
      m = max16(m, s[i]);
      PIN();
    });
    cm = splat16(-xhalf_max(m));
  }
  const int vb = LDS_VL + 2 * tl * VBLK + ((lane & 16) ? 512 : (lane >> 5) * 256 + (lane & 15) * 16);
  s[8] = mfma(kf[8], q, cm);
  s[0] = mfma(kf[0], q, cm);
  s[7] = mfma(kf[7], q, cm);
  const v4u mk0 = lds16(L, LDS_MK + lo), mk1 = lds16(L, LDS_MK + 1024 + lo);
  v4u va = lds16(L, vb + 16 * VBLK), vc = lds16(L, vb + 17 * VBLK), v0a = lds16(L, vb), v0c = lds16(L, vb + VBLK);
  asm volatile("s_nop 7\n\ts_nop 7" : "+v"(s[8]), "+v"(s[0]));
  PIN();
  f32x16 o;
  {
    static_for<16>([&](auto r) { s[8][r.value] = sel(low_mask(r.value), s[8][r.value], s[0][r.value]); });
    const v4u p0 = exp_pack(s[8], 0), l0 = p0 & mk0;
    o = mfma(va, l0, splat16(0.f));
    o = mfma(v0a, p0 ^ l0, o);
    const v4u p1 = exp_pack(s[8], 1), l1 = p1 & mk1;
    o = mfma(vc, l1, o);
    o = mfma(v0c, p1 ^ l1, o);
    va = lds16(L, vb + 14 * VBLK); vc = lds16(L, vb + 15 * VBLK);
    PIN();
  }
  static_for<7>([&](auto ic) {
    constexpr int i = 7 - ic.value;
    if constexpr (i > 1) s[i - 1] = mfma(kf[i - 1], q, cm);
    PIN();
    o = mfma(va, exp_pack(s[i], 0), o);
    if constexpr (i > 1) va = lds16(L, vb + (2 * i - 2) * VBLK);
    o = mfma(vc, exp_pack(s[i], 1), o);
    if constexpr (i > 1) vc = lds16(L, vb + (2 * i - 1) * VBLK);
    PIN();
  });
  return o;
}
DI v4u attn_unit(LAS unsigned char* L, int lane, int tl, int nv) {
  f32x16 o = attn_pass<false>(L, lane, tl, nv);
  float big = __builtin_fabsf(o[0]);
#pragma unroll
  for (int r = 1; r < 8; ++r) big += __builtin_fabsf(o[r]);
  if (__builtin_expect(!__all(o[8] > 0x1p-30f && o[8] < 0x1p60f && big < 0x1p120f), 0)) {
    asm volatile("" ::: "memory");
    o = attn_pass<true>(L, lane, tl, nv);
  }
  const float rl = __builtin_amdgcn_rcpf(o[8]);
  float y[8];
#pragma unroll
  for (int r = 0; r < 8; ++r) y[r] = o[r] * rl;
  return pack8(y);
}

struct Retile {
  v4u in[8];
  DI void load(const u16* m, int elems, int el, int ld, int rs4) {
    const rsrc_t r = __builtin_amdgcn_make_buffer_rsrc((void*)m, 0, elems * 2, 0x00020000);
    const int e4 = el + rs4 * ld;
#pragma unroll
    for (int j = 0; j < 8; ++j) in[j] = __builtin_amdgcn_raw_buffer_load_b128(r, (j < 4 ? el : e4) * 2, (j & 3) * ld * 2, 0);
    PIN();
  }
  unsigned d[8];
  DI void out(rsrc_t rs) {
#pragma unroll
    for (int i = 0; i < 8; ++i) st16(rs, in[i], d[i], 0);
  }
  DI void lines(LAS unsigned char* t, int lane, int sw, unsigned dst) {
#pragma unroll
    for (int e = 0; e < 8; ++e) {
      v4u o;
#pragma unroll
      for (int m = 0; m < 4; ++m) o[m] = __builtin_amdgcn_perm(in[2 * m + 1][e >> 1], in[2 * m][e >> 1], (e & 1) ? 0x07060302u : 0x05040100u);
      *(LAS v4u*)(t + lane * 128 + (e ^ sw ^ (lane & 7)) * 16) = o;
    }
    asm volatile("s_waitcnt lgkmcnt(0)" ::: "memory");
#pragma unroll
    for (int i = 0; i < 8; ++i) {
      in[i] = *(LAS v4u*)(t + i * 1024 + (lane ^ (lane >> 3)) * 16);
      d[i] = (unsigned)__builtin_amdgcn_ds_bpermute((8 * i + (lane >> 3)) * 4, (int)dst) + (lane & 7) * 16;
    }
    PIN();
  }
  DI void put(LAS unsigned char* L, int pb, int dd, int lp, int dl) {
#pragma unroll
    for (int e = 0; e < 8; ++e) {
      v4u o;
#pragma unroll
      for (int m = 0; m < 4; ++m) o[m] = __builtin_amdgcn_perm(in[2 * m + 1][e >> 1], in[2 * m][e >> 1], (e & 1) ? 0x07060302u : 0x05040100u);
      const int s = lp + (e & 3) + (e >> 2) * dl;
      *(LAS v4u*)(L + pb + (e >> 2) * dd + (s ^ ((s >> 3) & 7)) * 16) = o;
    }
  }
};

struct GS { int cs, j, kap, off; };
constexpr GS gu_step(int u) {
  if (u < 64) { const int k = u >> 2, j = u & 3; return GS{0, j, k, j < 3 ? (7 * (k >> 1) + 3 * (k & 1) + j) * 1024 : (7 * (k >> 1) + 6) * 1024 + (k & 1) * 512}; }
  const int v = u - 64, j = v / 17, i = v % 17;
  return GS{i == 16, j, 16 + i, 57344 + j * 16384 + i * (j < 3 ? 1024 : 512)};
}
constexpr int GA = 132, DB = 264, DA = 376, NST = 488;
#define NPQ_ 8
#define NPG_ 16
#define APRIO 1
constexpr int NPG = NPG_, NPQ = NPQ_;
#define HS1 28
#define HP1 11
#define HS2 72
#define HP2 12
#define UG 7
#define HB2 6
#define XQS 96
constexpr int ld_step(int id) {
  if (id < 32) return id < NPG ? -1 : 2 * (id - NPG);
  if (id < 64) { const int n = id - 32; return n < 12 ? 103 + (5 * n) / 2 : GA + 10 + 2 * (n - 12); }
  if (id < 92) { const int n = id - 64; return n < 12 ? (UG == 7 ? GA + 103 + n : GA + 88 + (9 * n) / 4) : DB - 15 + ((n - 12) * HB2) / 4; }
  if (id < 120) { const int n = id - 92; return n < 16 ? DB + HS1 + (HP1 * n) / 4 : DB + HS2 + ((n - 16) * HP2) / 4; }
  const int n = id - 120;
  return n < NPQ ? DA + XQS + ((112 - XQS) * n) / NPQ : -1;
}
constexpr int NEP = 56;
constexpr int ep_step(int k) { return 83 + 17 * (k / 16) + k % 16; }
struct Tab { short ld[NST][2]; short ep[NST]; short nvm[NST]; };
#define SL0 20
#define Q3T 75
#define XAE 0
#define XA0 18
constexpr int xa_step(int j) { const int a = XA0 + j, b = XAE == 0 ? (j < 12 ? 3 * j + 2 : 34 + j) : j < XAE ? 0 : j - XAE < 12 ? 3 * (j - XAE) + 2 : 34 + j - XAE; return a > b ? a : b; }
constexpr Tab make_tab() {
  Tab t{};
  for (int s = 0; s < NST; ++s) { t.ld[s][0] = t.ld[s][1] = t.ep[s] = -1; t.nvm[s] = 0; }
  for (int id = 0; id < 152; ++id) {
    const int s = ld_step(id);
    if (s < 0) continue;
    t.ld[s][t.ld[s][0] >= 0] = (short)id; ++t.nvm[s];
  }
  for (int job = 0; job < 2; ++job)
    for (int k = 0; k < NEP; ++k) {
      const int s = job * GA + ep_step(k);
      t.ep[s] = (short)(job * 64 + k);
      if (k == NEP - 1) t.nvm[s] += 2;
      if (k == (job ? 31 : NEP - 1)) t.nvm[s] += 2;
    }
  ++t.nvm[DB + 58]; ++t.nvm[DB + 59]; ++t.nvm[DA + 58]; ++t.nvm[DA + 59];
  return t;
}
constexpr Tab TAB = make_tab();
constexpr int vm_between(int a, int b) {
  int n = 0;
  for (int s = a + 1; s <= b; ++s) n += TAB.nvm[s];
  return n;
}

__global__ void __launch_bounds__(512, 2) k_main(const u16* __restrict__ x, const u16* __restrict__ wqkv, const u16* __restrict__ wo, const u16* __restrict__ wg,
                                                 const u16* __restrict__ wu, const u16* __restrict__ wd, unsigned char* __restrict__ ws,
                                                 u16* __restrict__ out) {
  extern __shared__ __attribute__((aligned(16))) unsigned char smem[];
  LAS unsigned char* L = (LAS unsigned char*)smem;
  const int tid = threadIdx.x, w0 = __builtin_amdgcn_readfirstlane(tid >> 6);
  const int b = blockIdx.x;
  auto touch = [&](rsrc_t r, unsigned voff, unsigned soff) { __builtin_amdgcn_raw_ptr_buffer_load_lds(r, (LAS void*)(L + LDS_BYTES - 256), 4, voff, soff, 0, AUX_SC1); };
  {
    unsigned long long pc0;
    asm volatile("s_getpc_b64 %0" : "=s"(pc0));
    const rsrc_t rc = __builtin_amdgcn_make_buffer_rsrc((void*)(pc0 & ~127ull), 0, CODE_BYTES, 0x00020000);
    if (w0 == 3) __builtin_amdgcn_raw_ptr_buffer_load_lds(rc, (LAS void*)(L + LDS_BYTES - 256), 4, (tid & 63) < CODE_LINES ? (tid & 63) * 128 : NONE, (b >> 3) * CODE_LINES * 128, 0, 0);
    PIN();
  }
  const int sp0 = (b & 6) + ((b >> 3) & 1), c0 = (b >> 4) + 16 * (b & 1);
  const rsrc_t rs = __builtin_amdgcn_make_buffer_rsrc((void*)ws, 0, (int)WS_BYTES, 0x00020000);
  const bool compute = w0 < 4;
  gu32* const fg = (gu32*)(ws + FLAG_G);
  gu32* const tmo = fg + FL_TMO * 32;
  unsigned tag = 0;
#define HOLD(k) do {} while (0)
#define FLAG(blk, line) st4(rs, tag, lane ? NONE : 0u, (blk) + (line) * 128)
#define POLL(blk, line, n) poll((gu32*)(ws + (blk)) + ((line) + lane) * 32, lane < (n), tmo, tag)
#define GETTAG() tag = (unsigned)__builtin_amdgcn_readfirstlane(*(LAS unsigned*)(L + LDS_TAG))
  auto blank = [&](int it, unsigned ly) {
    const int lane = lane_id(), sp = sopq(sp0), c = sopq(c0), w = it & 3, pb = it >= 34;
    const unsigned o = (unsigned)vopq(-1);
    if (it >= 16 && it < 24) st16(rs, (v4u){o, o, o, o}, ((lane >> 4) & 1) * 16384 + (lane >> 5) * 256 + (lane & 15) * 16, ly + L_O + (8 * sp + (it & 7)) * 32768 + c * 512);
    else if (it >= 24 && it < 28) st16(rs, (v4u){o, o, o, o}, (lane >> 4) * 512 + (lane & 15) * 16, ly + L_X1 + ((8 * sp + 4 + 2 * (c & 1) + (w >> 1)) * 32 + (c & ~1)) * 1024 + (w & 1) * 256);
    else if (it == 28 || it == 33 || it == 38) st8(rs, (v2u){o, o}, (lane >> 5) * 4096 + (lane & 31) * 8, ly + (it == 28 ? L_X1 : L_X) + MB2 + (8 * sp + (it == 33 ? 0 : 4) + 2 * (c & 1)) * 4096 + (c >> 1) * 256);
    else if (it >= 40) {
      const int sh = it < 48 ? 6 : it < 52 ? 5 : 4;
      st16(rs, (v4u){o, o, o, o}, __mul24(lane >> sh, 57344) + (lane & ((1 << sh) - 1)) * 16,
           ly + L_H + (16 * sp + (it < 48 ? it & 7 : it < 52 ? 2 * (it & 3) : 4 * (it & 1))) * 57344 + (it < 48 ? c * 1024 : it < 52 ? 32768 + c * 512 : 49152 + c * 256));
    } else st16(rs, (v4u){o, o, o, o}, lane * 16, ly + (it < 16 ? (it & 8 ? L_V : L_K) + ((8 * sp + (it & 7)) * 32 + c) * 1024
                                                  : L_X + ((16 * sp + 8 * pb + 4 * (c & 1) + w) * 16 + (c >> 1)) * 1024));
  };
  auto attention = [&](int layer, unsigned lay0) {
    COORDS();
    const int tl = compute ? 4 + w : w, T = 8 * sp + tl;
    const v4u o = attn_unit(L, lane, tl, sp == 0 ? 8 - tl : 0);
    st16(rs, nos4(o), (g4 & 1) * 16384 + hh * 256 + t16 * 16, lay + L_O + T * 32768 + c * 512);
    if (layer == 0) { DRAIN(); FLAG(flo, FL_O + T * 32 + c); }
    CST();
  };

  if (compute) {
    v4u xq[32];
    {
    const int layer = 0; const unsigned lay0 = LAY_OFF;
    COORDS();
    SETPRIO(2);
    const int r = lane >> 3, j = lane & 7;
    const u16* src = x + (size_t)(256 * sp + 8 * c + r) * D_ + 128 * w + 8 * j;
    v4u a[2] = {*(const v4u*)src, *(const v4u*)(src + 64)};
    if (w < 3) touch(rs, lane ? NONE : 0u, w == 0 ? lay + L_X + (16 * sp + n32) * 16384 : w == 1 ? lay + L_X + MB2 + 8 * sp * 4096 : (FL_XB + sp * 32 + c) * 128);
    PIN();
#pragma unroll
    for (int i = 0; i < 2; ++i) {
      a[i] = nos4(a[i]);
      st16(rs, a[i], (j >> 2) * 1024 + ((j & 3) * 16 + r) * 16, lay + L_X + (16 * sp + n32) * 16384 + (2 * w + i) * 2048 + rh * 128);
      auto quad = [](float v) {
        v += __int_as_float(__builtin_amdgcn_update_dpp(0, __float_as_int(v), 0xb1, 0xf, 0xf, true));
        return v + __int_as_float(__builtin_amdgcn_update_dpp(0, __float_as_int(v), 0x4e, 0xf, 0xf, true));
      };
      float sm = 0.f, q = 0.f;
#pragma unroll
      for (int m = 0; m < 4; ++m) sm += bflo(a[i][m]) + bfhi(a[i][m]);
      sm = quad(sm);
      const float mu = sm * (1.0f / 32.0f);
#pragma unroll
      for (int m = 0; m < 4; ++m) { const float e0 = bflo(a[i][m]) - mu, e1 = bfhi(a[i][m]) - mu; q += e0 * e0 + e1 * e1; }
      q = quad(q);
      st8(rs, (v2u){nos(__float_as_uint(sm)), nos(__float_as_uint(q))}, (j & 3) ? NONE : (j >> 2) * 256 + r * 8,
          lay + L_X + MB2 + (8 * sp + (c >> 2)) * 4096 + (4 * w + 2 * i) * 256 + (c & 3) * 64);
    }
    DRAIN();
    BAR();
    BAR();
    GETTAG();
    const int TB = 8 * sp + 4 + w;
    static_for<NPQ>([&](auto nc) { constexpr int n = nc.value; xq[n] = ld16(rs, lo + ((n >> 1) & 3) * 1024, lay + L_X + TB * 32768 + (n & 1) * 16384 + (n >> 3) * 4096); });
    BARX();
    }
  for (int layer = 0; layer < 2; ++layer) {
    const bool last = layer == 1;
    const unsigned lay0 = LAY_OFF + layer * LAY_BYTES;
    {
      COORDS();
      const int TB = 8 * sp + 4 + w, TA = TB - 4;
      v4u xa[32];
      auto ldx = [&](auto nc, int T) {
        constexpr int n = nc.value;
        return ld16(rs, lo + ((n >> 1) & 3) * 1024, lay + L_X + (2 * T + (n & 1)) * 16384 + (n >> 3) * 4096);
      };
      f32x4 aq[2][2], ak[2][2], av[2][2], r0, r1;
      v4u mf0, mf1; float rd0, rd1;
      unsigned o0, o1, o2, o3;
      const int kpos = (t16 + 16 * hh + 32 * (g4 & 1)) * 16;
      auto epi = [&](int k, int pa) {
        const int tl = pa ? w : 4 + w, st = (pa ? LDS_KL : LDS_ST) + w * 128;
        const unsigned kvb = lay + ((8 * sp + tl) * 32 + c) * 1024;
        if (k == 0) { r0 = lds4f(L, st + g4 * 16); r1 = lds4f(L, st + 64 + g4 * 16); }
        if (k == 2) { o0 = mulpk(ak[pa][0][0], rd0, ak[pa][0][1], rd0); o1 = mulpk(ak[pa][0][2], rd0, ak[pa][0][3], rd0); }
        if (k == 3) { o2 = mulpk(ak[pa][1][0], rd1, ak[pa][1][1], rd1); o3 = mulpk(ak[pa][1][2], rd1, ak[pa][1][3], rd1); }
        if (k == 4) {
          swap32(o0, o2); swap32(o1, o3);
          st16(rs, nos4((v4u){o0, o1, o2, o3}), kpos, kvb + L_K);
          *(LAS v4u*)(L + LDS_KL + (8 + tl) * 1024 + kpos) = (v4u){o0, o1, o2, o3};
        }
        if (k == 5) { o0 = mulpk(av[pa][0][0], r0[0], av[pa][0][1], r0[1]); o1 = mulpk(av[pa][0][2], r0[2], av[pa][0][3], r0[3]); }
        if (k == 6) { o2 = mulpk(av[pa][1][0], r1[0], av[pa][1][1], r1[1]); o3 = mulpk(av[pa][1][2], r1[2], av[pa][1][3], r1[3]); }
        if (k == 7) {
          swap32(o0, o2); swap32(o1, o3);
          st16(rs, nos4((v4u){o0, o1, o2, o3}), lo, kvb + L_V);
          *(LAS v4u*)(L + LDS_VL + (16 + 2 * tl + hh) * VBLK + (lane & 31) * 16) = (v4u){o0, o1, o2, o3};
        }
        if (k == 8) {
          asm volatile("s_nop 7" : "+v"(aq[pa][0]), "+v"(aq[pa][1]));
          const float s0 = rd0 * (0.25f * LOG2E), s1 = rd1 * (0.25f * LOG2E);
          o0 = mulpk(aq[pa][0][0], s0, aq[pa][0][1], s0); o1 = mulpk(aq[pa][0][2], s0, aq[pa][0][3], s0);
          o2 = mulpk(aq[pa][1][0], s1, aq[pa][1][1], s1); o3 = mulpk(aq[pa][1][2], s1, aq[pa][1][3], s1);
          swap32(o0, o2); swap32(o1, o3);
          *(LAS v4u*)(L + LDS_QL + tl * 1024 + kpos) = (v4u){o0, o1, o2, o3};
        }
      };
      pipe<102>(
          [&](auto fc) {
            constexpr int t = fc.value, u = t % 51;
            if constexpr (t == 28) { BARX(); CST(); }
            if constexpr (t == 12 || t == 44 || t == Q3T) BARX();
            if constexpr (u == 44 || u == 45 || u == 50) return csfrag(L, (u == 50 ? 0 : u - 43) * 16 + t16);
            else return lds16(L, (u < 44 ? u : u - 2) * 1024 + lsw);
          },
          [&](auto fc, const v4u& wfr, auto pc) {
            constexpr int t = fc.value, part = pc.value, u = t % 51;
            constexpr int ks = (u < 36 ? u / 3 : u < 44 ? (u - 12) >> 1 : u - 34) & 15, m = u < 36 ? (u % 3 + 1) % 3 : u < 46 ? 1 + (u & 1) : 0;
            constexpr bool pa = t >= 51, cs = u == 44 || u == 45 || u == 50;
            if constexpr (t < 0) return;
            else if constexpr (part == 0) {
              if constexpr (u < 44 && u % 12 == 0) {
                constexpr int n = u / 12 * 8;
                v4u* const X = pa ? xa : xq;
                if (__builtin_expect(missing<8>(X + n), 0)) {
                  unsigned spins = 0;
                  do { static_for<8>([&](auto i) { X[n + i.value] = ldx(IC<n + i.value>{}, pa ? TA : TB); }); DBGC(pa ? 0 : 7); } while (missing<8>(X + n) && ++spins < DATA_CAP);
                }
                TR();
                PIN();
              }
              const v4u& x0 = cs ? mf0 : pa ? xa[2 * ks] : xq[2 * ks];
              const v4u& x1 = cs ? mf1 : pa ? xa[2 * ks + 1] : xq[2 * ks + 1];
              if constexpr (m == 0) { aq[pa][0] = mfma16(wfr, x0, u < 3 ? zero4() : aq[pa][0]); aq[pa][1] = mfma16(wfr, x1, u < 3 ? zero4() : aq[pa][1]); }
              if constexpr (m == 1) { ak[pa][0] = mfma16(wfr, x0, u < 3 ? zero4() : ak[pa][0]); ak[pa][1] = mfma16(wfr, x1, u < 3 ? zero4() : ak[pa][1]); }
              if constexpr (m == 2) { av[pa][0] = mfma16(x0, wfr, u < 3 ? zero4() : av[pa][0]); av[pa][1] = mfma16(x1, wfr, u < 3 ? zero4() : av[pa][1]); }
            } else {
              if constexpr (2 * t < 32 - NPQ) { xq[NPQ + 2 * t] = ldx(IC<NPQ + 2 * t>{}, TB); xq[NPQ + 2 * t + 1] = ldx(IC<NPQ + 2 * t + 1>{}, TB); }
              if constexpr (u == 40) {
                const int st = (pa ? LDS_KL : LDS_ST) + w * 128;
                rd0 = lds1f(L, st + t16 * 4); rd1 = lds1f(L, st + 64 + t16 * 4);
                const int a = g4 ? LDS_TAG + 8 : st + 1024 + t16 * 4;
                mf0 = mfrag(L, a); mf1 = mfrag(L, g4 ? a : a + 64);
              }
              if constexpr (t >= 45 && t < 54) epi(t - 45, 0);
              if constexpr (t >= 96) epi(t - 96, 1);
              static_for<16>([&](auto j) { if constexpr (xa_step(j.value) == t) { xa[2 * j.value] = ldx(IC<2 * j.value>{}, TA); xa[2 * j.value + 1] = ldx(IC<2 * j.value + 1>{}, TA); } });
              if constexpr (t == 50) CST();
            }
          });
      epi(6, 1); epi(7, 1); epi(8, 1);
      BAR();
      CST();
    }
    attention(layer, lay0);
    {
      SETPRIO(0);
      v4u fr[120];
      {
        COORDS();
        const int TB = 8 * sp + 4 + 2 * rh + (w >> 1), SB = 2 * TB + (w & 1);
        const v4u rB = ld16(rs, lo, lay + L_X + (SB * 16 + n32) * 1024), rA = ld16(rs, lo, lay + L_X + (SB * 16 + n32) * 1024 - 8 * 16384);
        const unsigned xv = (g4 >> 1) * 1024 + (g4 & 1) * 512 + t16 * 16, sv = g4 ? NONE : t16 * 8;
        const unsigned xo = lay + L_X1 + (TB * 32 + 2 * n32) * 1024 + (w & 1) * 256, so = lay + L_X1 + MB2 + TB * 4096 + n32 * 256 + (w & 1) * 128;
        const int lswo = vopq(lsw + LDS_WO);
        while ((unsigned)__builtin_amdgcn_readfirstlane(__hip_atomic_load((LAS unsigned*)(L + LDS_TAG + 12), __ATOMIC_RELAXED, __HIP_MEMORY_SCOPE_WORKGROUP)) != 3u * layer + 3u)
          __builtin_amdgcn_s_sleep(1);
        asm volatile("" ::: "memory");
        v4u of[32];
        unsigned pk;
        f32x4 a0, a1;
        Fin fin;
        auto ldo = [&](auto i) { return ld16(rs, lo + (i.value & 3) * 1024, lay + L_O + (SB - 8 * (i.value >> 4)) * 16384 + ((i.value >> 2) & 3) * 4096); };
        pipe<64>(
            [&](auto fc) { return lds16(L, (fc.value & 31) * 1024 + lswo); },
            [&](auto fc, const v4u& wfr, auto pc) {
              constexpr int t = fc.value, part = pc.value, u = t & 31;
              if constexpr (part == 0) {
                if constexpr (u % 16 == 0) {
                  if (__builtin_expect(missing<8>(of + t / 2), 0)) {
                    unsigned spins = 0;
                    do { static_for<16 - u / 2>([&](auto i) { of[t / 2 + i.value] = ldo(IC<t / 2 + i.value>{}); }); DBGC(1 + (t >> 4)); SETTLE(); } while (missing<8>(of + t / 2) && ++spins < DATA_CAP);
                  }
                  if constexpr (u == 0) CST();
                  PIN();
                }
                if constexpr (u & 1) a1 = mfma16(wfr, of[t >> 1], u == 1 ? zero4() : a1);
                else a0 = mfma16(wfr, of[t >> 1], u == 0 ? zero4() : a0);
              } else {
                if constexpr (t == -1) {
                  if (!last) POLL(flo, FL_O + TB * 32, 32);
                  static_for<16>([&](auto i) { of[i.value] = ldo(i); });
                }
                if constexpr (t == 55) pk = __builtin_amdgcn_raw_buffer_load_b32(rs, (lane & 31) * 128, last ? NONE : (FL_X1B + (8 * sp + 4 + w) * 32) * 128, AUX_SC1);
                if constexpr (t == 63) { if (!last && !__all(pk == tag)) POLL(flo, FL_X1B + (8 * sp + 4 + w) * 32, 32); }
                if constexpr (t == 63) static_for<NPG>([&](auto i) { fr[i.value] = ld16(rs, lo + (i.value & 3) * 1024, lay + L_X1 + (8 * sp + 4 + w) * 32768 + (i.value >> 2) * 4096); });
                if constexpr (t == 31 || t == 63) {
                  asm volatile("s_nop 7" : "+v"(a0), "+v"(a1));
                  static_for<8>([&](auto k) { fin.template chunk<k.value>(a0, a1, t == 31 ? rB : rA, rs, xv, xo - (t == 31 ? 0 : 4 * 32768), rs, sv, so - (t == 31 ? 0 : 4 * 4096)); });
                  CST();
                }
                if constexpr (t == 31) {
                  if (!last) {
                    pk = __hip_atomic_load(fl + (FL_O + (TB - 4) * 32 + (lane & 31)) * 32, RLX_AGENT);
                    DRAIN();
                    FLAG(flo, FL_X1B + TB * 32 + 2 * n32 + (w & 1));
                    if (!__all(pk == tag)) POLL(flo, FL_O + (TB - 4) * 32, 32);
                  }
                  static_for<16>([&](auto i) { of[16 + i.value] = ldo(IC<16 + i.value>{}); });
                }
              }
            });
        CST();
      }
      SETPRIO(2);
      BAR();
      {
        COORDS();
        const int TB = 8 * sp + 4 + w;
        const int SD = 16 * sp + 8 + 4 * rh + w, kh = n32 & 1;
        const unsigned x1b = lay + L_X1 + TB * 32768, hb = lay + L_H + SD * 57344, xn = last ? NONE : lay + LAY_BYTES + L_X;
        const unsigned hsv = (g4 & 1) * 57344 + hh * 256 + t16 * 16, hso = lay + L_H + TB * 114688;
        const unsigned pso = lay + L_P + (2 * sp * 32 + (c ^ 2)) * 8192 + w * 2048;
        const int lsw2 = vopq(lsw + 65536), lh = (hh * 16 + t16) * 16, lh2 = vopq(lh + 65536);
        f32x16 acc[2][4];
        f32x4 dd[2][2];
        v4u mf, hv[4];
        float k1[2], den2[2], t0, t1, p0_, p1_;
        unsigned cm0, cm1;
        auto epi = [&](auto ec) {
          constexpr int job = ec.value >> 6, k = ec.value & 63, j = k >> 4, i = (k >> 2) & 3, uo = j < 3 ? 8 : 4;
          const f32x16& a = acc[job][j];
          if constexpr ((k & 3) == 0)
            asm volatile("v_mul_f32 %0, %2, %4\n\tv_mul_f32 %1, %3, %4\n\tv_exp_f32 %0, %0" : "=&v"(t0), "=&v"(t1) : "v"(a[2 * i]), "v"(a[2 * i + 1]), "v"(k1[job]));
          else if constexpr ((k & 3) == 1)
            asm volatile("v_exp_f32 %0, %0\n\tv_mul_f32 %1, %3, %5\n\tv_mul_f32 %2, %4, %6"
                : "+v"(t1), "=&v"(p0_), "=&v"(p1_) : "v"(a[2 * i]), "v"(a[2 * i + 1]), "v"(a[uo + 2 * i]), "v"(a[uo + 2 * i + 1]));
          else if constexpr ((k & 3) == 2)
            asm volatile("v_fma_f32 %0, %0, %2, %2\n\tv_fma_f32 %1, %1, %2, %2\n\tv_rcp_f32 %0, %0" : "+v"(t0), "+v"(t1) : "v"(den2[job]));
          else {
            unsigned d;
            asm volatile("v_rcp_f32 %2, %2\n\tv_mul_f32 %1, %1, %3\n\tv_mul_f32 %2, %2, %4\n\tv_cvt_pk_bf16_f32 %0, %1, %2\n\tv_min_u32 %0, -2, %0" : "=v"(d), "+v"(t0), "+v"(t1) : "v"(p0_), "v"(p1_));
            hv[j][i] = d;
            if constexpr (k == (job ? 31 : NEP - 1)) { st16(rs, hv[0], hsv, hso - job * 4 * 114688 + c * 1024); st16(rs, hv[1], hsv + 512, hso - job * 4 * 114688 + c * 1024); }
            if constexpr (k == NEP - 1) {
              st16(rs, hv[2], hsv, hso - job * 4 * 114688 + 32768 + c * 512);
              st8(rs, (v2u){hv[3][0], hv[3][1]}, hh ? hsv - 248 : hsv, hso - job * 4 * 114688 + 49152 + c * 256);
            }
          }
        };
        auto vload = [&](auto ic) {
          constexpr int id = ic.value;
          if constexpr (id < 64) fr[id] = ld16(rs, lo + (id & 3) * 1024, x1b - (id >> 5) * 4 * 32768 + ((id & 31) >> 2) * 4096);
          else if constexpr (id < 120) {
            constexpr int n = (id - 64) % 28;
            fr[id] = ld16(rs, lo + (n & 3) * 1024, hb - (id >= 92 ? 8 * 57344 : 0) + (n < 16 ? kh * 16384 : n < 24 ? 16384 + kh * 8192 : 24576 + kh * 4096) + (n >> 2) * 4096);
          } else {
            constexpr int n = id - 120;
            xq[n] = ld16(rs, lo + ((n >> 1) & 3) * 1024, xn + TB * 32768 + (n & 1) * 16384 + (n >> 3) * 4096);
          }
        };
        pipe<NST>(
            [&](auto fc) {
              constexpr int t = fc.value;
              if constexpr (t == 32) wait_vm<vm_between(-1, t - LA - 1)>();
              if constexpr (t == GA + 48) wait_vm<vm_between(ep_step(NEP - 1), t - LA - 1)>();
              if constexpr (t < DB) {
                constexpr GS s = gu_step(t % GA);
                if constexpr (s.cs) return csfrag(L, 48 + 32 * s.j + (s.j < 3 ? lane & 31 : t16));
                else {
                  if (s.off % 14336 == 0 && s.off >= (t < GA ? 14336 : 28672)) BARX();
                  return lds16(L, s.off < 65536 ? s.off + (s.j < 3 ? lsw : lh) : s.off - 65536 + (s.j < 3 ? lsw2 : lh2));
                }
              } else {
                constexpr int v = (t - DB) % 112;
                if (t < DA || v == 14 ? v % 14 == 0 : v % 28 == 0 && v) BARX();
                return lds16(L, v < 64 ? v * 1024 + lsw : (v < 96 ? v - 64 : v - 48) * 1024 + lsw2);
              }
            },
            [&](auto fc, const v4u& wfr, auto pc) {
              constexpr int t = fc.value, part = pc.value;
              if constexpr (t < 0) return;
              else if constexpr (part == 0) {
                if constexpr (t < DB) {
                  constexpr int job = t / GA, u = t % GA;
                  constexpr GS s = gu_step(u);
                  if constexpr (t == 0 || t == 16 || t == 32 || t == 64) {
                    constexpr int n0 = t < 64 ? t / 4 : 16, nn = t == 0 || t == 16 ? 4 : t == 32 ? 8 : 16;
                    if (__builtin_expect(t ? __builtin_amdgcn_ballot_w64((cm0 > cm1 ? cm0 : cm1) == 0xffffffffu) != 0 : missing<nn>(fr + n0), 0)) {
                      unsigned spins = 0;
                      do { static_for<(t < 64 ? 16 : 32) - n0>([&](auto i) { vload(IC<n0 + i.value>{}); }); DBGC(t < 64 ? 5 : 6); SETTLE(); } while (missing<nn>(fr + n0) && ++spins < DATA_CAP);
                    }
                    PIN();
                  }
                  acc[job][s.j] = mfma(wfr, s.cs ? mf : fr[32 * job + (s.kap & 31)], u < 4 ? splat16(0.f) : acc[job][s.j]);
                } else {
                  constexpr int job = t >= DA, v = (t - DB) % 112;
                  if constexpr (job && (v == 0 || v == 16 || v == 32 || v == 44)) {
                    constexpr int n0 = 92 + v / 2, nn = v < 32 ? 8 : 6;
                    if (__builtin_expect(__builtin_amdgcn_ballot_w64((cm0 > cm1 ? cm0 : cm1) == 0xffffffffu) != 0, 0)) {
                      unsigned spins = 0;
                      do { static_for<(v < 32 ? 108 : 120) - n0>([&](auto i) { vload(IC<n0 + i.value>{}); }); DBGC(v < 32 ? 4 : 7); SETTLE(); } while (missing<nn>(fr + n0) && ++spins < DATA_CAP);
                    }
                    PIN();
                  }
                  dd[v / 56][v & 1] = mfma16(wfr, fr[64 + 28 * job + ((v % 56) >> 1)], v % 56 < 2 ? zero4() : dd[v / 56][v & 1]);
                }
              } else {
                if constexpr (t < DB) {
                  constexpr int job = t / GA, u = t % GA;
                  if constexpr (u == 74) {
                    const int a = LDS_ST + w * 128 + (lane & 31) * 4;
                    k1[job] = lds1f(L, a); den2[job] = lds1f(L, a + 512); mf = mfrag(L, hh ? LDS_TAG + 8 : a + 1024);
                  }
                  if constexpr (u == 79) k1[job] *= -LOG2E;
                  if constexpr ((t >= 12 && t < 16) || (t >= 24 && t < 32) || (t >= 48 && t < 64)) {
                    constexpr int n = t < 16 ? t - 8 : t < 32 ? t - 16 : t - 32;
                    constexpr bool first = t == 12 || t == 24 || t == 48;
                    cm0 = umax3(first ? 0u : cm0, fr[n][0], fr[n][1]); cm1 = umax3(first ? 0u : cm1, fr[n][2], fr[n][3]);
                    asm volatile("" : "+v"(cm0), "+v"(cm1));
                  }
                }
                if constexpr ((t >= DA - 8 && t < DA) || (t >= DA + 8 && t < DA + 16) || (t >= DA + 26 && t < DA + 32) || (t >= DA + 38 && t < DA + 44)) {
                  constexpr int n = 92 + (t < DA ? t - DA + 8 : t < DA + 16 ? t - DA : t < DA + 32 ? t - DA - 10 : t - DA - 16);
                  constexpr bool first = t == DA - 8 || t == DA + 8 || t == DA + 26 || t == DA + 38;
                  cm0 = umax3(first ? 0u : cm0, fr[n][0], fr[n][1]); cm1 = umax3(first ? 0u : cm1, fr[n][2], fr[n][3]);
                  asm volatile("" : "+v"(cm0), "+v"(cm1));
                }
                if constexpr (TAB.ld[t][0] >= 0) vload(IC<TAB.ld[t][0]>{});
                if constexpr (TAB.ld[t][1] >= 0) vload(IC<TAB.ld[t][1]>{});
                if constexpr (TAB.ep[t] >= 0) epi(IC<TAB.ep[t]>{});
                if constexpr (t == DB + 58 || t == DB + 59 || t == DA + 58 || t == DA + 59)
                  st16(rs, pk24(dd[0][t & 1], tag), lo + (t & 1) * 1024, pso + (t < DA ? 32 * 8192 : 0));
                if constexpr (t == DA + 1 || t == DA + 2) *(LAS f32x4*)(L + LDS_QL + w * 2048 + (t & 1) * 1024 + lo) = dd[1][t & 1];
              }
            });
        CST();
        asm volatile("s_nop 7" : "+v"(dd[1][0]), "+v"(dd[1][1]));
        *(LAS f32x4*)(L + LDS_QL + w * 2048 + lo) = dd[1][0];
        *(LAS f32x4*)(L + LDS_QL + w * 2048 + 1024 + lo) = dd[1][1];
        BAR();
      }
    }
  }
  } else {
    {
    const int layer = 0; const unsigned lay0 = LAY_OFF;
    COORDS();
    SETPRIO(3);
    Retile rt;
    LAS unsigned char* tb = L + LDS_TB + w * 8192;
    Stats st;
    if (w == 0) {
      const int fi = lane >> 3, i8 = lane & 7, F = sopq(6 * sp) + (fi < 6 ? fi : 0);
      const int ks = F < 36 ? F / 3 : F < 44 ? (F - 12) >> 1 : F - 32, m = F < 36 ? (F - 3 * ks + 1) % 3 : F < 44 ? 1 + (F & 1) : 0;
      rt.load(wqkv, 3 * D_ * D_, __mul24(32 * ks + 8 * (i8 >> 1), 3 * D_) + m * D_ + 16 * c + 8 * (i8 & 1), 3 * D_, 4);
      const unsigned ep = __hip_atomic_load(fg + FL_EP * 32, RLX_AGENT);
      v4u mk[2];
#pragma unroll
      for (int k = 0; k < 8; ++k) {
        const int kq = ((2 * k) & 3) + 8 * ((2 * k) >> 2) + 4 * hh, ql = lane & 31;
        mk[k >> 2][k & 3] = (kq <= ql ? 0xffffu : 0u) | (kq + 1 <= ql ? 0xffff0000u : 0u);
      }
      *(LAS v4u*)(L + LDS_MK + lo) = mk[0];
      *(LAS v4u*)(L + LDS_MK + 1024 + lo) = mk[1];
      if (sp == 0) {
#pragma unroll
        for (int i = 0; i < 9; ++i) *(LAS v4u*)(L + LDS_VL + (i < 8 ? i * 1024 + lo : 8192 + t16 * 16)) = (v4u){0u, 0u, 0u, 0u};
      }
      const unsigned one = (sp == 0 && lane < 16) ? 0u : 0x3f803f80u;
      if (lane < 32) *(LAS v4u*)(L + LDS_VL + lane * VBLK + 512) = (v4u){one, one, one, one};
      PIN();
      rt.lines(tb, lane, i8, fi < 6 ? WQKV_OFF + c * 49152 + F * 1024 + i8 * 128 : NONE);
      rt.out(rs);
      tag = __builtin_amdgcn_readfirstlane(ep) + 1u;
      if (lane < 2) *(LAS v4u*)(L + LDS_TAG + lo) = (v4u){lane ? 0u : tag, 0u, 0u, 0u};
      DRAIN();
      HBAR();
      FLAG(0, FL_XB + sp * 32 + c);
      poll((gu32*)ws + (FL_XB + (lane < 32 ? sp * 32 + lane : (lane & 7) * 32 + c)) * 32, lane < 40, tmo, tag);
      st.load(rs, lane, lay + L_X + MB2 + (8 * sp + 4) * 4096);
    } else {
      const int id = 2 * sp + rh + 16 * (w - 1);
      const int t = 64 * id + lane, mo = t / 14, ch = t - __mul24(14, mo), mat = mo >> 6, o = mo & 63, c7 = ch >= 7 ? ch - 7 : ch, j = c7 >> 1, kap = o >> 1;
      const int dj = 4 * (id < 35 ? id - 28 : id - 35) + g4, dn = dj < 16 ? 16 * (n32 & 1) + dj : dj < 24 ? 16 + 8 * (n32 & 1) + dj : 24 + 4 * (n32 & 1) + dj;
      const int slot = (lane >> 2) & 3, m4 = lane & 3, q = (id < 42 ? 4 * dn : 16 * ((id - 42) & 3) + 4 * g4) + slot;
      const int cq = q < 128 ? q >> 2 : q < 192 ? (q - 128) >> 1 : q - 192, r = q < 128 ? q & 3 : q < 192 ? 4 + (q & 1) : 6;
      __builtin_amdgcn_s_sleep(SL0);
      rt.load(id < 14 ? wg : id < 28 ? wu : id < 42 ? wd : wo, id < 42 ? D_ * F_ : D_ * D_,
              id < 28 ? 8 * o * F_ + 112 * n32 + 8 * ch : (id < 42 ? __mul24(56, cq) + (r < 6 ? 16 * (r >> 1) + 4 * (r & 1) : 48) : 16 * (q >> 1) + 4 * (q & 1)) * D_ + 32 * (id < 35 ? n32 ^ 1 : n32) + 8 * m4,
              id < 28 ? F_ : D_, (id < 28 || (id < 42 && r == 6)) ? 4 : 8);
      HBAR();
      if (id < 28) {
        const int cell = kap < 16 ? 7 * (kap >> 1) + (j < 3 ? 3 * (kap & 1) + j : 6) : 56 + 16 * j + (j < 3 ? kap - 16 : (kap - 16) >> 1);
        const int rho = j < 3 ? 2 * mat + (c7 & 1) + 4 * (o & 1) : 4 * (kap & 1) + 2 * (o & 1) + mat;
        rt.lines(tb, lane, j < 3 ? rho : 0, WGU_OFF + (2 * n32 + (ch >= 7)) * 114688 + cell * 1024 + rho * 128);
      } else {
        rt.put(tb, g4 * 2048, 1024, 4 * m4 + 16 * slot, 0);
        asm volatile("s_waitcnt lgkmcnt(0)" ::: "memory");
#pragma unroll
        for (int i = 0; i < 8; ++i) {
          rt.in[i] = lds16(tb, i * 1024 + lo);
          rt.d[i] = (id < 42 ? WDN_OFF + n32 * 114688 + (id - 28) * 8192 : id < 46 ? WO_OFF + n32 * 32768 + (id - 42) * 8192 : NONE) + i * 1024 + lo;
        }
      }
#pragma unroll 1
      for (int it = w - 1; it < 16; it += 3) blank(it, lay);
    }
    HBAR();
    GETTAG();
    if (w) {
      const int l = w - 1;
      dma<4>(rs, L + l * 4096, lo, WQKV_OFF + c * 49152 + l * 4096);
      dma<7>(rs, L + (12 + 7 * l - (l >> 1)) * 1024, lo, WQKV_OFF + c * 49152 + (12 + 7 * l - (l >> 1)) * 1024);
      wait_vm<7>();
    }
    HOLD(0);
    HBAR();
    if (w) { dma<6>(rs, L + (27 + 5 * w) * 1024, lo, WQKV_OFF + c * 49152 + (27 + 5 * w) * 1024);
      rt.out(rs); wait_vm<14>();
    }
    else {
      st.all(L, lane);
      PIN();
      FLAG(0, FL_XA + sp * 32 + c);
      const int i = lane & 31;
      const int line = i < 8 ? FL_KV + (8 * sp + i) * 32 + c : i < 16 ? FL_O + (8 * sp + i - 8) * 32 + c
                     : i < 20 ? FL_X1B + (8 * sp + 4 + 2 * rh + ((i >> 1) & 1)) * 32 + 2 * n32 + (i & 1) : FL_X1A + (i - 20 < 6 ? i - 20 : 5) * 256 + sp * 32 + c;
      touch(rs, hh * FLAG_LAYER + line * 128, 0);
    }
    }
  for (int layer = 0; layer < 2; ++layer) {
    const bool last = layer == 1;
    const unsigned lay0 = LAY_OFF + layer * LAY_BYTES;
    auto ldw = [&](auto nc, unsigned src, int cell) {
      constexpr int N = nc.value, K = (N + 2) / 3;
      const int l = sopq(w0) - 5, c1 = cell + (l * K + K > N ? N - K : l * K);
      dma<K>(rs, L + c1 * 1024, lane_id() * 16, src + c1 * 1024);
    };
    auto ld_gu = [&](int s) { ldw(IC<14>{}, WGU_OFF + sopq(c0) * 114688, 14 * s); };
    {
      COORDS();
      const bool loader = w != 0;
      auto cs_qkv = [&]() {
        const unsigned one = (unsigned)vopq(0x3f803f80);
        const v4u ones = (v4u){one, one, one, one};
        f32x4 a = zero4();
        v4u f[16];
        const int fa = (w - 1) * 1024 + lsw, fb = (w < 3 ? 35 + w : 44) * 1024 + lsw, fs = w < 3 ? 2048 : 1024;
#pragma unroll
        for (int ks = 0; ks < 16; ++ks) asm volatile("ds_read_b128 %0, %1" : "=v"(f[ks]) : "v"(ks < 12 ? fa + ks * 3072 : fb + (ks - 12) * fs) : "memory");
        asm volatile("s_waitcnt lgkmcnt(0)" : "+v"(f[0]), "+v"(f[1]), "+v"(f[2]), "+v"(f[3]), "+v"(f[4]), "+v"(f[5]), "+v"(f[6]), "+v"(f[7]));
        asm volatile("" : "+v"(f[8]), "+v"(f[9]), "+v"(f[10]), "+v"(f[11]), "+v"(f[12]), "+v"(f[13]), "+v"(f[14]), "+v"(f[15]));
#pragma unroll
        for (int ks = 0; ks < 16; ++ks) a = mfma16(f[ks], ones, a);
        if (t16 == 0) {
#pragma unroll
          for (int r = 0; r < 4; ++r) cs_put(L, (w % 3) * 16 + 4 * g4 + r, a[r]);
        }
      };
      const unsigned wob = WO_OFF + n32 * 32768 - LDS_WO;
      if (layer == 0) {
        HBARS(false);
        if (loader) wait_vm<8>();
        HBAR();
        if (loader) { cs_qkv(); DRAIN(); }
        HBAR();
        if (!loader) FLAG(FLAG_G, FL_WR + n32 * 16 + 2 * sp + rh);
      } else {
        HBAR();
        if (loader) ldw(IC<16>{}, wob, 80);
        HBAR();
        if (loader) ldw(IC<16>{}, wob, 96);
      }
      {
        v4u kv[4];
        auto ldkv = [&]() {
          const unsigned so = sp ? lay + ((8 * sp - 5 + w) * 32 + c) * 1024 : NONE, s2 = w == 1 ? so + 3 * 32768 : NONE;
          kv[0] = ld16(rs, lo, so + L_K); kv[1] = ld16(rs, lo, so + L_V); kv[2] = ld16(rs, lo, s2 + L_K); kv[3] = ld16(rs, lo, s2 + L_V);
          PIN();
          DBGC(0);
        };
        if (!loader) {
          Stats st;
          const unsigned bl = lane < 16 ? FLAG_G + (FL_WR + n32 * 16 + lane) * 128 : (FL_XA + (sp ? sp - 1 : 0) * 32 + c) * 128;
          unsigned spins = 0;
          do { st.load(rs, lane, lay + L_X + MB2 + 8 * sp * 4096); DBGC(0); } while (missing<16>((const v4u*)st.p) && ++spins < DATA_CAP);
          const unsigned pv = __builtin_amdgcn_raw_buffer_load_b32(rs, bl, last ? NONE : 0u, AUX_SC1);
          PIN();
          st.all(L, lane, LDS_KL);
          if (!last && !__all(pv == tag)) poll((gu32*)(ws + bl), lane <= 16, tmo, tag);
        }
        HBARS(false);
        SETTLE();
        if (loader) {
          ldkv();
          ldw(IC<32>{}, last ? WGU_OFF + c * 114688 : wob, last ? 48 : 80);
          if (__builtin_expect(missing<4>(kv), 0)) {
            unsigned spins = 0;
            do ldkv(); while (missing<4>(kv) && ++spins < DATA_CAP);
          }
          const int va = LDS_VL + hh * VBLK + (lane & 31) * 16;
          asm volatile("ds_write_b128 %0, %1\n\tds_write_b128 %2, %3" ::"v"(LDS_KL + (3 + w) * 1024 + lo), "v"(kv[0]), "v"(va + (6 + 2 * w) * VBLK), "v"(kv[1]) : "memory");
          if (w == 1) asm volatile("ds_write_b128 %0, %1\n\tds_write_b128 %2, %3" ::"v"(LDS_KL + 7 * 1024 + lo), "v"(kv[2]), "v"(va + 14 * VBLK), "v"(kv[3]) : "memory");
        }
      }
      if (!last) HOLD(1);
      HBAR();
      CST(); CST();
      if (loader) {
        ld_gu(0); ld_gu(1); ld_gu(2); ldw(IC<6>{}, WGU_OFF + c * 114688, 42);
        if (!last) { ldw(IC<8>{}, WGU_OFF + c * 114688, 48); ld_gu(4); ldw(IC<10>{}, WGU_OFF + c * 114688, 70); wait_vm<29>(); }
        else wait_vm<28>();
        if (lane == 0) asm volatile("ds_add_u32 %0, %1" ::"v"(LDS_TAG + 12), "v"(1) : "memory");
      }
      if (sp > 0) {
        LAS unsigned* hw = (LAS unsigned*)(L + LDS_TAG + 4);
        CST();
        if (!loader) {
          v4u kv[8];
          unsigned spins = 0;
          do {
#pragma unroll
            for (int t = 0; t < 4; ++t) {
              const unsigned so = lay + ((8 * sp - 8 + t) * 32 + c) * 1024;
              kv[t] = ld16(rs, lo, so + L_K); kv[4 + t] = ld16(rs, lo, so + L_V);
            }
            PIN();
            DBGC(1);
          } while (missing<8>(kv) && ++spins < DATA_CAP);
#pragma unroll
          for (int t = 0; t < 4; ++t) {
            *(LAS v4u*)(L + LDS_KL + t * 1024 + lo) = kv[t];
            *(LAS v4u*)(L + LDS_VL + (2 * t + hh) * VBLK + (lane & 31) * 16) = kv[4 + t];
          }
          asm volatile("s_waitcnt lgkmcnt(0)" ::: "memory");
          __hip_atomic_store(hw, (unsigned)layer + 1u, __ATOMIC_RELAXED, __HIP_MEMORY_SCOPE_WORKGROUP);
        } else {
          unsigned v;
          do {
            __builtin_amdgcn_s_sleep(1);
            asm volatile("ds_read_b32 %0, %1\n\ts_waitcnt lgkmcnt(0)" : "=v"(v) : "v"(LDS_TAG + 4) : "memory");
          } while ((unsigned)__builtin_amdgcn_readfirstlane(v) != (unsigned)layer + 1u);
        }
      } else CST();
      CST();
      if (layer == 0 && !loader) touch(rs, LAY_OFF + L_O + lane * TSTR + c * 4096 + sp * 128, 0);
      SETPRIO(APRIO);
    }
    attention(layer, lay0);
    {
      COORDS();
      const bool poller = w == 0, loader = w != 0;
      const unsigned wq = WQKV_OFF + c * 49152, wb = WGU_OFF + c * 114688, wdn = WDN_OFF + n32 * 114688;
      unsigned pv = 0;
#define PEEK(blk, line) pv = __hip_atomic_load((gu32*)(ws + (blk)) + ((line) + (lane & 31)) * 32, RLX_AGENT)
#define GATE(blk, line) do { if (!__all(pv == tag)) POLL(blk, line, 32); } while (0)
      f32x4 ca = zero4();
      auto cs_gu = [&](int cell, int n2, bool put) {
        const unsigned one = (unsigned)vopq((w == 3 || t16 == (g4 & 1)) ? 0x3f803f80 : 0);
        const int a = cell * 1024 + (w < 3 ? lsw : lo), d2 = n2 == 1 ? 2048 : 7168;
        v4u f[8];
#pragma unroll
        for (int i = 0; i < 8; ++i) asm volatile("ds_read_b128 %0, %1" : "=v"(f[i]) : "v"(a + (i >> 1) * d2 + (i & 1) * n2 * 1024) : "memory");
        asm volatile("s_waitcnt lgkmcnt(0)" : "+v"(f[0]), "+v"(f[1]), "+v"(f[2]), "+v"(f[3]), "+v"(f[4]), "+v"(f[5]), "+v"(f[6]), "+v"(f[7]));
#pragma unroll
        for (int i = 0; i < 8; ++i) ca = mfma16(f[i], (v4u){one, one, one, one}, ca);
        if (put && t16 < (w < 3 ? 2 : 1)) {
#pragma unroll
          for (int r = 0; r < 4; ++r) cs_put(L, 48 + 32 * w + 16 * t16 + 4 * g4 + r, ca[r]);
        }
      };
      if (!last) {
        unsigned v;
        if (loader && lane == 0) asm volatile("ds_add_u32 %0, %1" ::"v"(LDS_TAG + 16), "v"(1) : "memory");
        do asm volatile("ds_read_b32 %0, %1\n\ts_waitcnt lgkmcnt(0)" : "=v"(v) : "v"(LDS_TAG + 16) : "memory");
        while ((unsigned)__builtin_amdgcn_readfirstlane(v) != 3u);
        for (int q = 0; q < 4 - w; ++q) {
          if (w == 3) cs_gu(6, 28, false);
          else cs_gu(q < 2 ? 28 * q + w : 40 + 16 * w + 8 * q, q < 2 ? 3 : 1, q == 3);
        }
      }
      auto tail = [&](auto pc) {
        constexpr bool P = pc.value;
        const int TD = 8 * sp + 4 + 2 * rh + (w >> 1), SD = 2 * TD + (w & 1);
        const unsigned xn = last ? NONE : lay + LAY_BYTES + L_X;
        const rsrc_t ro = last ? __builtin_amdgcn_make_buffer_rsrc((void*)out, 0, (int)MB2, 0x00020000) : rs;
        const unsigned ob = last ? SD * 16384 + n32 * 64 : xn + (SD * 16 + n32) * 1024, sb = xn + MB2 + TD * 4096 + n32 * 256 + (w & 1) * 128;
        const unsigned ov = lo + ((t16 * 1008 - g4 * 240) & -layer), sv = g4 ? NONE : t16 * 8;
        v4u pp[2], res;
        Stats st;
        auto ldp = [&](int pa) {
          const unsigned so = lay + L_P + ((2 * sp + 1 - pa) * 32 + c) * 8192 + w * 2048;
          pp[0] = ld16(rs, lo, so); pp[1] = ld16(rs, lo + 1024, so);
          PIN();
          DBGC(4 + pa);
        };
        auto ldr = [&](int pa) { res = ld16(rs, g4 * 512 + t16 * 16, lay + L_X1 + ((TD - 4 * pa) * 32 + 2 * n32) * 1024 + (w & 1) * 256); };
        auto finish = [&](int pa) {
          f32x4 a0 = lds4f(L, LDS_QL + w * 2048 + lo), a1 = lds4f(L, LDS_QL + w * 2048 + 1024 + lo);
          asm volatile("" : "+v"(a0), "+v"(a1));
          ldp(pa);
          if (P && pa) st.load(rs, lane, xn + MB2 + (8 * sp + 4) * 4096);
          if (pa && !last) { if (!P) wait_vm<2>(); HBARS(false); }
          if (!pa) { HBAR(); if (!P) ldw(IC<22>{}, last ? NONE : wq, 0); }
          TR();
          if (__builtin_expect(!__all(pp[0][3] == tag && pp[1][3] == tag), 0)) {
            unsigned spins = 0;
            do ldp(pa); while (!__all(pp[0][3] == tag && pp[1][3] == tag) && ++spins < DATA_CAP);
          }
          TR();
          a0 += un24(pp[0]); a1 += un24(pp[1]);
          Fin fin;
          static_for<8>([&](auto k) { fin.template chunk<k.value>(a0, a1, res, ro, ov, ob - pa * 8 * 16384, rs, sv, sb - pa * 4 * 4096); });
          TR();
        };
        SETPRIO(3);
        ldr(0);
        HBARS(false);
        HBARS(false);
        finish(0);
        HBARS(false);
        if (!P && !last) ldw(IC<26>{}, wq, 22);
        ldr(1);
        unsigned ev[4];
        const bool adv = last && blockIdx.x == 0;
        if (P) {
#pragma unroll
          for (int q = 0; q < 4; ++q) ev[q] = __builtin_amdgcn_raw_buffer_load_b32(rs, lane * 128, adv ? flo + (FL_HB + 64 * q) * 128 : NONE, AUX_SC1);
        }
        if (last) HOLD(3);
        HBAR();
        if (!P) wait_vm<10>();
        CST();
        if (!last) HOLD(2);
        HBAR();
        finish(1);
        if (P && !last) {
          if (__builtin_expect(missing<16>((const v4u*)st.p), 0)) {
            unsigned spins = 0;
            do { st.load(rs, lane, xn + MB2 + (8 * sp + 4) * 4096); DBGC(3); } while (missing<16>((const v4u*)st.p) && ++spins < DATA_CAP);
          }
          st.all(L, lane);
        }
        if (P && adv) {
          if (!__all(ev[0] == tag && ev[1] == tag && ev[2] == tag && ev[3] == tag))
            for (int q = 0; q < 4; ++q) POLL(flo, FL_HB + 64 * q, 64);
          if (lane == 0) __hip_atomic_store(fg + FL_EP * 32, tag, RLX_AGENT);
        }
      };
      SETPRIO(3);
      if (poller) {
        Stats st;
        const int h2l = FL_HB + (sp ? sp - 1 : 0) * 32 + c;
        SETPRIO(1);
        if (last) HOLD(4);
        HBAR();
        static_for<7>([&](auto nc) {
          constexpr int n = nc.value + 1;
          if (n <= 2) st.load(rs, lane, lay + L_X1 + MB2 + (8 * sp + 4) * 4096, 8 * n - 8, 8 * n);
          if (n == 3) { FLAG(flo, FL_X1A + sp * 32 + c); st.part1(); }
          if (n == 4) {
            if (missing<16>((const v4u*)st.p)) {
              unsigned spins = 0;
              do { st.load(rs, lane, lay + L_X1 + MB2 + (8 * sp + 4) * 4096); DBGC(2); } while (missing<16>((const v4u*)st.p) && ++spins < DATA_CAP);
              st.part1();
            }
            st.part2();
          }
          if (n == 5) st.part3(L, lane);
          if (n == 6) PEEK(flo, FL_X1A + sp * 32);
          if (n == 7) GATE(flo, FL_X1A + sp * 32);
          HBARS(n == 3 || n == 7);
        });
        st.load(rs, lane, lay + L_X1 + MB2 + 8 * sp * 4096);
        st.all(L, lane);
        static_for<6>([&](auto nc) {
          constexpr int n = nc.value + 2;
          if (n == 4) FLAG(flo, FL_HB + sp * 32 + c);
          if (n == UG - 1) PEEK(flo, FL_HB + sp * 32);
          if (n == UG) GATE(flo, FL_HB + sp * 32);
          HBARS(n == 2 || n == 7);
        });
        static_for<8>([&](auto nc) {
          constexpr int n = nc.value;
          if (n == 4) pv = __hip_atomic_load(fl + h2l * 32, RLX_AGENT);
          if (n == 7) { if (!__all(pv == tag)) poll(fl + h2l * 32, true, tmo, tag); }
          else HBARS(n == 0 || n == 3 || n == 6);
        });
        tail(IC<1>{});
      } else {
        DRAIN();
        HBAR();
        static_for<7>([&](auto nc) {
          constexpr int n = nc.value + 1;
          if (n >= 3) blank(30 + 3 * n + w, 30 + 3 * n + w > 53 ? NONE : lay);
          blank(6 * n - 7 + w, last ? NONE : LAY_OFF + LAY_BYTES);
          if (n < 7) blank(6 * n - 4 + w, last ? NONE : LAY_OFF + LAY_BYTES);
          if (n <= 3) ldw(IC<9>{}, wb, 71 + 9 * n);
          if (n == 4) { ldw(IC<5>{}, wb, 107); wait_vm<16>(); }
          if (n == 5) { ldw(IC<16>{}, wdn - 16384, 112); if (!last && w == 1) cs_gu(80, 1, true); wait_vm<20>(); }
          if (n == 6) { if (!last && w == 2) cs_gu(88, 1, false); wait_vm<12>(); }
          if (n == 7 && !last && w >= 2) cs_gu(80 + 8 * w, 1, true);
          HBARS(n == 3 || n == 7);
        });
        static_for<6>([&](auto nc) {
          constexpr int n = nc.value + 2;
          if (n == 3) wait_vm<0>();
          if (n > 3) ldw(IC<(n < 7 ? 21 : 16)>{}, wdn, 21 * n - 84);
          HBARS(n == 2 || n == 7);
        });
        static_for<8>([&](auto nc) {
          constexpr int n = nc.value;
          if (n == 0) ldw(IC<15>{}, wdn, 79);
          if (n == 1) ldw(IC<2>{}, wdn, 94);
          if (n == 2) ldw(IC<16>{}, NONE, 96);
          if (n < 7) wait_vm<(n == 0 ? 25 : n == 1 ? 19 : n == 2 ? 25 : n == 3 ? 18 : n == 4 ? 12 : n == 5 ? 7 : 6)>();
          if (n < 7) HBARS(n == 0 || n == 3 || n == 6);
        });
        tail(IC<0>{});
      }
    }
  }
  }
}

}

extern "C" void kernel_launch(void* const* d_in, const int* in_sizes, int n_in, void* d_out, int out_size, void* d_ws, size_t ws_size,
                              hipStream_t stream) {
  (void)in_sizes; (void)n_in; (void)out_size; (void)ws_size;
  (void)hipFuncSetAttribute((const void*)k_main, hipFuncAttributeMaxDynamicSharedMemorySize, LDS_BYTES);
  k_main<<<256, 512, LDS_BYTES, stream>>>((const u16*)d_in[0], (const u16*)d_in[1], (const u16*)d_in[2], (const u16*)d_in[4], (const u16*)d_in[5],
                                          (const u16*)d_in[6], (unsigned char*)d_ws, (u16*)d_out);
}
```

```cpp
#include <hip/hip_runtime.h>
#include <stdint.h>
#include <utility>

#define LAS __attribute__((address_space(3)))
#define GAS __attribute__((address_space(1)))
#define DI __device__ __forceinline__
#define RLX_AGENT __ATOMIC_RELAXED, __HIP_MEMORY_SCOPE_AGENT
typedef unsigned short u16;
typedef unsigned v4u __attribute__((ext_vector_type(4)));
typedef unsigned v2u __attribute__((ext_vector_type(2)));
typedef float f32x2 __attribute__((ext_vector_type(2)));
typedef float f32x4 __attribute__((ext_vector_type(4)));
typedef float f32x16 __attribute__((ext_vector_type(16)));
typedef __bf16 bf16x2 __attribute__((ext_vector_type(2)));
typedef __bf16 bf16x8 __attribute__((ext_vector_type(8)));
typedef GAS unsigned gu32;
typedef GAS unsigned long long gu64;

namespace {

constexpr int D_ = 512, F_ = 1792;
constexpr unsigned MB2 = 2097152u, STB = 262144u;
constexpr unsigned FLAG_LAYER = 1048576u, FLAG_G = 2 * FLAG_LAYER, FLAG_BYTES = FLAG_G + 65536;
constexpr unsigned WQKV_OFF = FLAG_BYTES;
constexpr unsigned WO_OFF = WQKV_OFF + 32 * 49152;
constexpr unsigned WGU_OFF = WO_OFF + 16 * 32768;
constexpr unsigned WDN_OFF = WGU_OFF + 32 * 114688;
constexpr unsigned LAY_OFF = WDN_OFF + 16 * 114688;
constexpr unsigned L_K = 0, L_V = MB2, L_O = 2 * MB2, L_X1 = 3 * MB2, L_X = 4 * MB2 + STB, L_H = 5 * MB2 + 2 * STB;
constexpr unsigned L_P = L_H + 128 * 57344;
constexpr unsigned LAY_BYTES = L_P + 2 * MB2;
constexpr unsigned WS_BYTES = LAY_OFF + 2 * LAY_BYTES;
constexpr unsigned NONE = 0x40000000u;
constexpr int FL_KV = 0, FL_O = 2048, FL_X1B = 4096, FL_X1A = 6144, FL_HB = 6400, FL_HA = 6656, FL_XB = 6912, FL_XA = 7168, FL_HA2 = 7424;
constexpr int FL_WR = 0, FL_EP = 256, FL_TMO = 257;
constexpr int VBLK = 528;
constexpr int LDS_KL = 114688, LDS_VL = LDS_KL + 16384, LDS_QL = LDS_VL + 32 * VBLK, LDS_CS = LDS_QL + 8192, LDS_ST = LDS_CS + 1536;
constexpr int LDS_MK = LDS_ST + 1536, LDS_TAG = LDS_MK + 2048;
constexpr int LDS_WO = 81920, LDS_TB = 49152;
constexpr int LDS_BYTES = 163840;
#define TSTR 1048576u
#define CODE_BYTES_ 74240
constexpr int CODE_BYTES = CODE_BYTES_, CODE_LINES = (CODE_BYTES + 4095) / 4096;
constexpr unsigned SPIN_CAP = 1u << 16;
constexpr int AUX_SC1 = 16;
constexpr float LOG2E = 1.4426950408889634f;

DI f32x16 mfma(v4u a, v4u b, f32x16 c) {
  return __builtin_amdgcn_mfma_f32_32x32x16_bf16(__builtin_bit_cast(bf16x8, a), __builtin_bit_cast(bf16x8, b), c, 0, 0, 0);
}
DI f32x4 mfma16(v4u a, v4u b, f32x4 c) {
  return __builtin_amdgcn_mfma_f32_16x16x32_bf16(__builtin_bit_cast(bf16x8, a), __builtin_bit_cast(bf16x8, b), c, 0, 0, 0);
}
DI unsigned pk2(float lo, float hi) {
  f32x2 p = {lo, hi};
  return __builtin_bit_cast(unsigned, __builtin_convertvector(p, bf16x2));
}
DI float bfr(float x) { return __uint_as_float(pk2(x, 0.f) << 16); }
DI float bflo(unsigned u) { return __uint_as_float(u << 16); }
DI float bfhi(unsigned u) { return __uint_as_float(u & 0xffff0000u); }
DI float amul(float a, float b) { float d; asm("v_mul_f32 %0, %1, %2" : "=v"(d) : "v"(a), "v"(b)); return d; }
DI float afma(float a, float b, float c) { float d; asm("v_fma_f32 %0, %1, %2, %3" : "=v"(d) : "v"(a), "v"(b), "v"(c)); return d; }
DI float aadd(float a, float b) { float d; asm("v_add_f32 %0, %1, %2" : "=v"(d) : "v"(a), "v"(b)); return d; }
DI float asub(float a, float b) { float d; asm("v_sub_f32 %0, %1, %2" : "=v"(d) : "v"(a), "v"(b)); return d; }
DI float xhalf_sum(float v) {
  float t;
  asm("v_mov_b32 %1, %0\n\ts_nop 1\n\tv_permlane32_swap_b32 %0, %1\n\tv_add_f32 %0, %0, %1" : "+v"(v), "=&v"(t));
  return v;
}
DI float xrow_sum(float v) {
  float t;
  asm("v_mov_b32 %1, %0\n\ts_nop 1\n\tv_permlane16_swap_b32 %0, %1\n\tv_add_f32 %0, %0, %1" : "+v"(v), "=&v"(t));
  return v;
}
DI float xhalf_max(float v) {
  auto r = __builtin_amdgcn_permlane32_swap(__float_as_uint(v), __float_as_uint(v), false, false);
  return fmaxf(__uint_as_float(r[0]), __uint_as_float(r[1]));
}
DI void swap16(unsigned& a, unsigned& b) { asm("s_nop 1\n\tv_permlane16_swap_b32 %0, %1" : "+v"(a), "+v"(b)); }
DI void swap32(unsigned& a, unsigned& b) { asm("s_nop 1\n\tv_permlane32_swap_b32 %0, %1" : "+v"(a), "+v"(b)); }
DI f32x16 splat16(float v) {
  f32x16 z;
#pragma unroll
  for (int i = 0; i < 16; ++i) z[i] = v;
  return z;
}
DI f32x4 zero4() { return (f32x4){0.f, 0.f, 0.f, 0.f}; }
DI int vopq(int x) { asm volatile("" : "+v"(x)); return x; }
DI int lane_id() { int l; asm volatile("v_mbcnt_lo_u32_b32 %0, -1, 0\n\tv_mbcnt_hi_u32_b32 %0, -1, %0" : "=v"(l)); return l; }
template <int LN> DI unsigned wrl(unsigned v, unsigned s) { asm volatile("v_writelane_b32 %0, %1, %2" : "+v"(v) : "s"(s), "i"(LN)); return v; }
template <class T> DI void undef(T& x) { asm volatile("" : "=v"(x)); }
DI int sopq(int x) { asm volatile("" : "+s"(x)); return x; }
#define SWZ(l) ((l) ^ (((l) >> 3) & 7))
#define COORDS() \
  const int lane = lane_id(), lo = vopq(lane * 16), hh = lane >> 5, t16 = lane & 15, g4 = lane >> 4, lsw = SWZ(lane) * 16; \
  const int w = sopq(w0) & 3, sp = sopq(sp0), c = sopq(c0), n32 = c >> 1, rh = c & 1; \
  const unsigned lay = (unsigned)sopq((int)lay0); \
  const unsigned flo = sopq(layer) * FLAG_LAYER; \
  gu32* const fl = (gu32*)(ws + flo); \
  (void)lo; (void)hh; (void)t16; (void)g4; (void)lsw; (void)n32; (void)rh; (void)lay; (void)sp; (void)c; (void)w; (void)fl;
#define HBARS(c) asm volatile("s_waitcnt lgkmcnt(0)\n\ts_barrier" ::: "memory")
#define CST() do {} while (0)
#define DBGC(k) asm volatile("" ::: "memory")
#define HBAR() asm volatile("s_waitcnt lgkmcnt(0)\n\ts_barrier" ::: "memory")
#define TR() do {} while (0)
#define BAR() asm volatile("s_waitcnt lgkmcnt(0)\n\ts_barrier" ::: "memory")
#define BARX() asm volatile("s_barrier" ::: "memory")
#define DRAIN() asm volatile("s_waitcnt vmcnt(0)" ::: "memory")
#define SETTLE() __builtin_amdgcn_s_waitcnt(0x0f70)
#define PIN() __builtin_amdgcn_sched_barrier(0)
#define SETPRIO(n) __builtin_amdgcn_s_setprio(n)
template <int N> DI void wait_vm() { static_assert(N >= 0 && N < 64, ""); asm volatile("s_waitcnt vmcnt(%0)" ::"i"(N) : "memory"); }

typedef __amdgpu_buffer_rsrc_t rsrc_t;
DI v4u ld16(rsrc_t rs, unsigned voff, unsigned soff) { return __builtin_amdgcn_raw_buffer_load_b128(rs, voff, soff, AUX_SC1); }
DI f32x2 ld8f(rsrc_t rs, unsigned voff, unsigned soff) { return __builtin_bit_cast(f32x2, __builtin_amdgcn_raw_buffer_load_b64(rs, voff, soff, AUX_SC1)); }
DI void st16(rsrc_t rs, v4u v, unsigned voff, unsigned soff) {
  __builtin_amdgcn_raw_buffer_store_b128(v, rs, voff, soff, AUX_SC1);
  asm volatile("s_nop 1" ::"v"(v));
}
DI void st8(rsrc_t rs, v2u v, unsigned voff, unsigned soff) { __builtin_amdgcn_raw_buffer_store_b64(v, rs, voff, soff, AUX_SC1); }
DI void st4(rsrc_t rs, unsigned v, unsigned voff, unsigned soff) { __builtin_amdgcn_raw_buffer_store_b32(v, rs, voff, soff, AUX_SC1); }
DI v4u lds16(LAS unsigned char* L, int off) { return *(LAS v4u*)(L + off); }
DI f32x4 lds4f(LAS unsigned char* L, int off) { return *(LAS f32x4*)(L + off); }

DI void poll(gu32* word, bool active, gu32* tmo, unsigned tag) {
  for (unsigned spins = 0;;) {
    unsigned v = tag;
    if (active) v = __hip_atomic_load(word, RLX_AGENT);
    if (__all(v == tag)) break;
    ++spins;
    if (spins > SPIN_CAP) { __hip_atomic_store(tmo, tag, RLX_AGENT); break; }
    if ((spins & 255u) == 0u && (unsigned)__builtin_amdgcn_readfirstlane(__hip_atomic_load(tmo, RLX_AGENT)) == tag) break;
  }
  __builtin_amdgcn_fence(__ATOMIC_ACQUIRE, "wavefront");
}

template <int I> DI void dma1(rsrc_t rs, LAS unsigned char* dst, unsigned voff, unsigned soff) {
  __builtin_amdgcn_raw_ptr_buffer_load_lds(rs, (LAS void*)dst, 16, voff, soff, I * 1024, AUX_SC1);
}
template <int V> using IC = std::integral_constant<int, V>;
template <class FN, int... I> DI void static_for_(FN fn, std::integer_sequence<int, I...>) { (fn(IC<I>{}), ...); }
template <int N, class FN> DI void static_for(FN fn) { static_for_(fn, std::make_integer_sequence<int, N>{}); }
template <int N> DI void dma(rsrc_t rs, LAS unsigned char* dst, unsigned lo, unsigned src) {
  static_for<N>([&](auto i) { dma1<i.value & 3>(rs, dst + (i.value >> 2) * 4096, lo, src + (i.value >> 2) * 4096); });
}

struct Stats {
  f32x4 p[16];
  float m0, m1, q0, q1;
  DI void load(rsrc_t rs, int lane, unsigned base, int i0 = 0, int i1 = 16) {
#pragma unroll
    for (int i = i0; i < i1; ++i) p[i] = __builtin_bit_cast(f32x4, ld16(rs, (lane >> 4) * 4096 + (lane & 15) * 16 + i * 256, base));
    PIN();
  }
  DI void part1() {
    m0 = m1 = q0 = q1 = 0.f;
#pragma unroll
    for (int i = 0; i < 16; ++i) { m0 += p[i][0]; q0 += p[i][1]; m1 += p[i][2]; q1 += p[i][3]; }
    asm volatile("" : "+v"(m0), "+v"(m1), "+v"(q0), "+v"(q1));
  }
  DI void part2() {
    const float a0 = m0 * (1.0f / 16.0f), a1 = m1 * (1.0f / 16.0f);
    q0 *= 32.0f; q1 *= 32.0f;
#pragma unroll
    for (int i = 0; i < 16; ++i) { const float d0 = p[i][0] - a0, d1 = p[i][2] - a1; q0 += d0 * d0; q1 += d1 * d1; }
    asm volatile("" : "+v"(q0), "+v"(q1));
  }
  DI void all(LAS unsigned char* L, int lane, int dst = LDS_ST) { part1(); part2(); part3(L, lane, dst); }
  DI void part3(LAS unsigned char* L, int lane, int dst = LDS_ST) {
    const float den0 = bfr(__builtin_amdgcn_sqrtf(bfr(bfr(q0 * (1.0f / (32.0f * D_))) + 1e-5f)));
    const float den1 = bfr(__builtin_amdgcn_sqrtf(bfr(bfr(q1 * (1.0f / (32.0f * D_))) + 1e-5f)));
    *(LAS f32x2*)(L + dst + lane * 8) = (f32x2){__builtin_amdgcn_rcpf(den0), __builtin_amdgcn_rcpf(den1)};
    *(LAS f32x2*)(L + dst + 512 + lane * 8) = (f32x2){den0 * den0, den1 * den1};
    *(LAS v2u*)(L + dst + 1024 + lane * 8) = (v2u){pk2(m0 * (-1.0f / D_), m0 * (-1.0f / D_)), pk2(m1 * (-1.0f / D_), m1 * (-1.0f / D_))};
  }
};
DI float lds1f(LAS unsigned char* L, int off) { return *(LAS float*)(L + off); }
DI v4u mfrag(LAS unsigned char* L, int off) { const unsigned n = *(LAS unsigned*)(L + off); return (v4u){n, n, 0u, 0u}; }

constexpr int LA = 6;
template <int NF, class RD, class STEP>
DI void pipe(RD rd, STEP step) {
  v4u wf[8];
  static_for<LA>([&](auto i) { wf[i.value] = rd(i); });
  step(IC<-1>{}, wf[0], IC<1>{});
  PIN();
  static_for<NF>([&](auto fc) {
    constexpr int f = fc.value;
    if constexpr ((f & 1) == 0 && f + LA <= NF) __builtin_amdgcn_s_waitcnt(0xc07f | ((LA - 2) << 8));
    step(fc, wf[f & 7], IC<0>{});
    if constexpr (f + LA < NF) wf[(f + LA) & 7] = rd(IC<f + LA>{});
    step(fc, wf[f & 7], IC<1>{});
    PIN();
  });
}
DI v4u csfrag(LAS unsigned char* L, int entry) {
  const v2u t = *(LAS v2u*)(L + LDS_CS + entry * 8);
  return (v4u){t.x, t.y, 0u, 0u};
}
DI void cs_put(LAS unsigned char* L, int entry, float cs) {
  const float hi = bfr(cs), mid = bfr(cs - hi), lw = (cs - hi) - mid;
  *(LAS v2u*)(L + LDS_CS + entry * 8) = (v2u){pk2(hi, mid), pk2(lw, 0.f)};
}
DI unsigned mulpk(float a, float s0, float b, float s1) {
  unsigned d; float t;
  asm("v_mul_f32 %0, %2, %3\n\tv_mul_f32 %1, %4, %5\n\tv_cvt_pk_bf16_f32 %0, %0, %1" : "=&v"(d), "=&v"(t) : "v"(a), "v"(s0), "v"(b), "v"(s1));
  return d;
}
DI unsigned nos(unsigned v) { return v < 0xfffffffeu ? v : 0xfffffffeu; }
DI v4u nos4(v4u v) { return (v4u){nos(v[0]), nos(v[1]), nos(v[2]), nos(v[3])}; }
DI unsigned umax3(unsigned a, unsigned b, unsigned c) { const unsigned t = a > b ? a : b; return t > c ? t : c; }
template <int N> DI bool missing(const v4u* f, int step = 1) {
  unsigned m0 = 0, m1 = 0;
#pragma unroll
  for (int i = 0; i < N; ++i) { m0 = umax3(m0, f[i * step][0], f[i * step][1]); m1 = umax3(m1, f[i * step][2], f[i * step][3]); }
  return __builtin_amdgcn_ballot_w64((m0 > m1 ? m0 : m1) == 0xffffffffu) != 0;
}
constexpr unsigned DATA_CAP = 1u << 14;
DI v4u pk24(const f32x4& a, unsigned tag) {
  const v4u u = __builtin_bit_cast(v4u, a);
  return (v4u){__builtin_amdgcn_perm(u[0], u[3], 0x07060501u), __builtin_amdgcn_perm(u[1], u[3], 0x07060502u), __builtin_amdgcn_perm(u[2], u[3], 0x07060503u), tag};
}
DI f32x4 un24(const v4u& u) {
  return __builtin_bit_cast(f32x4, (v4u){u[0], u[1], u[2], __builtin_amdgcn_perm(u[2], __builtin_amdgcn_perm(u[1], u[0], 0x0c0c0400u), 0x0401000cu)});
}
DI v4u pack8(const float* y) { return (v4u){pk2(y[0], y[1]), pk2(y[2], y[3]), pk2(y[4], y[5]), pk2(y[6], y[7])}; }

struct Fin {
  float y[8], s;
  v4u o;
  v2u sq;
  template <int K, bool ST = true> DI void chunk(const f32x4& a0, const f32x4& a1, const v4u& r, rsrc_t ro, unsigned vo, unsigned so, rsrc_t rs, unsigned vs, unsigned ss) {
    if constexpr (K == 0) { y[0] = aadd(a0[0], bflo(r[0])); y[1] = aadd(a0[1], bfhi(r[0])); y[2] = aadd(a0[2], bflo(r[1])); y[3] = aadd(a0[3], bfhi(r[1])); }
    if constexpr (K == 1) { y[4] = aadd(a1[0], bflo(r[2])); y[5] = aadd(a1[1], bfhi(r[2])); y[6] = aadd(a1[2], bflo(r[3])); y[7] = aadd(a1[3], bfhi(r[3])); }
    if constexpr (K == 2) { o = nos4(pack8(y)); if constexpr (ST) st16(ro, o, vo, so); }
    if constexpr (K == 3) {
#pragma unroll
      for (int m = 0; m < 4; ++m) { y[2 * m] = bflo(o[m]); y[2 * m + 1] = bfhi(o[m]); }
    }
    if constexpr (K == 4) s = xrow_sum(aadd(aadd(aadd(y[0], y[1]), aadd(y[2], y[3])), aadd(aadd(y[4], y[5]), aadd(y[6], y[7]))));
    if constexpr (K == 5) {
      s = xhalf_sum(s);
      const float mu = s * (1.0f / 32.0f);
#pragma unroll
      for (int i = 0; i < 8; ++i) y[i] = asub(y[i], mu);
    }
    if constexpr (K == 6) {
      y[0] = amul(y[0], y[0]); y[1] = amul(y[1], y[1]);
#pragma unroll
      for (int i = 2; i < 8; ++i) y[i & 1] = afma(y[i], y[i], y[i & 1]);
    }
    if constexpr (K == 7) {
      sq = (v2u){nos(__float_as_uint(s)), nos(__float_as_uint(xhalf_sum(xrow_sum(aadd(y[0], y[1])))))};
      if constexpr (ST) st8(rs, sq, vs, ss);
    }
  }
};

constexpr unsigned long long low_mask(int r) {
  unsigned long long m = 0;
  for (int l = 0; l < 64; ++l)
    if ((r & 3) + 8 * (r >> 2) + 4 * (l >> 5) <= (l & 31)) m |= 1ull << l;
  return m;
}
DI float sel(unsigned long long mask, float set, float unset) {
  float d;
  asm("v_cndmask_b32_e64 %0, %1, %2, %3" : "=v"(d) : "v"(unset), "v"(set), "s"(mask));
  return d;
}
DI float max16(float m, const f32x16& s) {
#pragma unroll
  for (int r = 0; r < 16; r += 2) m = __builtin_fmaxf(__builtin_fmaxf(m, s[r]), s[r + 1]);
  return m;
}
DI v4u exp_pack(const f32x16& s, int h) {
  float pr[8];
#pragma unroll
  for (int r = 0; r < 8; ++r) pr[r] = __builtin_amdgcn_exp2f(s[8 * h + r]);
  return pack8(pr);
}
template <bool EXACT>
DI f32x16 attn_pass(LAS unsigned char* L, int lane, int tl, int nv) {
  const int lo = lane * 16;
  const int kb = LDS_KL + tl * 1024 + lo, kf0 = LDS_KL + (tl + 8) * 1024 + (lane >> 5) * 512;
  const v4u q = lds16(L, LDS_QL + tl * 1024 + lo);
  v4u kf[9];
  kf[8] = lds16(L, kb + 8192); kf[0] = lds16(L, nv <= 0 ? kb : kf0);
#pragma unroll
  for (int i = 7; i >= 1; --i) kf[i] = lds16(L, (nv <= i ? kb : kf0 - i * 1024) + i * 1024);
  f32x16 s[9], cm = splat16(0.f);
  if constexpr (EXACT) {
    s[8] = mfma(kf[8], q, splat16(0.f));
    s[0] = mfma(kf[0], q, splat16(0.f));
    s[7] = mfma(kf[7], q, splat16(0.f));
    s[6] = mfma(kf[6], q, splat16(0.f));
    asm volatile("s_nop 7\n\ts_nop 7" : "+v"(s[8]), "+v"(s[0]));
    PIN();
    static_for<16>([&](auto r) { s[8][r.value] = sel(low_mask(r.value), s[8][r.value], s[0][r.value]); });
    float m = max16(s[8][0], s[8]);
    PIN();
    static_for<7>([&](auto ic) {
      constexpr int i = 7 - ic.value;
      if constexpr (i - 2 >= 1) s[i - 2] = mfma(kf[i - 2], q, splat16(0.f));
      m = max16(m, s[i]);
      PIN();
    });
    cm = splat16(-xhalf_max(m));
  }
  const int vb = LDS_VL + 2 * tl * VBLK + ((lane & 16) ? 512 : (lane >> 5) * 256 + (lane & 15) * 16);
  s[8] = mfma(kf[8], q, cm);
  s[0] = mfma(kf[0], q, cm);
  s[7] = mfma(kf[7], q, cm);
  const v4u mk0 = lds16(L, LDS_MK + lo), mk1 = lds16(L, LDS_MK + 1024 + lo);
  v4u va = lds16(L, vb + 16 * VBLK), vc = lds16(L, vb + 17 * VBLK), v0a = lds16(L, vb), v0c = lds16(L, vb + VBLK);
  asm volatile("s_nop 7\n\ts_nop 7" : "+v"(s[8]), "+v"(s[0]));
  PIN();
  f32x16 o;
  {
    static_for<16>([&](auto r) { s[8][r.value] = sel(low_mask(r.value), s[8][r.value], s[0][r.value]); });
    const v4u p0 = exp_pack(s[8], 0), l0 = p0 & mk0;
    o = mfma(va, l0, splat16(0.f));
    o = mfma(v0a, p0 ^ l0, o);
    const v4u p1 = exp_pack(s[8], 1), l1 = p1 & mk1;
    o = mfma(vc, l1, o);
    o = mfma(v0c, p1 ^ l1, o);
    va = lds16(L, vb + 14 * VBLK); vc = lds16(L, vb + 15 * VBLK);
    PIN();
  }
  static_for<7>([&](auto ic) {
    constexpr int i = 7 - ic.value;
    if constexpr (i > 1) s[i - 1] = mfma(kf[i - 1], q, cm);
    PIN();
    o = mfma(va, exp_pack(s[i], 0), o);
    if constexpr (i > 1) va = lds16(L, vb + (2 * i - 2) * VBLK);
    o = mfma(vc, exp_pack(s[i], 1), o);
    if constexpr (i > 1) vc = lds16(L, vb + (2 * i - 1) * VBLK);
    PIN();
  });
  return o;
}
DI v4u attn_unit(LAS unsigned char* L, int lane, int tl, int nv) {
  f32x16 o = attn_pass<false>(L, lane, tl, nv);
  float big = __builtin_fabsf(o[0]);
#pragma unroll
  for (int r = 1; r < 8; ++r) big += __builtin_fabsf(o[r]);
  if (__builtin_expect(!__all(o[8] > 0x1p-30f && o[8] < 0x1p60f && big < 0x1p120f), 0)) {
    asm volatile("" ::: "memory");
    o = attn_pass<true>(L, lane, tl, nv);
  }
  const float rl = __builtin_amdgcn_rcpf(o[8]);
  float y[8];
#pragma unroll
  for (int r = 0; r < 8; ++r) y[r] = o[r] * rl;
  return pack8(y);
}

struct Retile {
  v4u in[8];
  DI void load(const u16* m, int elems, int el, int ld, int rs4) {
    const rsrc_t r = __builtin_amdgcn_make_buffer_rsrc((void*)m, 0, elems * 2, 0x00020000);
    const int e4 = el + rs4 * ld;
#pragma unroll
    for (int j = 0; j < 8; ++j) in[j] = __builtin_amdgcn_raw_buffer_load_b128(r, (j < 4 ? el : e4) * 2, (j & 3) * ld * 2, 0);
    PIN();
  }
  unsigned d[8];
  DI void out(rsrc_t rs) {
#pragma unroll
    for (int i = 0; i < 8; ++i) st16(rs, in[i], d[i], 0);
  }
  DI void lines(LAS unsigned char* t, int lane, int sw, unsigned dst) {
#pragma unroll
    for (int e = 0; e < 8; ++e) {
      v4u o;
#pragma unroll
      for (int m = 0; m < 4; ++m) o[m] = __builtin_amdgcn_perm(in[2 * m + 1][e >> 1], in[2 * m][e >> 1], (e & 1) ? 0x07060302u : 0x05040100u);
      *(LAS v4u*)(t + lane * 128 + (e ^ sw ^ (lane & 7)) * 16) = o;
    }
    asm volatile("s_waitcnt lgkmcnt(0)" ::: "memory");
#pragma unroll
    for (int i = 0; i < 8; ++i) {
      in[i] = *(LAS v4u*)(t + i * 1024 + (lane ^ (lane >> 3)) * 16);
      d[i] = (unsigned)__builtin_amdgcn_ds_bpermute((8 * i + (lane >> 3)) * 4, (int)dst) + (lane & 7) * 16;
    }
    PIN();
  }
  DI void put(LAS unsigned char* L, int pb, int dd, int lp, int dl) {
#pragma unroll
    for (int e = 0; e < 8; ++e) {
      v4u o;
#pragma unroll
      for (int m = 0; m < 4; ++m) o[m] = __builtin_amdgcn_perm(in[2 * m + 1][e >> 1], in[2 * m][e >> 1], (e & 1) ? 0x07060302u : 0x05040100u);
      const int s = lp + (e & 3) + (e >> 2) * dl;
      *(LAS v4u*)(L + pb + (e >> 2) * dd + (s ^ ((s >> 3) & 7)) * 16) = o;
    }
  }
};

struct GS { int cs, j, kap, off; };
constexpr GS gu_step(int u) {
  if (u < 64) { const int k = u >> 2, j = u & 3; return GS{0, j, k, j < 3 ? (7 * (k >> 1) + 3 * (k & 1) + j) * 1024 : (7 * (k >> 1) + 6) * 1024 + (k & 1) * 512}; }
  const int v = u - 64, j = v / 17, i = v % 17;
  return GS{i == 16, j, 16 + i, 57344 + j * 16384 + i * (j < 3 ? 1024 : 512)};
}
constexpr int GA = 132, DB = 264, DA = 376, NST = 488;
#define NPQ_ 8
#define NPG_ 16
#define APRIO 1
constexpr int NPG = NPG_, NPQ = NPQ_;
#define HS1 28
#define HP1 11
#define HS2 72
#define HP2 12
#define UG 7
#define HB2 6
#define XQS 96
constexpr int ld_step(int id) {
  if (id < 32) return id < NPG ? -1 : 2 * (id - NPG);
  if (id < 64) { const int n = id - 32; return n < 12 ? 103 + (5 * n) / 2 : GA + 10 + 2 * (n - 12); }
  if (id < 92) { const int n = id - 64; return n < 12 ? (UG == 7 ? GA + 103 + n : GA + 88 + (9 * n) / 4) : DB - 15 + ((n - 12) * HB2) / 4; }
  if (id < 120) { const int n = id - 92; return n < 16 ? DB + HS1 + (HP1 * n) / 4 : DB + HS2 + ((n - 16) * HP2) / 4; }
  const int n = id - 120;
  return n < NPQ ? DA + XQS + ((112 - XQS) * n) / NPQ : -1;
}
constexpr int NEP = 56;
constexpr int ep_step(int k) { return 83 + 17 * (k / 16) + k % 16; }
struct Tab { short ld[NST][2]; short ep[NST]; short nvm[NST]; };
#define SL0 20
#define Q3T 75
#define XAE 0
#define XA0 18
constexpr int xa_step(int j) { const int a = XA0 + j, b = XAE == 0 ? (j < 12 ? 3 * j + 2 : 34 + j) : j < XAE ? 0 : j - XAE < 12 ? 3 * (j - XAE) + 2 : 34 + j - XAE; return a > b ? a : b; }
constexpr Tab make_tab() {
  Tab t{};
  for (int s = 0; s < NST; ++s) { t.ld[s][0] = t.ld[s][1] = t.ep[s] = -1; t.nvm[s] = 0; }
  for (int id = 0; id < 152; ++id) {
    const int s = ld_step(id);
    if (s < 0) continue;
    t.ld[s][t.ld[s][0] >= 0] = (short)id; ++t.nvm[s];
  }
  for (int job = 0; job < 2; ++job)
    for (int k = 0; k < NEP; ++k) {
      const int s = job * GA + ep_step(k);
      t.ep[s] = (short)(job * 64 + k);
      if (k == NEP - 1) t.nvm[s] += 2;
      if (k == (job ? 31 : NEP - 1)) t.nvm[s] += 2;
    }
  ++t.nvm[DB + 58]; ++t.nvm[DB + 59]; ++t.nvm[DA + 58]; ++t.nvm[DA + 59];
  return t;
}
constexpr Tab TAB = make_tab();
constexpr int vm_between(int a, int b) {
  int n = 0;
  for (int s = a + 1; s <= b; ++s) n += TAB.nvm[s];
  return n;
}

__global__ void __launch_bounds__(512, 2) k_main(const u16* __restrict__ x, const u16* __restrict__ wqkv, const u16* __restrict__ wo, const u16* __restrict__ wg,
                                                 const u16* __restrict__ wu, const u16* __restrict__ wd, unsigned char* __restrict__ ws,
                                                 u16* __restrict__ out) {
  extern __shared__ __attribute__((aligned(16))) unsigned char smem[];
  LAS unsigned char* L = (LAS unsigned char*)smem;
  const int tid = threadIdx.x, w0 = __builtin_amdgcn_readfirstlane(tid >> 6);
  const int b = blockIdx.x;
  auto touch = [&](rsrc_t r, unsigned voff, unsigned soff) { __builtin_amdgcn_raw_ptr_buffer_load_lds(r, (LAS void*)(L + LDS_BYTES - 256), 4, voff, soff, 0, AUX_SC1); };
  {
    unsigned long long pc0;
    asm volatile("s_getpc_b64 %0" : "=s"(pc0));
    const rsrc_t rc = __builtin_amdgcn_make_buffer_rsrc((void*)(pc0 & ~127ull), 0, CODE_BYTES, 0x00020000);
    if (w0 == 3) __builtin_amdgcn_raw_ptr_buffer_load_lds(rc, (LAS void*)(L + LDS_BYTES - 256), 4, (tid & 63) < CODE_LINES ? (tid & 63) * 128 : NONE, (b >> 3) * CODE_LINES * 128, 0, 0);
    PIN();
  }
  const int sp0 = (b & 6) + ((b >> 3) & 1), c0 = (b >> 4) + 16 * (b & 1);
  const rsrc_t rs = __builtin_amdgcn_make_buffer_rsrc((void*)ws, 0, (int)WS_BYTES, 0x00020000);
  const bool compute = w0 < 4;
  gu32* const fg = (gu32*)(ws + FLAG_G);
  gu32* const tmo = fg + FL_TMO * 32;
  unsigned tag = 0;
#define HOLD(k) do {} while (0)
#define FLAG(blk, line) st4(rs, tag, lane ? NONE : 0u, (blk) + (line) * 128)
#define POLL(blk, line, n) poll((gu32*)(ws + (blk)) + ((line) + lane) * 32, lane < (n), tmo, tag)
#define GETTAG() tag = (unsigned)__builtin_amdgcn_readfirstlane(*(LAS unsigned*)(L + LDS_TAG))
  auto blank = [&](int it, unsigned ly) {
    const int lane = lane_id(), sp = sopq(sp0), c = sopq(c0), w = it & 3, pb = it >= 34;
    const unsigned o = (unsigned)vopq(-1);
    if (it >= 16 && it < 24) st16(rs, (v4u){o, o, o, o}, ((lane >> 4) & 1) * 16384 + (lane >> 5) * 256 + (lane & 15) * 16, ly + L_O + (8 * sp + (it & 7)) * 32768 + c * 512);
    else if (it >= 24 && it < 28) st16(rs, (v4u){o, o, o, o}, (lane >> 4) * 512 + (lane & 15) * 16, ly + L_X1 + ((8 * sp + 4 + 2 * (c & 1) + (w >> 1)) * 32 + (c & ~1)) * 1024 + (w & 1) * 256);
    else if (it == 28 || it == 33 || it == 38) st8(rs, (v2u){o, o}, (lane >> 5) * 4096 + (lane & 31) * 8, ly + (it == 28 ? L_X1 : L_X) + MB2 + (8 * sp + (it == 33 ? 0 : 4) + 2 * (c & 1)) * 4096 + (c >> 1) * 256);
    else if (it >= 40) {
      const int sh = it < 48 ? 6 : it < 52 ? 5 : 4;
      st16(rs, (v4u){o, o, o, o}, __mul24(lane >> sh, 57344) + (lane & ((1 << sh) - 1)) * 16,
           ly + L_H + (16 * sp + (it < 48 ? it & 7 : it < 52 ? 2 * (it & 3) : 4 * (it & 1))) * 57344 + (it < 48 ? c * 1024 : it < 52 ? 32768 + c * 512 : 49152 + c * 256));
    } else st16(rs, (v4u){o, o, o, o}, lane * 16, ly + (it < 16 ? (it & 8 ? L_V : L_K) + ((8 * sp + (it & 7)) * 32 + c) * 1024
                                                  : L_X + ((16 * sp + 8 * pb + 4 * (c & 1) + w) * 16 + (c >> 1)) * 1024));
  };
  auto attention = [&](int layer, unsigned lay0) {
    COORDS();
    const int tl = compute ? 4 + w : w, T = 8 * sp + tl;
    const v4u o = attn_unit(L, lane, tl, sp == 0 ? 8 - tl : 0);
    st16(rs, nos4(o), (g4 & 1) * 16384 + hh * 256 + t16 * 16, lay + L_O + T * 32768 + c * 512);
    if (layer == 0) { DRAIN(); FLAG(flo, FL_O + T * 32 + c); }
    CST();
  };

  if (compute) {
    v4u xq[32];
    {
    const int layer = 0; const unsigned lay0 = LAY_OFF;
    COORDS();
    SETPRIO(2);
    const int r = lane >> 3, j = lane & 7;
    const u16* src = x + (size_t)(256 * sp + 8 * c + r) * D_ + 128 * w + 8 * j;
    v4u a[2] = {*(const v4u*)src, *(const v4u*)(src + 64)};
    if (w < 3) touch(rs, lane ? NONE : 0u, w == 0 ? lay + L_X + (16 * sp + n32) * 16384 : w == 1 ? lay + L_X + MB2 + 8 * sp * 4096 : (FL_XB + sp * 32 + c) * 128);
    PIN();
#pragma unroll
    for (int i = 0; i < 2; ++i) {
      a[i] = nos4(a[i]);
      st16(rs, a[i], (j >> 2) * 1024 + ((j & 3) * 16 + r) * 16, lay + L_X + (16 * sp + n32) * 16384 + (2 * w + i) * 2048 + rh * 128);
      auto quad = [](float v) {
        v += __int_as_float(__builtin_amdgcn_update_dpp(0, __float_as_int(v), 0xb1, 0xf, 0xf, true));
        return v + __int_as_float(__builtin_amdgcn_update_dpp(0, __float_as_int(v), 0x4e, 0xf, 0xf, true));
      };
      float sm = 0.f, q = 0.f;
#pragma unroll
      for (int m = 0; m < 4; ++m) sm += bflo(a[i][m]) + bfhi(a[i][m]);
      sm = quad(sm);
      const float mu = sm * (1.0f / 32.0f);
#pragma unroll
      for (int m = 0; m < 4; ++m) { const float e0 = bflo(a[i][m]) - mu, e1 = bfhi(a[i][m]) - mu; q += e0 * e0 + e1 * e1; }
      q = quad(q);
      st8(rs, (v2u){nos(__float_as_uint(sm)), nos(__float_as_uint(q))}, (j & 3) ? NONE : (j >> 2) * 256 + r * 8,
          lay + L_X + MB2 + (8 * sp + (c >> 2)) * 4096 + (4 * w + 2 * i) * 256 + (c & 3) * 64);
    }
    DRAIN();
    BAR();
    BAR();
    GETTAG();
    const int TB = 8 * sp + 4 + w;
    static_for<NPQ>([&](auto nc) { constexpr int n = nc.value; xq[n] = ld16(rs, lo + ((n >> 1) & 3) * 1024, lay + L_X + TB * 32768 + (n & 1) * 16384 + (n >> 3) * 4096); });
    BARX();
    }
  for (int layer = 0; layer < 2; ++layer) {
    const bool last = layer == 1;
    const unsigned lay0 = LAY_OFF + layer * LAY_BYTES;
    {
      COORDS();
      const int TB = 8 * sp + 4 + w, TA = TB - 4;
      v4u xa[32];
      auto ldx = [&](auto nc, int T) {
        constexpr int n = nc.value;
        return ld16(rs, lo + ((n >> 1) & 3) * 1024, lay + L_X + (2 * T + (n & 1)) * 16384 + (n >> 3) * 4096);
      };
      f32x4 aq[2][2], ak[2][2], av[2][2], r0, r1;
      v4u mf0, mf1; float rd0, rd1;
      unsigned o0, o1, o2, o3;
      const int kpos = (t16 + 16 * hh + 32 * (g4 & 1)) * 16;
      auto epi = [&](int k, int pa) {
        const int tl = pa ? w : 4 + w, st = (pa ? LDS_KL : LDS_ST) + w * 128;
        const unsigned kvb = lay + ((8 * sp + tl) * 32 + c) * 1024;
        if (k == 0) { r0 = lds4f(L, st + g4 * 16); r1 = lds4f(L, st + 64 + g4 * 16); }
        if (k == 2) { o0 = mulpk(ak[pa][0][0], rd0, ak[pa][0][1], rd0); o1 = mulpk(ak[pa][0][2], rd0, ak[pa][0][3], rd0); }
        if (k == 3) { o2 = mulpk(ak[pa][1][0], rd1, ak[pa][1][1], rd1); o3 = mulpk(ak[pa][1][2], rd1, ak[pa][1][3], rd1); }
        if (k == 4) {
          swap32(o0, o2); swap32(o1, o3);
          st16(rs, nos4((v4u){o0, o1, o2, o3}), kpos, kvb + L_K);
          *(LAS v4u*)(L + LDS_KL + (8 + tl) * 1024 + kpos) = (v4u){o0, o1, o2, o3};
        }
        if (k == 5) { o0 = mulpk(av[pa][0][0], r0[0], av[pa][0][1], r0[1]); o1 = mulpk(av[pa][0][2], r0[2], av[pa][0][3], r0[3]); }
        if (k == 6) { o2 = mulpk(av[pa][1][0], r1[0], av[pa][1][1], r1[1]); o3 = mulpk(av[pa][1][2], r1[2], av[pa][1][3], r1[3]); }
        if (k == 7) {
          swap32(o0, o2); swap32(o1, o3);
          st16(rs, nos4((v4u){o0, o1, o2, o3}), lo, kvb + L_V);
          *(LAS v4u*)(L + LDS_VL + (16 + 2 * tl + hh) * VBLK + (lane & 31) * 16) = (v4u){o0, o1, o2, o3};
        }
        if (k == 8) {
          asm volatile("s_nop 7" : "+v"(aq[pa][0]), "+v"(aq[pa][1]));
          const float s0 = rd0 * (0.25f * LOG2E), s1 = rd1 * (0.25f * LOG2E);
          o0 = mulpk(aq[pa][0][0], s0, aq[pa][0][1], s0); o1 = mulpk(aq[pa][0][2], s0, aq[pa][0][3], s0);
          o2 = mulpk(aq[pa][1][0], s1, aq[pa][1][1], s1); o3 = mulpk(aq[pa][1][2], s1, aq[pa][1][3], s1);
          swap32(o0, o2); swap32(o1, o3);
          *(LAS v4u*)(L + LDS_QL + tl * 1024 + kpos) = (v4u){o0, o1, o2, o3};
        }
      };
      pipe<102>(
          [&](auto fc) {
            constexpr int t = fc.value, u = t % 51;
            if constexpr (t == 28) { BARX(); CST(); }
            if constexpr (t == 12 || t == 44 || t == Q3T) BARX();
            if constexpr (u == 44 || u == 45 || u == 50) return csfrag(L, (u == 50 ? 0 : u - 43) * 16 + t16);
            else return lds16(L, (u < 44 ? u : u - 2) * 1024 + lsw);
          },
          [&](auto fc, const v4u& wfr, auto pc) {
            constexpr int t = fc.value, part = pc.value, u = t % 51;
            constexpr int ks = (u < 36 ? u / 3 : u < 44 ? (u - 12) >> 1 : u - 34) & 15, m = u < 36 ? (u % 3 + 1) % 3 : u < 46 ? 1 + (u & 1) : 0;
            constexpr bool pa = t >= 51, cs = u == 44 || u == 45 || u == 50;
            if constexpr (t < 0) return;
            else if constexpr (part == 0) {
              if constexpr (u < 44 && u % 12 == 0) {
                constexpr int n = u / 12 * 8;
                v4u* const X = pa ? xa : xq;
                if (__builtin_expect(missing<8>(X + n), 0)) {
                  unsigned spins = 0;
                  do { static_for<8>([&](auto i) { X[n + i.value] = ldx(IC<n + i.value>{}, pa ? TA : TB); }); DBGC(pa ? 0 : 7); } while (missing<8>(X + n) && ++spins < DATA_CAP);
                }
                TR();
                PIN();
              }
              const v4u& x0 = cs ? mf0 : pa ? xa[2 * ks] : xq[2 * ks];
              const v4u& x1 = cs ? mf1 : pa ? xa[2 * ks + 1] : xq[2 * ks + 1];
              if constexpr (m == 0) { aq[pa][0] = mfma16(wfr, x0, u < 3 ? zero4() : aq[pa][0]); aq[pa][1] = mfma16(wfr, x1, u < 3 ? zero4() : aq[pa][1]); }
              if constexpr (m == 1) { ak[pa][0] = mfma16(wfr, x0, u < 3 ? zero4() : ak[pa][0]); ak[pa][1] = mfma16(wfr, x1, u < 3 ? zero4() : ak[pa][1]); }
              if constexpr (m == 2) { av[pa][0] = mfma16(x0, wfr, u < 3 ? zero4() : av[pa][0]); av[pa][1] = mfma16(x1, wfr, u < 3 ? zero4() : av[pa][1]); }
            } else {
              if constexpr (2 * t < 32 - NPQ) { xq[NPQ + 2 * t] = ldx(IC<NPQ + 2 * t>{}, TB); xq[NPQ + 2 * t + 1] = ldx(IC<NPQ + 2 * t + 1>{}, TB); }
              if constexpr (u == 40) {
                const int st = (pa ? LDS_KL : LDS_ST) + w * 128;
                rd0 = lds1f(L, st + t16 * 4); rd1 = lds1f(L, st + 64 + t16 * 4);
                const int a = g4 ? LDS_TAG + 8 : st + 1024 + t16 * 4;
                mf0 = mfrag(L, a); mf1 = mfrag(L, g4 ? a : a + 64);
              }
              if constexpr (t >= 45 && t < 54) epi(t - 45, 0);
              if constexpr (t >= 96) epi(t - 96, 1);
              static_for<16>([&](auto j) { if constexpr (xa_step(j.value) == t) { xa[2 * j.value] = ldx(IC<2 * j.value>{}, TA); xa[2 * j.value + 1] = ldx(IC<2 * j.value + 1>{}, TA); } });
              if constexpr (t == 50) CST();
            }
          });
      epi(6, 1); epi(7, 1); epi(8, 1);
      BAR();
      CST();
    }
    attention(layer, lay0);
    {
      SETPRIO(0);
      v4u fr[120];
      {
        COORDS();
        const int TB = 8 * sp + 4 + 2 * rh + (w >> 1), SB = 2 * TB + (w & 1);
        const v4u rB = ld16(rs, lo, lay + L_X + (SB * 16 + n32) * 1024), rA = ld16(rs, lo, lay + L_X + (SB * 16 + n32) * 1024 - 8 * 16384);
        const unsigned xv = (g4 >> 1) * 1024 + (g4 & 1) * 512 + t16 * 16, sv = g4 ? NONE : t16 * 8;
        const unsigned xo = lay + L_X1 + (TB * 32 + 2 * n32) * 1024 + (w & 1) * 256, so = lay + L_X1 + MB2 + TB * 4096 + n32 * 256 + (w & 1) * 128;
        const int lswo = vopq(lsw + LDS_WO);
        while ((unsigned)__builtin_amdgcn_readfirstlane(__hip_atomic_load((LAS unsigned*)(L + LDS_TAG + 12), __ATOMIC_RELAXED, __HIP_MEMORY_SCOPE_WORKGROUP)) != 3u * layer + 3u)
          __builtin_amdgcn_s_sleep(1);
        asm volatile("" ::: "memory");
        v4u of[32];
        unsigned pk;
        f32x4 a0, a1;
        Fin fin;
        auto ldo = [&](auto i) { return ld16(rs, lo + (i.value & 3) * 1024, lay + L_O + (SB - 8 * (i.value >> 4)) * 16384 + ((i.value >> 2) & 3) * 4096); };
        pipe<64>(
            [&](auto fc) { return lds16(L, (fc.value & 31) * 1024 + lswo); },
            [&](auto fc, const v4u& wfr, auto pc) {
              constexpr int t = fc.value, part = pc.value, u = t & 31;
              if constexpr (part == 0) {
                if constexpr (u % 16 == 0) {
                  if (__builtin_expect(missing<8>(of + t / 2), 0)) {
                    unsigned spins = 0;
                    do { static_for<16 - u / 2>([&](auto i) { of[t / 2 + i.value] = ldo(IC<t / 2 + i.value>{}); }); DBGC(1 + (t >> 4)); SETTLE(); } while (missing<8>(of + t / 2) && ++spins < DATA_CAP);
                  }
                  if constexpr (u == 0) CST();
                  PIN();
                }
                if constexpr (u & 1) a1 = mfma16(wfr, of[t >> 1], u == 1 ? zero4() : a1);
                else a0 = mfma16(wfr, of[t >> 1], u == 0 ? zero4() : a0);
              } else {
                if constexpr (t == -1) {
                  if (!last) POLL(flo, FL_O + TB * 32, 32);
                  static_for<16>([&](auto i) { of[i.value] = ldo(i); });
                }
                if constexpr (t == 55) pk = __builtin_amdgcn_raw_buffer_load_b32(rs, (lane & 31) * 128, last ? NONE : (FL_X1B + (8 * sp + 4 + w) * 32) * 128, AUX_SC1);
                if constexpr (t == 63) { if (!last && !__all(pk == tag)) POLL(flo, FL_X1B + (8 * sp + 4 + w) * 32, 32); }
                if constexpr (t == 63) static_for<NPG>([&](auto i) { fr[i.value] = ld16(rs, lo + (i.value & 3) * 1024, lay + L_X1 + (8 * sp + 4 + w) * 32768 + (i.value >> 2) * 4096); });
                if constexpr (t == 31 || t == 63) {
                  asm volatile("s_nop 7" : "+v"(a0), "+v"(a1));
                  static_for<8>([&](auto k) { fin.template chunk<k.value>(a0, a1, t == 31 ? rB : rA, rs, xv, xo - (t == 31 ? 0 : 4 * 32768), rs, sv, so - (t == 31 ? 0 : 4 * 4096)); });
                  CST();
                }
                if constexpr (t == 31) {
                  if (!last) {
                    pk = __hip_atomic_load(fl + (FL_O + (TB - 4) * 32 + (lane & 31)) * 32, RLX_AGENT);
                    DRAIN();
                    FLAG(flo, FL_X1B + TB * 32 + 2 * n32 + (w & 1));
                    if (!__all(pk == tag)) POLL(flo, FL_O + (TB - 4) * 32, 32);
                  }
                  static_for<16>([&](auto i) { of[16 + i.value] = ldo(IC<16 + i.value>{}); });
                }
              }
            });
        CST();
      }
      SETPRIO(2);
      BAR();
      {
        COORDS();
        const int TB = 8 * sp + 4 + w;
        const int SD = 16 * sp + 8 + 4 * rh + w, kh = n32 & 1;
        const unsigned x1b = lay + L_X1 + TB * 32768, hb = lay + L_H + SD * 57344, xn = last ? NONE : lay + LAY_BYTES + L_X;
        const unsigned hsv = (g4 & 1) * 57344 + hh * 256 + t16 * 16, hso = lay + L_H + TB * 114688;
        const unsigned pso = lay + L_P + (2 * sp * 32 + (c ^ 2)) * 8192 + w * 2048;
        const int lsw2 = vopq(lsw + 65536), lh = (hh * 16 + t16) * 16, lh2 = vopq(lh + 65536);
        f32x16 acc[2][4];
        f32x4 dd[2][2];
        v4u mf, hv[4];
        float k1[2], den2[2], t0, t1, p0_, p1_;
        unsigned cm0, cm1;
        auto epi = [&](auto ec) {
          constexpr int job = ec.value >> 6, k = ec.value & 63, j = k >> 4, i = (k >> 2) & 3, uo = j < 3 ? 8 : 4;
          const f32x16& a = acc[job][j];
          if constexpr ((k & 3) == 0)
            asm volatile("v_mul_f32 %0, %2, %4\n\tv_mul_f32 %1, %3, %4\n\tv_exp_f32 %0, %0" : "=&v"(t0), "=&v"(t1) : "v"(a[2 * i]), "v"(a[2 * i + 1]), "v"(k1[job]));
          else if constexpr ((k & 3) == 1)
            asm volatile("v_exp_f32 %0, %0\n\tv_mul_f32 %1, %3, %5\n\tv_mul_f32 %2, %4, %6"
                : "+v"(t1), "=&v"(p0_), "=&v"(p1_) : "v"(a[2 * i]), "v"(a[2 * i + 1]), "v"(a[uo + 2 * i]), "v"(a[uo + 2 * i + 1]));
          else if constexpr ((k & 3) == 2)
            asm volatile("v_fma_f32 %0, %0, %2, %2\n\tv_fma_f32 %1, %1, %2, %2\n\tv_rcp_f32 %0, %0" : "+v"(t0), "+v"(t1) : "v"(den2[job]));
          else {
            unsigned d;
            asm volatile("v_rcp_f32 %2, %2\n\tv_mul_f32 %1, %1, %3\n\tv_mul_f32 %2, %2, %4\n\tv_cvt_pk_bf16_f32 %0, %1, %2\n\tv_min_u32 %0, -2, %0" : "=v"(d), "+v"(t0), "+v"(t1) : "v"(p0_), "v"(p1_));
            hv[j][i] = d;
            if constexpr (k == (job ? 31 : NEP - 1)) { st16(rs, hv[0], hsv, hso - job * 4 * 114688 + c * 1024); st16(rs, hv[1], hsv + 512, hso - job * 4 * 114688 + c * 1024); }
            if constexpr (k == NEP - 1) {
              st16(rs, hv[2], hsv, hso - job * 4 * 114688 + 32768 + c * 512);
              st8(rs, (v2u){hv[3][0], hv[3][1]}, hh ? hsv - 248 : hsv, hso - job * 4 * 114688 + 49152 + c * 256);
            }
          }
        };
        auto vload = [&](auto ic) {
          constexpr int id = ic.value;
          if constexpr (id < 64) fr[id] = ld16(rs, lo + (id & 3) * 1024, x1b - (id >> 5) * 4 * 32768 + ((id & 31) >> 2) * 4096);
          else if constexpr (id < 120) {
            constexpr int n = (id - 64) % 28;
            fr[id] = ld16(rs, lo + (n & 3) * 1024, hb - (id >= 92 ? 8 * 57344 : 0) + (n < 16 ? kh * 16384 : n < 24 ? 16384 + kh * 8192 : 24576 + kh * 4096) + (n >> 2) * 4096);
          } else {
            constexpr int n = id - 120;
            xq[n] = ld16(rs, lo + ((n >> 1) & 3) * 1024, xn + TB * 32768 + (n & 1) * 16384 + (n >> 3) * 4096);
          }
        };
        pipe<NST>(
            [&](auto fc) {
              constexpr int t = fc.value;
              if constexpr (t == 32) wait_vm<vm_between(-1, t - LA - 1)>();
              if constexpr (t == GA + 48) wait_vm<vm_between(ep_step(NEP - 1), t - LA - 1)>();
              if constexpr (t < DB) {
                constexpr GS s = gu_step(t % GA);
                if constexpr (s.cs) return csfrag(L, 48 + 32 * s.j + (s.j < 3 ? lane & 31 : t16));
                else {
                  if (s.off % 14336 == 0 && s.off >= (t < GA ? 14336 : 28672)) BARX();
                  return lds16(L, s.off < 65536 ? s.off + (s.j < 3 ? lsw : lh) : s.off - 65536 + (s.j < 3 ? lsw2 : lh2));
                }
              } else {
                constexpr int v = (t - DB) % 112;
                if (t < DA || v == 14 ? v % 14 == 0 : v % 28 == 0 && v) BARX();
                return lds16(L, v < 64 ? v * 1024 + lsw : (v < 96 ? v - 64 : v - 48) * 1024 + lsw2);
              }
            },
            [&](auto fc, const v4u& wfr, auto pc) {
              constexpr int t = fc.value, part = pc.value;
              if constexpr (t < 0) return;
              else if constexpr (part == 0) {
                if constexpr (t < DB) {
                  constexpr int job = t / GA, u = t % GA;
                  constexpr GS s = gu_step(u);
                  if constexpr (t == 0 || t == 16 || t == 32 || t == 64) {
                    constexpr int n0 = t < 64 ? t / 4 : 16, nn = t == 0 || t == 16 ? 4 : t == 32 ? 8 : 16;
                    if (__builtin_expect(t ? __builtin_amdgcn_ballot_w64((cm0 > cm1 ? cm0 : cm1) == 0xffffffffu) != 0 : missing<nn>(fr + n0), 0)) {
                      unsigned spins = 0;
                      do { static_for<(t < 64 ? 16 : 32) - n0>([&](auto i) { vload(IC<n0 + i.value>{}); }); DBGC(t < 64 ? 5 : 6); SETTLE(); } while (missing<nn>(fr + n0) && ++spins < DATA_CAP);
                    }
                    PIN();
                  }
                  acc[job][s.j] = mfma(wfr, s.cs ? mf : fr[32 * job + (s.kap & 31)], u < 4 ? splat16(0.f) : acc[job][s.j]);
                } else {
                  constexpr int job = t >= DA, v = (t - DB) % 112;
                  if constexpr (job && (v == 0 || v == 16 || v == 32 || v == 44)) {
                    constexpr int n0 = 92 + v / 2, nn = v < 32 ? 8 : 6;
                    if (__builtin_expect(__builtin_amdgcn_ballot_w64((cm0 > cm1 ? cm0 : cm1) == 0xffffffffu) != 0, 0)) {
                      unsigned spins = 0;
                      do { static_for<(v < 32 ? 108 : 120) - n0>([&](auto i) { vload(IC<n0 + i.value>{}); }); DBGC(v < 32 ? 4 : 7); SETTLE(); } while (missing<nn>(fr + n0) && ++spins < DATA_CAP);
                    }
                    PIN();
                  }
                  dd[v / 56][v & 1] = mfma16(wfr, fr[64 + 28 * job + ((v % 56) >> 1)], v % 56 < 2 ? zero4() : dd[v / 56][v & 1]);
                }
              } else {
                if constexpr (t < DB) {
                  constexpr int job = t / GA, u = t % GA;
                  if constexpr (u == 74) {
                    const int a = LDS_ST + w * 128 + (lane & 31) * 4;
                    k1[job] = lds1f(L, a); den2[job] = lds1f(L, a + 512); mf = mfrag(L, hh ? LDS_TAG + 8 : a + 1024);
                  }
                  if constexpr (u == 79) k1[job] *= -LOG2E;
                  if constexpr ((t >= 12 && t < 16) || (t >= 24 && t < 32) || (t >= 48 && t < 64)) {
                    constexpr int n = t < 16 ? t - 8 : t < 32 ? t - 16 : t - 32;
                    constexpr bool first = t == 12 || t == 24 || t == 48;
                    cm0 = umax3(first ? 0u : cm0, fr[n][0], fr[n][1]); cm1 = umax3(first ? 0u : cm1, fr[n][2], fr[n][3]);
                    asm volatile("" : "+v"(cm0), "+v"(cm1));
                  }
                }
                if constexpr ((t >= DA - 8 && t < DA) || (t >= DA + 8 && t < DA + 16) || (t >= DA + 26 && t < DA + 32) || (t >= DA + 38 && t < DA + 44)) {
                  constexpr int n = 92 + (t < DA ? t - DA + 8 : t < DA + 16 ? t - DA : t < DA + 32 ? t - DA - 10 : t - DA - 16);
                  constexpr bool first = t == DA - 8 || t == DA + 8 || t == DA + 26 || t == DA + 38;
                  cm0 = umax3(first ? 0u : cm0, fr[n][0], fr[n][1]); cm1 = umax3(first ? 0u : cm1, fr[n][2], fr[n][3]);
                  asm volatile("" : "+v"(cm0), "+v"(cm1));
                }
                if constexpr (TAB.ld[t][0] >= 0) vload(IC<TAB.ld[t][0]>{});
                if constexpr (TAB.ld[t][1] >= 0) vload(IC<TAB.ld[t][1]>{});
                if constexpr (TAB.ep[t] >= 0) epi(IC<TAB.ep[t]>{});
                if constexpr (t == DB + 58 || t == DB + 59 || t == DA + 58 || t == DA + 59)
                  st16(rs, pk24(dd[0][t & 1], tag), lo + (t & 1) * 1024, pso + (t < DA ? 32 * 8192 : 0));
                if constexpr (t == DA + 1 || t == DA + 2) *(LAS f32x4*)(L + LDS_QL + w * 2048 + (t & 1) * 1024 + lo) = dd[1][t & 1];
              }
            });
        CST();
        asm volatile("s_nop 7" : "+v"(dd[1][0]), "+v"(dd[1][1]));
        *(LAS f32x4*)(L + LDS_QL + w * 2048 + lo) = dd[1][0];
        *(LAS f32x4*)(L + LDS_QL + w * 2048 + 1024 + lo) = dd[1][1];
        BAR();
      }
    }
  }
  } else {
    {
    const int layer = 0; const unsigned lay0 = LAY_OFF;
    COORDS();
    SETPRIO(3);
    Retile rt;
    LAS unsigned char* tb = L + LDS_TB + w * 8192;
    Stats st;
    if (w == 0) {
      const int fi = lane >> 3, i8 = lane & 7, F = sopq(6 * sp) + (fi < 6 ? fi : 0);
      const int ks = F < 36 ? F / 3 : F < 44 ? (F - 12) >> 1 : F - 32, m = F < 36 ? (F - 3 * ks + 1) % 3 : F < 44 ? 1 + (F & 1) : 0;
      rt.load(wqkv, 3 * D_ * D_, __mul24(32 * ks + 8 * (i8 >> 1), 3 * D_) + m * D_ + 16 * c + 8 * (i8 & 1), 3 * D_, 4);
      const unsigned ep = __hip_atomic_load(fg + FL_EP * 32, RLX_AGENT);
      v4u mk[2];
#pragma unroll
      for (int k = 0; k < 8; ++k) {
        const int kq = ((2 * k) & 3) + 8 * ((2 * k) >> 2) + 4 * hh, ql = lane & 31;
        mk[k >> 2][k & 3] = (kq <= ql ? 0xffffu : 0u) | (kq + 1 <= ql ? 0xffff0000u : 0u);
      }
      *(LAS v4u*)(L + LDS_MK + lo) = mk[0];
      *(LAS v4u*)(L + LDS_MK + 1024 + lo) = mk[1];
      if (sp == 0) {
#pragma unroll
        for (int i = 0; i < 9; ++i) *(LAS v4u*)(L + LDS_VL + (i < 8 ? i * 1024 + lo : 8192 + t16 * 16)) = (v4u){0u, 0u, 0u, 0u};
      }
      const unsigned one = (sp == 0 && lane < 16) ? 0u : 0x3f803f80u;
      if (lane < 32) *(LAS v4u*)(L + LDS_VL + lane * VBLK + 512) = (v4u){one, one, one, one};
      PIN();
      rt.lines(tb, lane, i8, fi < 6 ? WQKV_OFF + c * 49152 + F * 1024 + i8 * 128 : NONE);
      rt.out(rs);
      tag = __builtin_amdgcn_readfirstlane(ep) + 1u;
      if (lane < 2) *(LAS v4u*)(L + LDS_TAG + lo) = (v4u){lane ? 0u : tag, 0u, 0u, 0u};
      DRAIN();
      HBAR();
      FLAG(0, FL_XB + sp * 32 + c);
      poll((gu32*)ws + (FL_XB + (lane < 32 ? sp * 32 + lane : (lane & 7) * 32 + c)) * 32, lane < 40, tmo, tag);
      st.load(rs, lane, lay + L_X + MB2 + (8 * sp + 4) * 4096);
    } else {
      const int id = 2 * sp + rh + 16 * (w - 1);
      const int t = 64 * id + lane, mo = t / 14, ch = t - __mul24(14, mo), mat = mo >> 6, o = mo & 63, c7 = ch >= 7 ? ch - 7 : ch, j = c7 >> 1, kap = o >> 1;
      const int dj = 4 * (id < 35 ? id - 28 : id - 35) + g4, dn = dj < 16 ? 16 * (n32 & 1) + dj : dj < 24 ? 16 + 8 * (n32 & 1) + dj : 24 + 4 * (n32 & 1) + dj;
      const int slot = (lane >> 2) & 3, m4 = lane & 3, q = (id < 42 ? 4 * dn : 16 * ((id - 42) & 3) + 4 * g4) + slot;
      const int cq = q < 128 ? q >> 2 : q < 192 ? (q - 128) >> 1 : q - 192, r = q < 128 ? q & 3 : q < 192 ? 4 + (q & 1) : 6;
      __builtin_amdgcn_s_sleep(SL0);
      rt.load(id < 14 ? wg : id < 28 ? wu : id < 42 ? wd : wo, id < 42 ? D_ * F_ : D_ * D_,
              id < 28 ? 8 * o * F_ + 112 * n32 + 8 * ch : (id < 42 ? __mul24(56, cq) + (r < 6 ? 16 * (r >> 1) + 4 * (r & 1) : 48) : 16 * (q >> 1) + 4 * (q & 1)) * D_ + 32 * (id < 35 ? n32 ^ 1 : n32) + 8 * m4,
              id < 28 ? F_ : D_, (id < 28 || (id < 42 && r == 6)) ? 4 : 8);
      HBAR();
      if (id < 28) {
        const int cell = kap < 16 ? 7 * (kap >> 1) + (j < 3 ? 3 * (kap & 1) + j : 6) : 56 + 16 * j + (j < 3 ? kap - 16 : (kap - 16) >> 1);
        const int rho = j < 3 ? 2 * mat + (c7 & 1) + 4 * (o & 1) : 4 * (kap & 1) + 2 * (o & 1) + mat;
        rt.lines(tb, lane, j < 3 ? rho : 0, WGU_OFF + (2 * n32 + (ch >= 7)) * 114688 + cell * 1024 + rho * 128);
      } else {
        rt.put(tb, g4 * 2048, 1024, 4 * m4 + 16 * slot, 0);
        asm volatile("s_waitcnt lgkmcnt(0)" ::: "memory");
#pragma unroll
        for (int i = 0; i < 8; ++i) {
          rt.in[i] = lds16(tb, i * 1024 + lo);
          rt.d[i] = (id < 42 ? WDN_OFF + n32 * 114688 + (id - 28) * 8192 : id < 46 ? WO_OFF + n32 * 32768 + (id - 42) * 8192 : NONE) + i * 1024 + lo;
        }
      }
#pragma unroll 1
      for (int it = w - 1; it < 16; it += 3) blank(it, lay);
    }
    HBAR();
    GETTAG();
    if (w) {
      const int l = w - 1;
      dma<4>(rs, L + l * 4096, lo, WQKV_OFF + c * 49152 + l * 4096);
      dma<7>(rs, L + (12 + 7 * l - (l >> 1)) * 1024, lo, WQKV_OFF + c * 49152 + (12 + 7 * l - (l >> 1)) * 1024);
      wait_vm<7>();
    }
    HOLD(0);
    HBAR();
    if (w) { dma<6>(rs, L + (27 + 5 * w) * 1024, lo, WQKV_OFF + c * 49152 + (27 + 5 * w) * 1024);
      rt.out(rs); wait_vm<14>();
    }
    else {
      st.all(L, lane);
      PIN();
      FLAG(0, FL_XA + sp * 32 + c);
      const int i = lane & 31;
      const int line = i < 8 ? FL_KV + (8 * sp + i) * 32 + c : i < 16 ? FL_O + (8 * sp + i - 8) * 32 + c
                     : i < 20 ? FL_X1B + (8 * sp + 4 + 2 * rh + ((i >> 1) & 1)) * 32 + 2 * n32 + (i & 1) : FL_X1A + (i - 20 < 6 ? i - 20 : 5) * 256 + sp * 32 + c;
      touch(rs, hh * FLAG_LAYER + line * 128, 0);
    }
    }
  for (int layer = 0; layer < 2; ++layer) {
    const bool last = layer == 1;
    const unsigned lay0 = LAY_OFF + layer * LAY_BYTES;
    auto ldw = [&](auto nc, unsigned src, int cell) {
      constexpr int N = nc.value, K = (N + 2) / 3;
      const int l = sopq(w0) - 5, c1 = cell + (l * K + K > N ? N - K : l * K);
      dma<K>(rs, L + c1 * 1024, lane_id() * 16, src + c1 * 1024);
    };
    auto ld_gu = [&](int s) { ldw(IC<14>{}, WGU_OFF + sopq(c0) * 114688, 14 * s); };
    {
      COORDS();
      const bool loader = w != 0;
      auto cs_qkv = [&]() {
        const unsigned one = (unsigned)vopq(0x3f803f80);
        const v4u ones = (v4u){one, one, one, one};
        f32x4 a = zero4();
        v4u f[16];
        const int fa = (w - 1) * 1024 + lsw, fb = (w < 3 ? 35 + w : 44) * 1024 + lsw, fs = w < 3 ? 2048 : 1024;
#pragma unroll
        for (int ks = 0; ks < 16; ++ks) asm volatile("ds_read_b128 %0, %1" : "=v"(f[ks]) : "v"(ks < 12 ? fa + ks * 3072 : fb + (ks - 12) * fs) : "memory");
        asm volatile("s_waitcnt lgkmcnt(0)" : "+v"(f[0]), "+v"(f[1]), "+v"(f[2]), "+v"(f[3]), "+v"(f[4]), "+v"(f[5]), "+v"(f[6]), "+v"(f[7]));
        asm volatile("" : "+v"(f[8]), "+v"(f[9]), "+v"(f[10]), "+v"(f[11]), "+v"(f[12]), "+v"(f[13]), "+v"(f[14]), "+v"(f[15]));
#pragma unroll
        for (int ks = 0; ks < 16; ++ks) a = mfma16(f[ks], ones, a);
        if (t16 == 0) {
#pragma unroll
          for (int r = 0; r < 4; ++r) cs_put(L, (w % 3) * 16 + 4 * g4 + r, a[r]);
        }
      };
      const unsigned wob = WO_OFF + n32 * 32768 - LDS_WO;
      if (layer == 0) {
        HBARS(false);
        if (loader) wait_vm<8>();
        HBAR();
        if (loader) { cs_qkv(); DRAIN(); }
        HBAR();
        if (!loader) FLAG(FLAG_G, FL_WR + n32 * 16 + 2 * sp + rh);
      } else {
        HBAR();
        if (loader) ldw(IC<8>{}, wob, 80);
        HBAR();
        if (loader) ldw(IC<8>{}, wob, 88);
      }
      {
        v4u kv[4];
        auto ldkv = [&]() {
          const unsigned so = sp ? lay + ((8 * sp - 5 + w) * 32 + c) * 1024 : NONE, s2 = w == 1 ? so + 3 * 32768 : NONE;
          kv[0] = ld16(rs, lo, so + L_K); kv[1] = ld16(rs, lo, so + L_V); kv[2] = ld16(rs, lo, s2 + L_K); kv[3] = ld16(rs, lo, s2 + L_V);
          PIN();
          DBGC(0);
        };
        if (!loader) {
          Stats st;
          const unsigned bl = lane < 16 ? FLAG_G + (FL_WR + n32 * 16 + lane) * 128 : (FL_XA + (sp ? sp - 1 : 0) * 32 + c) * 128;
          unsigned spins = 0;
          do { st.load(rs, lane, lay + L_X + MB2 + 8 * sp * 4096); DBGC(0); } while (missing<16>((const v4u*)st.p) && ++spins < DATA_CAP);
          const unsigned pv = __builtin_amdgcn_raw_buffer_load_b32(rs, bl, last ? NONE : 0u, AUX_SC1);
          PIN();
          st.all(L, lane, LDS_KL);
          if (!last && !__all(pv == tag)) poll((gu32*)(ws + bl), lane <= 16, tmo, tag);
        }
        HBARS(false);
        SETTLE();
        if (loader) {
          ldkv();
          ldw(IC<32>{}, last ? WGU_OFF + c * 114688 : wob, last ? 48 : 80);
          if (__builtin_expect(missing<4>(kv), 0)) {
            unsigned spins = 0;
            do ldkv(); while (missing<4>(kv) && ++spins < DATA_CAP);
          }
          const int va = LDS_VL + hh * VBLK + (lane & 31) * 16;
          asm volatile("ds_write_b128 %0, %1\n\tds_write_b128 %2, %3" ::"v"(LDS_KL + (3 + w) * 1024 + lo), "v"(kv[0]), "v"(va + (6 + 2 * w) * VBLK), "v"(kv[1]) : "memory");
          if (w == 1) asm volatile("ds_write_b128 %0, %1\n\tds_write_b128 %2, %3" ::"v"(LDS_KL + 7 * 1024 + lo), "v"(kv[2]), "v"(va + 14 * VBLK), "v"(kv[3]) : "memory");
        }
      }
      if (!last) HOLD(1);
      HBAR();
      CST(); CST();
      if (loader) {
        ld_gu(0); ld_gu(1); ld_gu(2); ldw(IC<6>{}, WGU_OFF + c * 114688, 42);
        if (!last) { ldw(IC<8>{}, WGU_OFF + c * 114688, 48); ld_gu(4); ldw(IC<10>{}, WGU_OFF + c * 114688, 70); wait_vm<29>(); }
        else wait_vm<28>();
        if (lane == 0) asm volatile("ds_add_u32 %0, %1" ::"v"(LDS_TAG + 12), "v"(1) : "memory");
      }
      if (sp > 0) {
        LAS unsigned* hw = (LAS unsigned*)(L + LDS_TAG + 4);
        CST();
        if (!loader) {
          v4u kv[8];
          unsigned spins = 0;
          do {
#pragma unroll
            for (int t = 0; t < 4; ++t) {
              const unsigned so = lay + ((8 * sp - 8 + t) * 32 + c) * 1024;
              kv[t] = ld16(rs, lo, so + L_K); kv[4 + t] = ld16(rs, lo, so + L_V);
            }
            PIN();
            DBGC(1);
          } while (missing<8>(kv) && ++spins < DATA_CAP);
#pragma unroll
          for (int t = 0; t < 4; ++t) {
            *(LAS v4u*)(L + LDS_KL + t * 1024 + lo) = kv[t];
            *(LAS v4u*)(L + LDS_VL + (2 * t + hh) * VBLK + (lane & 31) * 16) = kv[4 + t];
          }
          asm volatile("s_waitcnt lgkmcnt(0)" ::: "memory");
          __hip_atomic_store(hw, (unsigned)layer + 1u, __ATOMIC_RELAXED, __HIP_MEMORY_SCOPE_WORKGROUP);
        } else {
          unsigned v;
          do {
            __builtin_amdgcn_s_sleep(1);
            asm volatile("ds_read_b32 %0, %1\n\ts_waitcnt lgkmcnt(0)" : "=v"(v) : "v"(LDS_TAG + 4) : "memory");
          } while ((unsigned)__builtin_amdgcn_readfirstlane(v) != (unsigned)layer + 1u);
        }
      } else CST();
      CST();
      if (layer == 0 && !loader) touch(rs, LAY_OFF + L_O + lane * TSTR + c * 4096 + sp * 128, 0);
      SETPRIO(APRIO);
    }
    attention(layer, lay0);
    {
      COORDS();
      const bool poller = w == 0, loader = w != 0;
      const unsigned wq = WQKV_OFF + c * 49152, wb = WGU_OFF + c * 114688, wdn = WDN_OFF + n32 * 114688;
      unsigned pv = 0;
#define PEEK(blk, line) pv = __hip_atomic_load((gu32*)(ws + (blk)) + ((line) + (lane & 31)) * 32, RLX_AGENT)
#define GATE(blk, line) do { if (!__all(pv == tag)) POLL(blk, line, 32); } while (0)
      f32x4 ca = zero4();
      auto cs_gu = [&](int cell, int n2, bool put) {
        const unsigned one = (unsigned)vopq((w == 3 || t16 == (g4 & 1)) ? 0x3f803f80 : 0);
        const int a = cell * 1024 + (w < 3 ? lsw : lo), d2 = n2 == 1 ? 2048 : 7168;
        v4u f[8];
#pragma unroll
        for (int i = 0; i < 8; ++i) asm volatile("ds_read_b128 %0, %1" : "=v"(f[i]) : "v"(a + (i >> 1) * d2 + (i & 1) * n2 * 1024) : "memory");
        asm volatile("s_waitcnt lgkmcnt(0)" : "+v"(f[0]), "+v"(f[1]), "+v"(f[2]), "+v"(f[3]), "+v"(f[4]), "+v"(f[5]), "+v"(f[6]), "+v"(f[7]));
#pragma unroll
        for (int i = 0; i < 8; ++i) ca = mfma16(f[i], (v4u){one, one, one, one}, ca);
        if (put && t16 < (w < 3 ? 2 : 1)) {
#pragma unroll
          for (int r = 0; r < 4; ++r) cs_put(L, 48 + 32 * w + 16 * t16 + 4 * g4 + r, ca[r]);
        }
      };
      if (!last) {
        unsigned v;
        if (loader && lane == 0) asm volatile("ds_add_u32 %0, %1" ::"v"(LDS_TAG + 16), "v"(1) : "memory");
        do asm volatile("ds_read_b32 %0, %1\n\ts_waitcnt lgkmcnt(0)" : "=v"(v) : "v"(LDS_TAG + 16) : "memory");
        while ((unsigned)__builtin_amdgcn_readfirstlane(v) != 3u);
        for (int q = 0; q < 4 - w; ++q) {
          if (w == 3) cs_gu(6, 28, false);
          else cs_gu(q < 2 ? 28 * q + w : 40 + 16 * w + 8 * q, q < 2 ? 3 : 1, q == 3);
        }
      }
      auto tail = [&](auto pc) {
        constexpr bool P = pc.value;
        const int TD = 8 * sp + 4 + 2 * rh + (w >> 1), SD = 2 * TD + (w & 1);
        const unsigned xn = last ? NONE : lay + LAY_BYTES + L_X;
        const rsrc_t ro = last ? __builtin_amdgcn_make_buffer_rsrc((void*)out, 0, (int)MB2, 0x00020000) : rs;
        const unsigned ob = last ? SD * 16384 + n32 * 64 : xn + (SD * 16 + n32) * 1024, sb = xn + MB2 + TD * 4096 + n32 * 256 + (w & 1) * 128;
        const unsigned ov = lo + ((t16 * 1008 - g4 * 240) & -layer), sv = g4 ? NONE : t16 * 8;
        v4u pp[2], res;
        Stats st;
        auto ldp = [&](int pa) {
          const unsigned so = lay + L_P + ((2 * sp + 1 - pa) * 32 + c) * 8192 + w * 2048;
          pp[0] = ld16(rs, lo, so); pp[1] = ld16(rs, lo + 1024, so);
          PIN();
          DBGC(4 + pa);
        };
        auto ldr = [&](int pa) { res = ld16(rs, g4 * 512 + t16 * 16, lay + L_X1 + ((TD - 4 * pa) * 32 + 2 * n32) * 1024 + (w & 1) * 256); };
        auto finish = [&](int pa) {
          f32x4 a0 = lds4f(L, LDS_QL + w * 2048 + lo), a1 = lds4f(L, LDS_QL + w * 2048 + 1024 + lo);
          asm volatile("" : "+v"(a0), "+v"(a1));
          ldp(pa);
          if (P && pa) st.load(rs, lane, xn + MB2 + (8 * sp + 4) * 4096);
          if (pa && !last) { if (!P) wait_vm<2>(); HBARS(false); }
          if (!pa) { HBAR(); if (!P) ldw(IC<22>{}, last ? NONE : wq, 0); }
          TR();
          if (__builtin_expect(!__all(pp[0][3] == tag && pp[1][3] == tag), 0)) {
            unsigned spins = 0;
            do ldp(pa); while (!__all(pp[0][3] == tag && pp[1][3] == tag) && ++spins < DATA_CAP);
          }
          TR();
          a0 += un24(pp[0]); a1 += un24(pp[1]);
          Fin fin;
          static_for<8>([&](auto k) { fin.template chunk<k.value>(a0, a1, res, ro, ov, ob - pa * 8 * 16384, rs, sv, sb - pa * 4 * 4096); });
          TR();
        };
        SETPRIO(3);
        ldr(0);
        HBARS(false);
        HBARS(false);
        finish(0);
        HBARS(false);
        if (!P && !last) ldw(IC<26>{}, wq, 22);
        ldr(1);
        unsigned ev[4];
        const bool adv = last && blockIdx.x == 0;
        if (P) {
#pragma unroll
          for (int q = 0; q < 4; ++q) ev[q] = __builtin_amdgcn_raw_buffer_load_b32(rs, lane * 128, adv ? flo + (FL_HB + 64 * q) * 128 : NONE, AUX_SC1);
        }
        if (last) HOLD(3);
        HBAR();
        if (!P) wait_vm<10>();
        CST();
        if (!last) HOLD(2);
        HBAR();
        finish(1);
        if (P && !last) {
          if (__builtin_expect(missing<16>((const v4u*)st.p), 0)) {
            unsigned spins = 0;
            do { st.load(rs, lane, xn + MB2 + (8 * sp + 4) * 4096); DBGC(3); } while (missing<16>((const v4u*)st.p) && ++spins < DATA_CAP);
          }
          st.all(L, lane);
        }
        if (P && adv) {
          if (!__all(ev[0] == tag && ev[1] == tag && ev[2] == tag && ev[3] == tag))
            for (int q = 0; q < 4; ++q) POLL(flo, FL_HB + 64 * q, 64);
          if (lane == 0) __hip_atomic_store(fg + FL_EP * 32, tag, RLX_AGENT);
        }
      };
      SETPRIO(3);
      if (poller) {
        Stats st;
        const int h2l = FL_HB + (sp ? sp - 1 : 0) * 32 + c;
        SETPRIO(1);
        if (last) HOLD(4);
        HBAR();
        static_for<7>([&](auto nc) {
          constexpr int n = nc.value + 1;
          if (n <= 2) st.load(rs, lane, lay + L_X1 + MB2 + (8 * sp + 4) * 4096, 8 * n - 8, 8 * n);
          if (n == 3) { FLAG(flo, FL_X1A + sp * 32 + c); st.part1(); }
          if (n == 4) {
            if (missing<16>((const v4u*)st.p)) {
              unsigned spins = 0;
              do { st.load(rs, lane, lay + L_X1 + MB2 + (8 * sp + 4) * 4096); DBGC(2); } while (missing<16>((const v4u*)st.p) && ++spins < DATA_CAP);
              st.part1();
            }
            st.part2();
          }
          if (n == 5) st.part3(L, lane);
          if (n == 6) PEEK(flo, FL_X1A + sp * 32);
          if (n == 7) GATE(flo, FL_X1A + sp * 32);
          HBARS(n == 3 || n == 7);
        });
        st.load(rs, lane, lay + L_X1 + MB2 + 8 * sp * 4096);
        st.all(L, lane);
        static_for<6>([&](auto nc) {
          constexpr int n = nc.value + 2;
          if (n == 4) FLAG(flo, FL_HB + sp * 32 + c);
          if (n == UG - 1) PEEK(flo, FL_HB + sp * 32);
          if (n == UG) GATE(flo, FL_HB + sp * 32);
          HBARS(n == 2 || n == 7);
        });
        static_for<8>([&](auto nc) {
          constexpr int n = nc.value;
          if (n == 4) pv = __hip_atomic_load(fl + h2l * 32, RLX_AGENT);
          if (n == 7) { if (!__all(pv == tag)) poll(fl + h2l * 32, true, tmo, tag); }
          else HBARS(n == 0 || n == 3 || n == 6);
        });
        tail(IC<1>{});
      } else {
        DRAIN();
        HBAR();
        static_for<7>([&](auto nc) {
          constexpr int n = nc.value + 1;
          if (n >= 3) blank(30 + 3 * n + w, 30 + 3 * n + w > 53 ? NONE : lay);
          blank(6 * n - 7 + w, last ? NONE : LAY_OFF + LAY_BYTES);
          if (n < 7) blank(6 * n - 4 + w, last ? NONE : LAY_OFF + LAY_BYTES);
          if (n <= 3) ldw(IC<9>{}, wb, 71 + 9 * n);
          if (n == 4) { ldw(IC<5>{}, wb, 107); wait_vm<16>(); }
          if (n == 5) { ldw(IC<16>{}, wdn - 16384, 112); if (!last && w == 1) cs_gu(80, 1, true); wait_vm<20>(); }
          if (n == 6) { if (!last && w == 2) cs_gu(88, 1, false); wait_vm<12>(); }
          if (n == 7 && !last && w >= 2) cs_gu(80 + 8 * w, 1, true);
          HBARS(n == 3 || n == 7);
        });
        static_for<6>([&](auto nc) {
          constexpr int n = nc.value + 2;
          if (n == 3) wait_vm<0>();
          if (n > 3) ldw(IC<(n < 7 ? 21 : 16)>{}, wdn, 21 * n - 84);
          HBARS(n == 2 || n == 7);
        });
        static_for<8>([&](auto nc) {
          constexpr int n = nc.value;
          if (n == 0) ldw(IC<15>{}, wdn, 79);
          if (n == 1) ldw(IC<2>{}, wdn, 94);
          if (n == 2) ldw(IC<16>{}, last ? NONE : WO_OFF + n32 * 32768 - LDS_WO, 96);
          if (n < 7) wait_vm<(n == 0 ? 25 : n == 1 ? 19 : n == 2 ? 25 : n == 3 ? 18 : n == 4 ? 12 : n == 5 ? 7 : 6)>();
          if (n < 7) HBARS(n == 0 || n == 3 || n == 6);
        });
        tail(IC<0>{});
      }
    }
  }
  }
}

}

extern "C" void kernel_launch(void* const* d_in, const int* in_sizes, int n_in, void* d_out, int out_size, void* d_ws, size_t ws_size,
                              hipStream_t stream) {
  (void)in_sizes; (void)n_in; (void)out_size; (void)ws_size;
  (void)hipFuncSetAttribute((const void*)k_main, hipFuncAttributeMaxDynamicSharedMemorySize, LDS_BYTES);
  k_main<<<256, 512, LDS_BYTES, stream>>>((const u16*)d_in[0], (const u16*)d_in[1], (const u16*)d_in[2], (const u16*)d_in[4], (const u16*)d_in[5],
                                          (const u16*)d_in[6], (unsigned char*)d_ws, (u16*)d_out);
}
```

```cpp
#include <hip/hip_runtime.h>
#include <stdint.h>
#include <utility>

#define LAS __attribute__((address_space(3)))
#define GAS __attribute__((address_space(1)))
#define DI __device__ __forceinline__
#define RLX_AGENT __ATOMIC_RELAXED, __HIP_MEMORY_SCOPE_AGENT
typedef unsigned short u16;
typedef unsigned v4u __attribute__((ext_vector_type(4)));
typedef unsigned v2u __attribute__((ext_vector_type(2)));
typedef float f32x2 __attribute__((ext_vector_type(2)));
typedef float f32x4 __attribute__((ext_vector_type(4)));
typedef float f32x16 __attribute__((ext_vector_type(16)));
typedef __bf16 bf16x2 __attribute__((ext_vector_type(2)));
typedef __bf16 bf16x8 __attribute__((ext_vector_type(8)));
typedef GAS unsigned gu32;
typedef GAS unsigned long long gu64;

namespace {

constexpr int D_ = 512, F_ = 1792;
constexpr unsigned MB2 = 2097152u, STB = 262144u;
constexpr unsigned FLAG_LAYER = 1048576u, FLAG_G = 2 * FLAG_LAYER, FLAG_BYTES = FLAG_G + 65536;
constexpr unsigned WQKV_OFF = FLAG_BYTES;
constexpr unsigned WO_OFF = WQKV_OFF + 32 * 49152;
constexpr unsigned WGU_OFF = WO_OFF + 16 * 32768;
constexpr unsigned WDN_OFF = WGU_OFF + 32 * 114688;
constexpr unsigned LAY_OFF = WDN_OFF + 16 * 114688;
constexpr unsigned L_K = 0, L_V = MB2, L_O = 2 * MB2, L_X1 = 3 * MB2, L_X = 4 * MB2 + STB, L_H = 5 * MB2 + 2 * STB;
constexpr unsigned L_P = L_H + 128 * 57344;
constexpr unsigned LAY_BYTES = L_P + 2 * MB2;
constexpr unsigned WS_BYTES = LAY_OFF + 2 * LAY_BYTES;
constexpr unsigned NONE = 0x40000000u;
constexpr int FL_KV = 0, FL_O = 2048, FL_X1B = 4096, FL_X1A = 6144, FL_HB = 6400, FL_HA = 6656, FL_XB = 6912, FL_XA = 7168, FL_HA2 = 7424;
constexpr int FL_WR = 0, FL_EP = 256, FL_TMO = 257;
constexpr int VBLK = 528;
constexpr int LDS_KL = 114688, LDS_VL = LDS_KL + 16384, LDS_QL = LDS_VL + 32 * VBLK, LDS_CS = LDS_QL + 8192, LDS_ST = LDS_CS + 1536;
constexpr int LDS_MK = LDS_ST + 1536, LDS_TAG = LDS_MK + 2048;
constexpr int LDS_WO = 81920, LDS_TB = 49152;
constexpr int LDS_BYTES = 163840;
#define PSL 4
#define TPOS 0
#define TSTR 1048576u
#define CODE_BYTES_ 74240
constexpr int CODE_BYTES = CODE_BYTES_, CODE_LINES = (CODE_BYTES + 4095) / 4096;
constexpr unsigned SPIN_CAP = 1u << 16;
constexpr int AUX_SC1 = 16;
constexpr float LOG2E = 1.4426950408889634f;

DI f32x16 mfma(v4u a, v4u b, f32x16 c) {
  return __builtin_amdgcn_mfma_f32_32x32x16_bf16(__builtin_bit_cast(bf16x8, a), __builtin_bit_cast(bf16x8, b), c, 0, 0, 0);
}
DI f32x4 mfma16(v4u a, v4u b, f32x4 c) {
  return __builtin_amdgcn_mfma_f32_16x16x32_bf16(__builtin_bit_cast(bf16x8, a), __builtin_bit_cast(bf16x8, b), c, 0, 0, 0);
}
DI unsigned pk2(float lo, float hi) {
  f32x2 p = {lo, hi};
  return __builtin_bit_cast(unsigned, __builtin_convertvector(p, bf16x2));
}
DI float bfr(float x) { return __uint_as_float(pk2(x, 0.f) << 16); }
DI float bflo(unsigned u) { return __uint_as_float(u << 16); }
DI float bfhi(unsigned u) { return __uint_as_float(u & 0xffff0000u); }
DI float amul(float a, float b) { float d; asm("v_mul_f32 %0, %1, %2" : "=v"(d) : "v"(a), "v"(b)); return d; }
DI float afma(float a, float b, float c) { float d; asm("v_fma_f32 %0, %1, %2, %3" : "=v"(d) : "v"(a), "v"(b), "v"(c)); return d; }
DI float aadd(float a, float b) { float d; asm("v_add_f32 %0, %1, %2" : "=v"(d) : "v"(a), "v"(b)); return d; }
DI float asub(float a, float b) { float d; asm("v_sub_f32 %0, %1, %2" : "=v"(d) : "v"(a), "v"(b)); return d; }
DI float xhalf_sum(float v) {
  float t;
  asm("v_mov_b32 %1, %0\n\ts_nop 1\n\tv_permlane32_swap_b32 %0, %1\n\tv_add_f32 %0, %0, %1" : "+v"(v), "=&v"(t));
  return v;
}
DI float xrow_sum(float v) {
  float t;
  asm("v_mov_b32 %1, %0\n\ts_nop 1\n\tv_permlane16_swap_b32 %0, %1\n\tv_add_f32 %0, %0, %1" : "+v"(v), "=&v"(t));
  return v;
}
DI float xhalf_max(float v) {
  auto r = __builtin_amdgcn_permlane32_swap(__float_as_uint(v), __float_as_uint(v), false, false);
  return fmaxf(__uint_as_float(r[0]), __uint_as_float(r[1]));
}
DI void swap16(unsigned& a, unsigned& b) { asm("s_nop 1\n\tv_permlane16_swap_b32 %0, %1" : "+v"(a), "+v"(b)); }
DI void swap32(unsigned& a, unsigned& b) { asm("s_nop 1\n\tv_permlane32_swap_b32 %0, %1" : "+v"(a), "+v"(b)); }
DI f32x16 splat16(float v) {
  f32x16 z;
#pragma unroll
  for (int i = 0; i < 16; ++i) z[i] = v;
  return z;
}
DI f32x4 zero4() { return (f32x4){0.f, 0.f, 0.f, 0.f}; }
DI int vopq(int x) { asm volatile("" : "+v"(x)); return x; }
DI int lane_id() { int l; asm volatile("v_mbcnt_lo_u32_b32 %0, -1, 0\n\tv_mbcnt_hi_u32_b32 %0, -1, %0" : "=v"(l)); return l; }
template <int LN> DI unsigned wrl(unsigned v, unsigned s) { asm volatile("v_writelane_b32 %0, %1, %2" : "+v"(v) : "s"(s), "i"(LN)); return v; }
template <class T> DI void undef(T& x) { asm volatile("" : "=v"(x)); }
DI int sopq(int x) { asm volatile("" : "+s"(x)); return x; }
#define SWZ(l) ((l) ^ (((l) >> 3) & 7))
#define COORDS() \
  const int lane = lane_id(), lo = vopq(lane * 16), hh = lane >> 5, t16 = lane & 15, g4 = lane >> 4, lsw = SWZ(lane) * 16; \
  const int w = sopq(w0) & 3, sp = sopq(sp0), c = sopq(c0), n32 = c >> 1, rh = c & 1; \
  const unsigned lay = (unsigned)sopq((int)lay0); \
  const unsigned flo = sopq(layer) * FLAG_LAYER; \
  gu32* const fl = (gu32*)(ws + flo); \
  (void)lo; (void)hh; (void)t16; (void)g4; (void)lsw; (void)n32; (void)rh; (void)lay; (void)sp; (void)c; (void)w; (void)fl;
#define HBARS(c) asm volatile("s_waitcnt lgkmcnt(0)\n\ts_barrier" ::: "memory")
#define CST() do {} while (0)
#define DBGC(k) asm volatile("" ::: "memory")
#define HBAR() asm volatile("s_waitcnt lgkmcnt(0)\n\ts_barrier" ::: "memory")
#define TR() do {} while (0)
#define BAR() asm volatile("s_waitcnt lgkmcnt(0)\n\ts_barrier" ::: "memory")
#define BARX() asm volatile("s_barrier" ::: "memory")
#define DRAIN() asm volatile("s_waitcnt vmcnt(0)" ::: "memory")
#define SETTLE() __builtin_amdgcn_s_waitcnt(0x0f70)
#define PIN() __builtin_amdgcn_sched_barrier(0)
#define SETPRIO(n) __builtin_amdgcn_s_setprio(n)
template <int N> DI void wait_vm() { static_assert(N >= 0 && N < 64, ""); asm volatile("s_waitcnt vmcnt(%0)" ::"i"(N) : "memory"); }

typedef __amdgpu_buffer_rsrc_t rsrc_t;
DI v4u ld16(rsrc_t rs, unsigned voff, unsigned soff) { return __builtin_amdgcn_raw_buffer_load_b128(rs, voff, soff, AUX_SC1); }
DI f32x2 ld8f(rsrc_t rs, unsigned voff, unsigned soff) { return __builtin_bit_cast(f32x2, __builtin_amdgcn_raw_buffer_load_b64(rs, voff, soff, AUX_SC1)); }
DI void st16(rsrc_t rs, v4u v, unsigned voff, unsigned soff) {
  __builtin_amdgcn_raw_buffer_store_b128(v, rs, voff, soff, AUX_SC1);
  asm volatile("s_nop 1" ::"v"(v));
}
DI void st8(rsrc_t rs, v2u v, unsigned voff, unsigned soff) { __builtin_amdgcn_raw_buffer_store_b64(v, rs, voff, soff, AUX_SC1); }
DI void st4(rsrc_t rs, unsigned v, unsigned voff, unsigned soff) { __builtin_amdgcn_raw_buffer_store_b32(v, rs, voff, soff, AUX_SC1); }
DI v4u lds16(LAS unsigned char* L, int off) { return *(LAS v4u*)(L + off); }
DI f32x4 lds4f(LAS unsigned char* L, int off) { return *(LAS f32x4*)(L + off); }

DI void poll(gu32* word, bool active, gu32* tmo, unsigned tag) {
  for (unsigned spins = 0;;) {
    unsigned v = tag;
    if (active) v = __hip_atomic_load(word, RLX_AGENT);
    if (__all(v == tag)) break;
    ++spins;
    if (spins > SPIN_CAP) { __hip_atomic_store(tmo, tag, RLX_AGENT); break; }
    if ((spins & 255u) == 0u && (unsigned)__builtin_amdgcn_readfirstlane(__hip_atomic_load(tmo, RLX_AGENT)) == tag) break;
  }
  __builtin_amdgcn_fence(__ATOMIC_ACQUIRE, "wavefront");
}

template <int I> DI void dma1(rsrc_t rs, LAS unsigned char* dst, unsigned voff, unsigned soff) {
  __builtin_amdgcn_raw_ptr_buffer_load_lds(rs, (LAS void*)dst, 16, voff, soff, I * 1024, AUX_SC1);
}
template <int V> using IC = std::integral_constant<int, V>;
template <class FN, int... I> DI void static_for_(FN fn, std::integer_sequence<int, I...>) { (fn(IC<I>{}), ...); }
template <int N, class FN> DI void static_for(FN fn) { static_for_(fn, std::make_integer_sequence<int, N>{}); }
template <int N> DI void dma(rsrc_t rs, LAS unsigned char* dst, unsigned lo, unsigned src) {
  static_for<N>([&](auto i) { dma1<i.value & 3>(rs, dst + (i.value >> 2) * 4096, lo, src + (i.value >> 2) * 4096); });
}

struct Stats {
  f32x4 p[16];
  float m0, m1, q0, q1;
  DI void load(rsrc_t rs, int lane, unsigned base, int i0 = 0, int i1 = 16) {
#pragma unroll
    for (int i = i0; i < i1; ++i) p[i] = __builtin_bit_cast(f32x4, ld16(rs, (lane >> 4) * 4096 + (lane & 15) * 16 + i * 256, base));
    PIN();
  }
  DI void part1() {
    m0 = m1 = q0 = q1 = 0.f;
#pragma unroll
    for (int i = 0; i < 16; ++i) { m0 += p[i][0]; q0 += p[i][1]; m1 += p[i][2]; q1 += p[i][3]; }
    asm volatile("" : "+v"(m0), "+v"(m1), "+v"(q0), "+v"(q1));
  }
  DI void part2() {
    const float a0 = m0 * (1.0f / 16.0f), a1 = m1 * (1.0f / 16.0f);
    q0 *= 32.0f; q1 *= 32.0f;
#pragma unroll
    for (int i = 0; i < 16; ++i) { const float d0 = p[i][0] - a0, d1 = p[i][2] - a1; q0 += d0 * d0; q1 += d1 * d1; }
    asm volatile("" : "+v"(q0), "+v"(q1));
  }
  DI void all(LAS unsigned char* L, int lane, int dst = LDS_ST) { part1(); part2(); part3(L, lane, dst); }
  DI void part3(LAS unsigned char* L, int lane, int dst = LDS_ST) {
    const float den0 = bfr(__builtin_amdgcn_sqrtf(bfr(bfr(q0 * (1.0f / (32.0f * D_))) + 1e-5f)));
    const float den1 = bfr(__builtin_amdgcn_sqrtf(bfr(bfr(q1 * (1.0f / (32.0f * D_))) + 1e-5f)));
    *(LAS f32x2*)(L + dst + lane * 8) = (f32x2){__builtin_amdgcn_rcpf(den0), __builtin_amdgcn_rcpf(den1)};
    *(LAS f32x2*)(L + dst + 512 + lane * 8) = (f32x2){den0 * den0, den1 * den1};
    *(LAS v2u*)(L + dst + 1024 + lane * 8) = (v2u){pk2(m0 * (-1.0f / D_), m0 * (-1.0f / D_)), pk2(m1 * (-1.0f / D_), m1 * (-1.0f / D_))};
  }
};
DI float lds1f(LAS unsigned char* L, int off) { return *(LAS float*)(L + off); }
DI v4u mfrag(LAS unsigned char* L, int off) { const unsigned n = *(LAS unsigned*)(L + off); return (v4u){n, n, 0u, 0u}; }

constexpr int LA = 6;
template <int NF, class RD, class STEP>
DI void pipe(RD rd, STEP step) {
  v4u wf[8];
  static_for<LA>([&](auto i) { wf[i.value] = rd(i); });
  step(IC<-1>{}, wf[0], IC<1>{});
  PIN();
  static_for<NF>([&](auto fc) {
    constexpr int f = fc.value;
    if constexpr ((f & 1) == 0 && f + LA <= NF) __builtin_amdgcn_s_waitcnt(0xc07f | ((LA - 2) << 8));
    step(fc, wf[f & 7], IC<0>{});
    if constexpr (f + LA < NF) wf[(f + LA) & 7] = rd(IC<f + LA>{});
    step(fc, wf[f & 7], IC<1>{});
    PIN();
  });
}
DI v4u csfrag(LAS unsigned char* L, int entry) {
  const v2u t = *(LAS v2u*)(L + LDS_CS + entry * 8);
  return (v4u){t.x, t.y, 0u, 0u};
}
DI void cs_put(LAS unsigned char* L, int entry, float cs) {
  const float hi = bfr(cs), mid = bfr(cs - hi), lw = (cs - hi) - mid;
  *(LAS v2u*)(L + LDS_CS + entry * 8) = (v2u){pk2(hi, mid), pk2(lw, 0.f)};
}
DI unsigned mulpk(float a, float s0, float b, float s1) {
  unsigned d; float t;
  asm("v_mul_f32 %0, %2, %3\n\tv_mul_f32 %1, %4, %5\n\tv_cvt_pk_bf16_f32 %0, %0, %1" : "=&v"(d), "=&v"(t) : "v"(a), "v"(s0), "v"(b), "v"(s1));
  return d;
}
DI unsigned nos(unsigned v) { return v < 0xfffffffeu ? v : 0xfffffffeu; }
DI v4u nos4(v4u v) { return (v4u){nos(v[0]), nos(v[1]), nos(v[2]), nos(v[3])}; }
DI unsigned umax3(unsigned a, unsigned b, unsigned c) { const unsigned t = a > b ? a : b; return t > c ? t : c; }
template <int N> DI bool missing(const v4u* f, int step = 1) {
  unsigned m0 = 0, m1 = 0;
#pragma unroll
  for (int i = 0; i < N; ++i) { m0 = umax3(m0, f[i * step][0], f[i * step][1]); m1 = umax3(m1, f[i * step][2], f[i * step][3]); }
  return __builtin_amdgcn_ballot_w64((m0 > m1 ? m0 : m1) == 0xffffffffu) != 0;
}
constexpr unsigned DATA_CAP = 1u << 14;
DI v4u pk24(const f32x4& a, unsigned tag) {
  const v4u u = __builtin_bit_cast(v4u, a);
  return (v4u){__builtin_amdgcn_perm(u[0], u[3], 0x07060501u), __builtin_amdgcn_perm(u[1], u[3], 0x07060502u), __builtin_amdgcn_perm(u[2], u[3], 0x07060503u), tag};
}
DI f32x4 un24(const v4u& u) {
  return __builtin_bit_cast(f32x4, (v4u){u[0], u[1], u[2], __builtin_amdgcn_perm(u[2], __builtin_amdgcn_perm(u[1], u[0], 0x0c0c0400u), 0x0401000cu)});
}
DI v4u pack8(const float* y) { return (v4u){pk2(y[0], y[1]), pk2(y[2], y[3]), pk2(y[4], y[5]), pk2(y[6], y[7])}; }

struct Fin {
  float y[8], s;
  v4u o;
  v2u sq;
  template <int K, bool ST = true> DI void chunk(const f32x4& a0, const f32x4& a1, const v4u& r, rsrc_t ro, unsigned vo, unsigned so, rsrc_t rs, unsigned vs, unsigned ss) {
    if constexpr (K == 0) { y[0] = aadd(a0[0], bflo(r[0])); y[1] = aadd(a0[1], bfhi(r[0])); y[2] = aadd(a0[2], bflo(r[1])); y[3] = aadd(a0[3], bfhi(r[1])); }
    if constexpr (K == 1) { y[4] = aadd(a1[0], bflo(r[2])); y[5] = aadd(a1[1], bfhi(r[2])); y[6] = aadd(a1[2], bflo(r[3])); y[7] = aadd(a1[3], bfhi(r[3])); }
    if constexpr (K == 2) { o = nos4(pack8(y)); if constexpr (ST) st16(ro, o, vo, so); }
    if constexpr (K == 3) {
#pragma unroll
      for (int m = 0; m < 4; ++m) { y[2 * m] = bflo(o[m]); y[2 * m + 1] = bfhi(o[m]); }
    }
    if constexpr (K == 4) s = xrow_sum(aadd(aadd(aadd(y[0], y[1]), aadd(y[2], y[3])), aadd(aadd(y[4], y[5]), aadd(y[6], y[7]))));
    if constexpr (K == 5) {
      s = xhalf_sum(s);
      const float mu = s * (1.0f / 32.0f);
#pragma unroll
      for (int i = 0; i < 8; ++i) y[i] = asub(y[i], mu);
    }
    if constexpr (K == 6) {
      y[0] = amul(y[0], y[0]); y[1] = amul(y[1], y[1]);
#pragma unroll
      for (int i = 2; i < 8; ++i) y[i & 1] = afma(y[i], y[i], y[i & 1]);
    }
    if constexpr (K == 7) {
      sq = (v2u){nos(__float_as_uint(s)), nos(__float_as_uint(xhalf_sum(xrow_sum(aadd(y[0], y[1])))))};
      if constexpr (ST) st8(rs, sq, vs, ss);
    }
  }
};

constexpr unsigned long long low_mask(int r) {
  unsigned long long m = 0;
  for (int l = 0; l < 64; ++l)
    if ((r & 3) + 8 * (r >> 2) + 4 * (l >> 5) <= (l & 31)) m |= 1ull << l;
  return m;
}
DI float sel(unsigned long long mask, float set, float unset) {
  float d;
  asm("v_cndmask_b32_e64 %0, %1, %2, %3" : "=v"(d) : "v"(unset), "v"(set), "s"(mask));
  return d;
}
DI float max16(float m, const f32x16& s) {
#pragma unroll
  for (int r = 0; r < 16; r += 2) m = __builtin_fmaxf(__builtin_fmaxf(m, s[r]), s[r + 1]);
  return m;
}
DI v4u exp_pack(const f32x16& s, int h) {
  float pr[8];
#pragma unroll
  for (int r = 0; r < 8; ++r) pr[r] = __builtin_amdgcn_exp2f(s[8 * h + r]);
  return pack8(pr);
}
template <bool EXACT>
DI f32x16 attn_pass(LAS unsigned char* L, int lane, int tl, int nv) {
  const int lo = lane * 16;
  const int kb = LDS_KL + tl * 1024 + lo, kf0 = LDS_KL + (tl + 8) * 1024 + (lane >> 5) * 512;
  const v4u q = lds16(L, LDS_QL + tl * 1024 + lo);
  v4u kf[9];
  kf[8] = lds16(L, kb + 8192); kf[0] = lds16(L, nv <= 0 ? kb : kf0);
#pragma unroll
  for (int i = 7; i >= 1; --i) kf[i] = lds16(L, (nv <= i ? kb : kf0 - i * 1024) + i * 1024);
  f32x16 s[9], cm = splat16(0.f);
  if constexpr (EXACT) {
    s[8] = mfma(kf[8], q, splat16(0.f));
    s[0] = mfma(kf[0], q, splat16(0.f));
    s[7] = mfma(kf[7], q, splat16(0.f));
    s[6] = mfma(kf[6], q, splat16(0.f));
    asm volatile("s_nop 7\n\ts_nop 7" : "+v"(s[8]), "+v"(s[0]));
    PIN();
    static_for<16>([&](auto r) { s[8][r.value] = sel(low_mask(r.value), s[8][r.value], s[0][r.value]); });
    float m = max16(s[8][0], s[8]);
    PIN();
    static_for<7>([&](auto ic) {
      constexpr int i = 7 - ic.value;
      if constexpr (i - 2 >= 1) s[i - 2] = mfma(kf[i - 2], q, splat16(0.f));
      m = max16(m, s[i]);
      PIN();
    });
    cm = splat16(-xhalf_max(m));
  }
  const int vb = LDS_VL + 2 * tl * VBLK + ((lane & 16) ? 512 : (lane >> 5) * 256 + (lane & 15) * 16);
  s[8] = mfma(kf[8], q, cm);
  s[0] = mfma(kf[0], q, cm);
  s[7] = mfma(kf[7], q, cm);
  const v4u mk0 = lds16(L, LDS_MK + lo), mk1 = lds16(L, LDS_MK + 1024 + lo);
  v4u va = lds16(L, vb + 16 * VBLK), vc = lds16(L, vb + 17 * VBLK), v0a = lds16(L, vb), v0c = lds16(L, vb + VBLK);
  asm volatile("s_nop 7\n\ts_nop 7" : "+v"(s[8]), "+v"(s[0]));
  PIN();
  f32x16 o;
  {
    static_for<16>([&](auto r) { s[8][r.value] = sel(low_mask(r.value), s[8][r.value], s[0][r.value]); });
    const v4u p0 = exp_pack(s[8], 0), l0 = p0 & mk0;
    o = mfma(va, l0, splat16(0.f));
    o = mfma(v0a, p0 ^ l0, o);
    const v4u p1 = exp_pack(s[8], 1), l1 = p1 & mk1;
    o = mfma(vc, l1, o);
    o = mfma(v0c, p1 ^ l1, o);
    va = lds16(L, vb + 14 * VBLK); vc = lds16(L, vb + 15 * VBLK);
    PIN();
  }
  static_for<7>([&](auto ic) {
    constexpr int i = 7 - ic.value;
    if constexpr (i > 1) s[i - 1] = mfma(kf[i - 1], q, cm);
    PIN();
    o = mfma(va, exp_pack(s[i], 0), o);
    if constexpr (i > 1) va = lds16(L, vb + (2 * i - 2) * VBLK);
    o = mfma(vc, exp_pack(s[i], 1), o);
    if constexpr (i > 1) vc = lds16(L, vb + (2 * i - 1) * VBLK);
    PIN();
  });
  return o;
}
DI v4u attn_unit(LAS unsigned char* L, int lane, int tl, int nv) {
  f32x16 o = attn_pass<false>(L, lane, tl, nv);
  float big = __builtin_fabsf(o[0]);
#pragma unroll
  for (int r = 1; r < 8; ++r) big += __builtin_fabsf(o[r]);
  if (__builtin_expect(!__all(o[8] > 0x1p-30f && o[8] < 0x1p60f && big < 0x1p120f), 0)) {
    asm volatile("" ::: "memory");
    o = attn_pass<true>(L, lane, tl, nv);
  }
  const float rl = __builtin_amdgcn_rcpf(o[8]);
  float y[8];
#pragma unroll
  for (int r = 0; r < 8; ++r) y[r] = o[r] * rl;
  return pack8(y);
}

struct Retile {
  v4u in[8];
  DI void load(const u16* m, int elems, int el, int ld, int rs4) {
    const rsrc_t r = __builtin_amdgcn_make_buffer_rsrc((void*)m, 0, elems * 2, 0x00020000);
    const int e4 = el + rs4 * ld;
#pragma unroll
    for (int j = 0; j < 8; ++j) in[j] = __builtin_amdgcn_raw_buffer_load_b128(r, (j < 4 ? el : e4) * 2, (j & 3) * ld * 2, 0);
    PIN();
  }
  unsigned d[8];
  DI void out(rsrc_t rs) {
#pragma unroll
    for (int i = 0; i < 8; ++i) st16(rs, in[i], d[i], 0);
  }
  DI void lines(LAS unsigned char* t, int lane, int sw, unsigned dst) {
#pragma unroll
    for (int e = 0; e < 8; ++e) {
      v4u o;
#pragma unroll
      for (int m = 0; m < 4; ++m) o[m] = __builtin_amdgcn_perm(in[2 * m + 1][e >> 1], in[2 * m][e >> 1], (e & 1) ? 0x07060302u : 0x05040100u);
      *(LAS v4u*)(t + lane * 128 + (e ^ sw ^ (lane & 7)) * 16) = o;
    }
    asm volatile("s_waitcnt lgkmcnt(0)" ::: "memory");
#pragma unroll
    for (int i = 0; i < 8; ++i) {
      in[i] = *(LAS v4u*)(t + i * 1024 + (lane ^ (lane >> 3)) * 16);
      d[i] = (unsigned)__builtin_amdgcn_ds_bpermute((8 * i + (lane >> 3)) * 4, (int)dst) + (lane & 7) * 16;
    }
    PIN();
  }
  DI void put(LAS unsigned char* L, int pb, int dd, int lp, int dl) {
#pragma unroll
    for (int e = 0; e < 8; ++e) {
      v4u o;
#pragma unroll
      for (int m = 0; m < 4; ++m) o[m] = __builtin_amdgcn_perm(in[2 * m + 1][e >> 1], in[2 * m][e >> 1], (e & 1) ? 0x07060302u : 0x05040100u);
      const int s = lp + (e & 3) + (e >> 2) * dl;
      *(LAS v4u*)(L + pb + (e >> 2) * dd + (s ^ ((s >> 3) & 7)) * 16) = o;
    }
  }
};

struct GS { int cs, j, kap, off; };
constexpr GS gu_step(int u) {
  if (u < 64) { const int k = u >> 2, j = u & 3; return GS{0, j, k, j < 3 ? (7 * (k >> 1) + 3 * (k & 1) + j) * 1024 : (7 * (k >> 1) + 6) * 1024 + (k & 1) * 512}; }
  const int v = u - 64, j = v / 17, i = v % 17;
  return GS{i == 16, j, 16 + i, 57344 + j * 16384 + i * (j < 3 ? 1024 : 512)};
}
constexpr int GA = 132, DB = 264, DA = 376, NST = 488;
#define NPQ_ 8
#define NPG_ 16
#define APRIO 1
constexpr int NPG = NPG_, NPQ = NPQ_;
#define HS1 28
#define HP1 11
#define HS2 72
#define HP2 12
#define UG 7
#define HB2 6
#define XQS 96
constexpr int ld_step(int id) {
  if (id < 32) return id < NPG ? -1 : 2 * (id - NPG);
  if (id < 64) { const int n = id - 32; return n < 12 ? 103 + (5 * n) / 2 : GA + 10 + 2 * (n - 12); }
  if (id < 92) { const int n = id - 64; return n < 12 ? (UG == 7 ? GA + 103 + n : GA + 88 + (9 * n) / 4) : DB - 15 + ((n - 12) * HB2) / 4; }
  if (id < 120) { const int n = id - 92; return n < 16 ? DB + HS1 + (HP1 * n) / 4 : DB + HS2 + ((n - 16) * HP2) / 4; }
  const int n = id - 120;
  return n < NPQ ? DA + XQS + ((112 - XQS) * n) / NPQ : -1;
}
constexpr int NEP = 56;
constexpr int ep_step(int k) { return 83 + 17 * (k / 16) + k % 16; }
struct Tab { short ld[NST][2]; short ep[NST]; short nvm[NST]; };
#define SL0 20
#define Q3T 75
#define XAE 0
#define XA0 18
constexpr int xa_step(int j) { const int a = XA0 + j, b = XAE == 0 ? (j < 12 ? 3 * j + 2 : 34 + j) : j < XAE ? 0 : j - XAE < 12 ? 3 * (j - XAE) + 2 : 34 + j - XAE; return a > b ? a : b; }
constexpr Tab make_tab() {
  Tab t{};
  for (int s = 0; s < NST; ++s) { t.ld[s][0] = t.ld[s][1] = t.ep[s] = -1; t.nvm[s] = 0; }
  for (int id = 0; id < 152; ++id) {
    const int s = ld_step(id);
    if (s < 0) continue;
    t.ld[s][t.ld[s][0] >= 0] = (short)id; ++t.nvm[s];
  }
  for (int job = 0; job < 2; ++job)
    for (int k = 0; k < NEP; ++k) {
      const int s = job * GA + ep_step(k);
      t.ep[s] = (short)(job * 64 + k);
      if (k == NEP - 1) t.nvm[s] += 2;
      if (k == (job ? 31 : NEP - 1)) t.nvm[s] += 2;
    }
  ++t.nvm[DB + 58]; ++t.nvm[DB + 59]; ++t.nvm[DA + 58]; ++t.nvm[DA + 59];
  return t;
}
constexpr Tab TAB = make_tab();
constexpr int vm_between(int a, int b) {
  int n = 0;
  for (int s = a + 1; s <= b; ++s) n += TAB.nvm[s];
  return n;
}

__global__ void __launch_bounds__(512, 2) k_main(const u16* __restrict__ x, const u16* __restrict__ wqkv, const u16* __restrict__ wo, const u16* __restrict__ wg,
                                                 const u16* __restrict__ wu, const u16* __restrict__ wd, unsigned char* __restrict__ ws,
                                                 u16* __restrict__ out) {
  extern __shared__ __attribute__((aligned(16))) unsigned char smem[];
  LAS unsigned char* L = (LAS unsigned char*)smem;
  const int tid = threadIdx.x, w0 = __builtin_amdgcn_readfirstlane(tid >> 6);
  const int b = blockIdx.x;
  auto touch = [&](rsrc_t r, unsigned voff, unsigned soff) { __builtin_amdgcn_raw_ptr_buffer_load_lds(r, (LAS void*)(L + LDS_BYTES - 256), 4, voff, soff, 0, AUX_SC1); };
  {
    unsigned long long pc0;
    asm volatile("s_getpc_b64 %0" : "=s"(pc0));
    const rsrc_t rc = __builtin_amdgcn_make_buffer_rsrc((void*)(pc0 & ~127ull), 0, CODE_BYTES, 0x00020000);
    if (w0 == 3) __builtin_amdgcn_raw_ptr_buffer_load_lds(rc, (LAS void*)(L + LDS_BYTES - 256), 4, (tid & 63) < CODE_LINES ? (tid & 63) * 128 : NONE, (b >> 3) * CODE_LINES * 128, 0, 0);
    PIN();
  }
  const int sp0 = (b & 6) + ((b >> 3) & 1), c0 = (b >> 4) + 16 * (b & 1);
  const rsrc_t rs = __builtin_amdgcn_make_buffer_rsrc((void*)ws, 0, (int)WS_BYTES, 0x00020000);
  const bool compute = w0 < 4;
  gu32* const fg = (gu32*)(ws + FLAG_G);
  gu32* const tmo = fg + FL_TMO * 32;
  unsigned tag = 0;
#define HOLD(k) do {} while (0)
#define FLAG(blk, line) st4(rs, tag, lane ? NONE : 0u, (blk) + (line) * 128)
#define POLL(blk, line, n) poll((gu32*)(ws + (blk)) + ((line) + lane) * 32, lane < (n), tmo, tag)
#define GETTAG() tag = (unsigned)__builtin_amdgcn_readfirstlane(*(LAS unsigned*)(L + LDS_TAG))
  auto blank = [&](int it, unsigned ly) {
    const int lane = lane_id(), sp = sopq(sp0), c = sopq(c0), w = it & 3, pb = it >= 34;
    const unsigned o = (unsigned)vopq(-1);
    if (it >= 16 && it < 24) st16(rs, (v4u){o, o, o, o}, ((lane >> 4) & 1) * 16384 + (lane >> 5) * 256 + (lane & 15) * 16, ly + L_O + (8 * sp + (it & 7)) * 32768 + c * 512);
    else if (it >= 24 && it < 28) st16(rs, (v4u){o, o, o, o}, (lane >> 4) * 512 + (lane & 15) * 16, ly + L_X1 + ((8 * sp + 4 + 2 * (c & 1) + (w >> 1)) * 32 + (c & ~1)) * 1024 + (w & 1) * 256);
    else if (it == 28 || it == 33 || it == 38) st8(rs, (v2u){o, o}, (lane >> 5) * 4096 + (lane & 31) * 8, ly + (it == 28 ? L_X1 : L_X) + MB2 + (8 * sp + (it == 33 ? 0 : 4) + 2 * (c & 1)) * 4096 + (c >> 1) * 256);
    else if (it >= 40) {
      const int sh = it < 48 ? 6 : it < 52 ? 5 : 4;
      st16(rs, (v4u){o, o, o, o}, __mul24(lane >> sh, 57344) + (lane & ((1 << sh) - 1)) * 16,
           ly + L_H + (16 * sp + (it < 48 ? it & 7 : it < 52 ? 2 * (it & 3) : 4 * (it & 1))) * 57344 + (it < 48 ? c * 1024 : it < 52 ? 32768 + c * 512 : 49152 + c * 256));
    } else st16(rs, (v4u){o, o, o, o}, lane * 16, ly + (it < 16 ? (it & 8 ? L_V : L_K) + ((8 * sp + (it & 7)) * 32 + c) * 1024
                                                  : L_X + ((16 * sp + 8 * pb + 4 * (c & 1) + w) * 16 + (c >> 1)) * 1024));
  };
  auto attention = [&](int layer, unsigned lay0) {
    COORDS();
    const int tl = compute ? 4 + w : w, T = 8 * sp + tl;
    const v4u o = attn_unit(L, lane, tl, sp == 0 ? 8 - tl : 0);
    st16(rs, nos4(o), (g4 & 1) * 16384 + hh * 256 + t16 * 16, lay + L_O + T * 32768 + c * 512);
    if (layer == 0) { DRAIN(); FLAG(flo, FL_O + T * 32 + c); }
    CST();
  };

  if (compute) {
    v4u xq[32];
    {
    const int layer = 0; const unsigned lay0 = LAY_OFF;
    COORDS();
    SETPRIO(2);
    const int r = lane >> 3, j = lane & 7;
    const u16* src = x + (size_t)(256 * sp + 8 * c + r) * D_ + 128 * w + 8 * j;
    v4u a[2] = {*(const v4u*)src, *(const v4u*)(src + 64)};
    if (w < 3) touch(rs, lane ? NONE : 0u, w == 0 ? lay + L_X + (16 * sp + n32) * 16384 : w == 1 ? lay + L_X + MB2 + 8 * sp * 4096 : (FL_XB + sp * 32 + c) * 128);
    PIN();
#pragma unroll
    for (int i = 0; i < 2; ++i) {
      a[i] = nos4(a[i]);
      st16(rs, a[i], (j >> 2) * 1024 + ((j & 3) * 16 + r) * 16, lay + L_X + (16 * sp + n32) * 16384 + (2 * w + i) * 2048 + rh * 128);
      auto quad = [](float v) {
        v += __int_as_float(__builtin_amdgcn_update_dpp(0, __float_as_int(v), 0xb1, 0xf, 0xf, true));
        return v + __int_as_float(__builtin_amdgcn_update_dpp(0, __float_as_int(v), 0x4e, 0xf, 0xf, true));
      };
      float sm = 0.f, q = 0.f;
#pragma unroll
      for (int m = 0; m < 4; ++m) sm += bflo(a[i][m]) + bfhi(a[i][m]);
      sm = quad(sm);
      const float mu = sm * (1.0f / 32.0f);
#pragma unroll
      for (int m = 0; m < 4; ++m) { const float e0 = bflo(a[i][m]) - mu, e1 = bfhi(a[i][m]) - mu; q += e0 * e0 + e1 * e1; }
      q = quad(q);
      st8(rs, (v2u){nos(__float_as_uint(sm)), nos(__float_as_uint(q))}, (j & 3) ? NONE : (j >> 2) * 256 + r * 8,
          lay + L_X + MB2 + (8 * sp + (c >> 2)) * 4096 + (4 * w + 2 * i) * 256 + (c & 3) * 64);
    }
    DRAIN();
    BAR();
    BAR();
    GETTAG();
    const int TB = 8 * sp + 4 + w;
    static_for<NPQ>([&](auto nc) { constexpr int n = nc.value; xq[n] = ld16(rs, lo + ((n >> 1) & 3) * 1024, lay + L_X + TB * 32768 + (n & 1) * 16384 + (n >> 3) * 4096); });
    BARX();
    }
  for (int layer = 0; layer < 2; ++layer) {
    const bool last = layer == 1;
    const unsigned lay0 = LAY_OFF + layer * LAY_BYTES;
    {
      COORDS();
      const int TB = 8 * sp + 4 + w, TA = TB - 4;
      v4u xa[32];
      auto ldx = [&](auto nc, int T) {
        constexpr int n = nc.value;
        return ld16(rs, lo + ((n >> 1) & 3) * 1024, lay + L_X + (2 * T + (n & 1)) * 16384 + (n >> 3) * 4096);
      };
      f32x4 aq[2][2], ak[2][2], av[2][2], r0, r1;
      v4u mf0, mf1; float rd0, rd1;
      unsigned o0, o1, o2, o3;
      const int kpos = (t16 + 16 * hh + 32 * (g4 & 1)) * 16;
      auto epi = [&](int k, int pa) {
        const int tl = pa ? w : 4 + w, st = (pa ? LDS_KL : LDS_ST) + w * 128;
        const unsigned kvb = lay + ((8 * sp + tl) * 32 + c) * 1024;
        if (k == 0) { r0 = lds4f(L, st + g4 * 16); r1 = lds4f(L, st + 64 + g4 * 16); }
        if (k == 2) { o0 = mulpk(ak[pa][0][0], rd0, ak[pa][0][1], rd0); o1 = mulpk(ak[pa][0][2], rd0, ak[pa][0][3], rd0); }
        if (k == 3) { o2 = mulpk(ak[pa][1][0], rd1, ak[pa][1][1], rd1); o3 = mulpk(ak[pa][1][2], rd1, ak[pa][1][3], rd1); }
        if (k == 4) {
          swap32(o0, o2); swap32(o1, o3);
          st16(rs, nos4((v4u){o0, o1, o2, o3}), kpos, kvb + L_K);
          *(LAS v4u*)(L + LDS_KL + (8 + tl) * 1024 + kpos) = (v4u){o0, o1, o2, o3};
        }
        if (k == 5) { o0 = mulpk(av[pa][0][0], r0[0], av[pa][0][1], r0[1]); o1 = mulpk(av[pa][0][2], r0[2], av[pa][0][3], r0[3]); }
        if (k == 6) { o2 = mulpk(av[pa][1][0], r1[0], av[pa][1][1], r1[1]); o3 = mulpk(av[pa][1][2], r1[2], av[pa][1][3], r1[3]); }
        if (k == 7) {
          swap32(o0, o2); swap32(o1, o3);
          st16(rs, nos4((v4u){o0, o1, o2, o3}), lo, kvb + L_V);
          *(LAS v4u*)(L + LDS_VL + (16 + 2 * tl + hh) * VBLK + (lane & 31) * 16) = (v4u){o0, o1, o2, o3};
        }
        if (k == 8) {
          asm volatile("s_nop 7" : "+v"(aq[pa][0]), "+v"(aq[pa][1]));
          const float s0 = rd0 * (0.25f * LOG2E), s1 = rd1 * (0.25f * LOG2E);
          o0 = mulpk(aq[pa][0][0], s0, aq[pa][0][1], s0); o1 = mulpk(aq[pa][0][2], s0, aq[pa][0][3], s0);
          o2 = mulpk(aq[pa][1][0], s1, aq[pa][1][1], s1); o3 = mulpk(aq[pa][1][2], s1, aq[pa][1][3], s1);
          swap32(o0, o2); swap32(o1, o3);
          *(LAS v4u*)(L + LDS_QL + tl * 1024 + kpos) = (v4u){o0, o1, o2, o3};
        }
      };
      pipe<102>(
          [&](auto fc) {
            constexpr int t = fc.value, u = t % 51;
            if constexpr (t == 28) { BARX(); CST(); }
            if constexpr (t == 12 || t == 44 || t == Q3T) BARX();
            if constexpr (u == 44 || u == 45 || u == 50) return csfrag(L, (u == 50 ? 0 : u - 43) * 16 + t16);
            else return lds16(L, (u < 44 ? u : u - 2) * 1024 + lsw);
          },
          [&](auto fc, const v4u& wfr, auto pc) {
            constexpr int t = fc.value, part = pc.value, u = t % 51;
            constexpr int ks = (u < 36 ? u / 3 : u < 44 ? (u - 12) >> 1 : u - 34) & 15, m = u < 36 ? (u % 3 + 1) % 3 : u < 46 ? 1 + (u & 1) : 0;
            constexpr bool pa = t >= 51, cs = u == 44 || u == 45 || u == 50;
            if constexpr (t < 0) return;
            else if constexpr (part == 0) {
              if constexpr (u < 44 && u % 12 == 0) {
                constexpr int n = u / 12 * 8;
                v4u* const X = pa ? xa : xq;
                if (__builtin_expect(missing<8>(X + n), 0)) {
                  unsigned spins = 0;
                  do { static_for<8>([&](auto i) { X[n + i.value] = ldx(IC<n + i.value>{}, pa ? TA : TB); }); DBGC(pa ? 0 : 7); } while (missing<8>(X + n) && ++spins < DATA_CAP);
                }
                TR();
                PIN();
              }
              const v4u& x0 = cs ? mf0 : pa ? xa[2 * ks] : xq[2 * ks];
              const v4u& x1 = cs ? mf1 : pa ? xa[2 * ks + 1] : xq[2 * ks + 1];
              if constexpr (m == 0) { aq[pa][0] = mfma16(wfr, x0, u < 3 ? zero4() : aq[pa][0]); aq[pa][1] = mfma16(wfr, x1, u < 3 ? zero4() : aq[pa][1]); }
              if constexpr (m == 1) { ak[pa][0] = mfma16(wfr, x0, u < 3 ? zero4() : ak[pa][0]); ak[pa][1] = mfma16(wfr, x1, u < 3 ? zero4() : ak[pa][1]); }
              if constexpr (m == 2) { av[pa][0] = mfma16(x0, wfr, u < 3 ? zero4() : av[pa][0]); av[pa][1] = mfma16(x1, wfr, u < 3 ? zero4() : av[pa][1]); }
            } else {
              if constexpr (2 * t < 32 - NPQ) { xq[NPQ + 2 * t] = ldx(IC<NPQ + 2 * t>{}, TB); xq[NPQ + 2 * t + 1] = ldx(IC<NPQ + 2 * t + 1>{}, TB); }
              if constexpr (u == 40) {
                const int st = (pa ? LDS_KL : LDS_ST) + w * 128;
                rd0 = lds1f(L, st + t16 * 4); rd1 = lds1f(L, st + 64 + t16 * 4);
                const int a = g4 ? LDS_TAG + 8 : st + 1024 + t16 * 4;
                mf0 = mfrag(L, a); mf1 = mfrag(L, g4 ? a : a + 64);
              }
              if constexpr (t >= 45 && t < 54) epi(t - 45, 0);
              if constexpr (t >= 96) epi(t - 96, 1);
              static_for<16>([&](auto j) { if constexpr (xa_step(j.value) == t) { xa[2 * j.value] = ldx(IC<2 * j.value>{}, TA); xa[2 * j.value + 1] = ldx(IC<2 * j.value + 1>{}, TA); } });
              if constexpr (t == 50) CST();
            }
          });
      epi(6, 1); epi(7, 1); epi(8, 1);
      BAR();
      CST();
    }
    attention(layer, lay0);
    {
      SETPRIO(0);
      v4u fr[120];
      {
        COORDS();
        const int TB = 8 * sp + 4 + 2 * rh + (w >> 1), SB = 2 * TB + (w & 1);
        const v4u rB = ld16(rs, lo, lay + L_X + (SB * 16 + n32) * 1024), rA = ld16(rs, lo, lay + L_X + (SB * 16 + n32) * 1024 - 8 * 16384);
        const unsigned xv = (g4 >> 1) * 1024 + (g4 & 1) * 512 + t16 * 16, sv = g4 ? NONE : t16 * 8;
        const unsigned xo = lay + L_X1 + (TB * 32 + 2 * n32) * 1024 + (w & 1) * 256, so = lay + L_X1 + MB2 + TB * 4096 + n32 * 256 + (w & 1) * 128;
        const int lswo = vopq(lsw + LDS_WO);
        while ((unsigned)__builtin_amdgcn_readfirstlane(__hip_atomic_load((LAS unsigned*)(L + LDS_TAG + 12), __ATOMIC_RELAXED, __HIP_MEMORY_SCOPE_WORKGROUP)) != 3u * layer + 3u)
          __builtin_amdgcn_s_sleep(1);
        asm volatile("" ::: "memory");
        v4u of[32];
        unsigned pk;
        f32x4 a0, a1;
        Fin fin;
        auto ldo = [&](auto i) { return ld16(rs, lo + (i.value & 3) * 1024, lay + L_O + (SB - 8 * (i.value >> 4)) * 16384 + ((i.value >> 2) & 3) * 4096); };
        pipe<64>(
            [&](auto fc) { return lds16(L, (fc.value & 31) * 1024 + lswo); },
            [&](auto fc, const v4u& wfr, auto pc) {
              constexpr int t = fc.value, part = pc.value, u = t & 31;
              if constexpr (part == 0) {
                if constexpr (u % 16 == 0) {
                  if (__builtin_expect(missing<8>(of + t / 2), 0)) {
                    unsigned spins = 0;
                    do { static_for<16 - u / 2>([&](auto i) { of[t / 2 + i.value] = ldo(IC<t / 2 + i.value>{}); }); DBGC(1 + (t >> 4)); SETTLE(); } while (missing<8>(of + t / 2) && ++spins < DATA_CAP);
                  }
                  if constexpr (u == 0) CST();
                  PIN();
                }
                if constexpr (u & 1) a1 = mfma16(wfr, of[t >> 1], u == 1 ? zero4() : a1);
                else a0 = mfma16(wfr, of[t >> 1], u == 0 ? zero4() : a0);
              } else {
                if constexpr (t == -1) {
                  if (!last) POLL(flo, FL_O + TB * 32, 32);
                  static_for<16>([&](auto i) { of[i.value] = ldo(i); });
                }
                if constexpr (t == 55) pk = __builtin_amdgcn_raw_buffer_load_b32(rs, (lane & 31) * 128, last ? NONE : (FL_X1B + (8 * sp + 4 + w) * 32) * 128, AUX_SC1);
                if constexpr (t == 63) { if (!last && !__all(pk == tag)) POLL(flo, FL_X1B + (8 * sp + 4 + w) * 32, 32); }
                if constexpr (t == 63) static_for<NPG>([&](auto i) { fr[i.value] = ld16(rs, lo + (i.value & 3) * 1024, lay + L_X1 + (8 * sp + 4 + w) * 32768 + (i.value >> 2) * 4096); });
                if constexpr (t == 31 || t == 63) {
                  asm volatile("s_nop 7" : "+v"(a0), "+v"(a1));
                  static_for<8>([&](auto k) { fin.template chunk<k.value>(a0, a1, t == 31 ? rB : rA, rs, xv, xo - (t == 31 ? 0 : 4 * 32768), rs, sv, so - (t == 31 ? 0 : 4 * 4096)); });
                  CST();
                }
                if constexpr (t == 31) {
                  if (!last) {
                    pk = __hip_atomic_load(fl + (FL_O + (TB - 4) * 32 + (lane & 31)) * 32, RLX_AGENT);
                    DRAIN();
                    FLAG(flo, FL_X1B + TB * 32 + 2 * n32 + (w & 1));
                    if (!__all(pk == tag)) POLL(flo, FL_O + (TB - 4) * 32, 32);
                  }
                  static_for<16>([&](auto i) { of[16 + i.value] = ldo(IC<16 + i.value>{}); });
                }
              }
            });
        CST();
      }
      SETPRIO(2);
      BAR();
      {
        COORDS();
        const int TB = 8 * sp + 4 + w;
        const int SD = 16 * sp + 8 + 4 * rh + w, kh = n32 & 1;
        const unsigned x1b = lay + L_X1 + TB * 32768, hb = lay + L_H + SD * 57344, xn = last ? NONE : lay + LAY_BYTES + L_X;
        const unsigned hsv = (g4 & 1) * 57344 + hh * 256 + t16 * 16, hso = lay + L_H + TB * 114688;
        const unsigned pso = lay + L_P + (2 * sp * 32 + (c ^ 2)) * 8192 + w * 2048;
        const int lsw2 = vopq(lsw + 65536), lh = (hh * 16 + t16) * 16, lh2 = vopq(lh + 65536);
        f32x16 acc[2][4];
        f32x4 dd[2][2];
        v4u mf, hv[4];
        float k1[2], den2[2], t0, t1, p0_, p1_;
        unsigned cm0, cm1;
        auto epi = [&](auto ec) {
          constexpr int job = ec.value >> 6, k = ec.value & 63, j = k >> 4, i = (k >> 2) & 3, uo = j < 3 ? 8 : 4;
          const f32x16& a = acc[job][j];
          if constexpr ((k & 3) == 0)
            asm volatile("v_mul_f32 %0, %2, %4\n\tv_mul_f32 %1, %3, %4\n\tv_exp_f32 %0, %0" : "=&v"(t0), "=&v"(t1) : "v"(a[2 * i]), "v"(a[2 * i + 1]), "v"(k1[job]));
          else if constexpr ((k & 3) == 1)
            asm volatile("v_exp_f32 %0, %0\n\tv_mul_f32 %1, %3, %5\n\tv_mul_f32 %2, %4, %6"
                : "+v"(t1), "=&v"(p0_), "=&v"(p1_) : "v"(a[2 * i]), "v"(a[2 * i + 1]), "v"(a[uo + 2 * i]), "v"(a[uo + 2 * i + 1]));
          else if constexpr ((k & 3) == 2)
            asm volatile("v_fma_f32 %0, %0, %2, %2\n\tv_fma_f32 %1, %1, %2, %2\n\tv_rcp_f32 %0, %0" : "+v"(t0), "+v"(t1) : "v"(den2[job]));
          else {
            unsigned d;
            asm volatile("v_rcp_f32 %2, %2\n\tv_mul_f32 %1, %1, %3\n\tv_mul_f32 %2, %2, %4\n\tv_cvt_pk_bf16_f32 %0, %1, %2\n\tv_min_u32 %0, -2, %0" : "=v"(d), "+v"(t0), "+v"(t1) : "v"(p0_), "v"(p1_));
            hv[j][i] = d;
            if constexpr (k == (job ? 31 : NEP - 1)) { st16(rs, hv[0], hsv, hso - job * 4 * 114688 + c * 1024); st16(rs, hv[1], hsv + 512, hso - job * 4 * 114688 + c * 1024); }
            if constexpr (k == NEP - 1) {
              st16(rs, hv[2], hsv, hso - job * 4 * 114688 + 32768 + c * 512);
              st8(rs, (v2u){hv[3][0], hv[3][1]}, hh ? hsv - 248 : hsv, hso - job * 4 * 114688 + 49152 + c * 256);
            }
          }
        };
        auto vload = [&](auto ic) {
          constexpr int id = ic.value;
          if constexpr (id < 64) fr[id] = ld16(rs, lo + (id & 3) * 1024, x1b - (id >> 5) * 4 * 32768 + ((id & 31) >> 2) * 4096);
          else if constexpr (id < 120) {
            constexpr int n = (id - 64) % 28;
            fr[id] = ld16(rs, lo + (n & 3) * 1024, hb - (id >= 92 ? 8 * 57344 : 0) + (n < 16 ? kh * 16384 : n < 24 ? 16384 + kh * 8192 : 24576 + kh * 4096) + (n >> 2) * 4096);
          } else {
            constexpr int n = id - 120;
            xq[n] = ld16(rs, lo + ((n >> 1) & 3) * 1024, xn + TB * 32768 + (n & 1) * 16384 + (n >> 3) * 4096);
          }
        };
        pipe<NST>(
            [&](auto fc) {
              constexpr int t = fc.value;
              if constexpr (t == 32) wait_vm<vm_between(-1, t - LA - 1)>();
              if constexpr (t == GA + 48) wait_vm<vm_between(ep_step(NEP - 1), t - LA - 1)>();
              if constexpr (t < DB) {
                constexpr GS s = gu_step(t % GA);
                if constexpr (s.cs) return csfrag(L, 48 + 32 * s.j + (s.j < 3 ? lane & 31 : t16));
                else {
                  if (s.off % 14336 == 0 && s.off >= (t < GA ? 14336 : 28672)) BARX();
                  return lds16(L, s.off < 65536 ? s.off + (s.j < 3 ? lsw : lh) : s.off - 65536 + (s.j < 3 ? lsw2 : lh2));
                }
              } else {
                constexpr int v = (t - DB) % 112;
                if (t < DA || v == 14 ? v % 14 == 0 : v % 28 == 0 && v) BARX();
                return lds16(L, v < 64 ? v * 1024 + lsw : (v < 96 ? v - 64 : v - 48) * 1024 + lsw2);
              }
            },
            [&](auto fc, const v4u& wfr, auto pc) {
              constexpr int t = fc.value, part = pc.value;
              if constexpr (t < 0) return;
              else if constexpr (part == 0) {
                if constexpr (t < DB) {
                  constexpr int job = t / GA, u = t % GA;
                  constexpr GS s = gu_step(u);
                  if constexpr (t == 0 || t == 16 || t == 32 || t == 64) {
                    constexpr int n0 = t < 64 ? t / 4 : 16, nn = t == 0 || t == 16 ? 4 : t == 32 ? 8 : 16;
                    if (__builtin_expect(t ? __builtin_amdgcn_ballot_w64((cm0 > cm1 ? cm0 : cm1) == 0xffffffffu) != 0 : missing<nn>(fr + n0), 0)) {
                      unsigned spins = 0;
                      do { static_for<(t < 64 ? 16 : 32) - n0>([&](auto i) { vload(IC<n0 + i.value>{}); }); DBGC(t < 64 ? 5 : 6); SETTLE(); } while (missing<nn>(fr + n0) && ++spins < DATA_CAP);
                    }
                    PIN();
                  }
                  acc[job][s.j] = mfma(wfr, s.cs ? mf : fr[32 * job + (s.kap & 31)], u < 4 ? splat16(0.f) : acc[job][s.j]);
                } else {
                  constexpr int job = t >= DA, v = (t - DB) % 112;
                  if constexpr (job && (v == 0 || v == 16 || v == 32 || v == 44)) {
                    constexpr int n0 = 92 + v / 2, nn = v < 32 ? 8 : 6;
                    if (__builtin_expect(__builtin_amdgcn_ballot_w64((cm0 > cm1 ? cm0 : cm1) == 0xffffffffu) != 0, 0)) {
                      unsigned spins = 0;
                      do { static_for<(v < 32 ? 108 : 120) - n0>([&](auto i) { vload(IC<n0 + i.value>{}); }); DBGC(v < 32 ? 4 : 7); SETTLE(); } while (missing<nn>(fr + n0) && ++spins < DATA_CAP);
                    }
                    PIN();
                  }
                  dd[v / 56][v & 1] = mfma16(wfr, fr[64 + 28 * job + ((v % 56) >> 1)], v % 56 < 2 ? zero4() : dd[v / 56][v & 1]);
                }
              } else {
                if constexpr (t < DB) {
                  constexpr int job = t / GA, u = t % GA;
                  if constexpr (u == 74) {
                    const int a = LDS_ST + w * 128 + (lane & 31) * 4;
                    k1[job] = lds1f(L, a); den2[job] = lds1f(L, a + 512); mf = mfrag(L, hh ? LDS_TAG + 8 : a + 1024);
                  }
                  if constexpr (u == 79) k1[job] *= -LOG2E;
                  if constexpr ((t >= 12 && t < 16) || (t >= 24 && t < 32) || (t >= 48 && t < 64)) {
                    constexpr int n = t < 16 ? t - 8 : t < 32 ? t - 16 : t - 32;
                    constexpr bool first = t == 12 || t == 24 || t == 48;
                    cm0 = umax3(first ? 0u : cm0, fr[n][0], fr[n][1]); cm1 = umax3(first ? 0u : cm1, fr[n][2], fr[n][3]);
                    asm volatile("" : "+v"(cm0), "+v"(cm1));
                  }
                }
                if constexpr ((t >= DA - 8 && t < DA) || (t >= DA + 8 && t < DA + 16) || (t >= DA + 26 && t < DA + 32) || (t >= DA + 38 && t < DA + 44)) {
                  constexpr int n = 92 + (t < DA ? t - DA + 8 : t < DA + 16 ? t - DA : t < DA + 32 ? t - DA - 10 : t - DA - 16);
                  constexpr bool first = t == DA - 8 || t == DA + 8 || t == DA + 26 || t == DA + 38;
                  cm0 = umax3(first ? 0u : cm0, fr[n][0], fr[n][1]); cm1 = umax3(first ? 0u : cm1, fr[n][2], fr[n][3]);
                  asm volatile("" : "+v"(cm0), "+v"(cm1));
                }
                if constexpr (TAB.ld[t][0] >= 0) vload(IC<TAB.ld[t][0]>{});
                if constexpr (TAB.ld[t][1] >= 0) vload(IC<TAB.ld[t][1]>{});
                if constexpr (TAB.ep[t] >= 0) epi(IC<TAB.ep[t]>{});
                if constexpr (t == DB + 58 || t == DB + 59 || t == DA + 58 || t == DA + 59)
                  st16(rs, pk24(dd[0][t & 1], tag), lo + (t & 1) * 1024, pso + (t < DA ? 32 * 8192 : 0));
                if constexpr (t == DA + 1 || t == DA + 2) *(LAS f32x4*)(L + LDS_QL + w * 2048 + (t & 1) * 1024 + lo) = dd[1][t & 1];
              }
            });
        CST();
        asm volatile("s_nop 7" : "+v"(dd[1][0]), "+v"(dd[1][1]));
        *(LAS f32x4*)(L + LDS_QL + w * 2048 + lo) = dd[1][0];
        *(LAS f32x4*)(L + LDS_QL + w * 2048 + 1024 + lo) = dd[1][1];
        BAR();
      }
    }
  }
  } else {
    {
    const int layer = 0; const unsigned lay0 = LAY_OFF;
    COORDS();
    SETPRIO(3);
    Retile rt;
    LAS unsigned char* tb = L + LDS_TB + w * 8192;
    Stats st;
    if (w == 0) {
      const int fi = lane >> 3, i8 = lane & 7, F = sopq(6 * sp) + (fi < 6 ? fi : 0);
      const int ks = F < 36 ? F / 3 : F < 44 ? (F - 12) >> 1 : F - 32, m = F < 36 ? (F - 3 * ks + 1) % 3 : F < 44 ? 1 + (F & 1) : 0;
      rt.load(wqkv, 3 * D_ * D_, __mul24(32 * ks + 8 * (i8 >> 1), 3 * D_) + m * D_ + 16 * c + 8 * (i8 & 1), 3 * D_, 4);
      const unsigned ep = __hip_atomic_load(fg + FL_EP * 32, RLX_AGENT);
      v4u mk[2];
#pragma unroll
      for (int k = 0; k < 8; ++k) {
        const int kq = ((2 * k) & 3) + 8 * ((2 * k) >> 2) + 4 * hh, ql = lane & 31;
        mk[k >> 2][k & 3] = (kq <= ql ? 0xffffu : 0u) | (kq + 1 <= ql ? 0xffff0000u : 0u);
      }
      *(LAS v4u*)(L + LDS_MK + lo) = mk[0];
      *(LAS v4u*)(L + LDS_MK + 1024 + lo) = mk[1];
      if (sp == 0) {
#pragma unroll
        for (int i = 0; i < 9; ++i) *(LAS v4u*)(L + LDS_VL + (i < 8 ? i * 1024 + lo : 8192 + t16 * 16)) = (v4u){0u, 0u, 0u, 0u};
      }
      const unsigned one = (sp == 0 && lane < 16) ? 0u : 0x3f803f80u;
      if (lane < 32) *(LAS v4u*)(L + LDS_VL + lane * VBLK + 512) = (v4u){one, one, one, one};
      PIN();
      rt.lines(tb, lane, i8, fi < 6 ? WQKV_OFF + c * 49152 + F * 1024 + i8 * 128 : NONE);
      rt.out(rs);
      tag = __builtin_amdgcn_readfirstlane(ep) + 1u;
      if (lane < 2) *(LAS v4u*)(L + LDS_TAG + lo) = (v4u){lane ? 0u : tag, 0u, 0u, 0u};
      DRAIN();
      HBAR();
      FLAG(0, FL_XB + sp * 32 + c);
      poll((gu32*)ws + (FL_XB + (lane < 32 ? sp * 32 + lane : (lane & 7) * 32 + c)) * 32, lane < 40, tmo, tag);
      st.load(rs, lane, lay + L_X + MB2 + (8 * sp + 4) * 4096);
    } else {
      const int id = 2 * sp + rh + 16 * (w - 1);
      const int t = 64 * id + lane, mo = t / 14, ch = t - __mul24(14, mo), mat = mo >> 6, o = mo & 63, c7 = ch >= 7 ? ch - 7 : ch, j = c7 >> 1, kap = o >> 1;
      const int dj = 4 * (id < 35 ? id - 28 : id - 35) + g4, dn = dj < 16 ? 16 * (n32 & 1) + dj : dj < 24 ? 16 + 8 * (n32 & 1) + dj : 24 + 4 * (n32 & 1) + dj;
      const int slot = (lane >> 2) & 3, m4 = lane & 3, q = (id < 42 ? 4 * dn : 16 * ((id - 42) & 3) + 4 * g4) + slot;
      const int cq = q < 128 ? q >> 2 : q < 192 ? (q - 128) >> 1 : q - 192, r = q < 128 ? q & 3 : q < 192 ? 4 + (q & 1) : 6;
      __builtin_amdgcn_s_sleep(SL0);
      rt.load(id < 14 ? wg : id < 28 ? wu : id < 42 ? wd : wo, id < 42 ? D_ * F_ : D_ * D_,
              id < 28 ? 8 * o * F_ + 112 * n32 + 8 * ch : (id < 42 ? __mul24(56, cq) + (r < 6 ? 16 * (r >> 1) + 4 * (r & 1) : 48) : 16 * (q >> 1) + 4 * (q & 1)) * D_ + 32 * (id < 35 ? n32 ^ 1 : n32) + 8 * m4,
              id < 28 ? F_ : D_, (id < 28 || (id < 42 && r == 6)) ? 4 : 8);
      HBAR();
      if (id < 28) {
        const int cell = kap < 16 ? 7 * (kap >> 1) + (j < 3 ? 3 * (kap & 1) + j : 6) : 56 + 16 * j + (j < 3 ? kap - 16 : (kap - 16) >> 1);
        const int rho = j < 3 ? 2 * mat + (c7 & 1) + 4 * (o & 1) : 4 * (kap & 1) + 2 * (o & 1) + mat;
        rt.lines(tb, lane, j < 3 ? rho : 0, WGU_OFF + (2 * n32 + (ch >= 7)) * 114688 + cell * 1024 + rho * 128);
      } else {
        rt.put(tb, g4 * 2048, 1024, 4 * m4 + 16 * slot, 0);
        asm volatile("s_waitcnt lgkmcnt(0)" ::: "memory");
#pragma unroll
        for (int i = 0; i < 8; ++i) {
          rt.in[i] = lds16(tb, i * 1024 + lo);
          rt.d[i] = (id < 42 ? WDN_OFF + n32 * 114688 + (id - 28) * 8192 : id < 46 ? WO_OFF + n32 * 32768 + (id - 42) * 8192 : NONE) + i * 1024 + lo;
        }
      }
#pragma unroll 1
      for (int it = w - 1; it < 16; it += 3) blank(it, lay);
    }
    HBAR();
    GETTAG();
    if (w) {
      const int l = w - 1;
      dma<4>(rs, L + l * 4096, lo, WQKV_OFF + c * 49152 + l * 4096);
      dma<7>(rs, L + (12 + 7 * l - (l >> 1)) * 1024, lo, WQKV_OFF + c * 49152 + (12 + 7 * l - (l >> 1)) * 1024);
      wait_vm<7>();
    }
    HOLD(0);
    HBAR();
    if (w) { dma<6>(rs, L + (27 + 5 * w) * 1024, lo, WQKV_OFF + c * 49152 + (27 + 5 * w) * 1024);
      rt.out(rs); wait_vm<14>();
    }
    else {
      st.all(L, lane);
      PIN();
      FLAG(0, FL_XA + sp * 32 + c);
      const int i = lane & 31;
      const int line = i < 8 ? FL_KV + (8 * sp + i) * 32 + c : i < 16 ? FL_O + (8 * sp + i - 8) * 32 + c
                     : i < 20 ? FL_X1B + (8 * sp + 4 + 2 * rh + ((i >> 1) & 1)) * 32 + 2 * n32 + (i & 1) : FL_X1A + (i - 20 < 6 ? i - 20 : 5) * 256 + sp * 32 + c;
      touch(rs, hh * FLAG_LAYER + line * 128, 0);
    }
    }
  for (int layer = 0; layer < 2; ++layer) {
    const bool last = layer == 1;
    const unsigned lay0 = LAY_OFF + layer * LAY_BYTES;
    auto ldw = [&](auto nc, unsigned src, int cell) {
      constexpr int N = nc.value, K = (N + 2) / 3;
      const int l = sopq(w0) - 5, c1 = cell + (l * K + K > N ? N - K : l * K);
      dma<K>(rs, L + c1 * 1024, lane_id() * 16, src + c1 * 1024);
    };
    auto ld_gu = [&](int s) { ldw(IC<14>{}, WGU_OFF + sopq(c0) * 114688, 14 * s); };
    {
      COORDS();
      const bool loader = w != 0;
      auto cs_qkv = [&]() {
        const unsigned one = (unsigned)vopq(0x3f803f80);
        const v4u ones = (v4u){one, one, one, one};
        f32x4 a = zero4();
        v4u f[16];
        const int fa = (w - 1) * 1024 + lsw, fb = (w < 3 ? 35 + w : 44) * 1024 + lsw, fs = w < 3 ? 2048 : 1024;
#pragma unroll
        for (int ks = 0; ks < 16; ++ks) asm volatile("ds_read_b128 %0, %1" : "=v"(f[ks]) : "v"(ks < 12 ? fa + ks * 3072 : fb + (ks - 12) * fs) : "memory");
        asm volatile("s_waitcnt lgkmcnt(0)" : "+v"(f[0]), "+v"(f[1]), "+v"(f[2]), "+v"(f[3]), "+v"(f[4]), "+v"(f[5]), "+v"(f[6]), "+v"(f[7]));
        asm volatile("" : "+v"(f[8]), "+v"(f[9]), "+v"(f[10]), "+v"(f[11]), "+v"(f[12]), "+v"(f[13]), "+v"(f[14]), "+v"(f[15]));
#pragma unroll
        for (int ks = 0; ks < 16; ++ks) a = mfma16(f[ks], ones, a);
        if (t16 == 0) {
#pragma unroll
          for (int r = 0; r < 4; ++r) cs_put(L, (w % 3) * 16 + 4 * g4 + r, a[r]);
        }
      };
      const unsigned wob = WO_OFF + n32 * 32768 - LDS_WO;
      if (layer == 0) {
        HBARS(false);
        if (loader) wait_vm<8>();
        HBAR();
        if (loader) { cs_qkv(); DRAIN(); }
        HBAR();
        if (!loader) FLAG(FLAG_G, FL_WR + n32 * 16 + 2 * sp + rh);
        if (TPOS == 2 && w == 3) touch(rs, LAY_OFF + L_O + lane * TSTR + c * 4096 + sp * 128, 0);
      } else {
        HBAR();
        if (loader) ldw(IC<8>{}, wob, 80);
        HBAR();
        if (loader) ldw(IC<8>{}, wob, 88);
      }
      {
        v4u kv[4];
        auto ldkv = [&]() {
          const unsigned so = sp ? lay + ((8 * sp - 5 + w) * 32 + c) * 1024 : NONE, s2 = w == 1 ? so + 3 * 32768 : NONE;
          kv[0] = ld16(rs, lo, so + L_K); kv[1] = ld16(rs, lo, so + L_V); kv[2] = ld16(rs, lo, s2 + L_K); kv[3] = ld16(rs, lo, s2 + L_V);
          PIN();
          DBGC(0);
        };
        if (!loader) {
          Stats st;
          const unsigned bl = lane < 16 ? FLAG_G + (FL_WR + n32 * 16 + lane) * 128 : (FL_XA + (sp ? sp - 1 : 0) * 32 + c) * 128;
          unsigned spins = 0;
          do { st.load(rs, lane, lay + L_X + MB2 + 8 * sp * 4096); DBGC(0); } while (missing<16>((const v4u*)st.p) && ++spins < DATA_CAP);
          const unsigned pv = __builtin_amdgcn_raw_buffer_load_b32(rs, bl, last ? NONE : 0u, AUX_SC1);
          PIN();
          st.all(L, lane, LDS_KL);
          if (!last && !__all(pv == tag)) poll((gu32*)(ws + bl), lane <= 16, tmo, tag);
        }
        HBARS(false);
        SETTLE();
        if (loader) {
          ldkv();
          ldw(IC<32>{}, last ? WGU_OFF + c * 114688 : wob, last ? 48 : 80);
          if (__builtin_expect(missing<4>(kv), 0)) {
            unsigned spins = 0;
            do ldkv(); while (missing<4>(kv) && ++spins < DATA_CAP);
          }
          const int va = LDS_VL + hh * VBLK + (lane & 31) * 16;
          asm volatile("ds_write_b128 %0, %1\n\tds_write_b128 %2, %3" ::"v"(LDS_KL + (3 + w) * 1024 + lo), "v"(kv[0]), "v"(va + (6 + 2 * w) * VBLK), "v"(kv[1]) : "memory");
          if (w == 1) asm volatile("ds_write_b128 %0, %1\n\tds_write_b128 %2, %3" ::"v"(LDS_KL + 7 * 1024 + lo), "v"(kv[2]), "v"(va + 14 * VBLK), "v"(kv[3]) : "memory");
        }
      }
      if (!last) HOLD(1);
      HBAR();
      CST(); CST();
      if (loader) {
        ld_gu(0); ld_gu(1); ld_gu(2); ldw(IC<6>{}, WGU_OFF + c * 114688, 42);
        if (!last) { ldw(IC<8>{}, WGU_OFF + c * 114688, 48); ld_gu(4); ldw(IC<10>{}, WGU_OFF + c * 114688, 70); wait_vm<29>(); }
        else wait_vm<28>();
        if (lane == 0) asm volatile("ds_add_u32 %0, %1" ::"v"(LDS_TAG + 12), "v"(1) : "memory");
      }
      if (sp > 0) {
        LAS unsigned* hw = (LAS unsigned*)(L + LDS_TAG + 4);
        CST();
        if (!loader) {
          v4u kv[8];
          unsigned spins = 0;
          do {
#pragma unroll
            for (int t = 0; t < 4; ++t) {
              const unsigned so = lay + ((8 * sp - 8 + t) * 32 + c) * 1024;
              kv[t] = ld16(rs, lo, so + L_K); kv[4 + t] = ld16(rs, lo, so + L_V);
            }
            PIN();
            DBGC(1);
          } while (missing<8>(kv) && ++spins < DATA_CAP);
#pragma unroll
          for (int t = 0; t < 4; ++t) {
            *(LAS v4u*)(L + LDS_KL + t * 1024 + lo) = kv[t];
            *(LAS v4u*)(L + LDS_VL + (2 * t + hh) * VBLK + (lane & 31) * 16) = kv[4 + t];
          }
          asm volatile("s_waitcnt lgkmcnt(0)" ::: "memory");
          __hip_atomic_store(hw, (unsigned)layer + 1u, __ATOMIC_RELAXED, __HIP_MEMORY_SCOPE_WORKGROUP);
        } else {
          unsigned v;
          do {
            __builtin_amdgcn_s_sleep(1);
            asm volatile("ds_read_b32 %0, %1\n\ts_waitcnt lgkmcnt(0)" : "=v"(v) : "v"(LDS_TAG + 4) : "memory");
          } while ((unsigned)__builtin_amdgcn_readfirstlane(v) != (unsigned)layer + 1u);
        }
      } else CST();
      CST();
      if (TPOS == 0 && layer == 0 && !loader) touch(rs, LAY_OFF + L_O + lane * TSTR + c * 4096 + sp * 128, 0);
      SETPRIO(APRIO);
    }
    attention(layer, lay0);
    {
      COORDS();
      const bool poller = w == 0, loader = w != 0;
      const unsigned wq = WQKV_OFF + c * 49152, wb = WGU_OFF + c * 114688, wdn = WDN_OFF + n32 * 114688;
      unsigned pv = 0;
#define PEEK(blk, line) pv = __hip_atomic_load((gu32*)(ws + (blk)) + ((line) + (lane & 31)) * 32, RLX_AGENT)
#define GATE(blk, line) do { if (!__all(pv == tag)) POLL(blk, line, 32); } while (0)
      f32x4 ca = zero4();
      auto cs_gu = [&](int cell, int n2, bool put) {
        const unsigned one = (unsigned)vopq((w == 3 || t16 == (g4 & 1)) ? 0x3f803f80 : 0);
        const int a = cell * 1024 + (w < 3 ? lsw : lo), d2 = n2 == 1 ? 2048 : 7168;
        v4u f[8];
#pragma unroll
        for (int i = 0; i < 8; ++i) asm volatile("ds_read_b128 %0, %1" : "=v"(f[i]) : "v"(a + (i >> 1) * d2 + (i & 1) * n2 * 1024) : "memory");
        asm volatile("s_waitcnt lgkmcnt(0)" : "+v"(f[0]), "+v"(f[1]), "+v"(f[2]), "+v"(f[3]), "+v"(f[4]), "+v"(f[5]), "+v"(f[6]), "+v"(f[7]));
#pragma unroll
        for (int i = 0; i < 8; ++i) ca = mfma16(f[i], (v4u){one, one, one, one}, ca);
        if (put && t16 < (w < 3 ? 2 : 1)) {
#pragma unroll
          for (int r = 0; r < 4; ++r) cs_put(L, 48 + 32 * w + 16 * t16 + 4 * g4 + r, ca[r]);
        }
      };
      if (!last) {
        unsigned v;
        if (TPOS == 1 && !loader) touch(rs, LAY_OFF + L_O + lane * TSTR + c * 4096 + sp * 128, 0);
        if (loader && lane == 0) asm volatile("ds_add_u32 %0, %1" ::"v"(LDS_TAG + 16), "v"(1) : "memory");
        do asm volatile("ds_read_b32 %0, %1\n\ts_waitcnt lgkmcnt(0)" : "=v"(v) : "v"(LDS_TAG + 16) : "memory");
        while ((unsigned)__builtin_amdgcn_readfirstlane(v) != 3u);
        for (int q = 0; q < 4 - w; ++q) {
          if (w == 3) cs_gu(6, 28, false);
          else cs_gu(q < 2 ? 28 * q + w : 40 + 16 * w + 8 * q, q < 2 ? 3 : 1, q == 3);
        }
      }
      auto tail = [&](auto pc) {
        constexpr bool P = pc.value;
        const int TD = 8 * sp + 4 + 2 * rh + (w >> 1), SD = 2 * TD + (w & 1);
        const unsigned xn = last ? NONE : lay + LAY_BYTES + L_X;
        const rsrc_t ro = last ? __builtin_amdgcn_make_buffer_rsrc((void*)out, 0, (int)MB2, 0x00020000) : rs;
        const unsigned ob = last ? SD * 16384 + n32 * 64 : xn + (SD * 16 + n32) * 1024, sb = xn + MB2 + TD * 4096 + n32 * 256 + (w & 1) * 128;
        const unsigned ov = lo + ((t16 * 1008 - g4 * 240) & -layer), sv = g4 ? NONE : t16 * 8;
        v4u pp[2], res;
        Stats st;
        auto ldp = [&](int pa) {
          const unsigned so = lay + L_P + ((2 * sp + 1 - pa) * 32 + c) * 8192 + w * 2048;
          pp[0] = ld16(rs, lo, so); pp[1] = ld16(rs, lo + 1024, so);
          PIN();
          DBGC(4 + pa);
        };
        auto ldr = [&](int pa) { res = ld16(rs, g4 * 512 + t16 * 16, lay + L_X1 + ((TD - 4 * pa) * 32 + 2 * n32) * 1024 + (w & 1) * 256); };
        auto finish = [&](int pa) {
          f32x4 a0 = lds4f(L, LDS_QL + w * 2048 + lo), a1 = lds4f(L, LDS_QL + w * 2048 + 1024 + lo);
          asm volatile("" : "+v"(a0), "+v"(a1));
          if (pa || PSL < 0) ldp(pa);
          if (P && pa) st.load(rs, lane, xn + MB2 + (8 * sp + 4) * 4096);
          if (pa && !last) { if (!P) wait_vm<2>(); HBARS(false); }
          if (!pa) { HBAR(); if (!P) ldw(IC<22>{}, last ? NONE : wq, 0); }
          TR();
          if (__builtin_expect(!__all(pp[0][3] == tag && pp[1][3] == tag), 0)) {
            unsigned spins = 0;
            do ldp(pa); while (!__all(pp[0][3] == tag && pp[1][3] == tag) && ++spins < DATA_CAP);
          }
          TR();
          a0 += un24(pp[0]); a1 += un24(pp[1]);
          Fin fin;
          static_for<8>([&](auto k) { fin.template chunk<k.value>(a0, a1, res, ro, ov, ob - pa * 8 * 16384, rs, sv, sb - pa * 4 * 4096); });
          TR();
        };
        SETPRIO(3);
        ldr(0);
        HBARS(false);
        if (PSL >= 0) { __builtin_amdgcn_s_sleep(PSL < 0 ? 0 : PSL); ldp(0); }
        HBARS(false);
        finish(0);
        HBARS(false);
        if (!P && !last) ldw(IC<26>{}, wq, 22);
        ldr(1);
        unsigned ev[4];
        const bool adv = last && blockIdx.x == 0;
        if (P) {
#pragma unroll
          for (int q = 0; q < 4; ++q) ev[q] = __builtin_amdgcn_raw_buffer_load_b32(rs, lane * 128, adv ? flo + (FL_HB + 64 * q) * 128 : NONE, AUX_SC1);
        }
        if (last) HOLD(3);
        HBAR();
        if (!P) wait_vm<10>();
        CST();
        if (!last) HOLD(2);
        HBAR();
        finish(1);
        if (P && !last) {
          if (__builtin_expect(missing<16>((const v4u*)st.p), 0)) {
            unsigned spins = 0;
            do { st.load(rs, lane, xn + MB2 + (8 * sp + 4) * 4096); DBGC(3); } while (missing<16>((const v4u*)st.p) && ++spins < DATA_CAP);
          }
          st.all(L, lane);
        }
        if (P && adv) {
          if (!__all(ev[0] == tag && ev[1] == tag && ev[2] == tag && ev[3] == tag))
            for (int q = 0; q < 4; ++q) POLL(flo, FL_HB + 64 * q, 64);
          if (lane == 0) __hip_atomic_store(fg + FL_EP * 32, tag, RLX_AGENT);
        }
      };
      SETPRIO(3);
      if (poller) {
        Stats st;
        const int h2l = FL_HB + (sp ? sp - 1 : 0) * 32 + c;
        SETPRIO(1);
        if (last) HOLD(4);
        HBAR();
        static_for<7>([&](auto nc) {
          constexpr int n = nc.value + 1;
          if (n <= 2) st.load(rs, lane, lay + L_X1 + MB2 + (8 * sp + 4) * 4096, 8 * n - 8, 8 * n);
          if (n == 3) { FLAG(flo, FL_X1A + sp * 32 + c); st.part1(); }
          if (n == 4) {
            if (missing<16>((const v4u*)st.p)) {
              unsigned spins = 0;
              do { st.load(rs, lane, lay + L_X1 + MB2 + (8 * sp + 4) * 4096); DBGC(2); } while (missing<16>((const v4u*)st.p) && ++spins < DATA_CAP);
              st.part1();
            }
            st.part2();
          }
          if (n == 5) st.part3(L, lane);
          if (n == 6) PEEK(flo, FL_X1A + sp * 32);
          if (n == 7) GATE(flo, FL_X1A + sp * 32);
          HBARS(n == 3 || n == 7);
        });
        st.load(rs, lane, lay + L_X1 + MB2 + 8 * sp * 4096);
        st.all(L, lane);
        static_for<6>([&](auto nc) {
          constexpr int n = nc.value + 2;
          if (n == 4) FLAG(flo, FL_HB + sp * 32 + c);
          if (n == UG - 1) PEEK(flo, FL_HB + sp * 32);
          if (n == UG) GATE(flo, FL_HB + sp * 32);
          HBARS(n == 2 || n == 7);
        });
        static_for<8>([&](auto nc) {
          constexpr int n = nc.value;
          if (n == 4) pv = __hip_atomic_load(fl + h2l * 32, RLX_AGENT);
          if (n == 7) { if (!__all(pv == tag)) poll(fl + h2l * 32, true, tmo, tag); }
          else HBARS(n == 0 || n == 3 || n == 6);
        });
        tail(IC<1>{});
      } else {
        DRAIN();
        HBAR();
        static_for<7>([&](auto nc) {
          constexpr int n = nc.value + 1;
          if (n >= 3) blank(30 + 3 * n + w, 30 + 3 * n + w > 53 ? NONE : lay);
          blank(6 * n - 7 + w, last ? NONE : LAY_OFF + LAY_BYTES);
          if (n < 7) blank(6 * n - 4 + w, last ? NONE : LAY_OFF + LAY_BYTES);
          if (n <= 3) ldw(IC<9>{}, wb, 71 + 9 * n);
          if (n == 4) { ldw(IC<5>{}, wb, 107); wait_vm<16>(); }
          if (n == 5) { ldw(IC<16>{}, wdn - 16384, 112); if (!last && w == 1) cs_gu(80, 1, true); wait_vm<20>(); }
          if (n == 6) { if (!last && w == 2) cs_gu(88, 1, false); wait_vm<12>(); }
          if (n == 7 && !last && w >= 2) cs_gu(80 + 8 * w, 1, true);
          HBARS(n == 3 || n == 7);
        });
        static_for<6>([&](auto nc) {
          constexpr int n = nc.value + 2;
          if (n == 3) wait_vm<0>();
          if (n > 3) ldw(IC<(n < 7 ? 21 : 16)>{}, wdn, 21 * n - 84);
          HBARS(n == 2 || n == 7);
        });
        static_for<8>([&](auto nc) {
          constexpr int n = nc.value;
          if (n == 0) ldw(IC<15>{}, wdn, 79);
          if (n == 1) ldw(IC<2>{}, wdn, 94);
          if (n == 2) ldw(IC<16>{}, last ? NONE : WO_OFF + n32 * 32768 - LDS_WO, 96);
          if (n < 7) wait_vm<(n == 0 ? 25 : n == 1 ? 19 : n == 2 ? 25 : n == 3 ? 18 : n == 4 ? 12 : n == 5 ? 7 : 6)>();
          if (n < 7) HBARS(n == 0 || n == 3 || n == 6);
        });
        tail(IC<0>{});
      }
    }
  }
  }
}

}

extern "C" void kernel_launch(void* const* d_in, const int* in_sizes, int n_in, void* d_out, int out_size, void* d_ws, size_t ws_size,
                              hipStream_t stream) {
  (void)in_sizes; (void)n_in; (void)out_size; (void)ws_size;
  (void)hipFuncSetAttribute((const void*)k_main, hipFuncAttributeMaxDynamicSharedMemorySize, LDS_BYTES);
  k_main<<<256, 512, LDS_BYTES, stream>>>((const u16*)d_in[0], (const u16*)d_in[1], (const u16*)d_in[2], (const u16*)d_in[4], (const u16*)d_in[5],
                                          (const u16*)d_in[6], (unsigned char*)d_ws, (u16*)d_out);
}
```

```cpp
#include <hip/hip_runtime.h>
#include <stdint.h>
#include <utility>

#define LAS __attribute__((address_space(3)))
#define GAS __attribute__((address_space(1)))
#define DI __device__ __forceinline__
#define RLX_AGENT __ATOMIC_RELAXED, __HIP_MEMORY_SCOPE_AGENT
typedef unsigned short u16;
typedef unsigned v4u __attribute__((ext_vector_type(4)));
typedef unsigned v2u __attribute__((ext_vector_type(2)));
typedef float f32x2 __attribute__((ext_vector_type(2)));
typedef float f32x4 __attribute__((ext_vector_type(4)));
typedef float f32x16 __attribute__((ext_vector_type(16)));
typedef __bf16 bf16x2 __attribute__((ext_vector_type(2)));
typedef __bf16 bf16x8 __attribute__((ext_vector_type(8)));
typedef GAS unsigned gu32;
typedef GAS unsigned long long gu64;

namespace {

constexpr int D_ = 512, F_ = 1792;
constexpr unsigned MB2 = 2097152u, STB = 262144u;
constexpr unsigned FLAG_LAYER = 1048576u, FLAG_G = 2 * FLAG_LAYER, FLAG_BYTES = FLAG_G + 65536;
constexpr unsigned WQKV_OFF = FLAG_BYTES;
constexpr unsigned WO_OFF = WQKV_OFF + 32 * 49152;
constexpr unsigned WGU_OFF = WO_OFF + 16 * 32768;
constexpr unsigned WDN_OFF = WGU_OFF + 32 * 114688;
constexpr unsigned LAY_OFF = WDN_OFF + 16 * 114688;
constexpr unsigned L_K = 0, L_V = MB2, L_O = 2 * MB2, L_X1 = 3 * MB2, L_X = 4 * MB2 + STB, L_H = 5 * MB2 + 2 * STB;
constexpr unsigned L_P = L_H + 128 * 57344;
constexpr unsigned LAY_BYTES = L_P + 2 * MB2;
constexpr unsigned WS_BYTES = LAY_OFF + 2 * LAY_BYTES;
constexpr unsigned NONE = 0x40000000u;
constexpr int FL_KV = 0, FL_O = 2048, FL_X1B = 4096, FL_X1A = 6144, FL_HB = 6400, FL_HA = 6656, FL_XB = 6912, FL_XA = 7168, FL_HA2 = 7424;
constexpr int FL_WR = 0, FL_EP = 256, FL_TMO = 257;
constexpr int VBLK = 528;
constexpr int LDS_KL = 114688, LDS_VL = LDS_KL + 16384, LDS_QL = LDS_VL + 32 * VBLK, LDS_CS = LDS_QL + 8192, LDS_ST = LDS_CS + 1536;
constexpr int LDS_MK = LDS_ST + 1536, LDS_TAG = LDS_MK + 2048;
constexpr int LDS_WO = 81920, LDS_TB = 49152;
constexpr int LDS_BYTES = 163840;
#define PSL 4
#define TPOS 0
#define TSTR 1048576u
#define CODE_BYTES_ 75264
constexpr int CODE_BYTES = CODE_BYTES_, CODE_LINES = (CODE_BYTES + 4095) / 4096;
constexpr unsigned SPIN_CAP = 1u << 16;
constexpr int AUX_SC1 = 16;
constexpr float LOG2E = 1.4426950408889634f;

DI f32x16 mfma(v4u a, v4u b, f32x16 c) {
  return __builtin_amdgcn_mfma_f32_32x32x16_bf16(__builtin_bit_cast(bf16x8, a), __builtin_bit_cast(bf16x8, b), c, 0, 0, 0);
}
DI f32x4 mfma16(v4u a, v4u b, f32x4 c) {
  return __builtin_amdgcn_mfma_f32_16x16x32_bf16(__builtin_bit_cast(bf16x8, a), __builtin_bit_cast(bf16x8, b), c, 0, 0, 0);
}
DI unsigned pk2(float lo, float hi) {
  f32x2 p = {lo, hi};
  return __builtin_bit_cast(unsigned, __builtin_convertvector(p, bf16x2));
}
DI float bfr(float x) { return __uint_as_float(pk2(x, 0.f) << 16); }
DI float bflo(unsigned u) { return __uint_as_float(u << 16); }
DI float bfhi(unsigned u) { return __uint_as_float(u & 0xffff0000u); }
DI float amul(float a, float b) { float d; asm("v_mul_f32 %0, %1, %2" : "=v"(d) : "v"(a), "v"(b)); return d; }
DI float afma(float a, float b, float c) { float d; asm("v_fma_f32 %0, %1, %2, %3" : "=v"(d) : "v"(a), "v"(b), "v"(c)); return d; }
DI float aadd(float a, float b) { float d; asm("v_add_f32 %0, %1, %2" : "=v"(d) : "v"(a), "v"(b)); return d; }
DI float asub(float a, float b) { float d; asm("v_sub_f32 %0, %1, %2" : "=v"(d) : "v"(a), "v"(b)); return d; }
DI float xhalf_sum(float v) {
  float t;
  asm("v_mov_b32 %1, %0\n\ts_nop 1\n\tv_permlane32_swap_b32 %0, %1\n\tv_add_f32 %0, %0, %1" : "+v"(v), "=&v"(t));
  return v;
}
DI float xrow_sum(float v) {
  float t;
  asm("v_mov_b32 %1, %0\n\ts_nop 1\n\tv_permlane16_swap_b32 %0, %1\n\tv_add_f32 %0, %0, %1" : "+v"(v), "=&v"(t));
  return v;
}
DI float xhalf_max(float v) {
  auto r = __builtin_amdgcn_permlane32_swap(__float_as_uint(v), __float_as_uint(v), false, false);
  return fmaxf(__uint_as_float(r[0]), __uint_as_float(r[1]));
}
DI void swap16(unsigned& a, unsigned& b) { asm("s_nop 1\n\tv_permlane16_swap_b32 %0, %1" : "+v"(a), "+v"(b)); }
DI void swap32(unsigned& a, unsigned& b) { asm("s_nop 1\n\tv_permlane32_swap_b32 %0, %1" : "+v"(a), "+v"(b)); }
DI f32x16 splat16(float v) {
  f32x16 z;
#pragma unroll
  for (int i = 0; i < 16; ++i) z[i] = v;
  return z;
}
DI f32x4 zero4() { return (f32x4){0.f, 0.f, 0.f, 0.f}; }
DI int vopq(int x) { asm volatile("" : "+v"(x)); return x; }
DI int lane_id() { int l; asm volatile("v_mbcnt_lo_u32_b32 %0, -1, 0\n\tv_mbcnt_hi_u32_b32 %0, -1, %0" : "=v"(l)); return l; }
template <int LN> DI unsigned wrl(unsigned v, unsigned s) { asm volatile("v_writelane_b32 %0, %1, %2" : "+v"(v) : "s"(s), "i"(LN)); return v; }
template <class T> DI void undef(T& x) { asm volatile("" : "=v"(x)); }
DI int sopq(int x) { asm volatile("" : "+s"(x)); return x; }
#define SWZ(l) ((l) ^ (((l) >> 3) & 7))
#define COORDS() \
  const int lane = lane_id(), lo = vopq(lane * 16), hh = lane >> 5, t16 = lane & 15, g4 = lane >> 4, lsw = SWZ(lane) * 16; \
  const int w = sopq(w0) & 3, sp = sopq(sp0), c = sopq(c0), n32 = c >> 1, rh = c & 1; \
  const unsigned lay = (unsigned)sopq((int)lay0); \
  const unsigned flo = sopq(layer) * FLAG_LAYER; \
  gu32* const fl = (gu32*)(ws + flo); \
  (void)lo; (void)hh; (void)t16; (void)g4; (void)lsw; (void)n32; (void)rh; (void)lay; (void)sp; (void)c; (void)w; (void)fl;
#define HBARS(c) asm volatile("s_waitcnt lgkmcnt(0)\n\ts_barrier" ::: "memory")
#define CST() do {} while (0)
#define DBGC(k) asm volatile("" ::: "memory")
#define HBAR() asm volatile("s_waitcnt lgkmcnt(0)\n\ts_barrier" ::: "memory")
#define TR() do {} while (0)
#define BAR() asm volatile("s_waitcnt lgkmcnt(0)\n\ts_barrier" ::: "memory")
#define BARX() asm volatile("s_barrier" ::: "memory")
#define DRAIN() asm volatile("s_waitcnt vmcnt(0)" ::: "memory")
#define SETTLE() __builtin_amdgcn_s_waitcnt(0x0f70)
#define PIN() __builtin_amdgcn_sched_barrier(0)
#define SETPRIO(n) __builtin_amdgcn_s_setprio(n)
template <int N> DI void wait_vm() { static_assert(N >= 0 && N < 64, ""); asm volatile("s_waitcnt vmcnt(%0)" ::"i"(N) : "memory"); }

typedef __amdgpu_buffer_rsrc_t rsrc_t;
DI v4u ld16(rsrc_t rs, unsigned voff, unsigned soff) { return __builtin_amdgcn_raw_buffer_load_b128(rs, voff, soff, AUX_SC1); }
DI f32x2 ld8f(rsrc_t rs, unsigned voff, unsigned soff) { return __builtin_bit_cast(f32x2, __builtin_amdgcn_raw_buffer_load_b64(rs, voff, soff, AUX_SC1)); }
DI void st16(rsrc_t rs, v4u v, unsigned voff, unsigned soff) {
  __builtin_amdgcn_raw_buffer_store_b128(v, rs, voff, soff, AUX_SC1);
  asm volatile("s_nop 1" ::"v"(v));
}
DI void st8(rsrc_t rs, v2u v, unsigned voff, unsigned soff) { __builtin_amdgcn_raw_buffer_store_b64(v, rs, voff, soff, AUX_SC1); }
DI void st4(rsrc_t rs, unsigned v, unsigned voff, unsigned soff) { __builtin_amdgcn_raw_buffer_store_b32(v, rs, voff, soff, AUX_SC1); }
DI v4u lds16(LAS unsigned char* L, int off) { return *(LAS v4u*)(L + off); }
DI f32x4 lds4f(LAS unsigned char* L, int off) { return *(LAS f32x4*)(L + off); }

DI void poll(gu32* word, bool active, gu32* tmo, unsigned tag) {
  for (unsigned spins = 0;;) {
    unsigned v = tag;
    if (active) v = __hip_atomic_load(word, RLX_AGENT);
    if (__all(v == tag)) break;
    ++spins;
    if (spins > SPIN_CAP) { __hip_atomic_store(tmo, tag, RLX_AGENT); break; }
    if ((spins & 255u) == 0u && (unsigned)__builtin_amdgcn_readfirstlane(__hip_atomic_load(tmo, RLX_AGENT)) == tag) break;
  }
  __builtin_amdgcn_fence(__ATOMIC_ACQUIRE, "wavefront");
}

template <int I> DI void dma1(rsrc_t rs, LAS unsigned char* dst, unsigned voff, unsigned soff) {
  __builtin_amdgcn_raw_ptr_buffer_load_lds(rs, (LAS void*)dst, 16, voff, soff, I * 1024, AUX_SC1);
}
template <int V> using IC = std::integral_constant<int, V>;
template <class FN, int... I> DI void static_for_(FN fn, std::integer_sequence<int, I...>) { (fn(IC<I>{}), ...); }
template <int N, class FN> DI void static_for(FN fn) { static_for_(fn, std::make_integer_sequence<int, N>{}); }
template <int N> DI void dma(rsrc_t rs, LAS unsigned char* dst, unsigned lo, unsigned src) {
  static_for<N>([&](auto i) { dma1<i.value & 3>(rs, dst + (i.value >> 2) * 4096, lo, src + (i.value >> 2) * 4096); });
}

struct Stats {
  f32x4 p[16];
  float m0, m1, q0, q1;
  DI void load(rsrc_t rs, int lane, unsigned base, int i0 = 0, int i1 = 16) {
#pragma unroll
    for (int i = i0; i < i1; ++i) p[i] = __builtin_bit_cast(f32x4, ld16(rs, (lane >> 4) * 4096 + (lane & 15) * 16 + i * 256, base));
    PIN();
  }
  DI void part1() {
    m0 = m1 = q0 = q1 = 0.f;
#pragma unroll
    for (int i = 0; i < 16; ++i) { m0 += p[i][0]; q0 += p[i][1]; m1 += p[i][2]; q1 += p[i][3]; }
    asm volatile("" : "+v"(m0), "+v"(m1), "+v"(q0), "+v"(q1));
  }
  DI void part2() {
    const float a0 = m0 * (1.0f / 16.0f), a1 = m1 * (1.0f / 16.0f);
    q0 *= 32.0f; q1 *= 32.0f;
#pragma unroll
    for (int i = 0; i < 16; ++i) { const float d0 = p[i][0] - a0, d1 = p[i][2] - a1; q0 += d0 * d0; q1 += d1 * d1; }
    asm volatile("" : "+v"(q0), "+v"(q1));
  }
  DI void all(LAS unsigned char* L, int lane, int dst = LDS_ST) { part1(); part2(); part3(L, lane, dst); }
  DI void part3(LAS unsigned char* L, int lane, int dst = LDS_ST) {
    const float den0 = bfr(__builtin_amdgcn_sqrtf(bfr(bfr(q0 * (1.0f / (32.0f * D_))) + 1e-5f)));
    const float den1 = bfr(__builtin_amdgcn_sqrtf(bfr(bfr(q1 * (1.0f / (32.0f * D_))) + 1e-5f)));
    *(LAS f32x2*)(L + dst + lane * 8) = (f32x2){__builtin_amdgcn_rcpf(den0), __builtin_amdgcn_rcpf(den1)};
    *(LAS f32x2*)(L + dst + 512 + lane * 8) = (f32x2){den0 * den0, den1 * den1};
    *(LAS v2u*)(L + dst + 1024 + lane * 8) = (v2u){pk2(m0 * (-1.0f / D_), m0 * (-1.0f / D_)), pk2(m1 * (-1.0f / D_), m1 * (-1.0f / D_))};
  }
};
DI float lds1f(LAS unsigned char* L, int off) { return *(LAS float*)(L + off); }
DI v4u mfrag(LAS unsigned char* L, int off) { const unsigned n = *(LAS unsigned*)(L + off); return (v4u){n, n, 0u, 0u}; }

constexpr int LA = 6;
template <int NF, class RD, class STEP>
DI void pipe(RD rd, STEP step) {
  v4u wf[8];
  static_for<LA>([&](auto i) { wf[i.value] = rd(i); });
  step(IC<-1>{}, wf[0], IC<1>{});
  PIN();
  static_for<NF>([&](auto fc) {
    constexpr int f = fc.value;
    if constexpr ((f & 1) == 0 && f + LA <= NF) __builtin_amdgcn_s_waitcnt(0xc07f | ((LA - 2) << 8));
    step(fc, wf[f & 7], IC<0>{});
    if constexpr (f + LA < NF) wf[(f + LA) & 7] = rd(IC<f + LA>{});
    step(fc, wf[f & 7], IC<1>{});
    PIN();
  });
}
DI v4u csfrag(LAS unsigned char* L, int entry) {
  const v2u t = *(LAS v2u*)(L + LDS_CS + entry * 8);
  return (v4u){t.x, t.y, 0u, 0u};
}
DI void cs_put(LAS unsigned char* L, int entry, float cs) {
  const float hi = bfr(cs), mid = bfr(cs - hi), lw = (cs - hi) - mid;
  *(LAS v2u*)(L + LDS_CS + entry * 8) = (v2u){pk2(hi, mid), pk2(lw, 0.f)};
}
DI unsigned mulpk(float a, float s0, float b, float s1) {
  unsigned d; float t;
  asm("v_mul_f32 %0, %2, %3\n\tv_mul_f32 %1, %4, %5\n\tv_cvt_pk_bf16_f32 %0, %0, %1" : "=&v"(d), "=&v"(t) : "v"(a), "v"(s0), "v"(b), "v"(s1));
  return d;
}
DI unsigned nos(unsigned v) { return v < 0xfffffffeu ? v : 0xfffffffeu; }
DI v4u nos4(v4u v) { return (v4u){nos(v[0]), nos(v[1]), nos(v[2]), nos(v[3])}; }
DI unsigned umax3(unsigned a, unsigned b, unsigned c) { const unsigned t = a > b ? a : b; return t > c ? t : c; }
template <int N> DI bool missing(const v4u* f, int step = 1) {
  unsigned m0 = 0, m1 = 0;
#pragma unroll
  for (int i = 0; i < N; ++i) { m0 = umax3(m0, f[i * step][0], f[i * step][1]); m1 = umax3(m1, f[i * step][2], f[i * step][3]); }
  return __builtin_amdgcn_ballot_w64((m0 > m1 ? m0 : m1) == 0xffffffffu) != 0;
}
constexpr unsigned DATA_CAP = 1u << 14;
DI v4u pk24(const f32x4& a, unsigned tag) {
  const v4u u = __builtin_bit_cast(v4u, a);
  return (v4u){__builtin_amdgcn_perm(u[0], u[3], 0x07060501u), __builtin_amdgcn_perm(u[1], u[3], 0x07060502u), __builtin_amdgcn_perm(u[2], u[3], 0x07060503u), tag};
}
DI f32x4 un24(const v4u& u) {
  return __builtin_bit_cast(f32x4, (v4u){u[0], u[1], u[2], __builtin_amdgcn_perm(u[2], __builtin_amdgcn_perm(u[1], u[0], 0x0c0c0400u), 0x0401000cu)});
}
DI v4u pack8(const float* y) { return (v4u){pk2(y[0], y[1]), pk2(y[2], y[3]), pk2(y[4], y[5]), pk2(y[6], y[7])}; }

struct Fin {
  float y[8], s;
  v4u o;
  v2u sq;
  template <int K, bool ST = true> DI void chunk(const f32x4& a0, const f32x4& a1, const v4u& r, rsrc_t ro, unsigned vo, unsigned so, rsrc_t rs, unsigned vs, unsigned ss) {
    if constexpr (K == 0) { y[0] = aadd(a0[0], bflo(r[0])); y[1] = aadd(a0[1], bfhi(r[0])); y[2] = aadd(a0[2], bflo(r[1])); y[3] = aadd(a0[3], bfhi(r[1])); }
    if constexpr (K == 1) { y[4] = aadd(a1[0], bflo(r[2])); y[5] = aadd(a1[1], bfhi(r[2])); y[6] = aadd(a1[2], bflo(r[3])); y[7] = aadd(a1[3], bfhi(r[3])); }
    if constexpr (K == 2) { o = nos4(pack8(y)); if constexpr (ST) st16(ro, o, vo, so); }
    if constexpr (K == 3) {
#pragma unroll
      for (int m = 0; m < 4; ++m) { y[2 * m] = bflo(o[m]); y[2 * m + 1] = bfhi(o[m]); }
    }
    if constexpr (K == 4) s = xrow_sum(aadd(aadd(aadd(y[0], y[1]), aadd(y[2], y[3])), aadd(aadd(y[4], y[5]), aadd(y[6], y[7]))));
    if constexpr (K == 5) {
      s = xhalf_sum(s);
      const float mu = s * (1.0f / 32.0f);
#pragma unroll
      for (int i = 0; i < 8; ++i) y[i] = asub(y[i], mu);
    }
    if constexpr (K == 6) {
      y[0] = amul(y[0], y[0]); y[1] = amul(y[1], y[1]);
#pragma unroll
      for (int i = 2; i < 8; ++i) y[i & 1] = afma(y[i], y[i], y[i & 1]);
    }
    if constexpr (K == 7) {
      sq = (v2u){nos(__float_as_uint(s)), nos(__float_as_uint(xhalf_sum(xrow_sum(aadd(y[0], y[1])))))};
      if constexpr (ST) st8(rs, sq, vs, ss);
    }
  }
};

constexpr unsigned long long low_mask(int r) {
  unsigned long long m = 0;
  for (int l = 0; l < 64; ++l)
    if ((r & 3) + 8 * (r >> 2) + 4 * (l >> 5) <= (l & 31)) m |= 1ull << l;
  return m;
}
DI float sel(unsigned long long mask, float set, float unset) {
  float d;
  asm("v_cndmask_b32_e64 %0, %1, %2, %3" : "=v"(d) : "v"(unset), "v"(set), "s"(mask));
  return d;
}
DI float max16(float m, const f32x16& s) {
#pragma unroll
  for (int r = 0; r < 16; r += 2) m = __builtin_fmaxf(__builtin_fmaxf(m, s[r]), s[r + 1]);
  return m;
}
DI v4u exp_pack(const f32x16& s, int h) {
  float pr[8];
#pragma unroll
  for (int r = 0; r < 8; ++r) pr[r] = __builtin_amdgcn_exp2f(s[8 * h + r]);
  return pack8(pr);
}
template <bool EXACT>
DI f32x16 attn_pass(LAS unsigned char* L, int lane, int tl, int nv) {
  const int lo = lane * 16;
  const int kb = LDS_KL + tl * 1024 + lo, kf0 = LDS_KL + (tl + 8) * 1024 + (lane >> 5) * 512;
  const v4u q = lds16(L, LDS_QL + tl * 1024 + lo);
  v4u kf[9];
  kf[8] = lds16(L, kb + 8192); kf[0] = lds16(L, nv <= 0 ? kb : kf0);
#pragma unroll
  for (int i = 7; i >= 1; --i) kf[i] = lds16(L, (nv <= i ? kb : kf0 - i * 1024) + i * 1024);
  f32x16 s[9], cm = splat16(0.f);
  if constexpr (EXACT) {
    s[8] = mfma(kf[8], q, splat16(0.f));
    s[0] = mfma(kf[0], q, splat16(0.f));
    s[7] = mfma(kf[7], q, splat16(0.f));
    s[6] = mfma(kf[6], q, splat16(0.f));
    asm volatile("s_nop 7\n\ts_nop 7" : "+v"(s[8]), "+v"(s[0]));
    PIN();
    static_for<16>([&](auto r) { s[8][r.value] = sel(low_mask(r.value), s[8][r.value], s[0][r.value]); });
    float m = max16(s[8][0], s[8]);
    PIN();
    static_for<7>([&](auto ic) {
      constexpr int i = 7 - ic.value;
      if constexpr (i - 2 >= 1) s[i - 2] = mfma(kf[i - 2], q, splat16(0.f));
      m = max16(m, s[i]);
      PIN();
    });
    cm = splat16(-xhalf_max(m));
  }
  const int vb = LDS_VL + 2 * tl * VBLK + ((lane & 16) ? 512 : (lane >> 5) * 256 + (lane & 15) * 16);
  s[8] = mfma(kf[8], q, cm);
  s[0] = mfma(kf[0], q, cm);
  s[7] = mfma(kf[7], q, cm);
  const v4u mk0 = lds16(L, LDS_MK + lo), mk1 = lds16(L, LDS_MK + 1024 + lo);
  v4u va = lds16(L, vb + 16 * VBLK), vc = lds16(L, vb + 17 * VBLK), v0a = lds16(L, vb), v0c = lds16(L, vb + VBLK);
  asm volatile("s_nop 7\n\ts_nop 7" : "+v"(s[8]), "+v"(s[0]));
  PIN();
  f32x16 o;
  {
    static_for<16>([&](auto r) { s[8][r.value] = sel(low_mask(r.value), s[8][r.value], s[0][r.value]); });
    const v4u p0 = exp_pack(s[8], 0), l0 = p0 & mk0;
    o = mfma(va, l0, splat16(0.f));
    o = mfma(v0a, p0 ^ l0, o);
    const v4u p1 = exp_pack(s[8], 1), l1 = p1 & mk1;
    o = mfma(vc, l1, o);
    o = mfma(v0c, p1 ^ l1, o);
    va = lds16(L, vb + 14 * VBLK); vc = lds16(L, vb + 15 * VBLK);
    PIN();
  }
  static_for<7>([&](auto ic) {
    constexpr int i = 7 - ic.value;
    if constexpr (i > 1) s[i - 1] = mfma(kf[i - 1], q, cm);
    PIN();
    o = mfma(va, exp_pack(s[i], 0), o);
    if constexpr (i > 1) va = lds16(L, vb + (2 * i - 2) * VBLK);
    o = mfma(vc, exp_pack(s[i], 1), o);
    if constexpr (i > 1) vc = lds16(L, vb + (2 * i - 1) * VBLK);
    PIN();
  });
  return o;
}
DI v4u attn_unit(LAS unsigned char* L, int lane, int tl, int nv) {
  f32x16 o = attn_pass<false>(L, lane, tl, nv);
  float big = __builtin_fabsf(o[0]);
#pragma unroll
  for (int r = 1; r < 8; ++r) big += __builtin_fabsf(o[r]);
  if (__builtin_expect(!__all(o[8] > 0x1p-30f && o[8] < 0x1p60f && big < 0x1p120f), 0)) {
    asm volatile("" ::: "memory");
    o = attn_pass<true>(L, lane, tl, nv);
  }
  const float rl = __builtin_amdgcn_rcpf(o[8]);
  float y[8];
#pragma unroll
  for (int r = 0; r < 8; ++r) y[r] = o[r] * rl;
  return pack8(y);
}

struct Retile {
  v4u in[8];
  DI void load(const u16* m, int elems, int el, int ld, int rs4) {
    const rsrc_t r = __builtin_amdgcn_make_buffer_rsrc((void*)m, 0, elems * 2, 0x00020000);
    const int e4 = el + rs4 * ld;
#pragma unroll
    for (int j = 0; j < 8; ++j) in[j] = __builtin_amdgcn_raw_buffer_load_b128(r, (j < 4 ? el : e4) * 2, (j & 3) * ld * 2, 0);
    PIN();
  }
  unsigned d[8];
  DI void out(rsrc_t rs) {
#pragma unroll
    for (int i = 0; i < 8; ++i) st16(rs, in[i], d[i], 0);
  }
  DI void lines(LAS unsigned char* t, int lane, int sw, unsigned dst) {
#pragma unroll
    for (int e = 0; e < 8; ++e) {
      v4u o;
#pragma unroll
      for (int m = 0; m < 4; ++m) o[m] = __builtin_amdgcn_perm(in[2 * m + 1][e >> 1], in[2 * m][e >> 1], (e & 1) ? 0x07060302u : 0x05040100u);
      *(LAS v4u*)(t + lane * 128 + (e ^ sw ^ (lane & 7)) * 16) = o;
    }
    asm volatile("s_waitcnt lgkmcnt(0)" ::: "memory");
#pragma unroll
    for (int i = 0; i < 8; ++i) {
      in[i] = *(LAS v4u*)(t + i * 1024 + (lane ^ (lane >> 3)) * 16);
      d[i] = (unsigned)__builtin_amdgcn_ds_bpermute((8 * i + (lane >> 3)) * 4, (int)dst) + (lane & 7) * 16;
    }
    PIN();
  }
  DI void put(LAS unsigned char* L, int pb, int dd, int lp, int dl) {
#pragma unroll
    for (int e = 0; e < 8; ++e) {
      v4u o;
#pragma unroll
      for (int m = 0; m < 4; ++m) o[m] = __builtin_amdgcn_perm(in[2 * m + 1][e >> 1], in[2 * m][e >> 1], (e & 1) ? 0x07060302u : 0x05040100u);
      const int s = lp + (e & 3) + (e >> 2) * dl;
      *(LAS v4u*)(L + pb + (e >> 2) * dd + (s ^ ((s >> 3) & 7)) * 16) = o;
    }
  }
};

struct GS { int cs, j, kap, off; };
constexpr GS gu_step(int u) {
  if (u < 64) { const int k = u >> 2, j = u & 3; return GS{0, j, k, j < 3 ? (7 * (k >> 1) + 3 * (k & 1) + j) * 1024 : (7 * (k >> 1) + 6) * 1024 + (k & 1) * 512}; }
  const int v = u - 64, j = v / 17, i = v % 17;
  return GS{i == 16, j, 16 + i, 57344 + j * 16384 + i * (j < 3 ? 1024 : 512)};
}
constexpr int GA = 132, DB = 264, DA = 376, NST = 488;
#define NPQ_ 8
#define NPG_ 16
#define APRIO 1
constexpr int NPG = NPG_, NPQ = NPQ_;
#define HS1 28
#define HP1 11
#define HS2 72
#define HP2 12
#define XW 2
#define HW 3
#define NB1 4
#define XAN 12
#define LK2 0
#define KHB 3
#define UG 7
#define HB2 6
#define XQS 96
constexpr int ld_step(int id) {
  if (id < 32) return id < NPG ? -1 : (NB1 * (id - NPG)) / 2;
  if (id < 64) { const int n = id - 32; return n < 12 ? 103 + (5 * n) / 2 : n < XAN ? GA + (n - 12) / 2 : GA + 10 + 2 * (n - XAN); }
  if (id < 92) { const int n = id - 64; return n < 12 ? (UG == 7 ? GA + 103 + n : GA + 88 + (9 * n) / 4) : DB - 15 + ((n - 12) * HB2) / 4; }
  if (id < 120) { const int n = id - 92; return n < 16 ? DB + HS1 + (HP1 * n) / 4 : DB + HS2 + ((n - 16) * HP2) / 4; }
  const int n = id - 120;
  return n < NPQ ? DA + XQS + ((112 - XQS) * n) / NPQ : -1;
}
constexpr int NEP = 56;
constexpr int ep_step(int k) { return 83 + 17 * (k / 16) + k % 16; }
struct Tab { short ld[NST][2]; short ep[NST]; short nvm[NST]; };
#define SL0 20
#define Q3T 75
#define XAE 0
#define XA0 18
constexpr int xa_step(int j) { const int a = XA0 + j, b = XAE == 0 ? (j < 12 ? 3 * j + 2 : 34 + j) : j < XAE ? 0 : j - XAE < 12 ? 3 * (j - XAE) + 2 : 34 + j - XAE; return a > b ? a : b; }
constexpr Tab make_tab() {
  Tab t{};
  for (int s = 0; s < NST; ++s) { t.ld[s][0] = t.ld[s][1] = t.ep[s] = -1; t.nvm[s] = 0; }
  for (int id = 0; id < 152; ++id) {
    const int s = ld_step(id);
    if (s < 0) continue;
    t.ld[s][t.ld[s][0] >= 0] = (short)id; ++t.nvm[s];
  }
  for (int job = 0; job < 2; ++job)
    for (int k = 0; k < NEP; ++k) {
      const int s = job * GA + ep_step(k);
      t.ep[s] = (short)(job * 64 + k);
      if (k == NEP - 1) t.nvm[s] += 2;
      if (k == (job ? 31 : NEP - 1)) t.nvm[s] += 2;
    }
  ++t.nvm[DB + 58]; ++t.nvm[DB + 59]; ++t.nvm[DA + 58]; ++t.nvm[DA + 59];
  return t;
}
constexpr Tab TAB = make_tab();
constexpr bool tab_ok() {
  int a = 0, b = 0;
  for (int id = 0; id < 152; ++id) a += ld_step(id) >= 0;
  for (int s = 0; s < NST; ++s) b += (TAB.ld[s][0] >= 0) + (TAB.ld[s][1] >= 0);
  return a == b;
}
static_assert(tab_ok(), "");
constexpr int vm_between(int a, int b) {
  int n = 0;
  for (int s = a + 1; s <= b; ++s) n += TAB.nvm[s];
  return n;
}

__global__ void __launch_bounds__(512, 2) k_main(const u16* __restrict__ x, const u16* __restrict__ wqkv, const u16* __restrict__ wo, const u16* __restrict__ wg,
                                                 const u16* __restrict__ wu, const u16* __restrict__ wd, unsigned char* __restrict__ ws,
                                                 u16* __restrict__ out) {
  extern __shared__ __attribute__((aligned(16))) unsigned char smem[];
  LAS unsigned char* L = (LAS unsigned char*)smem;
  const int tid = threadIdx.x, w0 = __builtin_amdgcn_readfirstlane(tid >> 6);
  const int b = blockIdx.x;
  auto touch = [&](rsrc_t r, unsigned voff, unsigned soff) { __builtin_amdgcn_raw_ptr_buffer_load_lds(r, (LAS void*)(L + LDS_BYTES - 256), 4, voff, soff, 0, AUX_SC1); };
  {
    unsigned long long pc0;
    asm volatile("s_getpc_b64 %0" : "=s"(pc0));
    const rsrc_t rc = __builtin_amdgcn_make_buffer_rsrc((void*)(pc0 & ~127ull), 0, CODE_BYTES, 0x00020000);
    if (w0 == 3) __builtin_amdgcn_raw_ptr_buffer_load_lds(rc, (LAS void*)(L + LDS_BYTES - 256), 4, (tid & 63) < CODE_LINES ? (tid & 63) * 128 : NONE, (b >> 3) * CODE_LINES * 128, 0, 0);
    PIN();
  }
  const int sp0 = (b & 6) + ((b >> 3) & 1), c0 = (b >> 4) + 16 * (b & 1);
  const rsrc_t rs = __builtin_amdgcn_make_buffer_rsrc((void*)ws, 0, (int)WS_BYTES, 0x00020000);
  const bool compute = w0 < 4;
  gu32* const fg = (gu32*)(ws + FLAG_G);
  gu32* const tmo = fg + FL_TMO * 32;
  unsigned tag = 0;
#define HOLD(k) do {} while (0)
#define FLAG(blk, line) st4(rs, tag, lane ? NONE : 0u, (blk) + (line) * 128)
#define POLL(blk, line, n) poll((gu32*)(ws + (blk)) + ((line) + lane) * 32, lane < (n), tmo, tag)
#define GETTAG() tag = (unsigned)__builtin_amdgcn_readfirstlane(*(LAS unsigned*)(L + LDS_TAG))
  auto blank = [&](int it, unsigned ly) {
    const int lane = lane_id(), sp = sopq(sp0), c = sopq(c0), w = it & 3, pb = it >= 34;
    const unsigned o = (unsigned)vopq(-1);
    if (it >= 16 && it < 24) st16(rs, (v4u){o, o, o, o}, ((lane >> 4) & 1) * 16384 + (lane >> 5) * 256 + (lane & 15) * 16, ly + L_O + (8 * sp + (it & 7)) * 32768 + c * 512);
    else if (it >= 24 && it < 28) st16(rs, (v4u){o, o, o, o}, (lane >> 4) * 512 + (lane & 15) * 16, ly + L_X1 + ((8 * sp + 4 + 2 * (c & 1) + (w >> 1)) * 32 + (c & ~1)) * 1024 + (w & 1) * 256);
    else if (it == 28 || it == 33 || it == 38) st8(rs, (v2u){o, o}, (lane >> 5) * 4096 + (lane & 31) * 8, ly + (it == 28 ? L_X1 : L_X) + MB2 + (8 * sp + (it == 33 ? 0 : 4) + 2 * (c & 1)) * 4096 + (c >> 1) * 256);
    else if (it >= 40) {
      const int sh = it < 48 ? 6 : it < 52 ? 5 : 4;
      st16(rs, (v4u){o, o, o, o}, __mul24(lane >> sh, 57344) + (lane & ((1 << sh) - 1)) * 16,
           ly + L_H + (16 * sp + (it < 48 ? it & 7 : it < 52 ? 2 * (it & 3) : 4 * (it & 1))) * 57344 + (it < 48 ? c * 1024 : it < 52 ? 32768 + c * 512 : 49152 + c * 256));
    } else st16(rs, (v4u){o, o, o, o}, lane * 16, ly + (it < 16 ? (it & 8 ? L_V : L_K) + ((8 * sp + (it & 7)) * 32 + c) * 1024
                                                  : L_X + ((16 * sp + 8 * pb + 4 * (c & 1) + w) * 16 + (c >> 1)) * 1024));
  };
  auto attention = [&](int layer, unsigned lay0) {
    COORDS();
    const int tl = compute ? 4 + w : w, T = 8 * sp + tl;
    const v4u o = attn_unit(L, lane, tl, sp == 0 ? 8 - tl : 0);
    st16(rs, nos4(o), (g4 & 1) * 16384 + hh * 256 + t16 * 16, lay + L_O + T * 32768 + c * 512);
    if (layer == 0) { DRAIN(); FLAG(flo, FL_O + T * 32 + c); }
    CST();
  };

  if (compute) {
    v4u xq[32];
    {
    const int layer = 0; const unsigned lay0 = LAY_OFF;
    COORDS();
    SETPRIO(2);
    const int r = lane >> 3, j = lane & 7;
    const u16* src = x + (size_t)(256 * sp + 8 * c + r) * D_ + 128 * w + 8 * j;
    v4u a[2] = {*(const v4u*)src, *(const v4u*)(src + 64)};
    if (w < 3) touch(rs, lane ? NONE : 0u, w == 0 ? lay + L_X + (16 * sp + n32) * 16384 : w == 1 ? lay + L_X + MB2 + 8 * sp * 4096 : (FL_XB + sp * 32 + c) * 128);
    PIN();
#pragma unroll
    for (int i = 0; i < 2; ++i) {
      a[i] = nos4(a[i]);
      st16(rs, a[i], (j >> 2) * 1024 + ((j & 3) * 16 + r) * 16, lay + L_X + (16 * sp + n32) * 16384 + (2 * w + i) * 2048 + rh * 128);
      auto quad = [](float v) {
        v += __int_as_float(__builtin_amdgcn_update_dpp(0, __float_as_int(v), 0xb1, 0xf, 0xf, true));
        return v + __int_as_float(__builtin_amdgcn_update_dpp(0, __float_as_int(v), 0x4e, 0xf, 0xf, true));
      };
      float sm = 0.f, q = 0.f;
#pragma unroll
      for (int m = 0; m < 4; ++m) sm += bflo(a[i][m]) + bfhi(a[i][m]);
      sm = quad(sm);
      const float mu = sm * (1.0f / 32.0f);
#pragma unroll
      for (int m = 0; m < 4; ++m) { const float e0 = bflo(a[i][m]) - mu, e1 = bfhi(a[i][m]) - mu; q += e0 * e0 + e1 * e1; }
      q = quad(q);
      st8(rs, (v2u){nos(__float_as_uint(sm)), nos(__float_as_uint(q))}, (j & 3) ? NONE : (j >> 2) * 256 + r * 8,
          lay + L_X + MB2 + (8 * sp + (c >> 2)) * 4096 + (4 * w + 2 * i) * 256 + (c & 3) * 64);
    }
    DRAIN();
    BAR();
    BAR();
    GETTAG();
    const int TB = 8 * sp + 4 + w;
    static_for<NPQ>([&](auto nc) { constexpr int n = nc.value; xq[n] = ld16(rs, lo + ((n >> 1) & 3) * 1024, lay + L_X + TB * 32768 + (n & 1) * 16384 + (n >> 3) * 4096); });
    BARX();
    }
  for (int layer = 0; layer < 2; ++layer) {
    const bool last = layer == 1;
    const unsigned lay0 = LAY_OFF + layer * LAY_BYTES;
    {
      COORDS();
      const int TB = 8 * sp + 4 + w, TA = TB - 4;
      v4u xa[32];
      auto ldx = [&](auto nc, int T) {
        constexpr int n = nc.value;
        return ld16(rs, lo + ((n >> 1) & 3) * 1024, lay + L_X + (2 * T + (n & 1)) * 16384 + (n >> 3) * 4096);
      };
      f32x4 aq[2][2], ak[2][2], av[2][2], r0, r1;
      v4u mf0, mf1; float rd0, rd1;
      unsigned o0, o1, o2, o3;
      const int kpos = (t16 + 16 * hh + 32 * (g4 & 1)) * 16;
      auto epi = [&](int k, int pa) {
        const int tl = pa ? w : 4 + w, st = (pa ? LDS_KL : LDS_ST) + w * 128;
        const unsigned kvb = lay + ((8 * sp + tl) * 32 + c) * 1024;
        if (k == 0) { r0 = lds4f(L, st + g4 * 16); r1 = lds4f(L, st + 64 + g4 * 16); }
        if (k == 2) { o0 = mulpk(ak[pa][0][0], rd0, ak[pa][0][1], rd0); o1 = mulpk(ak[pa][0][2], rd0, ak[pa][0][3], rd0); }
        if (k == 3) { o2 = mulpk(ak[pa][1][0], rd1, ak[pa][1][1], rd1); o3 = mulpk(ak[pa][1][2], rd1, ak[pa][1][3], rd1); }
        if (k == 4) {
          swap32(o0, o2); swap32(o1, o3);
          st16(rs, nos4((v4u){o0, o1, o2, o3}), kpos, kvb + L_K);
          *(LAS v4u*)(L + LDS_KL + (8 + tl) * 1024 + kpos) = (v4u){o0, o1, o2, o3};
        }
        if (k == 5) { o0 = mulpk(av[pa][0][0], r0[0], av[pa][0][1], r0[1]); o1 = mulpk(av[pa][0][2], r0[2], av[pa][0][3], r0[3]); }
        if (k == 6) { o2 = mulpk(av[pa][1][0], r1[0], av[pa][1][1], r1[1]); o3 = mulpk(av[pa][1][2], r1[2], av[pa][1][3], r1[3]); }
        if (k == 7) {
          swap32(o0, o2); swap32(o1, o3);
          st16(rs, nos4((v4u){o0, o1, o2, o3}), lo, kvb + L_V);
          *(LAS v4u*)(L + LDS_VL + (16 + 2 * tl + hh) * VBLK + (lane & 31) * 16) = (v4u){o0, o1, o2, o3};
        }
        if (k == 8) {
          asm volatile("s_nop 7" : "+v"(aq[pa][0]), "+v"(aq[pa][1]));
          const float s0 = rd0 * (0.25f * LOG2E), s1 = rd1 * (0.25f * LOG2E);
          o0 = mulpk(aq[pa][0][0], s0, aq[pa][0][1], s0); o1 = mulpk(aq[pa][0][2], s0, aq[pa][0][3], s0);
          o2 = mulpk(aq[pa][1][0], s1, aq[pa][1][1], s1); o3 = mulpk(aq[pa][1][2], s1, aq[pa][1][3], s1);
          swap32(o0, o2); swap32(o1, o3);
          *(LAS v4u*)(L + LDS_QL + tl * 1024 + kpos) = (v4u){o0, o1, o2, o3};
        }
      };
      pipe<102>(
          [&](auto fc) {
            constexpr int t = fc.value, u = t % 51;
            if constexpr (t == 28) { BARX(); CST(); }
            if constexpr (t == 12 || t == 44 || t == Q3T) BARX();
            if constexpr (u == 44 || u == 45 || u == 50) return csfrag(L, (u == 50 ? 0 : u - 43) * 16 + t16);
            else return lds16(L, (u < 44 ? u : u - 2) * 1024 + lsw);
          },
          [&](auto fc, const v4u& wfr, auto pc) {
            constexpr int t = fc.value, part = pc.value, u = t % 51;
            constexpr int ks = (u < 36 ? u / 3 : u < 44 ? (u - 12) >> 1 : u - 34) & 15, m = u < 36 ? (u % 3 + 1) % 3 : u < 46 ? 1 + (u & 1) : 0;
            constexpr bool pa = t >= 51, cs = u == 44 || u == 45 || u == 50;
            if constexpr (t < 0) return;
            else if constexpr (part == 0) {
              if constexpr (u < 44 && u % 12 == 0) {
                constexpr int n = u / 12 * 8;
                v4u* const X = pa ? xa : xq;
                if (__builtin_expect(missing<8>(X + n), 0)) {
                  unsigned spins = 0;
                  do { static_for<8>([&](auto i) { X[n + i.value] = ldx(IC<n + i.value>{}, pa ? TA : TB); }); DBGC(pa ? 0 : 7); } while (missing<8>(X + n) && ++spins < DATA_CAP);
                }
                TR();
                PIN();
              }
              const v4u& x0 = cs ? mf0 : pa ? xa[2 * ks] : xq[2 * ks];
              const v4u& x1 = cs ? mf1 : pa ? xa[2 * ks + 1] : xq[2 * ks + 1];
              if constexpr (m == 0) { aq[pa][0] = mfma16(wfr, x0, u < 3 ? zero4() : aq[pa][0]); aq[pa][1] = mfma16(wfr, x1, u < 3 ? zero4() : aq[pa][1]); }
              if constexpr (m == 1) { ak[pa][0] = mfma16(wfr, x0, u < 3 ? zero4() : ak[pa][0]); ak[pa][1] = mfma16(wfr, x1, u < 3 ? zero4() : ak[pa][1]); }
              if constexpr (m == 2) { av[pa][0] = mfma16(x0, wfr, u < 3 ? zero4() : av[pa][0]); av[pa][1] = mfma16(x1, wfr, u < 3 ? zero4() : av[pa][1]); }
            } else {
              if constexpr (2 * t < 32 - NPQ) { xq[NPQ + 2 * t] = ldx(IC<NPQ + 2 * t>{}, TB); xq[NPQ + 2 * t + 1] = ldx(IC<NPQ + 2 * t + 1>{}, TB); }
              if constexpr (u == 40) {
                const int st = (pa ? LDS_KL : LDS_ST) + w * 128;
                rd0 = lds1f(L, st + t16 * 4); rd1 = lds1f(L, st + 64 + t16 * 4);
                const int a = g4 ? LDS_TAG + 8 : st + 1024 + t16 * 4;
                mf0 = mfrag(L, a); mf1 = mfrag(L, g4 ? a : a + 64);
              }
              if constexpr (t >= 45 && t < 54) epi(t - 45, 0);
              if constexpr (t >= 96) epi(t - 96, 1);
              static_for<16>([&](auto j) { if constexpr (xa_step(j.value) == t) { xa[2 * j.value] = ldx(IC<2 * j.value>{}, TA); xa[2 * j.value + 1] = ldx(IC<2 * j.value + 1>{}, TA); } });
              if constexpr (t == 50) CST();
            }
          });
      epi(6, 1); epi(7, 1); epi(8, 1);
      BAR();
      CST();
    }
    attention(layer, lay0);
    {
      SETPRIO(0);
      v4u fr[120];
      {
        COORDS();
        const int TB = 8 * sp + 4 + 2 * rh + (w >> 1), SB = 2 * TB + (w & 1);
        const v4u rB = ld16(rs, lo, lay + L_X + (SB * 16 + n32) * 1024), rA = ld16(rs, lo, lay + L_X + (SB * 16 + n32) * 1024 - 8 * 16384);
        const unsigned xv = (g4 >> 1) * 1024 + (g4 & 1) * 512 + t16 * 16, sv = g4 ? NONE : t16 * 8;
        const unsigned xo = lay + L_X1 + (TB * 32 + 2 * n32) * 1024 + (w & 1) * 256, so = lay + L_X1 + MB2 + TB * 4096 + n32 * 256 + (w & 1) * 128;
        const int lswo = vopq(lsw + LDS_WO);
        while ((unsigned)__builtin_amdgcn_readfirstlane(__hip_atomic_load((LAS unsigned*)(L + LDS_TAG + 12), __ATOMIC_RELAXED, __HIP_MEMORY_SCOPE_WORKGROUP)) != 3u * layer + 3u)
          __builtin_amdgcn_s_sleep(1);
        asm volatile("" ::: "memory");
        v4u of[32];
        unsigned pk;
        f32x4 a0, a1;
        Fin fin;
        auto ldo = [&](auto i) { return ld16(rs, lo + (i.value & 3) * 1024, lay + L_O + (SB - 8 * (i.value >> 4)) * 16384 + ((i.value >> 2) & 3) * 4096); };
        pipe<64>(
            [&](auto fc) { return lds16(L, (fc.value & 31) * 1024 + lswo); },
            [&](auto fc, const v4u& wfr, auto pc) {
              constexpr int t = fc.value, part = pc.value, u = t & 31;
              if constexpr (part == 0) {
                if constexpr (u % 16 == 0) {
                  if (__builtin_expect(missing<8>(of + t / 2), 0)) {
                    unsigned spins = 0;
                    do { static_for<16 - u / 2>([&](auto i) { of[t / 2 + i.value] = ldo(IC<t / 2 + i.value>{}); }); DBGC(1 + (t >> 4)); SETTLE(); } while (missing<8>(of + t / 2) && ++spins < DATA_CAP);
                  }
                  if constexpr (u == 0) CST();
                  PIN();
                }
                if constexpr (u & 1) a1 = mfma16(wfr, of[t >> 1], u == 1 ? zero4() : a1);
                else a0 = mfma16(wfr, of[t >> 1], u == 0 ? zero4() : a0);
              } else {
                if constexpr (t == -1) {
                  if (!last) POLL(flo, FL_O + TB * 32, 32);
                  static_for<16>([&](auto i) { of[i.value] = ldo(i); });
                }
                if constexpr (t == 55) pk = __builtin_amdgcn_raw_buffer_load_b32(rs, (lane & 31) * 128, last ? NONE : (FL_X1B + (8 * sp + 4 + w) * 32) * 128, AUX_SC1);
                if constexpr (t == 63) { if (!last && !__all(pk == tag)) POLL(flo, FL_X1B + (8 * sp + 4 + w) * 32, 32); }
                if constexpr (t == 63) static_for<NPG>([&](auto i) { fr[i.value] = ld16(rs, lo + (i.value & 3) * 1024, lay + L_X1 + (8 * sp + 4 + w) * 32768 + (i.value >> 2) * 4096); });
                if constexpr (t == 31 || t == 63) {
                  asm volatile("s_nop 7" : "+v"(a0), "+v"(a1));
                  static_for<8>([&](auto k) { fin.template chunk<k.value>(a0, a1, t == 31 ? rB : rA, rs, xv, xo - (t == 31 ? 0 : 4 * 32768), rs, sv, so - (t == 31 ? 0 : 4 * 4096)); });
                  CST();
                }
                if constexpr (t == 31) {
                  if (!last) {
                    pk = __hip_atomic_load(fl + (FL_O + (TB - 4) * 32 + (lane & 31)) * 32, RLX_AGENT);
                    DRAIN();
                    FLAG(flo, FL_X1B + TB * 32 + 2 * n32 + (w & 1));
                    if (!__all(pk == tag)) POLL(flo, FL_O + (TB - 4) * 32, 32);
                  }
                  static_for<16>([&](auto i) { of[16 + i.value] = ldo(IC<16 + i.value>{}); });
                }
              }
            });
        CST();
      }
      SETPRIO(2);
      BAR();
      {
        COORDS();
        const int TB = 8 * sp + 4 + w;
        const int SD = 16 * sp + 8 + 4 * rh + w, kh = (n32 >> KHB) & 1;
        const unsigned x1b = lay + L_X1 + TB * 32768, hb = lay + L_H + SD * 57344, xn = last ? NONE : lay + LAY_BYTES + L_X;
        const unsigned hsv = (g4 & 1) * 57344 + hh * 256 + t16 * 16, hso = lay + L_H + TB * 114688;
        const unsigned pso = lay + L_P + (2 * sp * 32 + (c ^ (2 << KHB))) * 8192 + w * 2048;
        const int lsw2 = vopq(lsw + 65536), lh = (hh * 16 + t16) * 16, lh2 = vopq(lh + 65536);
        f32x16 acc[2][4];
        f32x4 dd[2][2];
        v4u mf, hv[4];
        float k1[2], den2[2], t0, t1, p0_, p1_;
        unsigned cm0, cm1;
        auto epi = [&](auto ec) {
          constexpr int job = ec.value >> 6, k = ec.value & 63, j = k >> 4, i = (k >> 2) & 3, uo = j < 3 ? 8 : 4;
          const f32x16& a = acc[job][j];
          if constexpr ((k & 3) == 0)
            asm volatile("v_mul_f32 %0, %2, %4\n\tv_mul_f32 %1, %3, %4\n\tv_exp_f32 %0, %0" : "=&v"(t0), "=&v"(t1) : "v"(a[2 * i]), "v"(a[2 * i + 1]), "v"(k1[job]));
          else if constexpr ((k & 3) == 1)
            asm volatile("v_exp_f32 %0, %0\n\tv_mul_f32 %1, %3, %5\n\tv_mul_f32 %2, %4, %6"
                : "+v"(t1), "=&v"(p0_), "=&v"(p1_) : "v"(a[2 * i]), "v"(a[2 * i + 1]), "v"(a[uo + 2 * i]), "v"(a[uo + 2 * i + 1]));
          else if constexpr ((k & 3) == 2)
            asm volatile("v_fma_f32 %0, %0, %2, %2\n\tv_fma_f32 %1, %1, %2, %2\n\tv_rcp_f32 %0, %0" : "+v"(t0), "+v"(t1) : "v"(den2[job]));
          else {
            unsigned d;
            asm volatile("v_rcp_f32 %2, %2\n\tv_mul_f32 %1, %1, %3\n\tv_mul_f32 %2, %2, %4\n\tv_cvt_pk_bf16_f32 %0, %1, %2\n\tv_min_u32 %0, -2, %0" : "=v"(d), "+v"(t0), "+v"(t1) : "v"(p0_), "v"(p1_));
            hv[j][i] = d;
            if constexpr (k == (job ? 31 : NEP - 1)) { st16(rs, hv[0], hsv, hso - job * 4 * 114688 + c * 1024); st16(rs, hv[1], hsv + 512, hso - job * 4 * 114688 + c * 1024); }
            if constexpr (k == NEP - 1) {
              st16(rs, hv[2], hsv, hso - job * 4 * 114688 + 32768 + c * 512);
              st8(rs, (v2u){hv[3][0], hv[3][1]}, hh ? hsv - 248 : hsv, hso - job * 4 * 114688 + 49152 + c * 256);
            }
          }
        };
        auto vload = [&](auto ic) {
          constexpr int id = ic.value;
          if constexpr (id < 64) fr[id] = ld16(rs, lo + (id & 3) * 1024, x1b - (id >> 5) * 4 * 32768 + ((id & 31) >> 2) * 4096);
          else if constexpr (id < 120) {
            constexpr int n = (id - 64) % 28;
            fr[id] = ld16(rs, lo + (n & 3) * 1024, hb - (id >= 92 ? 8 * 57344 : 0) + (n < 16 ? kh * 16384 : n < 24 ? 16384 + kh * 8192 : 24576 + kh * 4096) + (n >> 2) * 4096);
          } else {
            constexpr int n = id - 120;
            xq[n] = ld16(rs, lo + ((n >> 1) & 3) * 1024, xn + TB * 32768 + (n & 1) * 16384 + (n >> 3) * 4096);
          }
        };
        pipe<NST>(
            [&](auto fc) {
              constexpr int t = fc.value;
              if constexpr (t == 16 * XW) wait_vm<vm_between(-1, t - LA - 1)>();
              if constexpr (t == GA + 16 * HW) wait_vm<vm_between(ep_step(NEP - 1), t - LA - 1)>();
              if constexpr (t < DB) {
                constexpr GS s = gu_step(t % GA);
                if constexpr (s.cs) return csfrag(L, 48 + 32 * s.j + (s.j < 3 ? lane & 31 : t16));
                else {
                  if (s.off % 14336 == 0 && s.off >= (t < GA ? 14336 : 28672)) BARX();
                  return lds16(L, s.off < 65536 ? s.off + (s.j < 3 ? lsw : lh) : s.off - 65536 + (s.j < 3 ? lsw2 : lh2));
                }
              } else {
                constexpr int v = (t - DB) % 112;
                if (t < DA || v == 14 ? v % 14 == 0 : v % 28 == 0 && v) BARX();
                return lds16(L, v < 64 ? v * 1024 + lsw : (v < 96 ? v - 64 : v - 48) * 1024 + lsw2);
              }
            },
            [&](auto fc, const v4u& wfr, auto pc) {
              constexpr int t = fc.value, part = pc.value;
              if constexpr (t < 0) return;
              else if constexpr (part == 0) {
                if constexpr (t < DB) {
                  constexpr int job = t / GA, u = t % GA;
                  constexpr GS s = gu_step(u);
                  if constexpr (t == 0 || t == 16 || t == 32 || t == 64 || (LK2 && t == 72)) {
                    constexpr int n0 = t < 64 ? t / 4 : t - 48, nn = t == 0 || t == 16 ? 4 : t == 32 || LK2 ? 8 : 16;
                    if (__builtin_expect(t ? __builtin_amdgcn_ballot_w64((cm0 > cm1 ? cm0 : cm1) == 0xffffffffu) != 0 : missing<nn>(fr + n0), 0)) {
                      unsigned spins = 0;
                      do { static_for<(t < 64 ? 16 : 32) - n0>([&](auto i) { vload(IC<n0 + i.value>{}); }); DBGC(t < 64 ? 5 : 6); SETTLE(); } while (missing<nn>(fr + n0) && ++spins < DATA_CAP);
                    }
                    PIN();
                  }
                  acc[job][s.j] = mfma(wfr, s.cs ? mf : fr[32 * job + (s.kap & 31)], u < 4 ? splat16(0.f) : acc[job][s.j]);
                } else {
                  constexpr int job = t >= DA, v = (t - DB) % 112;
                  if constexpr (job && (v == 0 || v == 16 || v == 32 || v == 44)) {
                    constexpr int n0 = 92 + v / 2, nn = v < 32 ? 8 : 6;
                    if (__builtin_expect(__builtin_amdgcn_ballot_w64((cm0 > cm1 ? cm0 : cm1) == 0xffffffffu) != 0, 0)) {
                      unsigned spins = 0;
                      do { static_for<(v < 32 ? 108 : 120) - n0>([&](auto i) { vload(IC<n0 + i.value>{}); }); DBGC(v < 32 ? 4 : 7); SETTLE(); } while (missing<nn>(fr + n0) && ++spins < DATA_CAP);
                    }
                    PIN();
                  }
                  dd[v / 56][v & 1] = mfma16(wfr, fr[64 + 28 * job + ((v % 56) >> 1)], v % 56 < 2 ? zero4() : dd[v / 56][v & 1]);
                }
              } else {
                if constexpr (t < DB) {
                  constexpr int job = t / GA, u = t % GA;
                  if constexpr (u == 74) {
                    const int a = LDS_ST + w * 128 + (lane & 31) * 4;
                    k1[job] = lds1f(L, a); den2[job] = lds1f(L, a + 512); mf = mfrag(L, hh ? LDS_TAG + 8 : a + 1024);
                  }
                  if constexpr (u == 79) k1[job] *= -LOG2E;
                  if constexpr ((t >= 12 && t < 16) || (t >= 24 && t < 32) || (t >= 48 + 8 * LK2 && t < 64 + 8 * LK2)) {
                    constexpr int n = t < 16 ? t - 8 : t < 32 ? t - 16 : t - 32 - 8 * LK2;
                    constexpr bool first = t == 12 || t == 24 || t == 48 + 8 * LK2 || (LK2 && t == 64);
                    cm0 = umax3(first ? 0u : cm0, fr[n][0], fr[n][1]); cm1 = umax3(first ? 0u : cm1, fr[n][2], fr[n][3]);
                    asm volatile("" : "+v"(cm0), "+v"(cm1));
                  }
                }
                if constexpr ((t >= DA - 8 && t < DA) || (t >= DA + 8 && t < DA + 16) || (t >= DA + 26 && t < DA + 32) || (t >= DA + 38 && t < DA + 44)) {
                  constexpr int n = 92 + (t < DA ? t - DA + 8 : t < DA + 16 ? t - DA : t < DA + 32 ? t - DA - 10 : t - DA - 16);
                  constexpr bool first = t == DA - 8 || t == DA + 8 || t == DA + 26 || t == DA + 38;
                  cm0 = umax3(first ? 0u : cm0, fr[n][0], fr[n][1]); cm1 = umax3(first ? 0u : cm1, fr[n][2], fr[n][3]);
                  asm volatile("" : "+v"(cm0), "+v"(cm1));
                }
                if constexpr (TAB.ld[t][0] >= 0) vload(IC<TAB.ld[t][0]>{});
                if constexpr (TAB.ld[t][1] >= 0) vload(IC<TAB.ld[t][1]>{});
                if constexpr (TAB.ep[t] >= 0) epi(IC<TAB.ep[t]>{});
                if constexpr (t == DB + 58 || t == DB + 59 || t == DA + 58 || t == DA + 59)
                  st16(rs, pk24(dd[0][t & 1], tag), lo + (t & 1) * 1024, pso + (t < DA ? 32 * 8192 : 0));
                if constexpr (t == DA + 1 || t == DA + 2) *(LAS f32x4*)(L + LDS_QL + w * 2048 + (t & 1) * 1024 + lo) = dd[1][t & 1];
              }
            });
        CST();
        asm volatile("s_nop 7" : "+v"(dd[1][0]), "+v"(dd[1][1]));
        *(LAS f32x4*)(L + LDS_QL + w * 2048 + lo) = dd[1][0];
        *(LAS f32x4*)(L + LDS_QL + w * 2048 + 1024 + lo) = dd[1][1];
        BAR();
      }
    }
  }
  } else {
    {
    const int layer = 0; const unsigned lay0 = LAY_OFF;
    COORDS();
    SETPRIO(3);
    Retile rt;
    LAS unsigned char* tb = L + LDS_TB + w * 8192;
    Stats st;
    if (w == 0) {
      const int fi = lane >> 3, i8 = lane & 7, F = sopq(6 * sp) + (fi < 6 ? fi : 0);
      const int ks = F < 36 ? F / 3 : F < 44 ? (F - 12) >> 1 : F - 32, m = F < 36 ? (F - 3 * ks + 1) % 3 : F < 44 ? 1 + (F & 1) : 0;
      rt.load(wqkv, 3 * D_ * D_, __mul24(32 * ks + 8 * (i8 >> 1), 3 * D_) + m * D_ + 16 * c + 8 * (i8 & 1), 3 * D_, 4);
      const unsigned ep = __hip_atomic_load(fg + FL_EP * 32, RLX_AGENT);
      v4u mk[2];
#pragma unroll
      for (int k = 0; k < 8; ++k) {
        const int kq = ((2 * k) & 3) + 8 * ((2 * k) >> 2) + 4 * hh, ql = lane & 31;
        mk[k >> 2][k & 3] = (kq <= ql ? 0xffffu : 0u) | (kq + 1 <= ql ? 0xffff0000u : 0u);
      }
      *(LAS v4u*)(L + LDS_MK + lo) = mk[0];
      *(LAS v4u*)(L + LDS_MK + 1024 + lo) = mk[1];
      if (sp == 0) {
#pragma unroll
        for (int i = 0; i < 9; ++i) *(LAS v4u*)(L + LDS_VL + (i < 8 ? i * 1024 + lo : 8192 + t16 * 16)) = (v4u){0u, 0u, 0u, 0u};
      }
      const unsigned one = (sp == 0 && lane < 16) ? 0u : 0x3f803f80u;
      if (lane < 32) *(LAS v4u*)(L + LDS_VL + lane * VBLK + 512) = (v4u){one, one, one, one};
      PIN();
      rt.lines(tb, lane, i8, fi < 6 ? WQKV_OFF + c * 49152 + F * 1024 + i8 * 128 : NONE);
      rt.out(rs);
      tag = __builtin_amdgcn_readfirstlane(ep) + 1u;
      if (lane < 2) *(LAS v4u*)(L + LDS_TAG + lo) = (v4u){lane ? 0u : tag, 0u, 0u, 0u};
      DRAIN();
      HBAR();
      FLAG(0, FL_XB + sp * 32 + c);
      poll((gu32*)ws + (FL_XB + (lane < 32 ? sp * 32 + lane : (lane & 7) * 32 + c)) * 32, lane < 40, tmo, tag);
      st.load(rs, lane, lay + L_X + MB2 + (8 * sp + 4) * 4096);
    } else {
      const int id = 2 * sp + rh + 16 * (w - 1);
      const int t = 64 * id + lane, mo = t / 14, ch = t - __mul24(14, mo), mat = mo >> 6, o = mo & 63, c7 = ch >= 7 ? ch - 7 : ch, j = c7 >> 1, kap = o >> 1;
      const int dj = 4 * (id < 35 ? id - 28 : id - 35) + g4, kh = (n32 >> KHB) & 1, dn = dj < 16 ? 16 * kh + dj : dj < 24 ? 16 + 8 * kh + dj : 24 + 4 * kh + dj;
      const int slot = (lane >> 2) & 3, m4 = lane & 3, q = (id < 42 ? 4 * dn : 16 * ((id - 42) & 3) + 4 * g4) + slot;
      const int cq = q < 128 ? q >> 2 : q < 192 ? (q - 128) >> 1 : q - 192, r = q < 128 ? q & 3 : q < 192 ? 4 + (q & 1) : 6;
      __builtin_amdgcn_s_sleep(SL0);
      rt.load(id < 14 ? wg : id < 28 ? wu : id < 42 ? wd : wo, id < 42 ? D_ * F_ : D_ * D_,
              id < 28 ? 8 * o * F_ + 112 * n32 + 8 * ch : (id < 42 ? __mul24(56, cq) + (r < 6 ? 16 * (r >> 1) + 4 * (r & 1) : 48) : 16 * (q >> 1) + 4 * (q & 1)) * D_ + 32 * (id < 35 ? n32 ^ (1 << KHB) : n32) + 8 * m4,
              id < 28 ? F_ : D_, (id < 28 || (id < 42 && r == 6)) ? 4 : 8);
      HBAR();
      if (id < 28) {
        const int cell = kap < 16 ? 7 * (kap >> 1) + (j < 3 ? 3 * (kap & 1) + j : 6) : 56 + 16 * j + (j < 3 ? kap - 16 : (kap - 16) >> 1);
        const int rho = j < 3 ? 2 * mat + (c7 & 1) + 4 * (o & 1) : 4 * (kap & 1) + 2 * (o & 1) + mat;
        rt.lines(tb, lane, j < 3 ? rho : 0, WGU_OFF + (2 * n32 + (ch >= 7)) * 114688 + cell * 1024 + rho * 128);
      } else {
        rt.put(tb, g4 * 2048, 1024, 4 * m4 + 16 * slot, 0);
        asm volatile("s_waitcnt lgkmcnt(0)" ::: "memory");
#pragma unroll
        for (int i = 0; i < 8; ++i) {
          rt.in[i] = lds16(tb, i * 1024 + lo);
          rt.d[i] = (id < 42 ? WDN_OFF + n32 * 114688 + (id - 28) * 8192 : id < 46 ? WO_OFF + n32 * 32768 + (id - 42) * 8192 : NONE) + i * 1024 + lo;
        }
      }
#pragma unroll 1
      for (int it = w - 1; it < 16; it += 3) blank(it, lay);
    }
    HBAR();
    GETTAG();
    if (w) {
      const int l = w - 1;
      dma<4>(rs, L + l * 4096, lo, WQKV_OFF + c * 49152 + l * 4096);
      dma<7>(rs, L + (12 + 7 * l - (l >> 1)) * 1024, lo, WQKV_OFF + c * 49152 + (12 + 7 * l - (l >> 1)) * 1024);
      wait_vm<7>();
    }
    HOLD(0);
    HBAR();
    if (w) { dma<6>(rs, L + (27 + 5 * w) * 1024, lo, WQKV_OFF + c * 49152 + (27 + 5 * w) * 1024);
      rt.out(rs); wait_vm<14>();
    }
    else {
      st.all(L, lane);
      PIN();
      FLAG(0, FL_XA + sp * 32 + c);
      const int i = lane & 31;
      const int line = i < 8 ? FL_KV + (8 * sp + i) * 32 + c : i < 16 ? FL_O + (8 * sp + i - 8) * 32 + c
                     : i < 20 ? FL_X1B + (8 * sp + 4 + 2 * rh + ((i >> 1) & 1)) * 32 + 2 * n32 + (i & 1) : FL_X1A + (i - 20 < 6 ? i - 20 : 5) * 256 + sp * 32 + c;
      touch(rs, hh * FLAG_LAYER + line * 128, 0);
    }
    }
  for (int layer = 0; layer < 2; ++layer) {
    const bool last = layer == 1;
    const unsigned lay0 = LAY_OFF + layer * LAY_BYTES;
    auto ldw = [&](auto nc, unsigned src, int cell) {
      constexpr int N = nc.value, K = (N + 2) / 3;
      const int l = sopq(w0) - 5, c1 = cell + (l * K + K > N ? N - K : l * K);
      dma<K>(rs, L + c1 * 1024, lane_id() * 16, src + c1 * 1024);
    };
    auto ld_gu = [&](int s) { ldw(IC<14>{}, WGU_OFF + sopq(c0) * 114688, 14 * s); };
    {
      COORDS();
      const bool loader = w != 0;
      auto cs_qkv = [&]() {
        const unsigned one = (unsigned)vopq(0x3f803f80);
        const v4u ones = (v4u){one, one, one, one};
        f32x4 a = zero4();
        v4u f[16];
        const int fa = (w - 1) * 1024 + lsw, fb = (w < 3 ? 35 + w : 44) * 1024 + lsw, fs = w < 3 ? 2048 : 1024;
#pragma unroll
        for (int ks = 0; ks < 16; ++ks) asm volatile("ds_read_b128 %0, %1" : "=v"(f[ks]) : "v"(ks < 12 ? fa + ks * 3072 : fb + (ks - 12) * fs) : "memory");
        asm volatile("s_waitcnt lgkmcnt(0)" : "+v"(f[0]), "+v"(f[1]), "+v"(f[2]), "+v"(f[3]), "+v"(f[4]), "+v"(f[5]), "+v"(f[6]), "+v"(f[7]));
        asm volatile("" : "+v"(f[8]), "+v"(f[9]), "+v"(f[10]), "+v"(f[11]), "+v"(f[12]), "+v"(f[13]), "+v"(f[14]), "+v"(f[15]));
#pragma unroll
        for (int ks = 0; ks < 16; ++ks) a = mfma16(f[ks], ones, a);
        if (t16 == 0) {
#pragma unroll
          for (int r = 0; r < 4; ++r) cs_put(L, (w % 3) * 16 + 4 * g4 + r, a[r]);
        }
      };
      const unsigned wob = WO_OFF + n32 * 32768 - LDS_WO;
      if (layer == 0) {
        HBARS(false);
        if (loader) wait_vm<8>();
        HBAR();
        if (loader) { cs_qkv(); DRAIN(); }
        HBAR();
        if (!loader) FLAG(FLAG_G, FL_WR + n32 * 16 + 2 * sp + rh);
        if (TPOS == 2 && w == 3) touch(rs, LAY_OFF + L_O + lane * TSTR + c * 4096 + sp * 128, 0);
      } else {
        HBAR();
        if (loader) ldw(IC<8>{}, wob, 80);
        HBAR();
        if (loader) ldw(IC<8>{}, wob, 88);
      }
      {
        v4u kv[4];
        auto ldkv = [&]() {
          const unsigned so = sp ? lay + ((8 * sp - 5 + w) * 32 + c) * 1024 : NONE, s2 = w == 1 ? so + 3 * 32768 : NONE;
          kv[0] = ld16(rs, lo, so + L_K); kv[1] = ld16(rs, lo, so + L_V); kv[2] = ld16(rs, lo, s2 + L_K); kv[3] = ld16(rs, lo, s2 + L_V);
          PIN();
          DBGC(0);
        };
        if (!loader) {
          Stats st;
          const unsigned bl = lane < 16 ? FLAG_G + (FL_WR + n32 * 16 + lane) * 128 : (FL_XA + (sp ? sp - 1 : 0) * 32 + c) * 128;
          unsigned spins = 0;
          do { st.load(rs, lane, lay + L_X + MB2 + 8 * sp * 4096); DBGC(0); } while (missing<16>((const v4u*)st.p) && ++spins < DATA_CAP);
          const unsigned pv = __builtin_amdgcn_raw_buffer_load_b32(rs, bl, last ? NONE : 0u, AUX_SC1);
          PIN();
          st.all(L, lane, LDS_KL);
          if (!last && !__all(pv == tag)) poll((gu32*)(ws + bl), lane <= 16, tmo, tag);
        }
        HBARS(false);
        SETTLE();
        if (loader) {
          ldkv();
          ldw(IC<32>{}, last ? WGU_OFF + c * 114688 : wob, last ? 48 : 80);
          if (__builtin_expect(missing<4>(kv), 0)) {
            unsigned spins = 0;
            do ldkv(); while (missing<4>(kv) && ++spins < DATA_CAP);
          }
          const int va = LDS_VL + hh * VBLK + (lane & 31) * 16;
          asm volatile("ds_write_b128 %0, %1\n\tds_write_b128 %2, %3" ::"v"(LDS_KL + (3 + w) * 1024 + lo), "v"(kv[0]), "v"(va + (6 + 2 * w) * VBLK), "v"(kv[1]) : "memory");
          if (w == 1) asm volatile("ds_write_b128 %0, %1\n\tds_write_b128 %2, %3" ::"v"(LDS_KL + 7 * 1024 + lo), "v"(kv[2]), "v"(va + 14 * VBLK), "v"(kv[3]) : "memory");
        }
      }
      if (!last) HOLD(1);
      HBAR();
      CST(); CST();
      if (loader) {
        ld_gu(0); ld_gu(1); ld_gu(2); ldw(IC<6>{}, WGU_OFF + c * 114688, 42);
        if (!last) { ldw(IC<8>{}, WGU_OFF + c * 114688, 48); ld_gu(4); ldw(IC<10>{}, WGU_OFF + c * 114688, 70); wait_vm<29>(); }
        else wait_vm<28>();
        if (lane == 0) asm volatile("ds_add_u32 %0, %1" ::"v"(LDS_TAG + 12), "v"(1) : "memory");
      }
      if (sp > 0) {
        LAS unsigned* hw = (LAS unsigned*)(L + LDS_TAG + 4);
        CST();
        if (!loader) {
          v4u kv[8];
          unsigned spins = 0;
          do {
#pragma unroll
            for (int t = 0; t < 4; ++t) {
              const unsigned so = lay + ((8 * sp - 8 + t) * 32 + c) * 1024;
              kv[t] = ld16(rs, lo, so + L_K); kv[4 + t] = ld16(rs, lo, so + L_V);
            }
            PIN();
            DBGC(1);
          } while (missing<8>(kv) && ++spins < DATA_CAP);
#pragma unroll
          for (int t = 0; t < 4; ++t) {
            *(LAS v4u*)(L + LDS_KL + t * 1024 + lo) = kv[t];
            *(LAS v4u*)(L + LDS_VL + (2 * t + hh) * VBLK + (lane & 31) * 16) = kv[4 + t];
          }
          asm volatile("s_waitcnt lgkmcnt(0)" ::: "memory");
          __hip_atomic_store(hw, (unsigned)layer + 1u, __ATOMIC_RELAXED, __HIP_MEMORY_SCOPE_WORKGROUP);
        } else {
          unsigned v;
          do {
            __builtin_amdgcn_s_sleep(1);
            asm volatile("ds_read_b32 %0, %1\n\ts_waitcnt lgkmcnt(0)" : "=v"(v) : "v"(LDS_TAG + 4) : "memory");
          } while ((unsigned)__builtin_amdgcn_readfirstlane(v) != (unsigned)layer + 1u);
        }
      } else CST();
      CST();
      if (TPOS == 0 && layer == 0 && !loader) touch(rs, LAY_OFF + L_O + lane * TSTR + c * 4096 + sp * 128, 0);
      SETPRIO(APRIO);
    }
    attention(layer, lay0);
    {
      COORDS();
      const bool poller = w == 0, loader = w != 0;
      const unsigned wq = WQKV_OFF + c * 49152, wb = WGU_OFF + c * 114688, wdn = WDN_OFF + n32 * 114688;
      unsigned pv = 0;
#define PEEK(blk, line) pv = __hip_atomic_load((gu32*)(ws + (blk)) + ((line) + (lane & 31)) * 32, RLX_AGENT)
#define GATE(blk, line) do { if (!__all(pv == tag)) POLL(blk, line, 32); } while (0)
      f32x4 ca = zero4();
      auto cs_gu = [&](int cell, int n2, bool put) {
        const unsigned one = (unsigned)vopq((w == 3 || t16 == (g4 & 1)) ? 0x3f803f80 : 0);
        const int a = cell * 1024 + (w < 3 ? lsw : lo), d2 = n2 == 1 ? 2048 : 7168;
        v4u f[8];
#pragma unroll
        for (int i = 0; i < 8; ++i) asm volatile("ds_read_b128 %0, %1" : "=v"(f[i]) : "v"(a + (i >> 1) * d2 + (i & 1) * n2 * 1024) : "memory");
        asm volatile("s_waitcnt lgkmcnt(0)" : "+v"(f[0]), "+v"(f[1]), "+v"(f[2]), "+v"(f[3]), "+v"(f[4]), "+v"(f[5]), "+v"(f[6]), "+v"(f[7]));
#pragma unroll
        for (int i = 0; i < 8; ++i) ca = mfma16(f[i], (v4u){one, one, one, one}, ca);
        if (put && t16 < (w < 3 ? 2 : 1)) {
#pragma unroll
          for (int r = 0; r < 4; ++r) cs_put(L, 48 + 32 * w + 16 * t16 + 4 * g4 + r, ca[r]);
        }
      };
      if (!last) {
        unsigned v;
        if (TPOS == 1 && !loader) touch(rs, LAY_OFF + L_O + lane * TSTR + c * 4096 + sp * 128, 0);
        if (loader && lane == 0) asm volatile("ds_add_u32 %0, %1" ::"v"(LDS_TAG + 16), "v"(1) : "memory");
        do asm volatile("ds_read_b32 %0, %1\n\ts_waitcnt lgkmcnt(0)" : "=v"(v) : "v"(LDS_TAG + 16) : "memory");
        while ((unsigned)__builtin_amdgcn_readfirstlane(v) != 3u);
        for (int q = 0; q < 4 - w; ++q) {
          if (w == 3) cs_gu(6, 28, false);
          else cs_gu(q < 2 ? 28 * q + w : 40 + 16 * w + 8 * q, q < 2 ? 3 : 1, q == 3);
        }
      }
      auto tail = [&](auto pc) {
        constexpr bool P = pc.value;
        const int TD = 8 * sp + 4 + 2 * rh + (w >> 1), SD = 2 * TD + (w & 1);
        const unsigned xn = last ? NONE : lay + LAY_BYTES + L_X;
        const rsrc_t ro = last ? __builtin_amdgcn_make_buffer_rsrc((void*)out, 0, (int)MB2, 0x00020000) : rs;
        const unsigned ob = last ? SD * 16384 + n32 * 64 : xn + (SD * 16 + n32) * 1024, sb = xn + MB2 + TD * 4096 + n32 * 256 + (w & 1) * 128;
        const unsigned ov = lo + ((t16 * 1008 - g4 * 240) & -layer), sv = g4 ? NONE : t16 * 8;
        v4u pp[2], res;
        Stats st;
        unsigned ev[4];
        const unsigned hal = (FL_HA + (lane < 32 ? sp * 32 + lane : (sp ? sp - 1 : 0) * 32 + c)) * 128;
        auto ldp = [&](int pa) {
          const unsigned so = lay + L_P + ((2 * sp + 1 - pa) * 32 + c) * 8192 + w * 2048;
          pp[0] = ld16(rs, lo, so); pp[1] = ld16(rs, lo + 1024, so);
          PIN();
          DBGC(4 + pa);
        };
        auto ldr = [&](int pa) { res = ld16(rs, g4 * 512 + t16 * 16, lay + L_X1 + ((TD - 4 * pa) * 32 + 2 * n32) * 1024 + (w & 1) * 256); };
        auto finish = [&](int pa) {
          f32x4 a0 = lds4f(L, LDS_QL + w * 2048 + lo), a1 = lds4f(L, LDS_QL + w * 2048 + 1024 + lo);
          asm volatile("" : "+v"(a0), "+v"(a1));
          if (pa || PSL < 0) ldp(pa);
          if (P && pa) st.load(rs, lane, xn + MB2 + (8 * sp + 4) * 4096);
          if (pa && !last) {
            if (!P) wait_vm<2>();
            else if (!__all(ev[0] == tag)) poll((gu32*)(ws + hal), true, tmo, tag);
            HBARS(false);
          }
          if (!pa) { HBAR(); if (!P) ldw(IC<22>{}, last ? NONE : wq, 0); }
          TR();
          if (__builtin_expect(!__all(pp[0][3] == tag && pp[1][3] == tag), 0)) {
            unsigned spins = 0;
            do ldp(pa); while (!__all(pp[0][3] == tag && pp[1][3] == tag) && ++spins < DATA_CAP);
          }
          TR();
          a0 += un24(pp[0]); a1 += un24(pp[1]);
          Fin fin;
          static_for<8>([&](auto k) { fin.template chunk<k.value>(a0, a1, res, ro, ov, ob - pa * 8 * 16384, rs, sv, sb - pa * 4 * 4096); });
          TR();
        };
        SETPRIO(3);
        ldr(0);
        HBARS(false);
        if (PSL >= 0) { __builtin_amdgcn_s_sleep(PSL < 0 ? 0 : PSL); ldp(0); }
        HBARS(false);
        finish(0);
        HBARS(false);
        if (!P && !last) ldw(IC<26>{}, wq, 22);
        ldr(1);
        const bool adv = last && blockIdx.x == 0;
        if (P) {
#pragma unroll
          for (int q = 0; q < 4; ++q) ev[q] = __builtin_amdgcn_raw_buffer_load_b32(rs, q || last ? lane * 128 : hal, adv ? flo + (FL_HB + 64 * q) * 128 : q || last ? NONE : 0u, AUX_SC1);
        }
        if (last) HOLD(3);
        HBAR();
        if (!P) wait_vm<10>();
        CST();
        if (!last) HOLD(2);
        HBAR();
        finish(1);
        if (P && !last) {
          if (__builtin_expect(missing<16>((const v4u*)st.p), 0)) {
            unsigned spins = 0;
            do { st.load(rs, lane, xn + MB2 + (8 * sp + 4) * 4096); DBGC(3); } while (missing<16>((const v4u*)st.p) && ++spins < DATA_CAP);
          }
          st.all(L, lane);
        }
        if (P && adv) {
          if (!__all(ev[0] == tag && ev[1] == tag && ev[2] == tag && ev[3] == tag))
            for (int q = 0; q < 4; ++q) POLL(flo, FL_HB + 64 * q, 64);
          if (lane == 0) __hip_atomic_store(fg + FL_EP * 32, tag, RLX_AGENT);
        }
      };
      SETPRIO(3);
      if (poller) {
        Stats st;
        SETPRIO(1);
        if (last) HOLD(4);
        HBAR();
        static_for<7>([&](auto nc) {
          constexpr int n = nc.value + 1;
          if (n <= 2) st.load(rs, lane, lay + L_X1 + MB2 + (8 * sp + 4) * 4096, 8 * n - 8, 8 * n);
          if (n == XW + 1) FLAG(flo, FL_X1A + sp * 32 + c);
          if (n == 3) st.part1();
          if (n == 4) {
            if (missing<16>((const v4u*)st.p)) {
              unsigned spins = 0;
              do { st.load(rs, lane, lay + L_X1 + MB2 + (8 * sp + 4) * 4096); DBGC(2); } while (missing<16>((const v4u*)st.p) && ++spins < DATA_CAP);
              st.part1();
            }
            st.part2();
          }
          if (n == 5) st.part3(L, lane);
          if (n == 6) PEEK(flo, FL_X1A + sp * 32);
          if (n == 7) GATE(flo, FL_X1A + sp * 32);
          HBARS(n == 3 || n == 7);
        });
        st.load(rs, lane, lay + L_X1 + MB2 + 8 * sp * 4096);
        st.all(L, lane);
        static_for<6>([&](auto nc) {
          constexpr int n = nc.value + 2;
          if (n == HW + 1) FLAG(flo, FL_HB + sp * 32 + c);
          if (n == UG - 1) PEEK(flo, FL_HB + sp * 32);
          if (n == UG) GATE(flo, FL_HB + sp * 32);
          HBARS(n == 2 || n == 7);
        });
        static_for<8>([&](auto nc) {
          constexpr int n = nc.value;
          if (n == 7) { if (!last) FLAG(0, FL_HA + sp * 32 + c); }
          else HBARS(n == 0 || n == 3 || n == 6);
        });
        tail(IC<1>{});
      } else {
        DRAIN();
        HBAR();
        static_for<7>([&](auto nc) {
          constexpr int n = nc.value + 1;
          blank(n < 3 ? 30 + 3 * n + w : 30 + 3 * n + w + (n == 7 && w == 3 ? -1 : 0), n >= 3 ? lay : last || (n == 2 && w == 3) ? NONE : LAY_OFF + LAY_BYTES);
          if (n <= 3) ldw(IC<9>{}, wb, 71 + 9 * n);
          if (n == 4) { ldw(IC<5>{}, wb, 107); wait_vm<11>(); }
          if (n == 5) { ldw(IC<16>{}, wdn - 16384, 112); if (!last && w == 1) cs_gu(80, 1, true); wait_vm<14>(); }
          if (n == 6) { if (!last && w == 2) cs_gu(88, 1, false); wait_vm<8>(); }
          if (n == 7 && !last && w >= 2) cs_gu(80 + 8 * w, 1, true);
          HBARS(n == 3 || n == 7);
        });
        static_for<6>([&](auto nc) {
          constexpr int n = nc.value + 2;
          if (n == 3) wait_vm<0>();
          if (n >= 3 && !last) { blank(6 * n - 20 + 2 * w, LAY_OFF + LAY_BYTES); blank(6 * n - 19 + 2 * w, LAY_OFF + LAY_BYTES); }
          if (n > 3) ldw(IC<(n < 7 ? 21 : 16)>{}, wdn, 21 * n - 84);
          HBARS(n == 2 || n == 7);
        });
        static_for<8>([&](auto nc) {
          constexpr int n = nc.value;
          if (n == 0) ldw(IC<15>{}, wdn, 79);
          if (n == 1) ldw(IC<2>{}, wdn, 94);
          if (n == 2 && !last) { blank(29 + w, LAY_OFF + LAY_BYTES); if (w == 1) blank(33, LAY_OFF + LAY_BYTES); ldw(IC<16>{}, WO_OFF + n32 * 32768 - LDS_WO, 96); }
          if (n < 7) {
            if (last) wait_vm<(n == 0 ? 25 : n < 3 ? 19 : n == 3 ? 12 : n == 4 ? 6 : n == 5 ? 1 : 0)>();
            else wait_vm<(n == 0 ? 31 : n == 1 ? 23 : n == 2 ? 30 : n == 3 ? 21 : n == 4 ? 13 : n == 5 ? 8 : 6)>();
          }
          if (n < 7) HBARS(n == 0 || n == 3 || n == 6);
        });
        tail(IC<0>{});
      }
    }
  }
  }
}

}

extern "C" void kernel_launch(void* const* d_in, const int* in_sizes, int n_in, void* d_out, int out_size, void* d_ws, size_t ws_size,
                              hipStream_t stream) {
  (void)in_sizes; (void)n_in; (void)out_size; (void)ws_size;
  (void)hipFuncSetAttribute((const void*)k_main, hipFuncAttributeMaxDynamicSharedMemorySize, LDS_BYTES);
  k_main<<<256, 512, LDS_BYTES, stream>>>((const u16*)d_in[0], (const u16*)d_in[1], (const u16*)d_in[2], (const u16*)d_in[4], (const u16*)d_in[5],
                                          (const u16*)d_in[6], (unsigned char*)d_ws, (u16*)d_out);
}
```

```cpp
#include <hip/hip_runtime.h>
#include <stdint.h>
#include <utility>

#define LAS __attribute__((address_space(3)))
#define GAS __attribute__((address_space(1)))
#define DI __device__ __forceinline__
#define RLX_AGENT __ATOMIC_RELAXED, __HIP_MEMORY_SCOPE_AGENT
typedef unsigned short u16;
typedef unsigned v4u __attribute__((ext_vector_type(4)));
typedef unsigned v2u __attribute__((ext_vector_type(2)));
typedef float f32x2 __attribute__((ext_vector_type(2)));
typedef float f32x4 __attribute__((ext_vector_type(4)));
typedef float f32x16 __attribute__((ext_vector_type(16)));
typedef __bf16 bf16x2 __attribute__((ext_vector_type(2)));
typedef __bf16 bf16x8 __attribute__((ext_vector_type(8)));
typedef GAS unsigned gu32;
typedef GAS unsigned long long gu64;

namespace {

constexpr int D_ = 512, F_ = 1792;
constexpr unsigned MB2 = 2097152u, STB = 262144u;
constexpr unsigned FLAG_LAYER = 1048576u, FLAG_G = 2 * FLAG_LAYER, FLAG_BYTES = FLAG_G + 65536;
constexpr unsigned WQKV_OFF = FLAG_BYTES;
constexpr unsigned WO_OFF = WQKV_OFF + 32 * 49152;
constexpr unsigned WGU_OFF = WO_OFF + 16 * 32768;
constexpr unsigned WDN_OFF = WGU_OFF + 32 * 114688;
constexpr unsigned LAY_OFF = WDN_OFF + 16 * 114688;
constexpr unsigned L_K = 0, L_V = MB2, L_O = 2 * MB2, L_X1 = 3 * MB2, L_X = 4 * MB2 + STB, L_H = 5 * MB2 + 2 * STB;
constexpr unsigned L_P = L_H + 128 * 57344;
constexpr unsigned LAY_BYTES = L_P + 2 * MB2;
constexpr unsigned WS_BYTES = LAY_OFF + 2 * LAY_BYTES;
constexpr unsigned NONE = 0x40000000u;
constexpr int FL_X1A = 0, FL_HB = 256, FL_HA = 512, FL_XB = 768, FL_XA = 1024;
constexpr int FL_WR = 0, FL_EP = 256, FL_TMO = 257;
constexpr int VBLK = 528;
constexpr int LDS_KL = 114688, LDS_VL = LDS_KL + 16384, LDS_QL = LDS_VL + 32 * VBLK, LDS_CS = LDS_QL + 8192, LDS_ST = LDS_CS + 1536;
constexpr int LDS_MK = LDS_ST + 1536, LDS_TAG = LDS_MK + 2048;
constexpr int LDS_WO = 81920, LDS_TB = 49152;
constexpr int LDS_BYTES = 163840;
constexpr int CODE_BYTES = 71040, CODE_LINES = (CODE_BYTES + 4095) / 4096;
constexpr unsigned SPIN_CAP = 1u << 16;
constexpr int AUX_SC1 = 16;
constexpr float LOG2E = 1.4426950408889634f;

DI f32x16 mfma(v4u a, v4u b, f32x16 c) {
  return __builtin_amdgcn_mfma_f32_32x32x16_bf16(__builtin_bit_cast(bf16x8, a), __builtin_bit_cast(bf16x8, b), c, 0, 0, 0);
}
DI f32x4 mfma16(v4u a, v4u b, f32x4 c) {
  return __builtin_amdgcn_mfma_f32_16x16x32_bf16(__builtin_bit_cast(bf16x8, a), __builtin_bit_cast(bf16x8, b), c, 0, 0, 0);
}
template <bool FIRST, bool PAD = false> DI void mac16(f32x4& c, const v4u& a, const v4u& b) {
  if constexpr (FIRST) asm volatile("v_mfma_f32_16x16x32_bf16 %0, %1, %2, 0" : "=&v"(c) : "v"(a), "v"(b));
  else if constexpr (PAD) asm volatile("s_nop 1\n\tv_mfma_f32_16x16x32_bf16 %0, %1, %2, %0" : "+v"(c) : "v"(a), "v"(b));
  else asm volatile("v_mfma_f32_16x16x32_bf16 %0, %1, %2, %0" : "+v"(c) : "v"(a), "v"(b));
}
DI unsigned pk2(float lo, float hi) {
  f32x2 p = {lo, hi};
  return __builtin_bit_cast(unsigned, __builtin_convertvector(p, bf16x2));
}
DI float bfr(float x) { return __uint_as_float(pk2(x, 0.f) << 16); }
DI float bflo(unsigned u) { return __uint_as_float(u << 16); }
DI float bfhi(unsigned u) { return __uint_as_float(u & 0xffff0000u); }
DI float amul(float a, float b) { float d; asm("v_mul_f32 %0, %1, %2" : "=v"(d) : "v"(a), "v"(b)); return d; }
DI float afma(float a, float b, float c) { float d; asm("v_fma_f32 %0, %1, %2, %3" : "=v"(d) : "v"(a), "v"(b), "v"(c)); return d; }
DI float aadd(float a, float b) { float d; asm("v_add_f32 %0, %1, %2" : "=v"(d) : "v"(a), "v"(b)); return d; }
DI float asub(float a, float b) { float d; asm("v_sub_f32 %0, %1, %2" : "=v"(d) : "v"(a), "v"(b)); return d; }
DI float xhalf_sum(float v) {
  float t;
  asm("v_mov_b32 %1, %0\n\ts_nop 1\n\tv_permlane32_swap_b32 %0, %1\n\tv_add_f32 %0, %0, %1" : "+v"(v), "=&v"(t));
  return v;
}
DI float xrow_sum(float v) {
  float t;
  asm("v_mov_b32 %1, %0\n\ts_nop 1\n\tv_permlane16_swap_b32 %0, %1\n\tv_add_f32 %0, %0, %1" : "+v"(v), "=&v"(t));
  return v;
}
DI float xhalf_max(float v) {
  auto r = __builtin_amdgcn_permlane32_swap(__float_as_uint(v), __float_as_uint(v), false, false);
  return fmaxf(__uint_as_float(r[0]), __uint_as_float(r[1]));
}
DI void swap16(unsigned& a, unsigned& b) { asm("s_nop 1\n\tv_permlane16_swap_b32 %0, %1" : "+v"(a), "+v"(b)); }
DI void swap32(unsigned& a, unsigned& b) { asm("s_nop 1\n\tv_permlane32_swap_b32 %0, %1" : "+v"(a), "+v"(b)); }
DI f32x16 splat16(float v) {
  f32x16 z;
#pragma unroll
  for (int i = 0; i < 16; ++i) z[i] = v;
  return z;
}
DI f32x4 zero4() { return (f32x4){0.f, 0.f, 0.f, 0.f}; }
DI int vopq(int x) { asm volatile("" : "+v"(x)); return x; }
DI int lane_id() { int l; asm volatile("v_mbcnt_lo_u32_b32 %0, -1, 0\n\tv_mbcnt_hi_u32_b32 %0, -1, %0" : "=v"(l)); return l; }
template <int LN> DI unsigned wrl(unsigned v, unsigned s) { asm volatile("v_writelane_b32 %0, %1, %2" : "+v"(v) : "s"(s), "i"(LN)); return v; }
template <class T> DI void undef(T& x) { asm volatile("" : "=v"(x)); }
DI int sopq(int x) { asm volatile("" : "+s"(x)); return x; }
#define SWZ(l) ((l) ^ (((l) >> 3) & 7))
#define COORDS() \
  const int lane = lane_id(), lo = vopq(lane * 16), hh = lane >> 5, t16 = lane & 15, g4 = lane >> 4, lsw = SWZ(lane) * 16; \
  const int w = sopq(w0) & 3, sp = sopq(sp0), c = sopq(c0), n32 = c >> 1, rh = c & 1; \
  const unsigned lay = (unsigned)sopq((int)lay0); \
  const unsigned flo = sopq(layer) * FLAG_LAYER; \
  gu32* const fl = (gu32*)(ws + flo); \
  (void)lo; (void)hh; (void)t16; (void)g4; (void)lsw; (void)n32; (void)rh; (void)lay; (void)sp; (void)c; (void)w; (void)fl;
#define BAR() asm volatile("s_waitcnt lgkmcnt(0)\n\ts_barrier" ::: "memory")
#define AGAIN() asm volatile("" ::: "memory")
#define BARX() asm volatile("s_barrier" ::: "memory")
#define BAR0(z) asm volatile("s_cmp_lg_u32 %0, 0\n\ts_cbranch_scc1 1f\n\ts_barrier\n1:" ::"s"(z) : "memory", "scc")
#define DRAIN() asm volatile("s_waitcnt vmcnt(0)" ::: "memory")
#define SETTLE() __builtin_amdgcn_s_waitcnt(0x0f70)
#define PIN() __builtin_amdgcn_sched_barrier(0)
#define SETPRIO(n) __builtin_amdgcn_s_setprio(n)
template <int N> DI void wait_vm() { static_assert(N >= 0 && N < 64, ""); asm volatile("s_waitcnt vmcnt(%0)" ::"i"(N) : "memory"); }

typedef __amdgpu_buffer_rsrc_t rsrc_t;
DI v4u ld16(rsrc_t rs, unsigned voff, unsigned soff) { return __builtin_amdgcn_raw_buffer_load_b128(rs, voff, soff, AUX_SC1); }
DI f32x2 ld8f(rsrc_t rs, unsigned voff, unsigned soff) { return __builtin_bit_cast(f32x2, __builtin_amdgcn_raw_buffer_load_b64(rs, voff, soff, AUX_SC1)); }
DI void st16(rsrc_t rs, v4u v, unsigned voff, unsigned soff) {
  __builtin_amdgcn_raw_buffer_store_b128(v, rs, voff, soff, AUX_SC1);
  asm volatile("s_nop 1" ::"v"(v));
}
DI void st8(rsrc_t rs, v2u v, unsigned voff, unsigned soff) { __builtin_amdgcn_raw_buffer_store_b64(v, rs, voff, soff, AUX_SC1); }
DI void st4(rsrc_t rs, unsigned v, unsigned voff, unsigned soff) { __builtin_amdgcn_raw_buffer_store_b32(v, rs, voff, soff, AUX_SC1); }
DI v4u lds16(LAS unsigned char* L, int off) { return *(LAS v4u*)(L + off); }
DI f32x4 lds4f(LAS unsigned char* L, int off) { return *(LAS f32x4*)(L + off); }

DI void poll(gu32* word, bool active, gu32* tmo, unsigned tag) {
  for (unsigned spins = 0;;) {
    unsigned v = tag;
    if (active) v = __hip_atomic_load(word, RLX_AGENT);
    if (__all(v == tag)) break;
    ++spins;
    if (spins > SPIN_CAP) { __hip_atomic_store(tmo, tag, RLX_AGENT); break; }
    if ((spins & 255u) == 0u && (unsigned)__builtin_amdgcn_readfirstlane(__hip_atomic_load(tmo, RLX_AGENT)) == tag) break;
  }
  __builtin_amdgcn_fence(__ATOMIC_ACQUIRE, "wavefront");
}

DI void lds_wait(int a, int n) {
  unsigned v, u;
  asm volatile("1:\n\tds_read_b32 %0, %2\n\ts_waitcnt lgkmcnt(0)\n\tv_readfirstlane_b32 %1, %0\n\ts_cmp_eq_u32 %1, %3\n\ts_cbranch_scc1 2f\n\ts_sleep 1\n\ts_branch 1b\n2:"
               : "=&v"(v), "=&s"(u) : "v"(a), "s"(n) : "memory", "scc");
}
DI void lds_add(int a) { asm volatile("s_mov_b64 exec, 1\n\tds_add_u32 %0, %1\n\ts_mov_b64 exec, -1" ::"v"(vopq(a)), "v"(vopq(1)) : "memory"); }
template <int I> DI void dma1(rsrc_t rs, LAS unsigned char* dst, unsigned voff, unsigned soff) {
  __builtin_amdgcn_raw_ptr_buffer_load_lds(rs, (LAS void*)dst, 16, voff, soff, I * 1024, AUX_SC1);
}
template <int V> using IC = std::integral_constant<int, V>;
template <class FN, int... I> DI void static_for_(FN fn, std::integer_sequence<int, I...>) { (fn(IC<I>{}), ...); }
template <int N, class FN> DI void static_for(FN fn) { static_for_(fn, std::make_integer_sequence<int, N>{}); }
template <int N> DI void dma(rsrc_t rs, LAS unsigned char* dst, unsigned lo, unsigned src) {
  static_for<N>([&](auto i) { dma1<i.value & 3>(rs, dst + (i.value >> 2) * 4096, lo, src + (i.value >> 2) * 4096); });
}

struct Stats {
  f32x4 p[16];
  float m0, m1, q0, q1;
  DI void load(rsrc_t rs, int lane, unsigned base, int i0 = 0, int i1 = 16) {
#pragma unroll
    for (int i = i0; i < i1; ++i) p[i] = __builtin_bit_cast(f32x4, ld16(rs, (lane >> 4) * 4096 + (lane & 15) * 16 + i * 256, base));
    PIN();
  }
  DI void part1() {
    m0 = m1 = q0 = q1 = 0.f;
#pragma unroll
    for (int i = 0; i < 16; ++i) { m0 += p[i][0]; q0 += p[i][1]; m1 += p[i][2]; q1 += p[i][3]; }
    asm volatile("" : "+v"(m0), "+v"(m1), "+v"(q0), "+v"(q1));
  }
  DI void part2() {
    const float a0 = m0 * (1.0f / 16.0f), a1 = m1 * (1.0f / 16.0f);
    q0 *= 32.0f; q1 *= 32.0f;
#pragma unroll
    for (int i = 0; i < 16; ++i) { const float d0 = p[i][0] - a0, d1 = p[i][2] - a1; q0 += d0 * d0; q1 += d1 * d1; }
    asm volatile("" : "+v"(q0), "+v"(q1));
  }
  DI void all(LAS unsigned char* L, int lane, int dst = LDS_ST) { part1(); part2(); part3(L, lane, dst); }
  DI void part3(LAS unsigned char* L, int lane, int dst = LDS_ST) {
    const float den0 = bfr(__builtin_amdgcn_sqrtf(bfr(bfr(q0 * (1.0f / (32.0f * D_))) + 1e-5f)));
    const float den1 = bfr(__builtin_amdgcn_sqrtf(bfr(bfr(q1 * (1.0f / (32.0f * D_))) + 1e-5f)));
    *(LAS f32x2*)(L + dst + lane * 8) = (f32x2){__builtin_amdgcn_rcpf(den0), __builtin_amdgcn_rcpf(den1)};
    *(LAS f32x2*)(L + dst + 512 + lane * 8) = (f32x2){den0 * den0, den1 * den1};
    *(LAS v2u*)(L + dst + 1024 + lane * 8) = (v2u){pk2(m0 * (-1.0f / D_), m0 * (-1.0f / D_)), pk2(m1 * (-1.0f / D_), m1 * (-1.0f / D_))};
  }
};
DI float lds1f(LAS unsigned char* L, int off) { return *(LAS float*)(L + off); }
DI v4u mfrag(LAS unsigned char* L, int off) { const unsigned n = *(LAS unsigned*)(L + off); return (v4u){n, n, 0u, 0u}; }

constexpr int LA = 6;
template <int NF, class RD, class STEP>
DI void pipe(RD rd, STEP step) {
  v4u wf[8];
  static_for<LA>([&](auto i) { wf[i.value] = rd(i); });
  step(IC<-1>{}, wf[0], IC<1>{});
  PIN();
  static_for<NF>([&](auto fc) {
    constexpr int f = fc.value;
    if constexpr ((f & 1) == 0 && f + LA <= NF) __builtin_amdgcn_s_waitcnt(0xc07f | ((LA - 2) << 8));
    step(fc, wf[f & 7], IC<0>{});
    if constexpr (f + LA < NF) wf[(f + LA) & 7] = rd(IC<f + LA>{});
    step(fc, wf[f & 7], IC<1>{});
    PIN();
  });
}
DI v4u csfrag(LAS unsigned char* L, int entry) {
  const v2u t = *(LAS v2u*)(L + LDS_CS + entry * 8);
  return (v4u){t.x, t.y, 0u, 0u};
}
DI void cs_put(LAS unsigned char* L, int entry, float cs) {
  const float hi = bfr(cs), mid = bfr(cs - hi), lw = (cs - hi) - mid;
  *(LAS v2u*)(L + LDS_CS + entry * 8) = (v2u){pk2(hi, mid), pk2(lw, 0.f)};
}
DI unsigned mulpk(float a, float s0, float b, float s1) {
  unsigned d; float t;
  asm("v_mul_f32 %0, %2, %3\n\tv_mul_f32 %1, %4, %5\n\tv_cvt_pk_bf16_f32 %0, %0, %1" : "=&v"(d), "=&v"(t) : "v"(a), "v"(s0), "v"(b), "v"(s1));
  return d;
}
DI unsigned nos(unsigned v) { return v < 0xfffffffeu ? v : 0xfffffffeu; }
DI v4u nos4(v4u v) { return (v4u){nos(v[0]), v[1], nos(v[2]), v[3]}; }
DI unsigned umax3(unsigned a, unsigned b, unsigned c) { const unsigned t = a > b ? a : b; return t > c ? t : c; }
DI bool ones(unsigned m) { return __builtin_amdgcn_ballot_w64(m == 0xffffffffu) != 0; }
template <int N> DI bool missing(const v4u* f) {
  unsigned m0 = 0, m1 = 0;
#pragma unroll
  for (int i = 0; i < N; i += 2) { m0 = umax3(m0, f[i][0], f[i][2]); m1 = umax3(m1, f[i + 1][0], f[i + 1][2]); }
  return ones(m0 > m1 ? m0 : m1);
}
constexpr unsigned DATA_CAP = 1u << 14;
template <int N0, int N1, int G, class LD> DI void refetch(v4u* f, LD ld) {
  unsigned spins = 0;
  bool any;
  do {
    any = false;
    static_for<(N1 - N0) / G>([&](auto g) {
      if (missing<G>(f + N0 + G * g.value)) { static_for<G>([&](auto i) { ld(IC<N0 + G * g.value + i.value>{}); }); any = true; }
    });
    AGAIN();
    SETTLE();
  } while (any && ++spins < DATA_CAP);
}
DI v4u pk24(const f32x4& a, unsigned tag) {
  const v4u u = __builtin_bit_cast(v4u, a);
  return (v4u){__builtin_amdgcn_perm(u[0], u[3], 0x07060501u), __builtin_amdgcn_perm(u[1], u[3], 0x07060502u), __builtin_amdgcn_perm(u[2], u[3], 0x07060503u), tag};
}
DI f32x4 un24(const v4u& u) {
  return __builtin_bit_cast(f32x4, (v4u){u[0], u[1], u[2], __builtin_amdgcn_perm(u[2], __builtin_amdgcn_perm(u[1], u[0], 0x0c0c0400u), 0x0401000cu)});
}
DI v4u pack8(const float* y) { return (v4u){pk2(y[0], y[1]), pk2(y[2], y[3]), pk2(y[4], y[5]), pk2(y[6], y[7])}; }

struct Fin {
  float y[8], s;
  v4u o;
  v2u sq;
  template <int K, bool ST = true> DI void chunk(const f32x4& a0, const f32x4& a1, const v4u& r, rsrc_t ro, unsigned vo, unsigned so, rsrc_t rs, unsigned vs, unsigned ss) {
    if constexpr (K == 0) { y[0] = aadd(a0[0], bflo(r[0])); y[1] = aadd(a0[1], bfhi(r[0])); y[2] = aadd(a0[2], bflo(r[1])); y[3] = aadd(a0[3], bfhi(r[1])); }
    if constexpr (K == 1) { y[4] = aadd(a1[0], bflo(r[2])); y[5] = aadd(a1[1], bfhi(r[2])); y[6] = aadd(a1[2], bflo(r[3])); y[7] = aadd(a1[3], bfhi(r[3])); }
    if constexpr (K == 2) { o = nos4(pack8(y)); if constexpr (ST) st16(ro, o, vo, so); }
    if constexpr (K == 3) {
#pragma unroll
      for (int m = 0; m < 4; ++m) { y[2 * m] = bflo(o[m]); y[2 * m + 1] = bfhi(o[m]); }
    }
    if constexpr (K == 4) s = xrow_sum(aadd(aadd(aadd(y[0], y[1]), aadd(y[2], y[3])), aadd(aadd(y[4], y[5]), aadd(y[6], y[7]))));
    if constexpr (K == 5) {
      s = xhalf_sum(s);
      const float mu = s * (1.0f / 32.0f);
#pragma unroll
      for (int i = 0; i < 8; ++i) y[i] = asub(y[i], mu);
    }
    if constexpr (K == 6) {
      y[0] = amul(y[0], y[0]); y[1] = amul(y[1], y[1]);
#pragma unroll
      for (int i = 2; i < 8; ++i) y[i & 1] = afma(y[i], y[i], y[i & 1]);
    }
    if constexpr (K == 7) {
      sq = (v2u){nos(__float_as_uint(s)), __float_as_uint(xhalf_sum(xrow_sum(aadd(y[0], y[1]))))};
      if constexpr (ST) st8(rs, sq, vs, ss);
    }
  }
};

constexpr unsigned long long low_mask(int r) {
  unsigned long long m = 0;
  for (int l = 0; l < 64; ++l)
    if ((r & 3) + 8 * (r >> 2) + 4 * (l >> 5) <= (l & 31)) m |= 1ull << l;
  return m;
}
DI float sel(unsigned long long mask, float set, float unset) {
  float d;
  asm("v_cndmask_b32_e64 %0, %1, %2, %3" : "=v"(d) : "v"(unset), "v"(set), "s"(mask));
  return d;
}
DI float max16(float m, const f32x16& s) {
#pragma unroll
  for (int r = 0; r < 16; r += 2) m = __builtin_fmaxf(__builtin_fmaxf(m, s[r]), s[r + 1]);
  return m;
}
DI v4u exp_pack(const f32x16& s, int h) {
  float pr[8];
#pragma unroll
  for (int r = 0; r < 8; ++r) pr[r] = __builtin_amdgcn_exp2f(s[8 * h + r]);
  return pack8(pr);
}
DI f32x16 attn_exact(LAS unsigned char* L, int lane, int tl, int nv) {
  const int lo = lane * 16;
  const int kb = LDS_KL + tl * 1024 + lo, kf0 = LDS_KL + (tl + 8) * 1024 + (lane >> 5) * 512;
  const v4u q = lds16(L, LDS_QL + tl * 1024 + lo);
  auto kf = [&](int i) { return lds16(L, (i == 8 || nv <= i ? kb : kf0 - i * 1024) + i * 1024); };
  f32x16 s[9], cm;
  {
    s[8] = mfma(kf(8), q, splat16(0.f));
    s[0] = mfma(kf(0), q, splat16(0.f));
    s[7] = mfma(kf(7), q, splat16(0.f));
    s[6] = mfma(kf(6), q, splat16(0.f));
    asm volatile("s_nop 7\n\ts_nop 7" : "+v"(s[8]), "+v"(s[0]));
    PIN();
    static_for<16>([&](auto r) { s[8][r.value] = sel(low_mask(r.value), s[8][r.value], s[0][r.value]); });
    float m = max16(s[8][0], s[8]);
    PIN();
    static_for<7>([&](auto ic) {
      constexpr int i = 7 - ic.value;
      if constexpr (i - 2 >= 1) s[i - 2] = mfma(kf(i - 2), q, splat16(0.f));
      m = max16(m, s[i]);
      PIN();
    });
    cm = splat16(-xhalf_max(m));
    AGAIN();
  }
  const int vb = LDS_VL + 2 * tl * VBLK + ((lane & 16) ? 512 : (lane >> 5) * 256 + (lane & 15) * 16);
  s[8] = mfma(kf(8), q, cm);
  s[0] = mfma(kf(0), q, cm);
  const v4u mk0 = lds16(L, LDS_MK + lo), mk1 = lds16(L, LDS_MK + 1024 + lo);
  v4u va = lds16(L, vb + 16 * VBLK), vc = lds16(L, vb + 17 * VBLK), v0a = lds16(L, vb), v0c = lds16(L, vb + VBLK);
  asm volatile("s_nop 7\n\ts_nop 7" : "+v"(s[8]), "+v"(s[0]));
  PIN();
  f32x16 o;
  {
    static_for<16>([&](auto r) { s[8][r.value] = sel(low_mask(r.value), s[8][r.value], s[0][r.value]); });
    PIN();
    s[7] = mfma(kf(7), q, cm);
    const v4u p0 = exp_pack(s[8], 0), l0 = p0 & mk0;
    o = mfma(va, l0, splat16(0.f));
    o = mfma(v0a, p0 ^ l0, o);
    const v4u p1 = exp_pack(s[8], 1), l1 = p1 & mk1;
    o = mfma(vc, l1, o);
    o = mfma(v0c, p1 ^ l1, o);
    va = lds16(L, vb + 14 * VBLK); vc = lds16(L, vb + 15 * VBLK);
    PIN();
  }
  static_for<7>([&](auto ic) {
    constexpr int i = 7 - ic.value;
    if constexpr (i > 1) s[i - 1] = mfma(kf(i - 1), q, cm);
    PIN();
    o = mfma(va, exp_pack(s[i], 0), o);
    if constexpr (i > 1) va = lds16(L, vb + (2 * i - 2) * VBLK);
    o = mfma(vc, exp_pack(s[i], 1), o);
    if constexpr (i > 1) vc = lds16(L, vb + (2 * i - 1) * VBLK);
    PIN();
  });
  return o;
}
struct APre { int k[8], k8, vb, qm; };
DI APre attn_pre(int lane, int tl, int nv) {
  APre a;
  const int kb = LDS_KL + tl * 1024 + lane * 16, kf0 = LDS_KL + (tl + 8) * 1024 + (lane >> 5) * 512;
#pragma unroll
  for (int i = 0; i < 8; ++i) a.k[i] = vopq(nv <= i ? kb : kf0 - i * 1024);
  a.k8 = vopq(kb);
  a.vb = vopq(LDS_VL + 2 * tl * VBLK + ((lane & 16) ? 512 : (lane >> 5) * 256 + (lane & 15) * 16));
  a.qm = vopq(LDS_QL + lane * 16);
  return a;
}
DI f32x16 attn_fast(LAS unsigned char* L, const APre& a, int tl) {
  v4u kf[9], mk0, mk1, v0a, v0c;
  kf[7] = lds16(L, a.k[7] + 7 * 1024);
  const v4u q = lds16(L, a.qm + tl * 1024);
  kf[6] = lds16(L, a.k[6] + 6 * 1024);
  v4u va = lds16(L, a.vb + 14 * VBLK), vc = lds16(L, a.vb + 15 * VBLK);
  PIN();
#pragma unroll
  for (int i = 5; i >= 0; --i) kf[i] = lds16(L, a.k[i] + i * 1024);
  kf[8] = lds16(L, a.k8 + 8192);
  f32x16 s[9], o;
  s[7] = mfma(kf[7], q, splat16(0.f));
  PIN();
  static_for<7>([&](auto ic) {
    constexpr int i = 7 - ic.value;
    if constexpr (i > 1) s[i - 1] = mfma(kf[i - 1], q, splat16(0.f));
    if constexpr (i == 1) { s[8] = mfma(kf[8], q, splat16(0.f)); s[0] = mfma(kf[0], q, splat16(0.f)); }
    if constexpr (i == 2) { mk0 = lds16(L, a.qm + (LDS_MK - LDS_QL)); mk1 = lds16(L, a.qm + (LDS_MK - LDS_QL) + 1024); v0a = lds16(L, a.vb); v0c = lds16(L, a.vb + VBLK); }
    PIN();
    o = mfma(va, exp_pack(s[i], 0), i == 7 ? splat16(0.f) : o);
    va = lds16(L, a.vb + (i > 1 ? 2 * i - 2 : 16) * VBLK);
    o = mfma(vc, exp_pack(s[i], 1), o);
    vc = lds16(L, a.vb + (i > 1 ? 2 * i - 1 : 17) * VBLK);
    PIN();
  });
  static_for<16>([&](auto r) { s[8][r.value] = sel(low_mask(r.value), s[8][r.value], s[0][r.value]); });
  const v4u p0 = exp_pack(s[8], 0), l0 = p0 & mk0;
  o = mfma(va, l0, o);
  o = mfma(v0a, p0 ^ l0, o);
  const v4u p1 = exp_pack(s[8], 1), l1 = p1 & mk1;
  o = mfma(vc, l1, o);
  o = mfma(v0c, p1 ^ l1, o);
  return o;
}
DI v4u attn_unit(LAS unsigned char* L, int lane, int tl, int nv, const APre& ap) {
  f32x16 o = attn_fast(L, ap, tl);
  float big = __builtin_fabsf(o[0]);
#pragma unroll
  for (int r = 1; r < 8; ++r) big += __builtin_fabsf(o[r]);
  if (__builtin_expect(!__all(o[8] > 0x1p-30f && o[8] < 0x1p60f && big < 0x1p120f), 0)) {
    asm volatile("" ::: "memory");
    o = attn_exact(L, lane, tl, nv);
  }
  const float rl = __builtin_amdgcn_rcpf(o[8]);
  float y[8];
#pragma unroll
  for (int r = 0; r < 8; ++r) y[r] = o[r] * rl;
  return pack8(y);
}

struct Retile {
  v4u in[8];
  DI void load(const u16* m, int elems, int el, int ld, int rs4) {
    const rsrc_t r = __builtin_amdgcn_make_buffer_rsrc((void*)m, 0, elems * 2, 0x00020000);
    const int e4 = el + rs4 * ld;
#pragma unroll
    for (int j = 0; j < 8; ++j) in[j] = __builtin_amdgcn_raw_buffer_load_b128(r, (j < 4 ? el : e4) * 2, (j & 3) * ld * 2, 0);
    PIN();
  }
  unsigned d[8];
  DI void out(rsrc_t rs) {
#pragma unroll
    for (int i = 0; i < 8; ++i) st16(rs, in[i], d[i], 0);
  }
  DI void lines(LAS unsigned char* t, int lane, int sw, unsigned dst) {
#pragma unroll
    for (int e = 0; e < 8; ++e) {
      v4u o;
#pragma unroll
      for (int m = 0; m < 4; ++m) o[m] = __builtin_amdgcn_perm(in[2 * m + 1][e >> 1], in[2 * m][e >> 1], (e & 1) ? 0x07060302u : 0x05040100u);
      *(LAS v4u*)(t + lane * 128 + (e ^ sw ^ (lane & 7)) * 16) = o;
    }
    asm volatile("s_waitcnt lgkmcnt(0)" ::: "memory");
#pragma unroll
    for (int i = 0; i < 8; ++i) {
      in[i] = *(LAS v4u*)(t + i * 1024 + (lane ^ (lane >> 3)) * 16);
      d[i] = (unsigned)__builtin_amdgcn_ds_bpermute((8 * i + (lane >> 3)) * 4, (int)dst) + (lane & 7) * 16;
    }
    PIN();
  }
  DI void put(LAS unsigned char* L, int pb, int dd, int lp, int dl) {
#pragma unroll
    for (int e = 0; e < 8; ++e) {
      v4u o;
#pragma unroll
      for (int m = 0; m < 4; ++m) o[m] = __builtin_amdgcn_perm(in[2 * m + 1][e >> 1], in[2 * m][e >> 1], (e & 1) ? 0x07060302u : 0x05040100u);
      const int s = lp + (e & 3) + (e >> 2) * dl;
      *(LAS v4u*)(L + pb + (e >> 2) * dd + (s ^ ((s >> 3) & 7)) * 16) = o;
    }
  }
};

struct GS { int cs, j, kap, off; };
constexpr GS gu_step(int u) {
  if (u < 64) { const int k = u >> 2, j = u & 3; return GS{0, j, k, j < 3 ? (7 * (k >> 1) + 3 * (k & 1) + j) * 1024 : (7 * (k >> 1) + 6) * 1024 + (k & 1) * 512}; }
  const int v = u - 64, j = v / 17, i = v % 17;
  return GS{i == 16, j, 16 + i, 57344 + j * 16384 + i * (j < 3 ? 1024 : 512)};
}
constexpr int GA = 132, DB = 264, NST = 376;
constexpr int NPG = 16;
constexpr int ld_step(int id) {
  if (id < 32) return id < NPG ? -1 : 2 * (id - NPG);
  if (id < 64) { const int n = id - 32; return n < 12 ? 103 + (5 * n) / 2 : GA + 10 + 2 * (n - 12); }
  const int n = id - 64;
  return n < 12 ? GA + 103 + n : DB - 15 + ((n - 12) * 3) / 2;
}
constexpr int hg_end(int n) { return n < 3 ? 0 : n < 5 ? 6 * n - 12 : 6 * n - 14; }
constexpr int NEP = 56;
constexpr int ep_step(int k) { return 83 + 17 * (k / 16) + k % 16; }
struct Tab { short ld[NST][2]; short ep[NST]; short nvm[NST]; };
constexpr int xa_step(int j) { const int a = 18 + j, b = j < 12 ? 3 * j + 2 : 34 + j; return a > b ? a : b; }
constexpr Tab make_tab() {
  Tab t{};
  for (int s = 0; s < NST; ++s) { t.ld[s][0] = t.ld[s][1] = t.ep[s] = -1; t.nvm[s] = 0; }
  for (int id = 0; id < 92; ++id) {
    const int s = ld_step(id);
    if (s < 0) continue;
    t.ld[s][t.ld[s][0] >= 0] = (short)id; ++t.nvm[s];
  }
  for (int job = 0; job < 2; ++job)
    for (int k = 0; k < NEP; ++k) {
      const int s = job * GA + ep_step(k);
      t.ep[s] = (short)(job * 64 + k);
      if (k == NEP - 1) t.nvm[s] += 2;
      if (k == (job ? 31 : NEP - 1)) t.nvm[s] += 2;
    }
  return t;
}
constexpr Tab TAB = make_tab();
constexpr bool tab_ok() {
  int a = 0, b = 0;
  for (int id = 0; id < 92; ++id) a += ld_step(id) >= 0;
  for (int s = 0; s < NST; ++s) b += (TAB.ld[s][0] >= 0) + (TAB.ld[s][1] >= 0);
  return a == b;
}
static_assert(tab_ok(), "");
constexpr int vm_between(int a, int b) {
  int n = 0;
  for (int s = a + 1; s <= b; ++s) n += TAB.nvm[s];
  return n;
}

__global__ void __launch_bounds__(512, 2) k_main(const u16* __restrict__ x, const u16* __restrict__ wqkv, const u16* __restrict__ wo, const u16* __restrict__ wg,
                                                 const u16* __restrict__ wu, const u16* __restrict__ wd, unsigned char* __restrict__ ws,
                                                 u16* __restrict__ out) {
  extern __shared__ __attribute__((aligned(16))) unsigned char smem[];
  LAS unsigned char* L = (LAS unsigned char*)smem;
  const int tid = threadIdx.x, w0 = __builtin_amdgcn_readfirstlane(tid >> 6);
  const int b = blockIdx.x;
  auto touch = [&](rsrc_t r, unsigned voff, unsigned soff) { __builtin_amdgcn_raw_ptr_buffer_load_lds(r, (LAS void*)(L + LDS_BYTES - 256), 4, voff, soff, 0, AUX_SC1); };
  {
    unsigned long long pc0;
    asm volatile("s_getpc_b64 %0" : "=s"(pc0));
    const rsrc_t rc = __builtin_amdgcn_make_buffer_rsrc((void*)(pc0 & ~127ull), 0, CODE_BYTES, 0x00020000);
    if (w0 == 3) __builtin_amdgcn_raw_ptr_buffer_load_lds(rc, (LAS void*)(L + LDS_BYTES - 256), 4, (tid & 63) < CODE_LINES ? (tid & 63) * 128 : NONE, (b >> 3) * CODE_LINES * 128, 0, 0);
    PIN();
  }
  const int sp0 = (b & 6) + ((b >> 3) & 1), c0 = (b >> 4) + 16 * (b & 1);
  const rsrc_t rs = __builtin_amdgcn_make_buffer_rsrc((void*)ws, 0, (int)WS_BYTES, 0x00020000);
  const bool compute = w0 < 4;
  gu32* const fg = (gu32*)(ws + FLAG_G);
  gu32* const tmo = fg + FL_TMO * 32;
  unsigned tag = 0;
#define FLAG(blk, line) st4(rs, tag, lane ? NONE : 0u, (blk) + (line) * 128)
#define POLL(blk, line, n) poll((gu32*)(ws + (blk)) + ((line) + lane) * 32, lane < (n), tmo, tag)
#define GETTAG() tag = (unsigned)__builtin_amdgcn_readfirstlane(*(LAS unsigned*)(L + LDS_TAG))
  auto blank = [&](int it, unsigned ly) {
    const int lane = lane_id(), sp = sopq(sp0), c = sopq(c0), w = it & 3, pb = it >= 34;
    const unsigned o = (unsigned)vopq(-1);
    if (it >= 16 && it < 24) st16(rs, (v4u){o, o, o, o}, ((lane >> 4) & 1) * 16384 + (lane >> 5) * 256 + (lane & 15) * 16, ly + L_O + (8 * sp + (it & 7)) * 32768 + c * 512);
    else if (it >= 24 && it < 28) st16(rs, (v4u){o, o, o, o}, (lane >> 4) * 512 + (lane & 15) * 16, ly + L_X1 + ((8 * sp + 4 + 2 * (c & 1) + (w >> 1)) * 32 + (c & ~1)) * 1024 + (w & 1) * 256);
    else if (it == 28 || it == 33 || it == 38) st8(rs, (v2u){o, o}, (lane >> 5) * 4096 + (lane & 31) * 8, ly + (it == 28 ? L_X1 : L_X) + MB2 + (8 * sp + (it == 33 ? 0 : 4) + 2 * (c & 1)) * 4096 + (c >> 1) * 256);
    else if (it >= 40) {
      const int sh = it < 48 ? 6 : it < 52 ? 5 : 4;
      st16(rs, (v4u){o, o, o, o}, __mul24(lane >> sh, 57344) + (lane & ((1 << sh) - 1)) * 16,
           ly + L_H + (16 * sp + (it < 48 ? it & 7 : it < 52 ? 2 * (it & 3) : 4 * (it & 1))) * 57344 + (it < 48 ? c * 1024 : it < 52 ? 32768 + c * 512 : 49152 + c * 256));
    } else st16(rs, (v4u){o, o, o, o}, lane * 16, ly + (it < 16 ? (it & 8 ? L_V : L_K) + ((8 * sp + (it & 7)) * 32 + c) * 1024
                                                  : L_X + ((16 * sp + 8 * pb + 4 * (c & 1) + w) * 16 + (c >> 1)) * 1024));
  };
  auto attention = [&](int layer, unsigned lay0, const APre& ap) {
    COORDS();
    const int tl = compute ? 4 + w : w, T = 8 * sp + tl;
    unsigned pk;
    if (compute) pk = __builtin_amdgcn_raw_buffer_load_b32(rs, (lane & 31) * 128, layer ? NONE : (FL_HA + sp * 32) * 128, AUX_SC1);
    const v4u o = attn_unit(L, lane, tl, sp == 0 ? 8 - tl : 0, ap);
    st16(rs, nos4(o), (g4 & 1) * 16384 + hh * 256 + t16 * 16, lay + L_O + T * 32768 + c * 512);
    if (compute && !layer && !__all(pk == tag)) POLL(0, FL_HA + sp * 32, 32);
  };

  struct Out { unsigned ob, sb, ov, sv, po, ro; };
  auto out_at = [&](int layer, unsigned lay0, int pa) {
    COORDS();
    const int TD = 8 * sp + 4 - 4 * pa + 2 * rh + (w >> 1), SD = 2 * TD + (w & 1);
    const unsigned xn = layer ? NONE : lay + LAY_BYTES + L_X;
    return Out{layer ? SD * 16384 + n32 * 64 : xn + (SD * 16 + n32) * 1024, xn + MB2 + TD * 4096 + n32 * 256 + (w & 1) * 128,
               (unsigned)(lo + ((t16 * 1008 - g4 * 240) & -layer)), g4 ? NONE : t16 * 8,
               lay + L_P + ((2 * sp + 1 - pa) * 32 + c) * 8192 + w * 2048, lay + L_X1 + (TD * 32 + 2 * n32) * 1024 + (w & 1) * 256};
  };

  if (compute) {
    v4u xq[32];
    {
    const int layer = 0; const unsigned lay0 = LAY_OFF;
    COORDS();
    SETPRIO(2);
    const int r = lane >> 3, j = lane & 7;
    const u16* src = x + (size_t)(256 * sp + 8 * c + r) * D_ + 128 * w + 8 * j;
    v4u a[2] = {*(const v4u*)src, *(const v4u*)(src + 64)};
    if (w < 3) touch(rs, lane == 0 ? 0u : w < 2 || lane > 2 ? NONE : lane == 1 ? (FL_XA - FL_XB) * 128 : FLAG_G + (FL_WR + n32 * 16 + 2 * sp + rh - FL_XB - sp * 32 - c) * 128,
                     w == 0 ? lay + L_X + (16 * sp + n32) * 16384 : w == 1 ? lay + L_X + MB2 + 8 * sp * 4096 : (FL_XB + sp * 32 + c) * 128);
    PIN();
#pragma unroll
    for (int i = 0; i < 2; ++i) {
      a[i] = nos4(a[i]);
      st16(rs, a[i], (j >> 2) * 1024 + ((j & 3) * 16 + r) * 16, lay + L_X + (16 * sp + n32) * 16384 + (2 * w + i) * 2048 + rh * 128);
      auto quad = [](float v) {
        v += __int_as_float(__builtin_amdgcn_update_dpp(0, __float_as_int(v), 0xb1, 0xf, 0xf, true));
        return v + __int_as_float(__builtin_amdgcn_update_dpp(0, __float_as_int(v), 0x4e, 0xf, 0xf, true));
      };
      float sm = 0.f, q = 0.f;
#pragma unroll
      for (int m = 0; m < 4; ++m) sm += bflo(a[i][m]) + bfhi(a[i][m]);
      sm = quad(sm);
      const float mu = sm * (1.0f / 32.0f);
#pragma unroll
      for (int m = 0; m < 4; ++m) { const float e0 = bflo(a[i][m]) - mu, e1 = bfhi(a[i][m]) - mu; q += e0 * e0 + e1 * e1; }
      q = quad(q);
      st8(rs, (v2u){nos(__float_as_uint(sm)), __float_as_uint(q)}, (j & 3) ? NONE : (j >> 2) * 256 + r * 8,
          lay + L_X + MB2 + (8 * sp + (c >> 2)) * 4096 + (4 * w + 2 * i) * 256 + (c & 3) * 64);
    }
    DRAIN();
    BAR();
    BAR();
    GETTAG();
    const int TB = 8 * sp + 4 + w;
    static_for<8>([&](auto nc) { constexpr int n = nc.value; xq[n] = ld16(rs, lo + ((n >> 1) & 3) * 1024, lay + L_X + TB * 32768 + (n & 1) * 16384 + (n >> 3) * 4096); });
    BARX();
    }
  for (int layer = 0; layer < 2; ++layer) {
    const bool last = layer == 1;
    const unsigned lay0 = LAY_OFF + layer * LAY_BYTES;
    APre ap;
    {
      COORDS();
      const int TB = 8 * sp + 4 + w, TA = TB - 4;
      v4u xa[32];
      auto ldx = [&](auto nc, int T) {
        constexpr int n = nc.value;
        return ld16(rs, lo + ((n >> 1) & 3) * 1024, lay + L_X + (2 * T + (n & 1)) * 16384 + (n >> 3) * 4096);
      };
      f32x4 aq[2][2], ak[2][2], av[2][2], r0, r1;
      v4u mf0, mf1; float rd0, rd1;
      unsigned o0, o1, o2, o3, cm;
      const int kpos = (t16 + 16 * hh + 32 * (g4 & 1)) * 16;
      auto epi = [&](int k, int pa) {
        const int tl = pa ? w : 4 + w, st = (pa ? LDS_KL : LDS_ST) + w * 128;
        const unsigned kvb = lay + ((8 * sp + tl) * 32 + c) * 1024;
        if (k == 0) { r0 = lds4f(L, st + g4 * 16); r1 = lds4f(L, st + 64 + g4 * 16); }
        if (k == 2) { o0 = mulpk(ak[pa][0][0], rd0, ak[pa][0][1], rd0); o1 = mulpk(ak[pa][0][2], rd0, ak[pa][0][3], rd0); }
        if (k == 3) { o2 = mulpk(ak[pa][1][0], rd1, ak[pa][1][1], rd1); o3 = mulpk(ak[pa][1][2], rd1, ak[pa][1][3], rd1); }
        if (k == 4) {
          swap32(o0, o2); swap32(o1, o3);
          st16(rs, nos4((v4u){o0, o1, o2, o3}), kpos, kvb + L_K);
          *(LAS v4u*)(L + LDS_KL + (8 + tl) * 1024 + kpos) = (v4u){o0, o1, o2, o3};
        }
        if (k == 5) { o0 = mulpk(av[pa][0][0], r0[0], av[pa][0][1], r0[1]); o1 = mulpk(av[pa][0][2], r0[2], av[pa][0][3], r0[3]); }
        if (k == 6) { o2 = mulpk(av[pa][1][0], r1[0], av[pa][1][1], r1[1]); o3 = mulpk(av[pa][1][2], r1[2], av[pa][1][3], r1[3]); }
        if (k == 7) {
          swap32(o0, o2); swap32(o1, o3);
          st16(rs, nos4((v4u){o0, o1, o2, o3}), lo, kvb + L_V);
          *(LAS v4u*)(L + LDS_VL + (16 + 2 * tl + hh) * VBLK + (lane & 31) * 16) = (v4u){o0, o1, o2, o3};
        }
        if (k == 8) {
          asm volatile("s_nop 7" : "+v"(aq[pa][0]), "+v"(aq[pa][1]));
          const float s0 = rd0 * (0.25f * LOG2E), s1 = rd1 * (0.25f * LOG2E);
          o0 = mulpk(aq[pa][0][0], s0, aq[pa][0][1], s0); o1 = mulpk(aq[pa][0][2], s0, aq[pa][0][3], s0);
          o2 = mulpk(aq[pa][1][0], s1, aq[pa][1][1], s1); o3 = mulpk(aq[pa][1][2], s1, aq[pa][1][3], s1);
          swap32(o0, o2); swap32(o1, o3);
          *(LAS v4u*)(L + LDS_QL + tl * 1024 + kpos) = (v4u){o0, o1, o2, o3};
        }
      };
      pipe<102>(
          [&](auto fc) {
            constexpr int t = fc.value, u = t % 51;
            if constexpr (t == 12 || t == 28) BAR0(layer);
            if constexpr (t == 44 || t == 75) BARX();
            if constexpr (u == 44 || u == 45 || u == 50) return csfrag(L, (u == 50 ? 0 : u - 43) * 16 + t16);
            else return lds16(L, (u < 44 ? u : u - 2) * 1024 + lsw);
          },
          [&](auto fc, const v4u& wfr, auto pc) {
            constexpr int t = fc.value, part = pc.value, u = t % 51;
            constexpr int ks = (u < 36 ? u / 3 : u < 44 ? (u - 12) >> 1 : u - 34) & 15, m = u < 36 ? (u % 3 + 1) % 3 : u < 46 ? 1 + (u & 1) : 0;
            constexpr bool pa = t >= 51, cs = u == 44 || u == 45 || u == 50;
            if constexpr (t < 0) return;
            else if constexpr (part == 0) {
              if constexpr ((u < 44 && u % 12 == 0) || t == 6) {
                constexpr int n = u / 12 * 8;
                v4u* const X = pa ? xa : xq;
                if (__builtin_expect(t ? ones(cm) : missing<4>(X + n), 0)) {
                  refetch<n, n + 8, 4>(X, [&](auto i) { X[i.value] = ldx(i, pa ? TA : TB); });
                }
                PIN();
              }
              const v4u& x0 = cs ? mf0 : pa ? xa[2 * ks] : xq[2 * ks];
              const v4u& x1 = cs ? mf1 : pa ? xa[2 * ks + 1] : xq[2 * ks + 1];
              if constexpr (m == 0) { mac16<(u < 3), cs>(aq[pa][0], wfr, x0); mac16<(u < 3)>(aq[pa][1], wfr, x1); }
              if constexpr (m == 1) { mac16<(u < 3), cs>(ak[pa][0], wfr, x0); mac16<(u < 3)>(ak[pa][1], wfr, x1); }
              if constexpr (m == 2) { mac16<(u < 3), cs>(av[pa][0], x0, wfr); mac16<(u < 3)>(av[pa][1], x1, wfr); }
            } else {
              if constexpr (t < 12) { xq[8 + 2 * t] = ldx(IC<8 + 2 * t>{}, TB); xq[9 + 2 * t] = ldx(IC<9 + 2 * t>{}, TB); }
              if constexpr (u == 40) {
                const int st = (pa ? LDS_KL : LDS_ST) + w * 128;
                rd0 = lds1f(L, st + t16 * 4); rd1 = lds1f(L, st + 64 + t16 * 4);
                const int a = g4 ? LDS_TAG + 8 : st + 1024 + t16 * 4;
                mf0 = mfrag(L, a); mf1 = mfrag(L, g4 ? a : a + 64);
              }
              if constexpr (t >= 45 && t < 54) epi(t - 45, 0);
              if constexpr (t == 96) epi(0, 1);
              if constexpr (t == 97) epi(2, 1);
              if constexpr (t == 98) { epi(3, 1); epi(4, 1); }
              if constexpr (t >= 99) epi(t - 94, 1);
              static_for<16>([&](auto j) { if constexpr (xa_step(j.value) == t) { xa[2 * j.value] = ldx(IC<2 * j.value>{}, TA); xa[2 * j.value + 1] = ldx(IC<2 * j.value + 1>{}, TA); } });
              {
                constexpr int nx = u < 36 ? (u / 12 + 1) * 12 + (pa ? 51 : 0) : 51, i = t - (nx - 8);
                auto look = [&](const v4u& f, bool first) {
                  cm = umax3(first ? 0u : cm, f[0], f[2]);
                  asm volatile("" : "+v"(cm));
                };
                if constexpr (t < 12) {
                  if constexpr (t >= 2 && t < 6) look(xq[t + 2], t == 2);
                  if constexpr (t >= 6) look(xq[t + 2], t == 6);
                  if constexpr (t >= 10) look(xq[t + 4], false);
                } else if constexpr (i >= 0 && i < 8 && (u < 36 || !pa)) look((nx >= 51 ? xa : xq)[nx % 51 / 12 * 8 + i], i == 0);
              }
              if constexpr (t == 92) ap = attn_pre(lane, 4 + w, sp == 0 ? 4 - w : 0);
            }
          });
      epi(8, 1);
      BAR();
    }
    attention(layer, lay0, ap);
    {
      SETPRIO(0);
      v4u fr[92];
      {
        COORDS();
        const int TB = 8 * sp + 4 + 2 * rh + (w >> 1), SB = 2 * TB + (w & 1);
        const v4u rB = ld16(rs, lo, lay + L_X + (SB * 16 + n32) * 1024), rA = ld16(rs, lo, lay + L_X + (SB * 16 + n32) * 1024 - 8 * 16384);
        const unsigned xv = (g4 >> 1) * 1024 + (g4 & 1) * 512 + t16 * 16, sv = g4 ? NONE : t16 * 8;
        const unsigned xo = lay + L_X1 + (TB * 32 + 2 * n32) * 1024 + (w & 1) * 256, so = lay + L_X1 + MB2 + TB * 4096 + n32 * 256 + (w & 1) * 128;
        const int lswo = vopq(lsw + LDS_WO);
        v4u of[32];
        auto ldo = [&](auto i) { return ld16(rs, lo + (i.value & 3) * 1024, lay + L_O + (SB - 8 * (i.value >> 4)) * 16384 + ((i.value >> 2) & 3) * 4096); };
        while ((unsigned)__builtin_amdgcn_readfirstlane(__hip_atomic_load((LAS unsigned*)(L + LDS_TAG + 12), __ATOMIC_RELAXED, __HIP_MEMORY_SCOPE_WORKGROUP)) != 3u * layer + 3u)
          __builtin_amdgcn_s_sleep(1);
        asm volatile("" ::: "memory");
        unsigned cm;
        f32x4 a0, a1;
        Fin fin;
        pipe<64>(
            [&](auto fc) { return lds16(L, (fc.value & 31) * 1024 + lswo); },
            [&](auto fc, const v4u& wfr, auto pc) {
              constexpr int t = fc.value, part = pc.value, u = t & 31;
              if constexpr (part == 0) {
                if constexpr (u == 0 || u == 8 || u == 16) {
                  if (__builtin_expect(u ? ones(cm) : missing<4>(of + t / 2), 0)) {
                    refetch<t / 2, (t & 32) / 2 + 16, 4>(of, [&](auto i) { of[i.value] = ldo(i); });
                  }
                  PIN();
                }
                mac16<(u < 2)>(u & 1 ? a1 : a0, wfr, of[t >> 1]);
              } else {
                if constexpr (t >= 0 && u >= 4 && u < 16) {
                  const v4u& f = of[(t >> 5) * 16 + u];
                  cm = umax3(u == 4 || u == 8 ? 0u : cm, f[0], f[2]);
                  asm volatile("" : "+v"(cm));
                }
                if constexpr (t == -1) {
                  __builtin_amdgcn_s_sleep(12);
                  static_for<16>([&](auto i) { of[i.value] = ldo(i); if constexpr ((i.value & 3) == 3) PIN(); });
                }
                if constexpr (t == 63) static_for<NPG>([&](auto i) { fr[i.value] = ld16(rs, lo + (i.value & 3) * 1024, lay + L_X1 + (8 * sp + 4 + w) * 32768 + (i.value >> 2) * 4096); });
                if constexpr (t == 31 || t == 63) {
                  asm volatile("s_nop 7" : "+v"(a0), "+v"(a1));
                  static_for<8>([&](auto k) { fin.template chunk<k.value>(a0, a1, t == 31 ? rB : rA, rs, xv, xo - (t == 31 ? 0 : 4 * 32768), rs, sv, so - (t == 31 ? 0 : 4 * 4096)); });
                }
                if constexpr (t == 31) {
                  static_for<16>([&](auto i) { of[16 + i.value] = ldo(IC<16 + i.value>{}); if constexpr ((i.value & 3) == 3) PIN(); });
                }
              }
            });
      }
      SETPRIO(2);
      BAR();
      {
        COORDS();
        const int TB = 8 * sp + 4 + w;
        const int SD = 16 * sp + 8 + 4 * rh + w, kh = n32 >> 3;
        const unsigned x1b = lay + L_X1 + TB * 32768, hb = lay + L_H + SD * 57344;
        const unsigned hsv = (g4 & 1) * 57344 + hh * 256 + t16 * 16, hso = lay + L_H + TB * 114688;
        const unsigned pso = lay + L_P + ((2 * sp + 1) * 32 + (c ^ 16)) * 8192 + w * 2048;
        const Out oo = out_at(layer, lay0, 0);
        v4u pp[2], res;
        auto ldp = [&]() { pp[0] = ld16(rs, lo, oo.po); pp[1] = ld16(rs, lo + 1024, oo.po); PIN(); AGAIN(); };
        const int lsw2 = vopq(lsw + 65536), lh = (hh * 16 + t16) * 16 + (lane & 16) * 16384, lh2 = vopq(lh + 65536);
        f32x16 acc[2][4];
        f32x4 dd[2][2];
        v4u mf, hv[4];
        float k1[2], den2[2], t0, t1, p0_, p1_;
        unsigned cm;
        auto epi = [&](auto ec) {
          constexpr int job = ec.value >> 6, k = ec.value & 63, j = k >> 4, i = (k >> 2) & 3, uo = j < 3 ? 8 : 4;
          const f32x16& a = acc[job][j];
          if constexpr ((k & 3) == 0)
            asm volatile("v_mul_f32 %0, %2, %4\n\tv_mul_f32 %1, %3, %4\n\tv_exp_f32 %0, %0" : "=&v"(t0), "=&v"(t1) : "v"(a[2 * i]), "v"(a[2 * i + 1]), "v"(k1[job]));
          else if constexpr ((k & 3) == 1)
            asm volatile("v_exp_f32 %0, %0\n\tv_mul_f32 %1, %3, %5\n\tv_mul_f32 %2, %4, %6"
                : "+v"(t1), "=&v"(p0_), "=&v"(p1_) : "v"(a[2 * i]), "v"(a[2 * i + 1]), "v"(a[uo + 2 * i]), "v"(a[uo + 2 * i + 1]));
          else if constexpr ((k & 3) == 2)
            asm volatile("v_fma_f32 %0, %0, %2, %2\n\tv_fma_f32 %1, %1, %2, %2\n\tv_rcp_f32 %0, %0" : "+v"(t0), "+v"(t1) : "v"(den2[job]));
          else {
            unsigned d;
            asm volatile("v_rcp_f32 %2, %2\n\tv_mul_f32 %1, %1, %3\n\tv_mul_f32 %2, %2, %4\n\tv_cvt_pk_bf16_f32 %0, %1, %2" : "=v"(d), "+v"(t0), "+v"(t1) : "v"(p0_), "v"(p1_));
            if constexpr (job && !(i & 1)) asm volatile("v_min_u32 %0, -2, %0" : "+v"(d));
            hv[j][i] = d;
            if constexpr (k == (job ? 31 : NEP - 1)) { st16(rs, hv[0], hsv, hso - job * 4 * 114688 + c * 1024); st16(rs, hv[1], hsv + 512, hso - job * 4 * 114688 + c * 1024); }
            if constexpr (k == NEP - 1) {
              st16(rs, hv[2], hsv, hso - job * 4 * 114688 + 32768 + c * 512);
              st8(rs, (v2u){hv[3][0], hv[3][1]}, hh ? hsv - 248 : hsv, hso - job * 4 * 114688 + 49152 + c * 256);
            }
          }
        };
        auto vload = [&](auto ic) {
          constexpr int id = ic.value;
          if constexpr (id < 64) fr[id] = ld16(rs, lo + (id & 3) * 1024, x1b - (id >> 5) * 4 * 32768 + ((id & 31) >> 2) * 4096);
          else {
            constexpr int n = id - 64;
            fr[id] = ld16(rs, lo + (n & 3) * 1024, hb + (n < 16 ? kh * 16384 : n < 24 ? 16384 + kh * 8192 : 24576 + kh * 4096) + (n >> 2) * 4096);
          }
        };
        pipe<NST>(
            [&](auto fc) {
              constexpr int t = fc.value;
              if constexpr (t == 32) wait_vm<vm_between(-1, t - LA - 1)>();
              if constexpr (t == GA + 48) wait_vm<vm_between(ep_step(NEP - 1), t - LA - 1)>();
              if constexpr (t < DB) {
                constexpr GS s = gu_step(t % GA);
                if constexpr (s.cs) return csfrag(L, 48 + 32 * s.j + (s.j < 3 ? lane & 31 : t16));
                else {
                  if (s.off % 14336 == 0 && s.off >= (t < GA ? 14336 : 28672)) BARX();
                  return lds16(L, s.off < 65536 ? s.off + (s.j < 3 ? lsw : lh) : s.off - 65536 + (s.j < 3 ? lsw2 : lh2));
                }
              } else {
                constexpr int v = t - DB;
                if (v % 14 == 0 && v < 98) BARX();
                return lds16(L, v < 64 ? v * 1024 + lsw : (v < 96 ? v - 64 : v - 48) * 1024 + lsw2);
              }
            },
            [&](auto fc, const v4u& wfr, auto pc) {
              constexpr int t = fc.value, part = pc.value;
              if constexpr (t < 0) return;
              else if constexpr (part == 0) {
                if constexpr (t < DB) {
                  constexpr int job = t / GA, u = t % GA;
                  constexpr GS s = gu_step(u);
                  if constexpr (t == 0 || t == 16 || t == 32 || t == 64) {
                    constexpr int n0 = t < 64 ? t / 4 : 16, nn = t == 0 || t == 16 ? 4 : t == 32 ? 8 : 16;
                    if (__builtin_expect(t ? ones(cm) : missing<nn>(fr + n0), 0)) {
                      unsigned spins = 0;
                      do { static_for<(t < 64 ? 16 : 32) - n0>([&](auto i) { vload(IC<n0 + i.value>{}); }); AGAIN(); SETTLE(); } while (missing<nn>(fr + n0) && ++spins < DATA_CAP);
                    }
                    PIN();
                  }
                  acc[job][s.j] = mfma(wfr, s.cs ? mf : fr[32 * job + (s.kap & 31)], u < 4 ? splat16(0.f) : acc[job][s.j]);
                } else {
                  constexpr int v = t - DB;
                  mac16<(v % 56 < 2)>(dd[v / 56][v & 1], wfr, fr[64 + ((v % 56) >> 1)]);
                }
              } else {
                if constexpr (t < DB) {
                  constexpr int job = t / GA, u = t % GA;
                  if constexpr (u == 74) {
                    const int a = LDS_ST + w * 128 + (lane & 31) * 4;
                    k1[job] = lds1f(L, a); den2[job] = lds1f(L, a + 512); mf = mfrag(L, hh ? LDS_TAG + 8 : a + 1024);
                  }
                  if constexpr (u == 79) k1[job] *= -LOG2E;
                  if constexpr ((t >= 12 && t < 16) || (t >= 24 && t < 32) || (t >= 48 && t < 64)) {
                    constexpr int n = t < 16 ? t - 8 : t < 32 ? t - 16 : t - 32;
                    constexpr bool first = t == 12 || t == 24 || t == 48;
                    cm = umax3(first ? 0u : cm, fr[n][0], fr[n][2]);
                    asm volatile("" : "+v"(cm));
                  }
                }
                if constexpr (TAB.ld[t][0] >= 0) vload(IC<TAB.ld[t][0]>{});
                if constexpr (TAB.ld[t][1] >= 0) vload(IC<TAB.ld[t][1]>{});
                if constexpr (TAB.ep[t] >= 0) epi(IC<TAB.ep[t]>{});
                if constexpr (t == DB + 58 || t == DB + 59) st16(rs, pk24(dd[0][t & 1], tag), lo + (t & 1) * 1024, pso);
                if constexpr (t == DB + 84) res = ld16(rs, g4 * 512 + t16 * 16, oo.ro);
                if constexpr (t == DB + 106) ldp();
              }
            });
        SETPRIO(3);
        asm volatile("s_nop 7" : "+v"(dd[1][0]), "+v"(dd[1][1]));
        if (__builtin_expect(!__all(pp[0][3] == tag && pp[1][3] == tag), 0)) {
          unsigned spins = 0;
          do ldp(); while (!__all(pp[0][3] == tag && pp[1][3] == tag) && ++spins < DATA_CAP);
        }
        {
          const rsrc_t ro = last ? __builtin_amdgcn_make_buffer_rsrc((void*)out, 0, (int)MB2, 0x00020000) : rs;
          const f32x4 a0 = dd[1][0] + un24(pp[0]), a1 = dd[1][1] + un24(pp[1]);
          Fin fin;
          static_for<8>([&](auto k) { fin.template chunk<k.value>(a0, a1, res, ro, oo.ov, oo.ob, rs, oo.sv, oo.sb); });
        }
        static_for<8>([&](auto i) { undef(xq[i.value]); });
        if (last) {
          if (blockIdx.x == 0 && w == 0) {
            unsigned ev[4];
#pragma unroll
            for (int q = 0; q < 4; ++q) ev[q] = __builtin_amdgcn_raw_buffer_load_b32(rs, lane * 128, flo + (FL_HB + 64 * q) * 128, AUX_SC1);
            if (!__all(ev[0] == tag && ev[1] == tag && ev[2] == tag && ev[3] == tag))
              for (int q = 0; q < 4; ++q) POLL(flo, FL_HB + 64 * q, 64);
            if (lane == 0) __hip_atomic_store(fg + FL_EP * 32, tag, RLX_AGENT);
          }
          break;
        }
        SETPRIO(1);
        __builtin_amdgcn_s_sleep(16);
        static_for<8>([&](auto nc) { constexpr int n = nc.value; xq[n] = ld16(rs, lo + ((n >> 1) & 3) * 1024, lay + LAY_BYTES + L_X + TB * 32768 + (n & 1) * 16384 + (n >> 3) * 4096); });
        lds_wait(vopq(LDS_TAG + 24), 3);
        SETPRIO(2);
      }
    }
  }
  } else {
    {
    const int layer = 0; const unsigned lay0 = LAY_OFF;
    COORDS();
    SETPRIO(3);
    Retile rt;
    LAS unsigned char* tb = L + LDS_TB + w * 8192;
    Stats st;
    if (w == 0) {
      const int fi = lane >> 3, i8 = lane & 7, F = sopq(6 * sp) + (fi < 6 ? fi : 0);
      const int ks = F < 36 ? F / 3 : F < 44 ? (F - 12) >> 1 : F - 32, m = F < 36 ? (F - 3 * ks + 1) % 3 : F < 44 ? 1 + (F & 1) : 0;
      rt.load(wqkv, 3 * D_ * D_, __mul24(32 * ks + 8 * (i8 >> 1), 3 * D_) + m * D_ + 16 * c + 8 * (i8 & 1), 3 * D_, 4);
      const unsigned ep = __hip_atomic_load(fg + FL_EP * 32, RLX_AGENT);
      v4u mk[2];
#pragma unroll
      for (int k = 0; k < 8; ++k) {
        const int kq = ((2 * k) & 3) + 8 * ((2 * k) >> 2) + 4 * hh, ql = lane & 31;
        mk[k >> 2][k & 3] = (kq <= ql ? 0xffffu : 0u) | (kq + 1 <= ql ? 0xffff0000u : 0u);
      }
      *(LAS v4u*)(L + LDS_MK + lo) = mk[0];
      *(LAS v4u*)(L + LDS_MK + 1024 + lo) = mk[1];
      if (sp == 0) {
#pragma unroll
        for (int i = 0; i < 9; ++i) *(LAS v4u*)(L + LDS_VL + (i < 8 ? i * 1024 + lo : 8192 + t16 * 16)) = (v4u){0u, 0u, 0u, 0u};
      }
      const unsigned one = (sp == 0 && lane < 16) ? 0u : 0x3f803f80u;
      if (lane < 32) *(LAS v4u*)(L + LDS_VL + lane * VBLK + 512) = (v4u){one, one, one, one};
      PIN();
      rt.put(tb, fi * 1024, 0, 8 * i8, 4);
#pragma unroll
      for (int i = 0; i < 6; ++i) st16(rs, lds16(tb, i * 1024 + lo), lo + (i & 3) * 1024, WQKV_OFF + c * 49152 + (6 * sp + (i & 4)) * 1024);
      tag = __builtin_amdgcn_readfirstlane(ep) + 1u;
      if (lane < 2) *(LAS v4u*)(L + LDS_TAG + lo) = (v4u){lane ? 0u : tag, 0u, 0u, 0u};
      DRAIN();
      BAR();
      FLAG(0, FL_XB + sp * 32 + c);
      poll((gu32*)ws + (FL_XB + (lane < 32 ? sp * 32 + lane : (lane & 7) * 32 + c)) * 32, lane < 40, tmo, tag);
      st.load(rs, lane, lay + L_X + MB2 + (8 * sp + 4) * 4096);
    } else {
      const int id = 2 * sp + rh + 16 * (w - 1);
      const int t = 64 * id + lane, mo = t / 14, ch = t - __mul24(14, mo), mat = mo >> 6, o = mo & 63, c7 = ch >= 7 ? ch - 7 : ch, j = c7 >> 1, kap = o >> 1;
      const int dj = 4 * (id < 35 ? id - 28 : id - 35) + g4, kh = n32 >> 3, dn = dj < 16 ? 16 * kh + dj : dj < 24 ? 16 + 8 * kh + dj : 24 + 4 * kh + dj;
      const int slot = (lane >> 2) & 3, m4 = lane & 3, q = (id < 42 ? 4 * dn : 16 * ((id - 42) & 3) + 4 * g4) + slot;
      const int cq = q < 128 ? q >> 2 : q < 192 ? (q - 128) >> 1 : q - 192, r = q < 128 ? q & 3 : q < 192 ? 4 + (q & 1) : 6;
      __builtin_amdgcn_s_sleep(20);
      rt.load(id < 14 ? wg : id < 28 ? wu : id < 42 ? wd : wo, id < 42 ? D_ * F_ : D_ * D_,
              id < 28 ? 8 * o * F_ + 112 * n32 + 8 * ch : (id < 42 ? __mul24(56, cq) + (r < 6 ? 16 * (r >> 1) + 4 * (r & 1) : 48) : 16 * (q >> 1) + 4 * (q & 1)) * D_ + 32 * (id < 35 ? n32 ^ 8 : n32) + 8 * m4,
              id < 28 ? F_ : D_, (id < 28 || (id < 42 && r == 6)) ? 4 : 8);
      BAR();
      if (id < 28) {
        const int cell = kap < 16 ? 7 * (kap >> 1) + (j < 3 ? 3 * (kap & 1) + j : 6) : 56 + 16 * j + (j < 3 ? kap - 16 : (kap - 16) >> 1);
        const int rho = j < 3 ? 2 * mat + (c7 & 1) + 4 * (o & 1) : 4 * (kap & 1) + 2 * (o & 1) + mat;
        rt.lines(tb, lane, j < 3 ? rho : 0, WGU_OFF + (2 * n32 + (ch >= 7)) * 114688 + cell * 1024 + rho * 128);
      } else {
        rt.put(tb, g4 * 2048, 1024, 4 * m4 + 16 * slot, 0);
        asm volatile("s_waitcnt lgkmcnt(0)" ::: "memory");
#pragma unroll
        for (int i = 0; i < 8; ++i) {
          rt.in[i] = lds16(tb, i * 1024 + lo);
          rt.d[i] = (id < 42 ? WDN_OFF + n32 * 114688 + (id - 28) * 8192 : id < 46 ? WO_OFF + n32 * 32768 + (id - 42) * 8192 : NONE) + i * 1024 + lo;
        }
      }
#pragma unroll 1
      for (int it = w - 1; it < 16; it += 3) blank(it, lay);
    }
    BAR();
    GETTAG();
    if (w) {
      const int l = w - 1;
      dma<4>(rs, L + l * 4096, lo, WQKV_OFF + c * 49152 + l * 4096);
      dma<7>(rs, L + (12 + 7 * l - (l >> 1)) * 1024, lo, WQKV_OFF + c * 49152 + (12 + 7 * l - (l >> 1)) * 1024);
      wait_vm<7>();
    }
    BAR();
    if (w) { dma<6>(rs, L + (27 + 5 * w) * 1024, lo, WQKV_OFF + c * 49152 + (27 + 5 * w) * 1024);
      rt.out(rs); wait_vm<14>();
    }
    else {
      st.all(L, lane);
      PIN();
      FLAG(0, FL_XA + sp * 32 + c);
      touch(rs, (lane & 31) < 3 ? hh * FLAG_LAYER + (FL_X1A + (lane & 31) * 256 + sp * 32 + c) * 128 : NONE, 0);
    }
    }
  for (int layer = 0; layer < 2; ++layer) {
    const bool last = layer == 1;
    const unsigned lay0 = LAY_OFF + layer * LAY_BYTES;
    auto ldw = [&](auto nc, unsigned src, int cell) {
      constexpr int N = nc.value, K = (N + 2) / 3;
      const int l = sopq(w0) - 5, c1 = cell + (l * K + K > N ? N - K : l * K);
      dma<K>(rs, L + c1 * 1024, lane_id() * 16, src + c1 * 1024);
    };
    auto ld_gu = [&](int s) { ldw(IC<14>{}, WGU_OFF + sopq(c0) * 114688, 14 * s); };
    APre ap;
    v4u f[16];
    static_for<16>([&](auto i) { undef(f[i.value]); });
    v4u g[16];
    auto ldg = [&]() {
      COORDS();
      static_for<16>([&](auto i) { undef(g[i.value]); });
      static_for<2>([&](auto hc) {
        constexpr int h = hc.value;
        if (!last && (h ? w == 2 : w != 0)) {
#pragma unroll
          for (int i = 8 * h; i < 8 * h + 8; ++i) g[i] = ld16(rs, (w < 3 ? lsw : lo) + (i & 3) * 1024, WGU_OFF + c * 114688 + (w == 1 ? 80 : 56 + 16 * w) * 1024 + (i >> 2) * 4096);
        }
      });
      PIN();
    };
    {
      COORDS();
      const bool loader = w != 0;
      auto cs_qkv = [&]() {
        const unsigned one = (unsigned)vopq(0x3f803f80);
        const v4u ones = (v4u){one, one, one, one};
        f32x4 a = zero4();
        const int fa = (w - 1) * 1024 + lsw, fb = (w < 3 ? 35 + w : 44) * 1024 + lsw, fs = w < 3 ? 2048 : 1024;
#pragma unroll
        for (int ks = 0; ks < 16; ++ks) asm volatile("ds_read_b128 %0, %1" : "=v"(f[ks]) : "v"(ks < 12 ? fa + ks * 3072 : fb + (ks - 12) * fs) : "memory");
        asm volatile("s_waitcnt lgkmcnt(0)" : "+v"(f[0]), "+v"(f[1]), "+v"(f[2]), "+v"(f[3]), "+v"(f[4]), "+v"(f[5]), "+v"(f[6]), "+v"(f[7]));
        asm volatile("" : "+v"(f[8]), "+v"(f[9]), "+v"(f[10]), "+v"(f[11]), "+v"(f[12]), "+v"(f[13]), "+v"(f[14]), "+v"(f[15]));
#pragma unroll
        for (int ks = 0; ks < 16; ++ks) a = mfma16(f[ks], ones, a);
        if (t16 == 0) {
#pragma unroll
          for (int r = 0; r < 4; ++r) cs_put(L, (w % 3) * 16 + 4 * g4 + r, a[r]);
        }
      };
      const unsigned wob = WO_OFF + n32 * 32768 - LDS_WO;
      if (layer == 0) {
        BAR();
        if (loader) wait_vm<8>();
        BAR();
        if (loader) { cs_qkv(); DRAIN(); }
        BAR();
        if (!loader) FLAG(FLAG_G, FL_WR + n32 * 16 + 2 * sp + rh);
        else {
#pragma unroll 1
          for (int it = 15 + w; it < 29; it += 3) blank(it, lay);
          DRAIN();
        }
      } else {
        BAR();
        if (loader) ldw(IC<16>{}, wob, 80);
      }
      {
        v4u kv[4];
        auto ldkv = [&]() {
          const unsigned so = sp ? lay + ((8 * sp - 5 + w) * 32 + c) * 1024 : NONE, s2 = w == 1 ? so + 3 * 32768 : NONE;
          kv[0] = ld16(rs, lo, so + L_K); kv[1] = ld16(rs, lo, so + L_V); kv[2] = ld16(rs, lo, s2 + L_K); kv[3] = ld16(rs, lo, s2 + L_V);
          PIN();
          AGAIN();
        };
        if (!loader) {
          Stats st;
          const unsigned bl = lane < 16 ? FLAG_G + (FL_WR + n32 * 16 + lane) * 128 : (FL_XA + (sp ? sp - 1 : 0) * 32 + c) * 128;
          unsigned spins = 0;
          do { st.load(rs, lane, lay + L_X + MB2 + 8 * sp * 4096); AGAIN(); } while (missing<16>((const v4u*)st.p) && ++spins < DATA_CAP);
          const unsigned pv = __builtin_amdgcn_raw_buffer_load_b32(rs, bl, last ? NONE : 0u, AUX_SC1);
          PIN();
          st.all(L, lane, LDS_KL);
          if (!last && !__all(pv == tag)) poll((gu32*)(ws + bl), lane <= 16, tmo, tag);
        }
        BAR();
        SETTLE();
        if (!loader && !last) FLAG(0, FL_HA + sp * 32 + c);
        if (loader) {
          ldkv();
          ldw(IC<32>{}, last ? WGU_OFF + c * 114688 : wob, last ? 48 : 80);
          if (__builtin_expect(missing<4>(kv), 0)) {
            unsigned spins = 0;
            do ldkv(); while (missing<4>(kv) && ++spins < DATA_CAP);
          }
          const int va = LDS_VL + hh * VBLK + (lane & 31) * 16;
          asm volatile("ds_write_b128 %0, %1\n\tds_write_b128 %2, %3" ::"v"(LDS_KL + (3 + w) * 1024 + lo), "v"(kv[0]), "v"(va + (6 + 2 * w) * VBLK), "v"(kv[1]) : "memory");
          if (w == 1) asm volatile("ds_write_b128 %0, %1\n\tds_write_b128 %2, %3" ::"v"(LDS_KL + 7 * 1024 + lo), "v"(kv[2]), "v"(va + 14 * VBLK), "v"(kv[3]) : "memory");
          if (!last) ldw(IC<32>{}, WGU_OFF + c * 114688, 48);
        }
      }
      BAR();
      if (loader) {
        ld_gu(0); ld_gu(1); ld_gu(2); ldw(IC<6>{}, WGU_OFF + c * 114688, 42);
        wait_vm<28>();
        if (lane == 0) asm volatile("ds_add_u32 %0, %1" ::"v"(LDS_TAG + 12), "v"(1) : "memory");
      }
      ap = attn_pre(lane, w, sp == 0 ? 8 - w : 0);
      if (sp > 0) {
        LAS unsigned* hw = (LAS unsigned*)(L + LDS_TAG + 4);
        if (!loader) {
          v4u kv[8];
          unsigned spins = 0;
          __builtin_amdgcn_s_sleep(4);
          do {
#pragma unroll
            for (int t = 0; t < 4; ++t) {
              const unsigned so = lay + ((8 * sp - 8 + t) * 32 + c) * 1024;
              kv[t] = ld16(rs, lo, so + L_K); kv[4 + t] = ld16(rs, lo, so + L_V);
            }
            PIN();
            AGAIN();
          } while (missing<8>(kv) && ++spins < DATA_CAP);
#pragma unroll
          for (int t = 0; t < 4; ++t) {
            *(LAS v4u*)(L + LDS_KL + t * 1024 + lo) = kv[t];
            *(LAS v4u*)(L + LDS_VL + (2 * t + hh) * VBLK + (lane & 31) * 16) = kv[4 + t];
          }
          asm volatile("s_waitcnt lgkmcnt(0)" ::: "memory");
          __hip_atomic_store(hw, (unsigned)layer + 1u, __ATOMIC_RELAXED, __HIP_MEMORY_SCOPE_WORKGROUP);
        } else {
          unsigned v;
          do {
            __builtin_amdgcn_s_sleep(1);
            asm volatile("ds_read_b32 %0, %1\n\ts_waitcnt lgkmcnt(0)" : "=v"(v) : "v"(LDS_TAG + 4) : "memory");
          } while ((unsigned)__builtin_amdgcn_readfirstlane(v) != (unsigned)layer + 1u);
        }
      }
      if (layer == 0 && !loader) touch(rs, LAY_OFF + L_O + lane * 1048576u + c * 4096 + sp * 128, 0);
      ldg();
      SETPRIO(1);
    }
    attention(layer, lay0, ap);
    {
      COORDS();
      const bool poller = w == 0, loader = w != 0;
      const unsigned wb = WGU_OFF + c * 114688, wdn = WDN_OFF + n32 * 114688;
      unsigned pv = 0;
#define PEEK(blk, line) pv = __hip_atomic_load((gu32*)(ws + (blk)) + ((line) + (lane & 31)) * 32, RLX_AGENT)
#define GATE(blk, line) do { if (!__all(pv == tag)) POLL(blk, line, 32); } while (0)
      f32x4 ca = zero4();
      if (!last) {
        const unsigned one = (unsigned)vopq((w == 3 || t16 == (g4 & 1)) ? 0x3f803f80 : 0);
        const int fo = w < 3 ? lsw : lo;
        unsigned v;
        if (loader) wait_vm<1>();
        if (loader && lane == 0) asm volatile("ds_add_u32 %0, %1" ::"v"(LDS_TAG + 16), "v"(1) : "memory");
        do asm volatile("ds_read_b32 %0, %1\n\ts_waitcnt lgkmcnt(0)" : "=v"(v) : "v"(LDS_TAG + 16) : "memory");
        while ((unsigned)__builtin_amdgcn_readfirstlane(v) != 3u);
        for (int q = 0; q < 4 - w; ++q) {
          const int cell = w == 3 ? 6 : q < 2 ? 28 * q + w : 40 + 16 * w + 8 * q, n2 = w == 3 ? 28 : q < 2 ? 3 : 1;
          const int a = cell * 1024 + fo, d2 = n2 == 1 ? 2048 : 7168;
          v4u e[8];
#pragma unroll
          for (int i = 0; i < 8; ++i) asm volatile("ds_read_b128 %0, %1" : "=v"(e[i]) : "v"(a + (i >> 1) * d2 + (i & 1) * n2 * 1024) : "memory");
          asm volatile("s_waitcnt lgkmcnt(0)" : "+v"(e[0]), "+v"(e[1]), "+v"(e[2]), "+v"(e[3]), "+v"(e[4]), "+v"(e[5]), "+v"(e[6]), "+v"(e[7]));
#pragma unroll
          for (int i = 0; i < 8; ++i) ca = mfma16(e[i], (v4u){one, one, one, one}, ca);
        }
        if (loader) {
#pragma unroll
          for (int i = 0; i < 8; ++i) ca = mfma16(g[i], (v4u){one, one, one, one}, ca);
        }
        if (w == 2) {
#pragma unroll
          for (int i = 8; i < 16; ++i) ca = mfma16(g[i], (v4u){one, one, one, one}, ca);
        }
        if (t16 < (w < 3 ? 2 : 1)) {
#pragma unroll
          for (int r = 0; r < 4; ++r) cs_put(L, 48 + 32 * w + 16 * t16 + 4 * g4 + r, ca[r]);
        }
      }
      SETPRIO(3);
      if (poller) {
        Stats st;
        SETPRIO(1);
        BAR();
        static_for<7>([&](auto nc) {
          constexpr int n = nc.value + 1;
          if (n <= 2) st.load(rs, lane, lay + L_X1 + MB2 + (8 * sp + 4) * 4096, 8 * n - 8, 8 * n);
          if (n == 3) FLAG(flo, FL_X1A + sp * 32 + c);
          if (n == 3) st.part1();
          if (n == 4) {
            if (missing<16>((const v4u*)st.p)) {
              unsigned spins = 0;
              do { st.load(rs, lane, lay + L_X1 + MB2 + (8 * sp + 4) * 4096); AGAIN(); } while (missing<16>((const v4u*)st.p) && ++spins < DATA_CAP);
              st.part1();
            }
            st.part2();
          }
          if (n == 5) st.part3(L, lane);
          if (n == 6) PEEK(flo, FL_X1A + sp * 32);
          if (n == 7) GATE(flo, FL_X1A + sp * 32);
          BAR();
        });
        st.load(rs, lane, lay + L_X1 + MB2 + 8 * sp * 4096);
        st.all(L, lane);
        static_for<6>([&](auto nc) {
          constexpr int n = nc.value + 2;
          if (n == 4) FLAG(flo, FL_HB + sp * 32 + c);
          if (n == 6) PEEK(flo, FL_HB + sp * 32);
          if (n == 7) GATE(flo, FL_HB + sp * 32);
          BAR();
        });
      } else {
        DRAIN();
        BAR();
        static_for<7>([&](auto nc) {
          constexpr int n = nc.value + 1;
          if (n >= 3) blank(30 + 3 * n + w, 30 + 3 * n + w > 53 ? NONE : lay);
          if (!last) {
            blank(6 * n - 7 + w, LAY_OFF + LAY_BYTES);
            if (n < 7) blank(6 * n - 4 + w, LAY_OFF + LAY_BYTES);
          }
          if (n <= 3) ldw(IC<9>{}, wb, 71 + 9 * n);
          if (n == 4) ldw(IC<5>{}, wb, 107);
          if (n == 5) ldw(IC<16>{}, wdn - 16384, 112);
          if (n >= 4 && n <= 6) { if (last) wait_vm<(n == 4 ? 10 : n == 5 ? 14 : 8)>(); else wait_vm<(n == 4 ? 16 : n == 5 ? 20 : 12)>(); }
          BAR();
        });
        static_for<6>([&](auto nc) {
          constexpr int n = nc.value + 2;
          if (n == 3) wait_vm<0>();
          if (n > 3) ldw(IC<(n < 7 ? 21 : 16)>{}, wdn, 21 * n - 84);
          BAR();
        });
      }
    }
    {
      COORDS();
      const bool poller = w == 0, loader = w != 0;
      const unsigned wdn = WDN_OFF + n32 * 114688;
      const int kh = n32 >> 3, lsw2 = vopq(lsw + 65536);
      const unsigned hb = lay + L_H + (16 * sp + 4 * rh + w) * 57344, pso = lay + L_P + (2 * sp * 32 + (c ^ 16)) * 8192 + w * 2048;
      const int h2l = FL_HB + (sp ? sp - 1 : 0) * 32 + c;
      const Out oo = out_at(layer, lay0, 1);
      v4u ha[28], res;
      unsigned pv, cm;
      f32x4 dd[2][2];
      auto ldh = [&](auto nc) {
        constexpr int n = nc.value;
        ha[n] = ld16(rs, lo + (n & 3) * 1024, hb + (n < 16 ? kh * 16384 : n < 24 ? 16384 + kh * 8192 : 24576 + kh * 4096) + (n >> 2) * 4096);
      };
      SETPRIO(3);
      static_for<7>([&](auto nc) {
        constexpr int n = nc.value;
        static_for<hg_end(n) - hg_end(n - 1)>([&](auto i) { ldh(IC<hg_end(n - 1) + i.value>{}); });
        if (n == 4) pv = __builtin_amdgcn_raw_buffer_load_b32(rs, 0, poller ? flo + h2l * 128 : NONE, AUX_SC1);
        PIN();
        if (loader) {
          if (n == 0) ldw(IC<15>{}, wdn, 79);
          if (n == 1) ldw(IC<2>{}, wdn, 94);
          if (n == 2 && !last) ldw(IC<16>{}, WO_OFF + n32 * 32768 - LDS_WO, 96);
          constexpr int nw = (n == 0 ? 25 : n < 3 ? 19 : n == 3 ? 12 : n == 4 ? 6 : n == 5 ? 1 : 0) + hg_end(n) + (n >= 4);
          if (n < 2 || last) wait_vm<nw>(); else wait_vm<nw + 6>();
        }
        BAR();
      });
      static_for<28 - hg_end(6)>([&](auto i) { ldh(IC<hg_end(6) + i.value>{}); });
      res = ld16(rs, g4 * 512 + t16 * 16, oo.ro);
      v4u pp[2];
      auto ldp = [&]() { pp[0] = ld16(rs, lo, oo.po); pp[1] = ld16(rs, lo + 1024, oo.po); PIN(); AGAIN(); };
      SETPRIO(3);
      pipe<112>(
          [&](auto fc) {
            constexpr int v = fc.value;
            if (v == 54) lds_add(LDS_TAG + 20);
            return lds16(L, v < 64 ? v * 1024 + lsw : (v < 96 ? v - 64 : v - 48) * 1024 + lsw2);
          },
          [&](auto fc, const v4u& wfr, auto pc) {
            constexpr int v = fc.value, part = pc.value;
            if constexpr (v < 0) return;
            else if constexpr (part == 0) {
              if constexpr (v == 0 || v == 8 || v == 16 || v == 32 || v == 44) {
                if (__builtin_expect(v ? ones(cm) : missing<4>(ha), 0)) {
                  refetch<v / 2, (v < 32 ? 16 : 28), 2>(ha, ldh);
                }
                PIN();
              }
              mac16<(v % 56 < 2)>(dd[v / 56][v & 1], wfr, ha[(v % 56) >> 1]);
            } else {
              if constexpr ((v >= 4 && v < 16) || (v >= 26 && v < 32) || (v >= 38 && v < 44)) {
                constexpr int n = v < 16 ? v : v < 32 ? v - 10 : v - 16;
                constexpr bool first = v == 4 || v == 8 || v == 26 || v == 38;
                cm = umax3(first ? 0u : cm, ha[n][0], ha[n][2]);
                asm volatile("" : "+v"(cm));
              }
              if constexpr (v == 58 || v == 59) st16(rs, pk24(dd[0][v & 1], tag), lo + (v & 1) * 1024, pso);
              if constexpr (v == 60) {
                if (loader && !last) {
                  const int fa = (w - 1) * 1024 + lsw, fb = (w < 3 ? 35 + w : 44) * 1024 + lsw, fs = w < 3 ? 2048 : 1024;
                  lds_wait(vopq(LDS_TAG + 20), 4);
#pragma unroll
                  for (int ks = 0; ks < 16; ++ks) *(LAS v4u*)(L + (ks < 12 ? fa + ks * 3072 : fb + (ks - 12) * fs)) = f[ks];
                  lds_add(LDS_TAG + 24);
                }
              }
            }
          });
      asm volatile("s_nop 7" : "+v"(dd[1][0]), "+v"(dd[1][1]));
      auto tail = [&](auto pc) {
        constexpr bool P = pc.value;
        const rsrc_t ro = last ? __builtin_amdgcn_make_buffer_rsrc((void*)out, 0, (int)MB2, 0x00020000) : rs;
        const unsigned sn = last ? NONE : lay + LAY_BYTES + L_X + MB2 + (8 * sp + 4) * 4096;
        Stats st;
        ldp();
        if (P) st.load(rs, lane, sn);
        if (__builtin_expect(!__all(pp[0][3] == tag && pp[1][3] == tag), 0)) {
          unsigned spins = 0;
          do ldp(); while (!__all(pp[0][3] == tag && pp[1][3] == tag) && ++spins < DATA_CAP);
        }
        const f32x4 a0 = dd[1][0] + un24(pp[0]), a1 = dd[1][1] + un24(pp[1]);
        Fin fin;
        static_for<8>([&](auto k) { fin.template chunk<k.value>(a0, a1, res, ro, oo.ov, oo.ob, rs, oo.sv, oo.sb); });
        if (P && !last) {
          if (!__all(pv == tag)) poll(fl + h2l * 32, true, tmo, tag);
          if (__builtin_expect(missing<16>((const v4u*)st.p), 0)) {
            unsigned spins = 0;
            do { st.load(rs, lane, sn); AGAIN(); } while (missing<16>((const v4u*)st.p) && ++spins < DATA_CAP);
          }
          st.all(L, lane);
        }
      };
      if (poller) tail(IC<1>{}); else tail(IC<0>{});
    }
  }
  }
}

}

extern "C" void kernel_launch(void* const* d_in, const int* in_sizes, int n_in, void* d_out, int out_size, void* d_ws, size_t ws_size,
                              hipStream_t stream) {
  (void)in_sizes; (void)n_in; (void)out_size; (void)ws_size;
  (void)hipFuncSetAttribute((const void*)k_main, hipFuncAttributeMaxDynamicSharedMemorySize, LDS_BYTES);
  k_main<<<256, 512, LDS_BYTES, stream>>>((const u16*)d_in[0], (const u16*)d_in[1], (const u16*)d_in[2], (const u16*)d_in[4], (const u16*)d_in[5],
                                          (const u16*)d_in[6], (unsigned char*)d_ws, (u16*)d_out);
}
```
